```python
import torch

def _softmax(x, axis=-1):
    return torch.softmax(x, dim=axis)

def _gelu(x):
    return torch.nn.functional.gelu(x, approximate='tanh')

def _silu(x):
    return torch.nn.functional.silu(x)

def _sigmoid(x):
    return torch.sigmoid(x)

def _layernorm(x, axis=-1, eps=1e-05):
    m = x.mean(dim=axis, keepdim=True)
    v = x.var(dim=axis, keepdim=True, unbiased=False)
    return (x - m) / torch.sqrt(v + eps)

def _scatter_add(tgt, idx, src, axis=0):
    return tgt.clone().index_add_(axis, idx, src)

def _segment_sum(src, seg, n):
    out = torch.zeros((n,) + src.shape[1:], dtype=src.dtype)
    return out.index_add_(0, seg, src)

def _pad(x, pw):
    flat = tuple((v for pair in reversed(pw) for v in pair))
    return torch.nn.functional.pad(x, flat)
kernel_name = 'adaLN_cond__ssm_conv_h3__mixer_s_mlp__prompt'

def setup_inputs():

    _w = lambda a: (lambda t: t.to(torch.bfloat16) if t.is_floating_point() else t)(torch.as_tensor(a))

    def _A_setup():
        B, S, D = (1, 2048, 640)
        ks = 4
        return {'q': _w((lambda *s: torch.randn(*s, dtype=torch.bfloat16) * 0.05)(B, S, D)), 'k': _w((lambda *s: torch.randn(*s, dtype=torch.bfloat16) * 0.8)(B, S, D)), 'v': _w((lambda *s: torch.randn(*s, dtype=torch.bfloat16) * 0.8)(B, S, D)), 'shift_k': _w((lambda *s: torch.randn(*s, dtype=torch.bfloat16) * 0.8)(D, ks) * 0.5), 'ssm_k': _w((lambda *s: torch.randn(*s, dtype=torch.bfloat16) * 0.8)(D, S) * (1.0 / S ** 0.5)), 'ssm_D': _w((lambda *s: torch.randn(*s, dtype=torch.bfloat16) * 0.8)(D))}
    _A_in = _A_setup()

    def _B_setup():
        B, S, D, F = (1, 2048, 640, 1728)
        return {'x': _w((lambda *s: torch.randn(*s, dtype=torch.bfloat16) * 0.05)(B, S, D)), 'w1': _w((lambda *s: torch.randn(*s, dtype=torch.bfloat16) * 3.2)(D, D) * D ** (-0.5)), 'w2': _w((lambda *s: torch.randn(*s, dtype=torch.bfloat16) * 3.2)(D, D) * D ** (-0.5)), 'w3': _w((lambda *s: torch.randn(*s, dtype=torch.bfloat16) * 3.2)(D, F) * D ** (-0.5)), 'w4': _w((lambda *s: torch.randn(*s, dtype=torch.bfloat16) * 3.2)(F, D) * F ** (-0.5))}
    _B_in = _B_setup()
    return {'A_q': _A_in['q'], 'A_k': _A_in['k'], 'A_v': _A_in['v'], 'A_shift_k': _A_in['shift_k'], 'A_ssm_k': _A_in['ssm_k'], 'A_ssm_D': _A_in['ssm_D'], 'B_x': _B_in['x'], 'B_w1': _B_in['w1'], 'B_w2': _B_in['w2'], 'B_w3': _B_in['w3'], 'B_w4': _B_in['w4'], 'c': _w((lambda *s: torch.randn(*s, dtype=torch.bfloat16) * 0.1)(1, 640) * 8.0), 'w_mod': _w((lambda *s: torch.randn(*s, dtype=torch.bfloat16) * 0.1)(640, 6 * 640) * 640 ** (-0.5) * 8.0)}

def reference(A_q, A_k, A_v, A_shift_k, A_ssm_k, A_ssm_D, B_x, B_w1, B_w2, B_w3, B_w4, c, w_mod):

    def _A_ref(q, k, v, shift_k, ssm_k, ssm_D):
        B, S, D = q.shape
        ks = shift_k.shape[-1]
        k_pad = _pad(k, ((0, 0), (ks - 1, 0), (0, 0)))
        k_shift = torch.zeros_like(k)
        for t in range(ks):
            k_shift = k_shift + k_pad[:, t:t + S, :] * shift_k[:, ks - 1 - t][None, None, :]
        kv = k_shift * v
        L = 2 * S
        kv_pad = _pad(kv, ((0, 0), (0, S), (0, 0)))
        kf_pad = _pad(ssm_k, ((0, 0), (0, S)))
        kv_f = (lambda x, axes=(-2, -1): torch.fft.rfft2(x.float(), dim=axes))(kv_pad[:, None, :, :], axes=(1, 2))
        kf_f = (lambda x, axes=(-2, -1): torch.fft.rfft2(x.float(), dim=axes))(kf_pad[:, None, :], axes=(1, 2))
        kf_f = (lambda x, *d: x.permute(*d))(kf_f, 1, 2, 0)
        y_f = kv_f * kf_f[None, :, :, :]
        y = (lambda x, s, axes=(-2, -1): torch.fft.irfft2(x, s=s, dim=axes).to(torch.bfloat16))(y_f, (1, L), axes=(1, 2))[:, 0, :S, :]
        y = y + kv * ssm_D[None, None, :]
        return q * y

    def _B_ref(x, w1, w2, w3, w4):
        B, S, D = x.shape
        h = _gelu(_layernorm(x, axis=-1) @ w1)
        c = D // 4
        g0 = _pad(h[:, :, 0 * c:1 * c], ((0, 0), (1, 0), (0, 0)))[:, :S, :]
        g1 = _pad(h[:, :, 1 * c:2 * c], ((0, 0), (0, 1), (0, 0)))[:, 1:, :]
        g2 = _pad(h[:, :, 2 * c:3 * c], ((0, 0), (2, 0), (0, 0)))[:, :S, :]
        g3 = _pad(h[:, :, 3 * c:], ((0, 0), (0, 2), (0, 0)))[:, 2:, :]
        hs = torch.concatenate([g0, g1, g2, g3], axis=-1) @ w2
        x1 = x + hs
        h2 = _gelu(_layernorm(x1, axis=-1) @ w3) @ w4
        return x1 + h2
    x = A_q
    mod = (_silu(c) @ w_mod).reshape(c.shape[0], 6, 1, -1)
    sa, ca, ga, sb, cb, gb = (mod[:, 0].clone(), mod[:, 1].clone(), mod[:, 2].clone(), mod[:, 3].clone(), mod[:, 4].clone(), mod[:, 5].clone())
    h = _layernorm(x, axis=-1) * (1 + ca) + sa
    x = x + ga * _A_ref(h, A_k, A_v, A_shift_k, A_ssm_k, A_ssm_D)
    h = _layernorm(x, axis=-1) * (1 + cb) + sb
    x = x + gb * _B_ref(h, B_w1, B_w2, B_w3, B_w4)
    return x
```

```cpp
#define RTE 7
#include <hip/hip_runtime.h>
#include <hip/hip_bf16.h>
#include <stdint.h>

typedef __hip_bfloat16 bf16;
typedef __attribute__((ext_vector_type(8))) short bf16x8;
typedef __attribute__((ext_vector_type(4))) short s16x4;
typedef __attribute__((ext_vector_type(4))) float f32x4;
typedef __attribute__((ext_vector_type(2))) float f32x2;
typedef __attribute__((ext_vector_type(4))) unsigned u32x4;
typedef __attribute__((ext_vector_type(2))) unsigned u32x2;
#define LAS __attribute__((address_space(3)))

static constexpr int S_ = 2048;
static constexpr int D_ = 640;
static constexpr int F_ = 1728;
static constexpr unsigned SENT = 0xffffffffu;
static constexpr int RS3 = 0x00020000;
static constexpr unsigned OOB = 0x40000000u;

static constexpr unsigned SD2 = S_ * D_ * 2;
static constexpr unsigned DF2 = D_ * F_ * 2;
static constexpr unsigned O_WT3 = 0, O_WT4 = DF2;
static constexpr unsigned O_EP = 2 * DF2;
static constexpr unsigned O_MOD = O_EP + 256;
static constexpr unsigned O_MP = O_MOD + 16384;
static constexpr unsigned O_FL1 = O_MP + 131072;
static constexpr unsigned O_KVT = O_FL1 + 2048;
static constexpr unsigned O_HP = O_KVT, O_X1 = O_KVT;
static constexpr unsigned TS = 57344;
static constexpr unsigned O_H = O_KVT + 384 * 8192;
static constexpr unsigned O_Z = O_H, O_H1 = O_H + 20480;
static constexpr unsigned O_ST3 = O_H + 128 * TS;
static constexpr unsigned O_ST4 = O_ST3 + S_ * 8;
static constexpr unsigned O_XO = O_ST4 + S_ * 16 * 8;
static constexpr unsigned WS_USED = O_XO + SD2;
#define KVROW(c) ((c) < 384u ? O_KVT + (c) * 8192u : O_H + 40960u + (((c) - 384u) >> 1) * TS + ((c) & 1u) * 8192u)
static constexpr unsigned CODED = 0x7f7f7f7fu;
static constexpr unsigned CSH = 98u << 23;
static constexpr float PTHR = 0x1p48f, PMAX = 0x1p47f, CMUL = 0x1p98f, CMAX = 0x1p127f;
__device__ __forceinline__ float plainv(float x) { return __builtin_amdgcn_fmed3f(x, -PMAX, PMAX); }
__device__ __forceinline__ float codedv(float x) { return __builtin_copysignf(__builtin_amdgcn_fmed3f(__builtin_fabsf(x * CMUL), PTHR, CMAX), x); }

__device__ __forceinline__ float rbf(float x) { return __bfloat162float(__float2bfloat16(x)); }
__device__ __forceinline__ float lo2f(unsigned w) { return __uint_as_float(w << 16); }
__device__ __forceinline__ float hi2f(unsigned w) { return __uint_as_float(w & 0xffff0000u); }
typedef __bf16 bf16v2 __attribute__((ext_vector_type(2)));
__device__ __forceinline__ unsigned pack2(float a, float b) {
  bf16v2 r = __builtin_convertvector(f32x2{a, b}, bf16v2);
  return __builtin_bit_cast(unsigned, r);
}

static constexpr float GC1 = -2.f * 0.7978845608028654f * 1.4426950408889634f;
static constexpr float GC2 = GC1 * 0.044715f;
__device__ __forceinline__ float row16_sum(float v) {
  v += __builtin_bit_cast(float, __builtin_amdgcn_update_dpp(0, __builtin_bit_cast(int, v), 0xB1, 0xf, 0xf, true));
  v += __builtin_bit_cast(float, __builtin_amdgcn_update_dpp(0, __builtin_bit_cast(int, v), 0x4E, 0xf, 0xf, true));
  v += __builtin_bit_cast(float, __builtin_amdgcn_update_dpp(0, __builtin_bit_cast(int, v), 0x141, 0xf, 0xf, true));
  v += __builtin_bit_cast(float, __builtin_amdgcn_update_dpp(0, __builtin_bit_cast(int, v), 0x140, 0xf, 0xf, true));
  return v;
}
typedef volatile LAS unsigned* ldsw_t;
static constexpr int CV_STR = 4160;
static constexpr int CV_U = 8 * CV_STR;
static constexpr int CV_SLOT = CV_U + 4608;
static constexpr int SL_A = 46592, SL_C = SL_A + CV_SLOT, SL_B = SL_C + CV_SLOT;
static constexpr int L_KV = 0;
static constexpr int L_RT = L_KV + 24576;
static constexpr int L_MP = L_RT + 20480;
static constexpr int L_XCH = 139264;
static constexpr int L_XA = 81920, L_XB = 40960;
static constexpr int L_PB = 122880;
static constexpr int L_CTL = 160256;
static constexpr int L_MOD = 103424;
static constexpr int L_GB = L_CTL + 64;
static constexpr int L_CS3 = L_CTL + 384;
static constexpr int L_ST4 = L_CTL + 1408;
static constexpr int L_STAMP = L_CTL + 2432;
static constexpr int L_TOT = 163840;
static_assert(L_MP + 1024 <= SL_A && SL_B + CV_SLOT <= L_CTL && L_MOD >= SL_C + 16384 && L_MOD >= 102400 && L_MOD + 7680 <= SL_C + CV_U, "lds");
static constexpr int YROW = 1296;
enum { P_KV = 1, P_K, P_B, P_M, P_B2, P_U, P_C2, P_A2, P_RT, P_GA, P_ZC, P_X, P_X2, P_T, P_Z, P_RP, P_Z2, P_L1, P_XF1, P_G1, P_XF2, P_G2, P_SF, P_SA, P_S0, P_KA, P_H0, P_S1, P_KB, P_H1, P_P0 };
static constexpr unsigned SPIN_CAP = 1u << 21;
static constexpr unsigned RETRY_CAP = 1u << 15;

__device__ __forceinline__ float swap_add16(float v) {
  const unsigned u = __float_as_uint(v);
  auto r = __builtin_amdgcn_permlane16_swap(u, u, false, false);
  return __uint_as_float((unsigned)r[0]) + __uint_as_float((unsigned)r[1]);
}
__device__ __forceinline__ float swap_add32(float v) {
  const unsigned u = __float_as_uint(v);
  auto r = __builtin_amdgcn_permlane32_swap(u, u, false, false);
  return __uint_as_float((unsigned)r[0]) + __uint_as_float((unsigned)r[1]);
}
__device__ __forceinline__ float wave_sum2(float v) { return swap_add32(swap_add16(row16_sum(v))); }
__device__ __forceinline__ f32x2 up2(unsigned u) { return f32x2{lo2f(u), hi2f(u)}; }
__device__ __forceinline__ f32x2 rbf2(f32x2 v) { return up2(pack2(v[0], v[1])); }
#define FMA2 __builtin_elementwise_fma
__device__ __forceinline__ f32x2 rf2(f32x2 v, f32x2 cp, f32x2 cn) { return FMA2(v, cn, FMA2(v, cp, v)); }
__device__ __forceinline__ void ln5(const f32x2* in, unsigned* out) {
  const f32x2 s2 = ((in[0] + in[1]) + (in[2] + in[3])) + in[4];
  const float mean = wave_sum2(s2[0] + s2[1]) * (1.f / (float)D_);
  f32x2 d[5];
#pragma unroll
  for (int j = 0; j < 5; ++j) d[j] = in[j] - mean;
  const f32x2 q2 = (d[0] * d[0] + d[1] * d[1]) + (d[2] * d[2] + d[3] * d[3]) + d[4] * d[4];
  const float m = rbf(mean);
#pragma unroll
  for (int j = 0; j < 5; ++j) d[j] = rbf2(in[j] - m);
  const float q = wave_sum2(q2[0] + q2[1]);
  const float var = rbf(q * (1.f / (float)D_));
  const float den = rbf(__builtin_amdgcn_sqrtf(rbf(var + 1e-5f)));
  const float rinv = __builtin_amdgcn_rcpf(den);
#pragma unroll
  for (int j = 0; j < 5; ++j) { const f32x2 o = d[j] * rinv; out[j] = pack2(o[0], o[1]); }
}
#define PIN(x) asm volatile("" : "+v"(x))
#define STG(N, stmt) do { _Pragma("unroll") for (int i = 0; i < N; ++i) { stmt; } __builtin_amdgcn_sched_barrier(0); } while (0)
template <int N>
__device__ __forceinline__ void wave_sums(float* v) {
  STG(N, v[i] += __builtin_bit_cast(float, __builtin_amdgcn_update_dpp(0, __builtin_bit_cast(int, v[i]), 0xB1, 0xf, 0xf, true)); PIN(v[i]));
  STG(N, v[i] += __builtin_bit_cast(float, __builtin_amdgcn_update_dpp(0, __builtin_bit_cast(int, v[i]), 0x4E, 0xf, 0xf, true)); PIN(v[i]));
  STG(N, v[i] += __builtin_bit_cast(float, __builtin_amdgcn_update_dpp(0, __builtin_bit_cast(int, v[i]), 0x141, 0xf, 0xf, true)); PIN(v[i]));
  STG(N, v[i] += __builtin_bit_cast(float, __builtin_amdgcn_update_dpp(0, __builtin_bit_cast(int, v[i]), 0x140, 0xf, 0xf, true)); PIN(v[i]));
  unsigned a[N], b[N];
  STG(N, a[i] = __float_as_uint(v[i]); b[i] = a[i]; PIN(b[i]));
  STG(N, { auto r = __builtin_amdgcn_permlane16_swap(a[i], b[i], false, false); a[i] = r[0]; b[i] = r[1]; PIN(a[i]); PIN(b[i]); });
  STG(N, v[i] = __uint_as_float(a[i]) + __uint_as_float(b[i]); PIN(v[i]));
  STG(N, a[i] = __float_as_uint(v[i]); b[i] = a[i]; PIN(b[i]));
  STG(N, { auto r = __builtin_amdgcn_permlane32_swap(a[i], b[i], false, false); a[i] = r[0]; b[i] = r[1]; PIN(a[i]); PIN(b[i]); });
  STG(N, v[i] = __uint_as_float(a[i]) + __uint_as_float(b[i]); PIN(v[i]));
}
template <int N>
__device__ __forceinline__ void gelu_s(float* e, float ga_, float gb2, float gd, unsigned* pk) {
  float x[N];
  STG(N, x[i] = e[i] * e[i]; PIN(x[i]));
  STG(N, x[i] = __builtin_fmaf(x[i], gb2, ga_); PIN(x[i]));
  STG(N, x[i] *= e[i]; PIN(x[i]));
  STG(N, x[i] = __builtin_amdgcn_exp2f(x[i]); PIN(x[i]));
  STG(N, x[i] = __builtin_fmaf(x[i], gd, gd); PIN(x[i]));
  STG(N, x[i] = __builtin_amdgcn_rcpf(x[i]); PIN(x[i]));
  STG(N, e[i] *= x[i]; PIN(e[i]));
  STG(N / 4, e[4 * i + 1] = plainv(e[4 * i + 1]); PIN(e[4 * i + 1]));
  STG(N / 2, pk[i] = pack2(e[2 * i], e[2 * i + 1]); PIN(pk[i]));
}
__device__ __forceinline__ f32x4 mfma16(u32x4 a, u32x4 b, f32x4 c) {
  return __builtin_amdgcn_mfma_f32_16x16x32_bf16(__builtin_bit_cast(bf16x8, a), __builtin_bit_cast(bf16x8, b), c, 0, 0, 0);
}
template <bool FIRST> __device__ __forceinline__ void mf(f32x4& c, const u32x4& a, const u32x4& b) {
  if (FIRST) asm volatile("v_mfma_f32_16x16x32_bf16 %0, %1, %2, 0" : "=v"(c) : "v"(a), "v"(b));
  else asm volatile("v_mfma_f32_16x16x32_bf16 %0, %1, %2, %0" : "+v"(c) : "v"(a), "v"(b));
}
__device__ __forceinline__ unsigned canon(unsigned x) { return x < 0xfffffffeu ? x : 0xfffffffeu; }
__device__ __forceinline__ bool bad1(unsigned a) { return __builtin_amdgcn_ballot_w64(a == SENT) != 0ull; }
__device__ __forceinline__ float amax2(const u32x4& a) { return __builtin_amdgcn_fmed3f(__builtin_fabsf(__uint_as_float(a[0])), __builtin_fabsf(__uint_as_float(a[2])), CMAX); }
__device__ __forceinline__ float amin2(const u32x4& a) { return __builtin_amdgcn_fmed3f(__builtin_fabsf(__uint_as_float(a[0])), __builtin_fabsf(__uint_as_float(a[2])), 0.f); }
__device__ __forceinline__ float amax3(float m, const u32x4& a) { float r; asm("v_max3_f32 %0, %1, |%2|, |%3|" : "=v"(r) : "v"(m), "v"(a[0]), "v"(a[2])); return r; }
template <bool CD> __device__ __forceinline__ bool any_bad4(u32x4& a, float thr, unsigned csh, float cmx) {
  const float m = __builtin_amdgcn_fmed3f(__builtin_fabsf(__uint_as_float(a[0])), __builtin_fabsf(__uint_as_float(a[2])), CD ? 0.f : cmx);
  if (CD) { a[0] -= csh; a[2] -= csh; }
  return __builtin_amdgcn_ballot_w64(CD ? m < thr : m >= thr) != 0ull;
}
__device__ __forceinline__ u32x4 widen(unsigned x0, unsigned x1, unsigned y0, unsigned y1) {
  const auto p0 = __builtin_amdgcn_permlane16_swap(x0, y0, false, false);
  const auto p1 = __builtin_amdgcn_permlane16_swap(x1, y1, false, false);
  return u32x4{(unsigned)p0[0], (unsigned)p1[0], (unsigned)p0[1], (unsigned)p1[1]};
}
#define DMA1(M) asm volatile("s_mov_b32 m0, %3\n\ts_nop 0\n\t" "buffer_load_dwordx4 %0, %1, %2 offen " M "lds" :: "v"(voff), "s"(rs), "s"(soff), "s"(dst) : "memory")
#define DMA4(M) asm volatile("s_mov_b32 m0, %3\n\ts_nop 0\n\t" \
                   "buffer_load_dwordx4 %0, %1, %2 offen " M "lds\n\tbuffer_load_dwordx4 %0, %1, %2 offen offset:1024 " M "lds\n\t" \
                   "buffer_load_dwordx4 %0, %1, %2 offen offset:2048 " M "lds\n\tbuffer_load_dwordx4 %0, %1, %2 offen offset:3072 " M "lds" \
                   :: "v"(voff), "s"(rs), "s"(soff), "s"(dst) : "memory")
template <int N, int AUX>
__device__ __forceinline__ void dma(unsigned voff, u32x4 rs, unsigned soff, unsigned dst) {
  if (N == 4) { if (AUX == 1) DMA4("sc1 "); else if (AUX == 2) DMA4("nt "); else DMA4(""); }
  else { if (AUX == 1) DMA1("sc1 "); else if (AUX == 2) DMA1("nt "); else DMA1(""); }
}
__device__ __forceinline__ void st128(u32x4 v, __amdgpu_buffer_rsrc_t r, unsigned voff, unsigned base, int) {
  __builtin_amdgcn_raw_buffer_store_b128(v, r, voff + base, 0, 16);
}
__device__ __forceinline__ u32x4 mk_rs(const void* p, unsigned bytes) {
  const unsigned long long a = (unsigned long long)p;
  return u32x4{(unsigned)a, (unsigned)(a >> 32) & 0xffffu, bytes, (unsigned)RS3};
}

#define USLEEP 2
#define DLY0 8
#define DLY1 3
#define DLY2 0
#define DLY3 2
#define NWH 8
#define ZG 8
#define ZSLEEP 2
#define AW4 8
#define AD3 3
#define RSLEEP 1
#define PHI 2
#define CPW 4
#define CPG 128u
#define NZ2 5
#define NS2 5
#define RS0 6
#define DLY3B 0
#define MV0 7
#define H0S 6
#define H1S 3
#define PK0 2
#define PE0 1
#define PK1 2
#define PE1 1
#define P40 1
#define P41 2
#define GSA 2
#define GSB 1
#define GSC 2
#define EA0 4
#define EA1 28
#define EC0 4
#define EC1 22
#define PB1 26
#define PB2 20
#define NL1 14
#define NP1 2
#define NL2 12
#define ROT 1
#define NA1 3
#define NP0 0
#define SL1 6
#define SL2 6
#define PH1 2
#define PH2 2
#define KG 42
#define KC3 12
#define PK2 2
#define KC3B 40
#define NPK 10
#define HG 4
#define WSP 3
#define PR1 3
#define HPZ 8
#define WS3 1
#define RB1 0
#define HFI 1
#define TR 0
#define MGE 0
#define TZ 0
#define KE 0
#define TU1 200
#define TU2 260
#define TU 290
#define KL 5
#define PBE 18
#define NAF 6
#define W0U 4
#define U0 5
#define NPH 6
#define NGA 5
#define NR0 2
#define NR1 5
#define KVR 0
static_assert((NWH + NL1 + NP1) % 2 == 0 && NL2 % 2 == 0 && NWH + NL1 + NP1 + NL2 <= 40, "w3 schedule");
#define SPIN_STEP() do { ++spins; asm volatile("" : "+s"(spins)); } while (0)
#define SB() __builtin_amdgcn_sched_barrier(0)
#define FENCE() do { asm volatile("" ::: "memory"); __builtin_amdgcn_sched_barrier(0); } while (0)
#define LGKM0() do { __builtin_amdgcn_sched_barrier(0); asm volatile("s_waitcnt lgkmcnt(0)" ::: "memory"); } while (0)
#define BARRIER() do { LGKM0(); __builtin_amdgcn_s_barrier(); asm volatile("" ::: "memory"); } while (0)
#define VM0() __builtin_amdgcn_s_waitcnt(0x0F70)
#define PRIO(n) __builtin_amdgcn_s_setprio(n)
#define CHECK_A(CD, AV, i, hi, LD)                                                          \
  do {                                                                                      \
    if (__builtin_expect(any_bad4<CD>(AV, sthr, scsh, scmx), 0)) {                          \
      unsigned spins = 0;                                                                   \
      PRIO(0);                                                                              \
      do {                                                                                  \
        __builtin_amdgcn_s_sleep(RSLEEP);                                                   \
        _Pragma("unroll") for (int y_ = (i); y_ < (hi); ++y_) { LD(y_); }                   \
        SPIN_STEP();                                                                        \
      } while (any_bad4<CD>(AV, sthr, scsh, scmx) && spins < RETRY_CAP);                    \
      PRIO(PHI);                                                                            \
      VM0();                                                                                \
    }                                                                                       \
  } while (0)

#define ASM_MACROS \
  ".macro regno op\n.set r_, 0\n.set st_, 0\n.irpc c, \"\\op\"\n.ifc \\c, :\n.set st_, 2\n.endif\n.if st_ == 1\n.set r_, r_ * 10 + \\c\n.endif\n.ifc \\c, [\n.set st_, 1\n.endif\n.endr\n.endm\n" \
  ".macro retry n, cd, a, so, ct, t, vo, rs, sb, im, th, k, pr\n"                                   \
  "s_cbranch_vccnz Lc\\n\nLb\\n:\n.subsection 1\nLc\\n:\nregno \\a\n"              \
  "s_setprio 0\ns_add_u32 \\so, \\sb, \\im\ns_mov_b32 \\ct, 0\n"                                        \
  "Ll\\n:\ns_sleep 1\nbuffer_load_dwordx4 \\a, \\vo, \\rs, \\so offen sc1\ns_add_u32 \\ct, \\ct, 1\ns_waitcnt vmcnt(0)\n" \
  ".if \\cd\nv_med3_f32 \\t, |v[r_]|, |v[r_+2]|, 0\nv_cmp_gt_f32 vcc, \\th, \\t\n.else\nv_med3_f32 \\t, |v[r_]|, |v[r_+2]|, \\k\nv_cmp_le_f32 vcc, \\th, \\t\n.endif\n" \
  "s_cbranch_vccz Ld\\n\ns_cmp_lt_u32 \\ct, 0x8000\ns_cbranch_scc1 Ll\\n\n"                             \
  "Ld\\n:\n.if \\cd\nv_subrev_u32 v[r_], \\k, v[r_]\nv_subrev_u32 v[r_+2], \\k, v[r_+2]\n.endif\ns_setprio \\pr\ns_nop 1\ns_branch Lb\\n\n.subsection 0\n.endm\n" \
  ".macro need n, v, ct, ad, k\n"                                                                       \
  "v_cmp_gt_u32 vcc, \\k, \\v\ns_cbranch_vccnz Lc\\n\nLb\\n:\n.subsection 1\nLc\\n:\ns_mov_b32 \\ct, 0\n" \
  "Ll\\n:\nds_read_b32 \\v, \\ad\ns_waitcnt lgkmcnt(0)\nv_cmp_gt_u32 vcc, \\k, \\v\ns_cbranch_vccz Lb\\n\n" \
  "s_sleep 1\ns_add_u32 \\ct, \\ct, 1\ns_cmp_lt_u32 \\ct, 0x200000\ns_cbranch_scc1 Ll\\n\ns_branch Lb\\n\n.subsection 0\n.endm\n"
#define RETRY(CD, AV, BM, VOFF, SBASE, IMM, PR)                                                         \
  do {                                                                                                  \
    unsigned so_, ct_; float t_; register unsigned long long vc_ asm("vcc") = (BM);                                                                     \
    asm volatile("retry %=, %5, \"%0\", %1, %2, %3, %6, \"%7\", %8, %9, %10, %11, %12"                   \
                 : "+v"(AV), "=&s"(so_), "=&s"(ct_), "=&v"(t_), "+s"(vc_)                            \
                 : "i"((int)(CD)), "v"(VOFF), "s"(dW), "s"(SBASE), "i"(IMM), "s"(sthr), "s"((CD) ? scsh : __float_as_uint(scmx)), "i"(PR) : "scc", "memory"); \
  } while (0)
#define CMP_BAD(CD, BM, M) do { register unsigned long long vc_ asm("vcc");                             \
    if (CD) asm volatile("v_cmp_gt_f32 vcc, %1, %2" : "=s"(vc_) : "s"(sthr), "v"(M)); else asm volatile("v_cmp_le_f32 vcc, %1, %2" : "=s"(vc_) : "s"(sthr), "v"(M)); \
    BM = vc_; } while (0)
#define RETRY_C(AV, BM, VOFF, SBASE, IMM, PR) RETRY(1, AV, BM, VOFF, SBASE, IMM, PR)
#define NEED_AT(V, ADDR, K) do { unsigned ct_; asm volatile("need %=, %0, %1, %2, %3" : "+v"(V), "=&s"(ct_) : "v"(ADDR), "s"(K) : "vcc", "scc", "memory"); } while (0)

#define STAMP(i)
#define CLK(i)
#define KATTR
#define TS_(i)
#define TX_(i)
#define TSHI 62
__device__ __forceinline__ u32x4 rd_tr(const LAS char* p) {
  const s16x4 lo = __builtin_amdgcn_ds_read_tr16_b64_v4i16((LAS s16x4*)p);
  const s16x4 hi = __builtin_amdgcn_ds_read_tr16_b64_v4i16((LAS s16x4*)(p + 2560));
  const bf16x8 o = {lo[0], lo[1], lo[2], lo[3], hi[0], hi[1], hi[2], hi[3]};
  return __builtin_bit_cast(u32x4, o);
}
__device__ __forceinline__ float dpp_add(float v, int ctrl) {
  return v + __builtin_bit_cast(float, ctrl == 0 ? __builtin_amdgcn_update_dpp(0, __builtin_bit_cast(int, v), 0x128, 0xf, 0xf, true)
                                    : ctrl == 1 ? __builtin_amdgcn_update_dpp(0, __builtin_bit_cast(int, v), 0x124, 0xf, 0xf, true)
                                                : __builtin_amdgcn_update_dpp(0, __builtin_bit_cast(int, v), 0x4E, 0xf, 0xf, true));
}

template <int V> struct IC { static constexpr int v = V; };
template <int I, int N, class F> __device__ __forceinline__ void sfor(F&& f) { if constexpr (I < N) { f(IC<I>{}); sfor<I + 1, N>(f); } }
constexpr int cv_i(int m) { int i = 0; while (m >= 8 - i) { m -= 8 - i; ++i; } return i; }
constexpr int cv_d(int m) { int i = 0; while (m >= 8 - i) { m -= 8 - i; ++i; } return 7 - i - m; }
constexpr int kl_i(int m) { return m < 50 - 5 * KL ? m / 5 : m < 50 - 2 * KL ? 10 - KL + (m - 50 + 5 * KL) / 3 : 10 - KL + (m - 50 + 2 * KL) / 2; }
constexpr int kl_j(int m) { return m < 50 - 5 * KL ? m % 5 : m < 50 - 2 * KL ? (m - 50 + 5 * KL) % 3 : 3 + (m - 50 + 2 * KL) % 2; }
constexpr int cl_t(int m) { int t = 0; while (m > t) { m -= t + 1; ++t; } return t; }
constexpr int cl_i(int m) { int t = 0; while (m > t) { m -= t + 1; ++t; } return m; }
constexpr bool cv_free(int m, int k) {
  return k == 0 ? (m >= 64 && m < 111 && m != 65 && m != 69 && m != 72 && m != 75 && m != 77 && m != 94 && m != 95 && m != 104 && m != 110 && (m < 72 + RS0 || m >= 88 + RS0))
                : (m >= 123 && m != 126 && m != 131 && m != 132 && m != 139 && m != 140);
}
constexpr int cv_slot(int m, int k) { if (!cv_free(m, k)) return -1; int n = 0; for (int y = 0; y < m; ++y) n += cv_free(y, k); return n; }
static_assert(cv_slot(109, 0) >= 15 && cv_slot(143, 1) >= 15, "slots");
constexpr int cc_i(int J, int c) { for (int i = 0; i < 8; ++i) for (int t = 3; t >= 0; --t) if (i <= ((J >> (4 * t)) & 15) && c-- == 0) return i; return 0; }
constexpr int cc_t(int J, int c) { for (int i = 0; i < 8; ++i) for (int t = 3; t >= 0; --t) if (i <= ((J >> (4 * t)) & 15) && c-- == 0) return t; return 0; }
#define KWAIT
__global__ __launch_bounds__(512, 2) KATTR void k_main(const bf16* kin_, const bf16* vin_, char* ws_, const bf16* shift_k_, const bf16* ssm_k_,
                                                 const bf16* w_mod_, const bf16* cin_, const bf16* w3_,
                                                 const bf16* w4_, const bf16* q_, const bf16* w1_, const bf16* w2_,
                                                 const bf16* ssm_D_, bf16* out_) {
  typedef __attribute__((ext_vector_type(8))) unsigned long long u64x8;
  typedef __attribute__((ext_vector_type(4))) unsigned long long u64x4;
  typedef __attribute__((ext_vector_type(2))) unsigned long long u64x2;
  u64x8 ka;
  u64x4 kb;
  u64x2 kc;
  int tid_ = threadIdx.x, bx_ = blockIdx.x;
  unsigned long long pc0;
  unsigned code_bytes;
  asm volatile("s_load_dwordx16 %0, %6, 0x0\n\ts_load_dwordx8 %1, %6, 0x40\n\ts_load_dwordx4 %2, %6, 0x60\n\ts_getpc_b64 %3\n"
               "Lpc0:\n\ts_mov_b32 %4, Lcold0 - Lpc0\n" KWAIT ".subsection 1\nLcold0:\n.subsection 0"
               : "=&s"(ka), "=&s"(kb), "=&s"(kc), "=&s"(pc0), "=&s"(code_bytes), "+s"(bx_) : "s"(__builtin_amdgcn_kernarg_segment_ptr()));
  asm volatile(ASM_MACROS ".subsection 2\n.fill 256, 4, 0xbf800000\n.subsection 0" : "+v"(tid_));
  extern __shared__ __attribute__((aligned(16))) char lds_[];
  LAS char* lds = (LAS char*)lds_;
  ldsw_t LW = (ldsw_t)(lds + L_CTL);
  const int tid = tid_ & 511, lane = tid & 63;
  const int wv = __builtin_amdgcn_readfirstlane(tid >> 6) & 7;
  const int bx = bx_ & 255;
  const int mt = (bx & 7) * 4 + (bx >> 6), nt = (bx >> 3) & 7;
  const int n0 = nt * 80, m0 = mt * 64;
  const int l15 = lane & 15, g = lane >> 4;
  const int mg = wv & 3, kh = wv >> 2;
  const int gm = (wv >> 2) * 2 + (wv & 1), gk = ((wv >> 1) ^ (wv >> 2)) & 1, gp = wv ^ 2;
  const unsigned l16 = (unsigned)lane * 16u;
  TS_(0);
  STAMP(0);
  CLK(30);
  unsigned cp0 = 0;
  if (wv == CPW) {
    asm volatile("buffer_load_dword %0, %1, %2, 0 offen" : "=v"(cp0) : "v"((unsigned)((bx >> 3) + 32 * lane) * CPG), "s"(mk_rs((const void*)pc0, code_bytes)) : "memory");
  }
  FENCE();
  const int tok0 = 32 * ((bx & 7) * 8 + (bx >> 5)), ch0 = 160 * ((bx >> 3) & 3);
  unsigned ko[3], vo[3];
#pragma unroll
  for (int i = 0; i < 3; ++i) {
    const unsigned s = (unsigned)((mg + 4 * i) * 64 + lane);
    const unsigned gr = __umul24(s, 800u) >> 16, w = s - __umul24(82u, gr), rr = __umul24(w, 205u) >> 12;
    unsigned o = (unsigned)(tok0 * (D_ * 2) + ch0 * 2) + __umul24(gr, 4u * D_ * 2) + __umul24(rr, D_ * 2 - 320) + w * 16u;
    asm volatile("" : "+v"(o));
    ko[i] = (w < 80u && gr < 9u) ? o - 4u * D_ * 2 : OOB;
    vo[i] = (w < 80u && gr < 8u) ? o : OOB;
    asm volatile("" : "+v"(ko[i]), "+v"(vo[i]));
  }
  FENCE();
  asm volatile("s_waitcnt lgkmcnt(0)" : "+s"(ka), "+s"(kb), "+s"(kc));
  const bf16* kin = (const bf16*)ka[0];
  const bf16* vin = (const bf16*)ka[1];
  char* ws = (char*)ka[2];
  const bf16* shift_k = (const bf16*)ka[3];
  const bf16* ssm_k = (const bf16*)ka[4];
  const bf16* w_mod = (const bf16*)ka[5];
  const bf16* cin = (const bf16*)ka[6];
  const bf16* w3 = (const bf16*)ka[7];
  const bf16* w4 = (const bf16*)kb[0];
  const bf16* q = (const bf16*)kb[1];
  const bf16* w1 = (const bf16*)kb[2];
  const bf16* w2 = (const bf16*)kb[3];
  const bf16* ssm_D = (const bf16*)kc[0];
  bf16* out = (bf16*)kc[1];
  const __amdgpu_buffer_rsrc_t rW = __builtin_amdgcn_make_buffer_rsrc((void*)ws, 0, WS_USED, RS3);
  const u32x4 dW = mk_rs(ws, WS_USED);
  u32x2 ep;
  asm volatile("buffer_load_dwordx2 %0, off, %1, %2 sc1" : "=v"(ep) : "s"(dW), "s"(O_EP) : "memory");
  FENCE();
  if (kh == 0) {
    const u32x4 dK = mk_rs(kin, SD2), dV = mk_rs(vin, SD2);
#pragma unroll
    for (int i = 0; i < 3; ++i) dma<1, 0>(ko[i], dK, 0u, (unsigned)(L_KV + (mg + 4 * i) * 1024));
#pragma unroll
    for (int i = 0; i < 3; ++i) dma<1, 0>(vo[i], dV, 0u, (unsigned)(L_KV + 12288 + (mg + 4 * i) * 1024));
  }
  u32x4 sk[3];
  if (kh == 0) {
    asm volatile("buffer_load_dwordx4 %0, %3, %5, %6 offen\n\tbuffer_load_dwordx4 %1, %3, %5, %6 offen offset:16\n\tbuffer_load_dwordx4 %2, %4, %5, %7 offen"
                 : "=&v"(sk[0]), "=&v"(sk[1]), "=&v"(sk[2])
                 : "v"(((unsigned)lane >> 3) * 32u), "v"((unsigned)g * 16u), "s"(mk_rs(shift_k, D_ * 8)), "s"(ch0 * 8 + mg * 256), "s"(ch0 * 8 + 1024 + mg * 64) : "memory");
  }
  FENCE();
  if (tid >= 504) LW[tid - 504] = 0u;
  if (tid >= 256 && tid < 352) *(LAS u32x4*)(lds + SL_A + ((tid - 256) >> 5) * CV_SLOT + CV_U + (tid & 31) * 16) = u32x4{0u, 0u, 0u, 0u};
  if (tid >= 384 && tid < 408) *(LAS u32x4*)(lds + SL_A + ((tid - 384) >> 3) * CV_SLOT + (tid & 7) * CV_STR + 257 * 16) = u32x4{0u, 0u, 0u, 0u};
  STAMP(29);
  if constexpr (TSHI == 23) TX_(30);
  BARRIER();

  const unsigned psig = (unsigned)L_CTL + (unsigned)wv * 4u;
#define SIG(k) do { unsigned long long sv_; asm volatile("s_mov_b64 %0, exec\n\ts_mov_b64 exec, 1\n\tds_write_b32 %1, %2\n\ts_mov_b64 exec, %0" : "=&s"(sv_) : "v"(psig), "v"((unsigned)(k)) : "memory"); } while (0)
#define WAIT_AT(addr, k)                                                                                \
  do {                                                                                                  \
    unsigned tv_, tc_;                                                                                  \
    asm volatile("s_mov_b32 %1, 0\n"                                                                    \
                 "L%=_a:\n\tds_read_b32 %0, %2\n\ts_waitcnt lgkmcnt(0)\n\tv_cmp_gt_u32 vcc, %3, %0\n\ts_cbranch_vccz L%=_b\n\t"    \
                 "s_sleep 1\n\ts_add_u32 %1, %1, 1\n\ts_cmp_lt_u32 %1, 0x200000\n\ts_cbranch_scc1 L%=_a\n"            \
                 "L%=_b:" : "=&v"(tv_), "=&s"(tc_) : "v"(addr), "i"(k) : "vcc", "scc", "memory");       \
  } while (0)
#define WAIT_2(k0, k1)                                                                                 \
  do {                                                                                                  \
    unsigned tv_, tc_;                                                                                  \
    asm volatile("s_mov_b32 %1, 0\n"                                                                    \
                 "L%=_a:\n\tds_read_b32 %0, %2\n\ts_waitcnt lgkmcnt(0)\n\tv_cmp_gt_u32 vcc, %3, %0\n\ts_cbranch_vccz L%=_b\n\t"    \
                 "s_sleep 1\n\ts_add_u32 %1, %1, 1\n\ts_cmp_lt_u32 %1, 0x200000\n\ts_cbranch_scc1 L%=_a\n"            \
                 "L%=_b:" : "=&v"(tv_), "=&s"(tc_) : "v"((unsigned)L_CTL + ((unsigned)lane & 7u) * 4u), "v"((lane & 4) ? (unsigned)(k1) : (unsigned)(k0)) : "vcc", "scc", "memory"); \
  } while (0)
#define WAIT_ALL(k) WAIT_AT((unsigned)L_CTL + ((unsigned)lane & 7u) * 4u, k)
#define WAIT_HALF(h, k) WAIT_AT((unsigned)L_CTL + ((unsigned)lane & 3u) * 4u + (unsigned)(h) * 16u, k)

  const int sb3 = nt < 6 ? 14 * nt : 84 + 12 * (nt - 6);
  const bool two = kh == 0 || nt < 6;
  const int jl = kh ? 4 + wv : 2 * wv, jm = kh ? 12 + (wv & 1) : 2 * wv + 1, hb = wv > 5 ? 1 : 0;
  const int k4 = 7 * wv - (wv > 5 ? 2 : wv > 4 ? 1 : 0);
  const unsigned w4a = O_WT4 + (unsigned)(nt * 54 + k4) * 5120u;
  unsigned w3s[4] = {O_WT3 + (unsigned)(sb3 + jl) * 20480u + (unsigned)hb * 10240u, two ? O_WT3 + (unsigned)(sb3 + jm) * 20480u + (unsigned)hb * 10240u : OOB,
                     O_WT3 + (unsigned)(sb3 + jl) * 20480u - (unsigned)hb * 10240u, kh ? w4a - 10240u : O_WT3 + (unsigned)(sb3 + jm) * 20480u};
  f32x2 cp = {65536.f, 65536.f}, cn = {-65536.f, -65536.f};
  asm volatile("" : "+s"(cp), "+s"(cn));
  u32x4 W3[40];
#define LD_W3U(f) W3[f] = __builtin_amdgcn_raw_buffer_load_b128(rW, l16 + ((f) < 8 ? ((f) >> 1) * 1024 : 0), w3s[((f) & 1) + ((f) >= 20 ? 2 : 0)] + ((f) < 8 ? 0 : ((f) >> 1) * 1024), 16)
#define LD_W3R(a, b) do { _Pragma("unroll") for (int f_ = (a); f_ < (b); ++f_) LD_W3U(f_); } while (0)
#define VMW(n) asm volatile("s_waitcnt vmcnt(%0)" :: "i"(n) : "memory")
  const __amdgpu_buffer_rsrc_t rW1 = __builtin_amdgcn_make_buffer_rsrc((void*)w1, 0, D_ * D_ * 2, RS3);
  const __amdgpu_buffer_rsrc_t rW2 = __builtin_amdgcn_make_buffer_rsrc((void*)w2, 0, D_ * D_ * 2, RS3);
  const u32x4 dW1 = mk_rs(w1, D_ * D_ * 2), dW2 = mk_rs(w2, D_ * D_ * 2);
#define W12_G(gw) unsigned gw[5]; { unsigned ln_ = (unsigned)lane; asm volatile("" : "+v"(ln_)); \
    _Pragma("unroll") for (int j_ = 0; j_ < 5; ++j_) { const unsigned p_ = ln_ + 64u * j_, r_ = __umul24(p_, 6554u) >> 16; \
      gw[j_] = __umul24((r_ & 12u) * 2u + (r_ >> 4) * 4u + (r_ & 3u), D_ * 2) + (p_ - __umul24(r_, 10u)) * 16u + (unsigned)n0 * 2u; } }

  const int s0 = m0 + nt * 8;
  const int srow = s0 + wv;
  unsigned qw[5], hw[5];
  unsigned long long epn;
  unsigned tA, tB;
  u32x4 WS2[10];
#define LD_WS2U(a, b) _Pragma("unroll") for (int y_ = (a); y_ < (b); ++y_) asm volatile("buffer_load_dwordx4 %0, %1, %2, %3 offen ; ws2" : "=v"(WS2[y_]) : "v"(gw[y_ % 5]), "s"(dW2), "s"((wv + 8 * (y_ / 5)) * (32 * D_ * 2)) : "memory")
#define LD_WS2(a, b) _Pragma("unroll") for (int y_ = (a); y_ < (b); ++y_) WS2[y_] = __builtin_amdgcn_raw_buffer_load_b128(rW2, gw[y_ % 5], (wv + 8 * (y_ / 5)) * (32 * D_ * 2), 0)
  unsigned ry = (unsigned)(L_XCH + wv * YROW) + l16, rz = (unsigned)(L_XCH + wv * YROW + 1024 + 4 * lane);
  f32x2 xq[5], ha[5], ca[5], sb[5];
  float mc, mb;
  const int row = m0 + 16 * gm + l15;
  const int jb = 3 * gk;
  const unsigned ofr = (unsigned)(row >> 4) * 20480u + (unsigned)((n0 + 16 * jb + 4 * g) >> 3) * 256u + (unsigned)(g & 1) * 8u + (unsigned)l15 * 16u;
  {
    const int pi = (bx & 7) + 8 * (bx >> 4), half = (bx >> 3) & 1;
    const int chA = 2 * bx, chC = 512 + pi;
#define RD_MOD(v) do { const u32x4 b_ = *(const LAS u32x4*)(lds + L_MOD + (v) * (D_ * 2) + l16); mw[v][0] = b_[0]; mw[v][1] = b_[1]; mw[v][2] = b_[2]; mw[v][3] = b_[3]; \
                       mw[v][4] = *(const LAS unsigned*)(lds + L_MOD + (v) * (D_ * 2) + 1024 + lane * 4); } while (0)
#define LN1()                                                                                           \
  do {                                                                                                  \
    f32x2 xq_[5];                                                                                       \
    _Pragma("unroll") for (int j = 0; j < 5; ++j) xq_[j] = up2(qw[j]);                                  \
    ln5(xq_, hw);                                                                                       \
    _Pragma("unroll") for (int j = 0; j < 5; ++j) asm volatile("" : "+v"(hw[j]));                       \
  } while (0)
#define R_PRE() do {                                                                                    \
      unsigned mw[5][5];                                                                                \
      _Pragma("unroll") for (int v = 0; v < 5; ++v) RD_MOD(v);                                          \
      _Pragma("unroll") for (int j = 0; j < 5; ++j) {                                                   \
        xq[j] = up2(qw[j]);                                                                             \
        ha[j] = FMA2(up2(hw[j]), up2(mw[1][j]), up2(mw[0][j])) * up2(mw[2][j]); ca[j] = up2(mw[4][j]); sb[j] = up2(mw[3][j]); \
        asm volatile("" : "+v"(xq[j]), "+v"(ha[j]), "+v"(ca[j]), "+v"(sb[j]));                          \
      }                                                                                                 \
      const f32x2 c2_ = ((ca[0] + ca[1]) + (ca[2] + ca[3])) + ca[4], b2_ = ((sb[0] + sb[1]) + (sb[2] + sb[3])) + sb[4]; \
      mc = wave_sum2(c2_[0] + c2_[1]) * (1.f / (float)D_); mb = wave_sum2(b2_[0] + b2_[1]) * (1.f / (float)D_); \
      asm volatile("" : "+v"(mc), "+v"(mb));                                                            \
    } while (0)
    unsigned dv2;
#define GET_D() const unsigned dAB = __builtin_amdgcn_readlane(dv2, 0), dC_ = __builtin_amdgcn_readlane(dv2, 1), dC = (chC & 1) ? dC_ & 0xffff0000u : dC_ << 16
#define REQ_Q() do {                                                                                    \
      const __amdgpu_buffer_rsrc_t rQ = __builtin_amdgcn_make_buffer_rsrc((void*)q, 0, SD2, RS3);       \
      const u32x4 a = __builtin_amdgcn_raw_buffer_load_b128(rQ, l16, srow * (D_ * 2), 0);               \
      qw[0] = a[0]; qw[1] = a[1]; qw[2] = a[2]; qw[3] = a[3];                                           \
      qw[4] = __builtin_amdgcn_raw_buffer_load_b32(rQ, (unsigned)lane * 4u, srow * (D_ * 2) + 1024, 0); \
    } while (0)
#define REQ_D() dv2 = __builtin_amdgcn_raw_buffer_load_b32(__builtin_amdgcn_make_buffer_rsrc((void*)ssm_D, 0, D_ * 2, RS3), (unsigned)(lane ? (chC & ~1) : chA) * 2u, 0, 0)
#define TAGS()                                                                                          \
    {                                                                                                   \
      unsigned e0_, e1_;                                                                                \
      asm volatile("v_readfirstlane_b32 %0, %2\n\tv_readfirstlane_b32 %1, %3" : "=s"(e0_), "=s"(e1_) : "v"(ep[0]), "v"(ep[1])); \
      unsigned long long h = (unsigned long long)e1_ << 32 | e0_;                                       \
      epn = h + 1ull;                                                                                   \
      h += 0x9E3779B97F4A7C15ull;                                                                       \
      h = (h ^ (h >> 30)) * 0xBF58476D1CE4E5B9ull;                                                      \
      h = (h ^ (h >> 27)) * 0x94D049BB133111EBull;                                                      \
      h ^= h >> 31;                                                                                     \
      tA = (unsigned)h; tB = (unsigned)(h >> 32);                                                       \
    }
#define BUILD(SL, CC)                                                                                   \
  do {                                                                                                  \
    const u32x4 klo = *(const LAS u32x4*)(lds + (SL) + CV_U + 512 + 4064 - 16 * (CC)), khi = *(const LAS u32x4*)(lds + (SL) + CV_U + 512 + 4080 - 16 * (CC)); \
    const unsigned w[8] = {klo[0], klo[1], klo[2], klo[3], khi[0], khi[1], khi[2], khi[3]};             \
    _Pragma("unroll") for (int m = 0; m < 8; ++m) {                                                     \
      u32x4 o;                                                                                          \
      const int a = m >> 1;                                                                             \
      _Pragma("unroll") for (int t = 0; t < 4; ++t) {                                                   \
        if ((m & 1) == 0) o[t] = (w[a - t + 4] & 0xffffu) | (w[a - t + 3] & 0xffff0000u);               \
        else o[t] = (w[a - t + 4] >> 16) | (w[a - t + 4] << 16);                                        \
      }                                                                                                 \
      *(LAS u32x4*)(lds + (SL) + m * CV_STR + ((CC) + 1) * 16) = o;                                     \
    }                                                                                                   \
  } while (0)
#define ZCH(c) ((c) * 128u + ((c) / 160u) * (TS - 20480u))
#define ZOFF(JA, JB, CH) ((unsigned)((32 * ((g & 1) ? (JB) : (JA)) + 2 * l15 + (g >> 1)) >> 3) * (4u * TS) + ZCH((unsigned)(CH)) + (unsigned)((2 * l15 + (g >> 1)) & 7) * 16u)
#define Z2OFF(w) (O_Z + (unsigned)(4 * (w) + (l15 >> 2)) * (4u * TS) + ZCH((unsigned)chA) + (unsigned)((l15 & 3) * 64 + g * 16))
    const unsigned a00 = (unsigned)((l15 & 7) * CV_STR + (2032 - 8 * (l15 >> 3) + 8 * g) * 2) - 128u * mg;
    const unsigned b00 = (unsigned)(CV_U + 480 + 32 * l15 + 16 * g) - 128u * mg;
#define CV_RA(AV, SL, rd) _Pragma("unroll") for (int i = 0; i < 8; ++i) AV[8 * (rd) + i] = *(const LAS u32x4*)(lds + (SL) + a00 - 64 * (rd) - 512 * i)
#define CV_RB(BV, SL, rd) _Pragma("unroll") for (int i = 7; i >= 0; --i) BV[8 * (rd) + i] = *(const LAS u32x4*)(lds + (SL) + b00 + 512 * i - 64 * (rd))
    const int zja = mg ? (half ? 5 : 4) : (half ? 1 : 0), zjb = mg ? (half ? 6 : 7) : (half ? 2 : 3);
    const int t8 = tid & 255;
#define W_DMA(blk, j) dma<1, 0>(gw[j], (blk) < 20 ? dW1 : dW2, (unsigned)((blk) < 20 ? (blk) : (blk) - 4) * (32 * D_ * 2), (unsigned)((blk) * 5120 + (j) * 1024))
    if (__builtin_expect(kh != 0, 1)) {
      {
        const u32x4 dS = mk_rs(ssm_k, SD2);
        dma<1, 0>(l16, dS, (unsigned)(chC * (S_ * 2) + mg * 1024), (unsigned)(SL_C + CV_U + 512 + mg * 1024));
        dma<1, 0>(l16, dS, (unsigned)(chA * (S_ * 2) + mg * 1024), (unsigned)(SL_A + CV_U + 512 + mg * 1024));
        dma<1, 0>(l16, dS, (unsigned)((chA + 1) * (S_ * 2) + mg * 1024), (unsigned)(SL_B + CV_U + 512 + mg * 1024));
      }
      unsigned cpf = __builtin_amdgcn_raw_buffer_load_b32(rW, OOB, 0, 0);
      FENCE();
#define FILL_Z() do {                                                                                   \
      st128(u32x4{CODED, CODED, CODED, CODED}, rW, Z2OFF(mg), 0, 16);                                   \
      st128(u32x4{CODED, CODED, CODED, CODED}, rW, Z2OFF(wv), 0, 16);                                   \
      if (mg < 2) st128(u32x4{CODED, CODED, CODED, CODED}, rW, ZOFF(zja, zjb, chC), O_Z, 16);            \
    } while (0)
      unsigned fpa[3], fpv;
#pragma unroll
      for (int f_ = 0; f_ < 3; ++f_) {
        const unsigned o = (unsigned)(f_ * 4096 + t8 * 16);
        fpa[f_] = o < 10240u ? O_H1 + (unsigned)(bx >> 1) * TS + (bx & 1) * 10240u + o : o < 11264u ? O_ST4 + (unsigned)bx * 1024u + (o - 10240u)
                : (o < 11392u && (bx & 1)) ? O_ST3 + (unsigned)(bx >> 1) * 128u + (o - 11264u) : OOB;
        if (f_ == 2) fpv = o < 10240u ? CODED : SENT;
      }
#define FILL_P(a, b) _Pragma("unroll") for (int f_ = (a); f_ < (b); ++f_) { const unsigned fv = f_ < 2 ? CODED : fpv; st128(u32x4{fv, fv, fv, fv}, rW, fpa[f_], 0, 16); }
      u32x4 WS[10];
#define LD_WS(a, b) _Pragma("unroll") for (int x = (a); x < (b); ++x)                                   \
      WS[x] = __builtin_amdgcn_raw_buffer_load_b128(rW1, gw[x % 5], (9 + mg + 4 * (x / 5)) * (32 * D_ * 2), 0)
#define LD_WD(a, b) _Pragma("unroll") for (int x = (a); x < (b); ++x) {                                 \
      if (x < 10) W_DMA(mg + 4 * (x / 5), x % 5);                                                       \
      else if (x == 10) dma<1, 0>(mg == 0 ? gw[0] : mg == 1 ? gw[1] : mg == 2 ? gw[2] : gw[3], dW1, 8u * (32 * D_ * 2), (unsigned)(8 * 5120 + mg * 1024)); \
      else if (mg == 0) W_DMA(8, 4);                                                                    \
    }
      FILL_Z();
      FENCE();
      u32x4 wm[5];
      unsigned short cv;
      {
        const __amdgpu_buffer_rsrc_t rM = __builtin_amdgcn_make_buffer_rsrc((void*)w_mod, 0, D_ * 6 * D_ * 2, RS3);
        const __amdgpu_buffer_rsrc_t rC = __builtin_amdgcn_make_buffer_rsrc((void*)cin, 0, D_ * 2, RS3);
        const unsigned o = bx < 240 ? (unsigned)(t8 >> 3) * (6 * D_ * 2) + (unsigned)(t8 & 7) * 16u : OOB;
        cv = __builtin_amdgcn_raw_buffer_load_b16(rC, (unsigned)((t8 >> 3) + 32 * min(lane & 7, 4)) * 2u, (bx & 3) * 320, 0);
#pragma unroll
        for (int i = 0; i < 5; ++i) wm[i] = __builtin_amdgcn_raw_buffer_load_b128(rM, o, ((bx & 3) * 160 + i * 32) * (6 * D_ * 2) + (bx >> 2) * 128, 0);
      }
      FENCE();
      W12_G(gw);
#pragma unroll
      for (int j = 0; j < 5; ++j) PIN(gw[j]);
      const int job = bx * 4 + mg;
      const bool j3 = job < 540, jok = job < 972;
      const int jks = j3 ? job % 20 : (job - 540) % 54, jcg = j3 ? job / 20 : (job - 540) / 54;
      const unsigned rtl = (unsigned)(L_RT + mg * 5120);
      u32x4 rt[5];
      unsigned rta[5];
      {
        const unsigned v3 = ((unsigned)lane >> 3) * (F_ * 2) + ((unsigned)lane & 7u) * 16u + (unsigned)jks * (32 * F_ * 2) + (unsigned)jcg * 128u;
        const unsigned s4 = (unsigned)jks * (32 * D_ * 2) + (unsigned)(jcg - nt) * 160u;
#pragma unroll
        for (int i = 0; i < 5; ++i) { rta[i] = j3 ? (i < 4 ? v3 + i * (8 * F_ * 2) : OOB) : gw[i] + s4;
          PIN(rta[i]); }
      }
#define RT_LD() do {                                                                                    \
      const __amdgpu_buffer_rsrc_t rS = __builtin_amdgcn_make_buffer_rsrc((void*)(j3 ? w3 : w4), 0, jok ? DF2 : 0, RS3); \
      _Pragma("unroll") for (int i = 0; i < 4; ++i) rt[i] = __builtin_amdgcn_raw_buffer_load_b128(rS, rta[i], 0, 0); \
      if (!j3) rt[4] = __builtin_amdgcn_raw_buffer_load_b128(rS, rta[4], 0, 0);                         \
      FENCE();                                                                                          \
    } while (0)
      FENCE();
      STAMP(12);
      asm volatile("" : "+v"(cpf));
      asm volatile("" :: "v"(cp0));
      SIG(P_K);
      if constexpr (TSHI == 23) TX_(31);
      FENCE();
      TAGS();
      {
        float a[8];
#pragma unroll
        for (int e = 0; e < 8; ++e) a[e] = 0.f;
        const float c_ = lo2f(cv);
        const int sci = __float_as_int(rbf(c_ * __builtin_amdgcn_rcpf(1.f + __builtin_amdgcn_exp2f(c_ * -1.4426950408889634f))));
#pragma unroll
        for (int i = 0; i < 5; ++i) {
          const float sc = __int_as_float(__builtin_amdgcn_ds_bpermute(((lane & 56) + i) * 4, sci));
#pragma unroll
          for (int e = 0; e < 4; ++e) { a[2 * e] += sc * lo2f(wm[i][e]); a[2 * e + 1] += sc * hi2f(wm[i][e]); }
        }
#pragma unroll
        for (int e = 0; e < 4; ++e) {
          const auto r = __builtin_amdgcn_permlane32_swap(__float_as_uint(a[e]), __float_as_uint(a[e + 4]), false, false);
          a[e] = __uint_as_float((unsigned)r[0]) + __uint_as_float((unsigned)r[1]);
        }
#pragma unroll
        for (int e = 0; e < 2; ++e) {
          const auto r = __builtin_amdgcn_permlane16_swap(__float_as_uint(a[e]), __float_as_uint(a[e + 2]), false, false);
          a[e] = dpp_add(__uint_as_float((unsigned)r[0]) + __uint_as_float((unsigned)r[1]), 0);
        }
        if ((lane & 8) == 0) *(LAS f32x2*)(lds + L_MP + mg * 256 + (lane & 7) * 32 + g * 8) = f32x2{a[0], a[1]};
        SIG(P_M);
        TS_(52);
        if constexpr (TSHI == 23) TX_(32);
        FENCE();
        if (wv == 7) {
          WAIT_HALF(1, P_M);
          f32x2 s = *(const LAS f32x2*)(lds + L_MP + (lane & 31) * 8);
#pragma unroll
          for (int w = 1; w < 4; ++w) s += *(const LAS f32x2*)(lds + L_MP + w * 256 + (lane & 31) * 8);
          st128(u32x4{__float_as_uint(s[0]), tA, __float_as_uint(s[1]), tB}, rW, lane < 32 ? l16 : OOB, O_MP + (unsigned)bx * 512u, 16);
          __builtin_amdgcn_raw_buffer_store_b64(u32x2{tA, tB}, rW, lane == 0 ? (unsigned)bx * 8u : OOB, O_FL1, 16);
        }
        FENCE();
      }
      STAMP(2);
      {
      WAIT_HALF(1, P_K);
      BUILD(SL_A, t8);
      BUILD(SL_B, t8);
      SIG(P_B2);
      if constexpr (TSHI == 23) TX_(33);
      FENCE();
      STAMP(1);
      }
      WAIT_HALF(1, P_B2);
      REQ_Q();
      REQ_D();
      FENCE();
      FENCE();
#define RT_TR() do \
      { \
        _Pragma("unroll") \
        for (int i = 0; i < 5; ++i) *(LAS u32x4*)(lds + rtl + i * 1024 + l16) = rt[i]; \
        const LAS char* a0 = lds + rtl + (j3 ? (8 * g + (l15 >> 2)) * 128 : (lane >> 2) * 160) + (lane & 3) * 8; \
        const LAS char* a1 = a0 + (j3 ? 512 : 2560); \
        _Pragma("unroll") \
        for (int jn = 0; jn < 5; ++jn) { \
          const s16x4 lo = __builtin_amdgcn_ds_read_tr16_b64_v4i16((LAS s16x4*)(a0 + jn * 32)); \
          const s16x4 hi = __builtin_amdgcn_ds_read_tr16_b64_v4i16((LAS s16x4*)(a1 + jn * 32)); \
          rt[jn] = __builtin_bit_cast(u32x4, bf16x8{lo[0], lo[1], lo[2], lo[3], hi[0], hi[1], hi[2], hi[3]}); \
        } \
      } while (0)
#define RT_OUT() do {                                                                                   \
        const unsigned dst = j3 ? O_WT3 + (unsigned)(jcg * 80 + jks) * 1024u : O_WT4 + (unsigned)(jcg * 54 + jks) * 5120u, dstr = j3 ? 20480u : 1024u; \
        _Pragma("unroll") for (int jn = 0; jn < 4; ++jn) st128(rt[jn], rW, jok ? l16 : OOB, dst + jn * dstr, 16); \
        if (!j3) st128(rt[4], rW, jok ? l16 : OOB, dst + 4096u, 16);                                    \
        FENCE();                                                                                        \
      } while (0)
      SIG(P_U);
      const bool mfin = wv == 7 && bx < 240 && lane < 32;
      const unsigned po = mfin ? ((unsigned)lane >> 3) * 512u + ((unsigned)lane & 7u) * 16u : OOB;
      u32x4 mp4;
#define LD_P() mp4 = __builtin_amdgcn_raw_buffer_load_b128(rW, po, O_MP + (unsigned)(bx & ~3) * 512u + (unsigned)(bx & 3) * 128u, 16)
#define MOD_L2() do {                                                                                   \
      if (wv == 7) {                                                                                    \
        unsigned spins = 0;                                                                             \
        while (__builtin_amdgcn_ballot_w64(mfin && (mp4[1] != tA || mp4[3] != tB)) != 0ull) {           \
          SPIN_STEP();                                                                                  \
          if (spins > RETRY_CAP) break;                                                                 \
          __builtin_amdgcn_s_sleep(USLEEP);                                                             \
          LD_P();                                                                                       \
        }                                                                                               \
        const float one = ((bx >= 40 && bx < 80) || (bx >= 160 && bx < 200)) ? 1.f : 0.f;               \
        const float s0_ = swap_add16(dpp_add(__uint_as_float(mp4[0]), 0)), s1_ = swap_add16(dpp_add(__uint_as_float(mp4[2]), 0)); \
        const unsigned d = pack2(one + rbf(s0_), one + rbf(s1_));                                       \
        const unsigned dn = (unsigned)__builtin_amdgcn_update_dpp(0, (int)d, 0xB1, 0xf, 0xf, true);     \
        st128(u32x4{d, tA, dn, tB}, rW, (lane < 8 && (lane & 1) == 0) ? (unsigned)lane * 8u : OOB, O_MOD + (unsigned)bx * 64u, 16); \
      }                                                                                                 \
      FENCE();                                                                                          \
    } while (0)
      if (wv == 7) LD_P();
      FENCE();
      STAMP(3);
      FENCE();
      asm volatile("" : "+v"(fpa[0]), "+v"(fpa[1]), "+v"(fpa[2]), "+v"(fpv));
#define RT_GO() RT_LD()
      LN1();
      GET_D();
      if constexpr (TSHI == 23) TX_(34);
      MOD_L2();
      if constexpr (TSHI == 23) TX_(35);
      TS_(27);
      if constexpr (TSHI == 23) TX_(36);
      STAMP(22);
      WAIT_ALL(P_U);
      RT_LD();
      TS_(4);
      STAMP(28);
      FILL_P(0, 3);
      FENCE();
      RT_TR();
      SIG(P_RT);
      TS_(48);
      RT_OUT();
      LD_WD(0, 5);
      WAIT_HALF(1, P_RT);
      LD_WD(5, NGA);
      FENCE();
      VMW(NGA);
      SIG(P_GA);
      TS_(5);
      LD_WD(NGA, 12);
      FENCE();
      if (mg < 2) {
        u32x2 uc[2];
        f32x4 yc[8];
        float v[8];
        uc[0] = *(const LAS u32x2*)(lds + SL_C + CV_U + 512 + (256 * zja + 16 * l15 + 4 * g) * 2);
        uc[1] = *(const LAS u32x2*)(lds + SL_C + CV_U + 512 + (256 * zjb + 16 * l15 + 4 * g) * 2);
        unsigned zq = O_Z + ZOFF(zja, zjb, chC);
        PIN(zq);
        WAIT_HALF(0, P_C2);
#pragma unroll
        for (int c = 0; c < 8; ++c) yc[c] = *(const LAS f32x4*)(lds + SL_C + mg * 2048 + l16 + (c >> 1) * 4096 + (c & 1) * 1024);
        STG(8, v[i] = (i & 1) ? hi2f(uc[i >> 2][(i >> 1) & 1]) : lo2f(uc[i >> 2][(i >> 1) & 1]); PIN(v[i]));
        STG(8, v[i] = __builtin_fmaf(v[i], __uint_as_float(dC), yc[i >> 2][i & 3]); PIN(v[i]));
#pragma unroll
        for (int w = 1; w < 4; ++w) STG(8, v[i] += yc[2 * w + (i >> 2)][i & 3]; PIN(v[i]));
        st128(widen(pack2(v[0], plainv(v[1])), pack2(v[2], v[3]), pack2(v[4], plainv(v[5])), pack2(v[6], v[7])), rW, zq, 0, 16);
      }
      FENCE();
      SIG(P_ZC);
      FENCE();
      LD_WS(0, NS2);
      FENCE();
      if (HPZ) { WAIT_HALF(0, P_X2); __builtin_amdgcn_s_sleep(HPZ); }
      STAMP(15);
      LD_WS(NS2, 10);
      if (mg > 0) {
        WAIT_HALF(1, P_ZC);
#pragma unroll
        for (int j = 0; j < 5; ++j) W_DMA(16 + mg, j);
      }
      LD_WS2U(0, NZ2);
      FENCE();
      STAMP(5);
      TS_(6);
      WAIT_HALF(0, P_X2);
#pragma unroll
      for (int x = 0; x < 10; ++x) *(LAS u32x4*)(lds + (9 + mg + 4 * (x / 5)) * 5120 + (x % 5) * 1024 + l16) = WS[x];
      TS_(1);
      WAIT_HALF(0, P_T);
      R_PRE();
      VMW(NZ2);
      SIG(P_X);
      TS_(2);
      TS_(3);
      WAIT_HALF(0, P_Z);
      LD_WS2U(NZ2, 10);
      FENCE();
      WAIT_2(P_RP, P_X);
#pragma unroll
      for (int j = 0; j < 5; ++j) W_DMA(20 + mg, j);
      FENCE();
      TS_(7);
      STAMP(7);
    } else {
      PRIO(3);
      const unsigned rt0 = (unsigned)__builtin_amdgcn_s_memrealtime();
      unsigned kpa = (unsigned)L_KV + ((unsigned)lane & 7u) * 1312u + (unsigned)mg * 64u + ((unsigned)lane >> 3) * 8u;
      unsigned kpb = (unsigned)L_KV + ((unsigned)l15 >> 1) * 1312u + 256u + (unsigned)mg * 16u + (unsigned)g * 4u + ((unsigned)lane & 1u) * 640u, kpc = kpb + ((unsigned)lane & 1u) * 32u;
      const unsigned cha_ = (unsigned)ch0 + 32u * mg + ((unsigned)lane >> 3) * 4u, chb_ = (unsigned)ch0 + 128u + 8u * mg + 2u * g;
      unsigned koa = KVROW(cha_) + (unsigned)tok0 * 4u + ((unsigned)lane & 7u) * 16u, koa2 = KVROW(cha_ + 2u) + (unsigned)tok0 * 4u + ((unsigned)lane & 7u) * 16u;
      unsigned kob = KVROW(chb_) + (unsigned)tok0 * 4u + (unsigned)l15 * 8u;
      asm volatile("" : "+v"(kpa), "+v"(kpb), "+v"(kpc), "+v"(koa), "+v"(koa2), "+v"(kob));
      u32x4 ug[6];
      unsigned uo = (unsigned)mg * 2048u + l16;
      unsigned udone;
      asm volatile("s_mov_b32 %0, 0" : "=s"(udone));
      unsigned ubA = KVROW((unsigned)chA), ubC = KVROW((unsigned)chC), ubB = KVROW((unsigned)(chA + 1));
      asm volatile("" : "+s"(ubA), "+s"(ubB), "+s"(ubC));
      PIN(uo);
#pragma unroll
      for (int r = 0; r < 6; ++r) asm volatile("" : "=v"(ug[r]));
#define TRY_U(EL, T, CODE)                                                                               \
        asm volatile("s_cmp_lg_u32 %6, 0\n\ts_cbranch_scc1 Lu%=\n\ts_cmp_lt_u32 %[el], %[t]\n\ts_cbranch_scc1 Lu%=\n\t" \
                     "buffer_load_dwordx4 %2, %[vo], %[rs], %[c] offen sc1\n\tbuffer_load_dwordx4 %3, %[vo], %[rs], %[c] offen offset:1024 sc1\n\t" \
                     "buffer_load_dwordx4 %0, %[vo], %[rs], %[a] offen sc1\n\tbuffer_load_dwordx4 %1, %[vo], %[rs], %[a] offen offset:1024 sc1\n\t" \
                     "buffer_load_dwordx4 %4, %[vo], %[rs], %[b] offen sc1\n\tbuffer_load_dwordx4 %5, %[vo], %[rs], %[b] offen offset:1024 sc1\n\t" \
                     "s_mov_b32 %6, %[cd]\n"                                                            \
                     "Lu%=:" : "+v"(ug[0]), "+v"(ug[1]), "+v"(ug[2]), "+v"(ug[3]), "+v"(ug[4]), "+v"(ug[5]), "+s"(udone) \
                     : [el] "s"(EL), [t] "i"(T), [cd] "i"(CODE), [vo] "v"(uo), [rs] "s"(dW), [a] "s"(ubA), [b] "s"(ubB), [c] "s"(ubC) : "scc", "memory")
      VMW(9);
      TAGS();
      unsigned tAB = (lane & 1) ? tB : tA;
      PIN(tAB);
      asm volatile("s_waitcnt vmcnt(0)" : "+v"(sk[0]), "+v"(sk[1]), "+v"(sk[2]));
      const unsigned rt1 = (unsigned)__builtin_amdgcn_s_memrealtime();
      SIG(P_KV);
      FENCE();
      f32x2 wa[3][4];
#pragma unroll
      for (int c = 0; c < 3; ++c)
#pragma unroll
        for (int t = 0; t < 4; ++t) {
          const f32x2 w = (t & 1) ? f32x2{hi2f(sk[c][t >> 1]), hi2f(sk[c][2 + (t >> 1)])} : f32x2{lo2f(sk[c][t >> 1]), lo2f(sk[c][2 + (t >> 1)])};
          wa[c][t] = w;
          PIN(wa[c][t]);
        }
      asm volatile("" : "+v"(tAB));
      FENCE();
      TS_(1);
      STAMP(1);
      WAIT_HALF(0, P_KV);
      if (TU1) TRY_U(rt1 - rt0, TU1, 1);
      if constexpr (TSHI == 23) TX_(31);
      STAMP(12);
      {
        u32x2 kw[7], vw[4];
        unsigned kx[5], vx[2];
#pragma unroll
        for (int i = 0; i < 7; ++i) kw[i] = *(const LAS u32x2*)(lds + kpa + (i < 3 ? 320 + 320 * i : 1312 + 320 * (i - 3)));
#pragma unroll
        for (int i = 0; i < 5; ++i) kx[i] = *(const LAS unsigned*)(lds + ((i == 1 || i == 2) ? kpc : kpb) + (i < 3 ? 320 + 320 * i : 1312 + 320 * (i - 3)));
#pragma unroll
        for (int i = 0; i < 4; ++i) vw[i] = *(const LAS u32x2*)(lds + kpa + 12288 + 320 * i);
#pragma unroll
        for (int i = 0; i < 2; ++i) vx[i] = *(const LAS unsigned*)(lds + kpb + 12288 + 320 * i);
        __builtin_amdgcn_sched_barrier(0);
        f32x2 kf[19], a[10], p[10], t_[10];
#define KX(o, t) kf[((o) >> 2) * 7 + ((o) & 3) + (t)]
#define STR(lo, hi, stmt) do { _Pragma("unroll") for (int i = (lo); i < (hi); ++i) { stmt; } __builtin_amdgcn_sched_barrier(0); } while (0)
#define KV_GROUP(lo, hi) do {                                                                           \
        STR(((lo) >> 2) * 7, (hi) == 10 ? 19 : ((hi) >> 2) * 7, kf[i] = up2(i < 14 ? kw[i % 7][i / 7] : kx[i < 14 ? 0 : i - 14]); PIN(kf[i])); \
        {                                                                                               \
          STR(lo, hi, a[i] = KX(i, 0) * wa[i >> 2][3]; PIN(a[i]));                                      \
          _Pragma("unroll") for (int t = 1; t < 4; ++t) STR(lo, hi, a[i] = FMA2(KX(i, t), wa[i >> 2][3 - t], a[i]); PIN(a[i])); \
          STR(lo, hi, t_[i] = FMA2(a[i], cp, a[i]); PIN(t_[i]));                                        \
          STR(lo, hi, a[i] = FMA2(a[i], cn, t_[i]); PIN(a[i]));                                         \
        }                                                                                               \
        STR(lo, hi, p[i] = up2(i < 8 ? vw[i & 3][i >> 2] : vx[i & 1]); PIN(p[i]));                      \
        STR(lo, hi, a[i] *= p[i]; PIN(a[i]));                                                           \
        if constexpr (TSHI == 23) TX_(32 + (lo) / 2);                                                   \
        _Pragma("unroll") for (int c = (lo) >> 1; c < ((hi) < 8 ? (hi) : 8) >> 1; ++c) {                \
          const int o = (c >> 1) * 4, e = c & 1;                                                        \
          st128(u32x4{pack2(a[o][e], plainv(a[o + 1][e])), tA, pack2(a[o + 2][e], plainv(a[o + 3][e])), tB}, rW, c < 2 ? koa : koa2, e * 8192u, 16); \
        }                                                                                               \
        if ((hi) == 10) { _Pragma("unroll") for (int e = 0; e < 2; ++e) __builtin_amdgcn_raw_buffer_store_b64(u32x2{pack2(a[8][e], plainv(a[9][e])), tAB}, rW, kob, e * 8192, 16); } \
        __builtin_amdgcn_sched_barrier(0);                                                              \
      } while (0)
        KV_GROUP(0, 4);
        if constexpr (TSHI == 23) TX_(33);
        STAMP(5);
        KV_GROUP(4, 10);
        if constexpr (TSHI == 23) TX_(35);
      }
      FENCE();
      const unsigned rt2 = (unsigned)__builtin_amdgcn_s_memrealtime();
      REQ_Q();
      REQ_D();
      FENCE();
      if (TU2) TRY_U(rt2 - rt0, TU2, 2);
      STAMP(2);
      TS_(2);
      WAIT_HALF(1, P_K);
      BUILD(SL_C, t8);
      SIG(P_B);
      if constexpr (TSHI == 23) TX_(36);
      FENCE();
#define LD_U(r) ug[r] = __builtin_amdgcn_raw_buffer_load_b128(rW, uo + ((r) & 1) * 1024, (r) < 2 ? ubA : (r) < 4 ? ubC : ubB, 16)
      u32x4 A2[16], B2[16];
      WAIT_HALF(0, P_B);
      CV_RA(A2, SL_C, 0);
      CV_RA(A2, SL_C, 1);
      W12_G(gw);
#pragma unroll
      for (int j = 0; j < 5; ++j) PIN(gw[j]);
      unsigned zq = Z2OFF(wv);
      unsigned pa = a00 + SL_A - 3648, pb = b00 + SL_A - 64;
      unsigned xw = SL_A + mg * 8192 + l16, xc = SL_C + mg * 4096 + l16;
      asm volatile("" : "+v"(zq), "+v"(pa), "+v"(pb), "+v"(xw), "+v"(xc));
      FENCE();
      if (udone == 0u) while ((unsigned)__builtin_amdgcn_s_memrealtime() - rt0 < (unsigned)TU) __builtin_amdgcn_s_sleep(1);
      STAMP(3);
      TS_(3);
      TRY_U(rt0, 0, 3);
      FENCE();
      if constexpr (TSHI == 23) TX_(37);
      unsigned uw = (unsigned)(SL_A + CV_U + 512 + mg * 1024 + lane * 8);
      PIN(uw);
      WAIT_HALF(1, P_B2);
      asm volatile("s_cmp_eq_u32 %[d], 1\n\ts_cbranch_scc1 Lw%=\n\ts_waitcnt vmcnt(0)\nLw%=:\n\ts_waitcnt vmcnt(9)"
                   : "+v"(ug[0]), "+v"(ug[1]), "+v"(ug[2]), "+v"(ug[3]), "+v"(ug[4]), "+v"(ug[5]) : [d] "s"(udone) : "scc", "memory");
      if constexpr (TSHI == 23) { TX_(38); PIN(ug[5]); __builtin_amdgcn_s_sleep(1); TX_(39); }
      {
#define U_BAD(r) (ug[r][1] != tA || ug[r][3] != tB)
        if (__builtin_expect(__builtin_amdgcn_ballot_w64(U_BAD(0) || U_BAD(1) || U_BAD(2) || U_BAD(3) || U_BAD(4) || U_BAD(5)) != 0ull, 1)) {
          unsigned spins = 0;
          do {
            __builtin_amdgcn_s_sleep(USLEEP);
#pragma unroll
            for (int r = 0; r < 6; ++r) if (U_BAD(r)) LD_U(r);
            SPIN_STEP();
          } while (__builtin_amdgcn_ballot_w64(U_BAD(0) || U_BAD(1) || U_BAD(2) || U_BAD(3) || U_BAD(4) || U_BAD(5)) != 0ull && spins < RETRY_CAP);
          VM0();
        }
#pragma unroll
        for (int r = 2; r < 8; ++r) *(LAS u32x2*)(lds + uw + ((r % 6) >> 1) * CV_SLOT + (r & 1) * 512) = u32x2{ug[r % 6][0], ug[r % 6][2]};
      }
      SIG(P_U);
      TS_(56);
      if constexpr (TSHI == 23) TX_(40);
      STAMP(22);
      GET_D();
      LD_WS2(0, 10);
      FENCE();
      WAIT_HALF(0, P_U);
      CV_RB(B2, SL_C, 0);
      FENCE();
      STAMP(28);
      TS_(4);
      f32x4 acc[8], ac[4];
      u32x4 A[16], B[16];
#define RA(P, rd, i_) *(const LAS u32x4*)(lds + (P) + 3648 - 64 * (rd) - 512 * (i_))
#define RB(P, rd, i_) *(const LAS u32x4*)(lds + (P) + 64 - 64 * (rd) + 512 * (i_))
#define CONV_C(J, lo, hi)                                                                               \
      sfor<lo, hi>([&](auto C_) __attribute__((always_inline)) {                                         \
        constexpr int c = decltype(C_)::v, rd = c / 18, i = cc_i(J, c % 18), t = cc_t(J, c % 18);       \
        ac[t] = mfma16(A2[8 * rd + i], B2[8 * rd + (((J) >> (4 * t)) & 15) - i], (rd == 0 && i == 0) ? f32x4{0.f, 0.f, 0.f, 0.f} : ac[t]); \
        SB();                                                                                           \
        if constexpr (c < 8) B2[15 - c] = RB(pb + CV_SLOT, 1, 7 - c);                                   \
        if constexpr (c >= 18 && c < 34) { constexpr int x = (c - 18) >> 1; if constexpr ((c & 1) == 0) B[7 - x] = RB(pb, 0, 7 - x); else A[x] = RA(pa, 0, x); } \
        SB();                                                                                           \
      });
      if (half == 0) { CONV_C(0x7430, 0, 18) } else { CONV_C(0x6521, 0, 18) }
      SB();
      int half2 = half;
      asm volatile("" : "+s"(half2));
      if (half2 == 0) { CONV_C(0x7430, 18, 36) } else { CONV_C(0x6521, 18, 36) }
      u32x4 A3[16], B3[16];
      f32x4 ab_[8];
      unsigned pk_;
      float ud[16];
      u32x2 u2[4];
      f32x4 ya[8];
      unsigned ga_;
      unsigned pu = (unsigned)(SL_A + CV_U + 512 + (256 * wv + 16 * l15 + 4 * g) * 2), py = (unsigned)(SL_A + wv * 1024) + l16;
      const unsigned f0 = (unsigned)L_CTL + ((unsigned)lane & 3u) * 4u;
      sfor<0, 144>([&](auto M_) __attribute__((always_inline)) {
        constexpr int m = decltype(M_)::v, ch = m / 72, rd = (m / 36) & 1, r = m % 36, i = m < 108 ? cv_i(r) : cl_i(r), dd = m < 108 ? cv_d(r) : cl_t(r) - cl_i(r);
        if constexpr (ch == 0) acc[i + dd] = mfma16(A[8 * rd + i], B[8 * rd + dd], (rd == 0 && i == 0) ? f32x4{0.f, 0.f, 0.f, 0.f} : acc[i + dd]);
        else ab_[i + dd] = mfma16(A3[8 * rd + i], B3[8 * rd + dd], (rd == 0 && i == 0) ? f32x4{0.f, 0.f, 0.f, 0.f} : ab_[i + dd]);
        SB();
        if constexpr (m == 30) { pa += SL_B - SL_A; pb += SL_B - SL_A; asm volatile("" : "+v"(pa), "+v"(pb)); }
        if constexpr (m == 94) { xw += SL_B - SL_A; PIN(xw); }
        if constexpr (r >= RS0 && r < RS0 + 16 && m < 108) {
          constexpr int x = (r - RS0) >> 1;
          if constexpr (m < 36) { if constexpr ((r & 1) == 0) B[15 - x] = RB(pb, 1, 7 - x); else A[8 + x] = RA(pa, 1, x); }
          else if constexpr (m < 72) { if constexpr ((r & 1) == 0) B3[7 - x] = RB(pb, 0, 7 - x); else A3[x] = RA(pa, 0, x); }
          else { if constexpr ((r & 1) == 0) B3[15 - x] = RB(pb, 1, 7 - x); else A3[8 + x] = RA(pa, 1, x); }
        }
        if constexpr (m == 1 || m == 4 || m == 23 || m == 26) { constexpr int c = m == 1 ? 0 : m == 4 ? 1 : m == 23 ? 2 : 3; *(LAS f32x4*)(lds + xc + c * 1024) = ac[c]; }
        if constexpr (m == 28) SIG(P_C2);
        if constexpr (m == 38 || m == 104) pk_ = *(volatile LAS unsigned*)(lds + f0);
        if constexpr (m == 45) NEED_AT(pk_, f0, P_C2);
        if constexpr (m == 111) NEED_AT(pk_, f0, P_A2);
        constexpr int wa = m == 47 ? 0 : m == 54 ? 1 : m == 60 ? 2 : m == 65 ? 3 : m == 69 ? 4 : m == 72 ? 5 : m == 75 ? 6 : m == 77 ? 7 : -1;
        constexpr int wb = m == 113 ? 0 : m == 116 ? 1 : m == 119 ? 2 : m == 122 ? 3 : m == 126 ? 4 : m == 132 ? 5 : m == 139 ? 6 : -1;
        if constexpr (wa >= 0) *(LAS f32x4*)(lds + xw + wa * 1024) = acc[wa];
        if constexpr (wb >= 0) *(LAS f32x4*)(lds + xw + wb * 1024) = ab_[wb];
        if constexpr (m == 95) SIG(P_A2);
        if constexpr (m == 58) PIN(pu);
        if constexpr (m == 59 || m == 61) { constexpr int c = m - 59; u2[c] = *(const LAS u32x2*)(lds + pu + c * 1024); u2[c + 1] = *(const LAS u32x2*)(lds + pu + (SL_B - SL_A) + c * 1024); }
        constexpr int us = cv_slot(m, 0);
        if constexpr (us >= 0 && us < 16) {
#pragma unroll
          for (int o = 2 * us; o < 2 * us + 2; ++o) {
            const int v = (o >> 3) * 4 + (o & 3);
            if ((o & 4) == 0) ud[v] = (v & 1) ? hi2f(u2[v >> 2][(v >> 1) & 1]) : lo2f(u2[v >> 2][(v >> 1) & 1]);
            else ud[v] *= ((v >> 2) & 1) ? hi2f(dAB) : lo2f(dAB);
            PIN(ud[v]);
          }
        }
        if constexpr (m == 110) PIN(py);
        if constexpr (m == 112 || m == 114 || m == 115 || m == 117) { constexpr int c = m == 112 ? 0 : m == 114 ? 2 : m == 115 ? 4 : 6;
          ya[c] = *(const volatile LAS f32x4*)(lds + py + (c >> 2) * 4096 + (c & 3) * 8192); ya[c + 1] = *(const volatile LAS f32x4*)(lds + py + (c >> 2) * 4096 + (c & 3) * 8192 + 8192); }
        constexpr int zs = cv_slot(m, 1);
        if constexpr (zs >= 0 && zs < 16) {
#pragma unroll
          for (int o = 2 * zs; o < 2 * zs + 2; ++o) { const int tl = (o >> 2) & 1, e = o & 3; ud[tl * 8 + e] += ya[4 * tl + (o >> 3)][e]; PIN(ud[tl * 8 + e]); }
        }
        if constexpr (TSHI == 10 && (m % 18 == 0 || m == 143)) TX_(44 + (m + 1) / 18);
        if constexpr (m == 131) ga_ = *(volatile LAS unsigned*)(lds + f0 + 16);
        if constexpr (m == 140) { py += SL_B - SL_A; PIN(py); }
        SB();
      });
      *(LAS f32x4*)(lds + xw + 7 * 1024) = ab_[7];
      SIG(P_X);
      TS_(5);
      STAMP(4);
      u32x4 mg4[4];
#define LD_MG(i) mg4[i] = __builtin_amdgcn_raw_buffer_load_b128(rW, (unsigned)t8 * 16u, O_MOD + (i) * 4096, 16)
      u32x2 f1v;
#define LD_F1() f1v = __builtin_amdgcn_raw_buffer_load_b64(rW, (unsigned)t8 * 8u, O_FL1, 16)
      NEED_AT(ga_, f0 + 16, P_GA);
      {
        unsigned spins = 0;
        for (;;) {
          const unsigned fl = *(volatile LAS unsigned*)(lds + f0);
#pragma unroll
          for (int c = 0; c < 8; ++c) ya[c] = *(const volatile LAS f32x4*)(lds + py + (c >> 2) * 4096 + (c & 3) * 8192);
          if (__builtin_amdgcn_ballot_w64(fl < (unsigned)P_X) == 0ull) break;
          SPIN_STEP();
          if (spins > SPIN_CAP) break;
        }
#pragma unroll
        for (int tl = 0; tl < 2; ++tl) {
          const f32x4 yb = (ya[4 * tl] + ya[4 * tl + 1]) + (ya[4 * tl + 2] + ya[4 * tl + 3]);
          const f32x2 v2 = f32x2{yb[0], yb[1]} + f32x2{ud[8 * tl + 4], ud[8 * tl + 5]}, v3 = f32x2{yb[2], yb[3]} + f32x2{ud[8 * tl + 6], ud[8 * tl + 7]};
          st128(u32x4{pack2(ud[8 * tl], plainv(v2[0])), pack2(ud[8 * tl + 1], v2[1]), pack2(ud[8 * tl + 2], plainv(v3[0])), pack2(ud[8 * tl + 3], v3[1])}, rW, zq + tl * 64u * TS, 0, 16);
        }
      }
      FENCE();
      SIG(P_X2);
      TS_(6);
      LD_F1(); LD_MG(0); LD_MG(1); LD_MG(2); LD_MG(3);
      FENCE();
      LN1();
      STAMP(6);
      u32x4 za, zb, zc = {0u, 0u, 0u, 0u}, zd = {0u, 0u, 0u, 0u};
      const unsigned zo = (unsigned)mt * (4u * TS) + ZCH((unsigned)t8 & ~1u) + (unsigned)nt * 32u + ((unsigned)t8 & 1u) * 16u;
      const unsigned zo1 = (unsigned)mt * (4u * TS) + ZCH(256u + ((unsigned)t8 & ~1u)) + (unsigned)nt * 32u + ((unsigned)t8 & 1u) * 16u;
      const unsigned zo2 = (unsigned)mt * (4u * TS) + ZCH(2u * t8 + 128u) + (unsigned)nt * 16u;
      FENCE();
      {
#define MG_BAD(i) (mg4[i][1] != tA || mg4[i][3] != tB)
#define T_BAD() (__builtin_amdgcn_ballot_w64(f1v[0] != tA || f1v[1] != tB || MG_BAD(0) || MG_BAD(1) || MG_BAD(2) || MG_BAD(3)) != 0ull)
        if (__builtin_expect(T_BAD(), 0)) {
          unsigned spins = 0;
          do {
            __builtin_amdgcn_s_sleep(USLEEP);
            if (f1v[0] != tA || f1v[1] != tB) LD_F1();
#pragma unroll
            for (int i = 0; i < 4; ++i) if (MG_BAD(i)) LD_MG(i);
            SPIN_STEP();
          } while (T_BAD() && spins < RETRY_CAP);
          VM0();
        }
#pragma unroll
        for (int i = 0; i < 4; ++i) {
          const int n = (t8 + 256 * i) * 4;
          if (n < 6 * D_) *(LAS u32x2*)(lds + L_MOD + n * 2) = u32x2{mg4[i][0], mg4[i][2]};
          if (i == 3 && n >= 5 * D_ + n0 && n < 5 * D_ + n0 + 80)
            *(LAS f32x4*)(lds + L_GB + (n - 5 * D_ - n0) * 4) = f32x4{lo2f(mg4[i][0]), hi2f(mg4[i][0]), lo2f(mg4[i][2]), hi2f(mg4[i][2])};
        }
        SIG(P_T);
        if constexpr (TSHI == 23) TX_(41);
      }
      WAIT_HALF(0, P_T);
      if (DLY0) __builtin_amdgcn_s_sleep(DLY0);
      PRIO(3);
      STAMP(7);
      TS_(7);
      za = __builtin_amdgcn_raw_buffer_load_b128(rW, zo, O_Z, 16); zb = __builtin_amdgcn_raw_buffer_load_b128(rW, zo1, O_Z, 16);
      if (wv == 3) { zc = __builtin_amdgcn_raw_buffer_load_b128(rW, zo2, O_Z, 16); zd = __builtin_amdgcn_raw_buffer_load_b128(rW, zo2 + 16u * ZG, O_Z, 16); }
      FENCE();
      SIG(P_Z);
      R_PRE();
      SIG(P_RP);
      unsigned zw0 = (unsigned)(L_XCH + 4 * (t8 & 1) * YROW + 4 * (t8 >> 1)), zw1 = (unsigned)(L_XCH + 256 + 4 * t8);
      asm volatile("" : "+v"(zw0), "+v"(zw1), "+v"(ry), "+v"(rz));
      asm volatile("" : "+s"(w3s[0]), "+s"(w3s[1]));
      unsigned spins = 0;
      for (;;) {
        const bool ba = amax2(za) >= PTHR, bb_ = amax2(zb) >= PTHR, bc_ = amax2(zc) >= PTHR, bd = amax2(zd) >= PTHR;
        if (__builtin_amdgcn_ballot_w64(ba || bb_ || bc_ || bd) == 0ull) break;
        SPIN_STEP();
        if (spins > RETRY_CAP) break;
        __builtin_amdgcn_s_sleep(ZSLEEP);
        if (ba) za = __builtin_amdgcn_raw_buffer_load_b128(rW, zo, O_Z, 16);
        if (bb_) zb = __builtin_amdgcn_raw_buffer_load_b128(rW, zo1, O_Z, 16);
        if (bc_) zc = __builtin_amdgcn_raw_buffer_load_b128(rW, zo2, O_Z, 16);
        if (bd) zd = __builtin_amdgcn_raw_buffer_load_b128(rW, zo2 + 16u * ZG, O_Z, 16);
      }
#pragma unroll
      for (int i = 0; i < 4; ++i) {
        *(LAS unsigned*)(lds + zw0 + i * YROW) = za[i];
        *(LAS unsigned*)(lds + zw0 + i * YROW + 512) = zb[i];
      }
      if (wv == 3) {
#pragma unroll
        for (int i = 0; i < 4; ++i) {
          *(LAS unsigned*)(lds + zw1 + (2 * i) * YROW) = __builtin_amdgcn_perm(zd[i], zc[i], 0x05040100u);
          *(LAS unsigned*)(lds + zw1 + (2 * i + 1) * YROW) = __builtin_amdgcn_perm(zd[i], zc[i], 0x07060302u);
        }
      }
      VM0();
      SIG(P_Z2);
    }
  }
  {
    f32x2 hh[5], t_[5];
    STAMP(8);
    asm volatile("" : "+v"(ry), "+v"(rz));
    asm volatile("" : "+s"(w3s[0]), "+s"(w3s[1]));
    if (kh) PRIO(PR1); else PRIO(PHI);
    WAIT_HALF(0, P_Z2);
    STAMP(9);
    TS_(8);
    unsigned zw[5], xp[5], hp_[5];
    {
      const u32x4 a = *(LAS u32x4*)(lds + ry);
      zw[0] = a[0]; zw[1] = a[1]; zw[2] = a[2]; zw[3] = a[3];
      zw[4] = *(LAS unsigned*)(lds + rz);
    }
    FENCE();
#define RQ3(k) do { LD_W3R((k) == 0 ? 0 : (k) == 1 ? NR0 : NR1, (k) == 0 ? NR0 : (k) == 1 ? NR1 : NWH); FENCE(); } while (0)
    RQ3(0);
    FENCE();
    if constexpr (TSHI == 22) TX_(30);
    STG(5, hh[i] = up2(zw[i]); PIN(hh[i]));
    STG(5, xq[i] = FMA2(hh[i], ha[i], xq[i]); PIN(xq[i]));
    STG(5, t_[i] = FMA2(xq[i], cp, xq[i]); PIN(t_[i]));
    STG(5, xq[i] = FMA2(xq[i], cn, t_[i]); PIN(xq[i]));
    if constexpr (TSHI == 22) TX_(31);
    RQ3(1);
    if constexpr (TSHI == 22) TX_(32);
    float m3;
    {
      const f32x2 s2 = ((xq[0] + xq[1]) + (xq[2] + xq[3])) + xq[4];
      const f32x2 q2 = FMA2(xq[0], xq[0], xq[1] * xq[1]) + FMA2(xq[2], xq[2], FMA2(xq[3], xq[3], xq[4] * xq[4]));
      const f32x2 p2 = FMA2(xq[0], ca[0], xq[1] * ca[1]) + FMA2(xq[2], ca[2], FMA2(xq[3], ca[3], xq[4] * ca[4]));
      float sm3[3] = {s2[0] + s2[1], q2[0] + q2[1], p2[0] + p2[1]};
      wave_sums<3>(sm3);
      const float mean = sm3[0] * (1.f / (float)D_), qs = sm3[1], ps = sm3[2];
      const float m = rbf(mean);
      f32x2 d[5];
#pragma unroll
      for (int j = 0; j < 5; ++j) d[j] = (xq[j] - m) * ca[j];
      const float var = rbf(__builtin_fmaxf(__builtin_fmaf(-mean, mean, qs * (1.f / (float)D_)), 0.f));
      const float den = rbf(__builtin_amdgcn_sqrtf(rbf(var + 1e-5f)));
      const float rinv = __builtin_amdgcn_rcpf(den);
      m3 = rbf(__builtin_fmaf(__builtin_fmaf(-m, mc, ps * (1.f / (float)D_)), rinv, mb));
      __builtin_amdgcn_sched_barrier(0);
      if constexpr (TSHI == 22) TX_(33);
      RQ3(2);
      if constexpr (TSHI == 22) TX_(34);
      STG(5, hh[i] = FMA2(d[i], (f32x2{rinv, rinv}), sb[i]); PIN(hh[i]));
      STG(5, hh[i] -= m3; PIN(hh[i]));
    }
#pragma unroll
    for (int j = 0; j < 5; ++j) hp_[j] = pack2(hh[j][0], (j & 1) ? hh[j][1] : (j < 4 || (lane & 1) == 0) ? codedv(hh[j][1]) : hh[j][1]);
    *(LAS u32x4*)(lds + ry) = u32x4{hp_[0], hp_[1], hp_[2], hp_[3]};
    *(LAS unsigned*)(lds + rz) = hp_[4];
    BARRIER();
    {
      const unsigned tb = (unsigned)(s0 >> 4) * 20480u + (unsigned)(s0 & 15) * 16u + (unsigned)(tid >> 3) * 256u + (unsigned)(tid & 7) * 16u;
      const LAS char* pr = lds + L_XCH + (tid & 7) * YROW + (tid >> 3) * 16;
      const u32x4 v0 = *(const LAS u32x4*)(pr);
      const u32x4 v1 = *(const LAS u32x4*)(pr + (tid < 128 ? 1024 : 0));
      st128(v0, rW, tb, O_HP, 16);
      if (wv < 2) st128(v1, rW, tb + 64u * 256u, O_HP, 16);
    }
    FENCE();
    TS_(9);
    PRIO(1);
#pragma unroll
    for (int j = 0; j < 5; ++j) xp[j] = pack2(xq[j][0], xq[j][1]);
    *(LAS u32x4*)(lds + ry + 8 * YROW) = u32x4{xp[0], xp[1], xp[2], xp[3]};
    *(LAS unsigned*)(lds + rz + 8 * YROW) = xp[4];
    FENCE();
    STAMP(10);
    {
      const f32x2 s2 = ((hh[0] + hh[1]) + (hh[2] + hh[3])) + hh[4];
      const f32x2 q2 = FMA2(hh[0], hh[0], hh[1] * hh[1]) + FMA2(hh[2], hh[2], FMA2(hh[3], hh[3], hh[4] * hh[4]));
      const float mean = wave_sum2(s2[0] + s2[1]) * (1.f / (float)D_), qs = wave_sum2(q2[0] + q2[1]);
      const float var = rbf(__builtin_fmaxf(__builtin_fmaf(-mean, mean, qs * (1.f / (float)D_)), 0.f));
      const float den = rbf(__builtin_amdgcn_sqrtf(rbf(var + 1e-5f)));
      const u32x2 st = {canon(__float_as_uint(m3)), __float_as_uint(den)};
      __builtin_amdgcn_raw_buffer_store_b64(st, rW, lane == 0 ? (unsigned)srow * 8u : OOB, O_ST3, 16);
    }
    {
      BARRIER();
      const unsigned tb = (unsigned)(s0 >> 4) * 20480u + (unsigned)(s0 & 15) * 16u + (unsigned)(tid >> 3) * 256u + (unsigned)(tid & 7) * 16u;
      const LAS char* pr = lds + L_XCH + (8 + (tid & 7)) * YROW + (tid >> 3) * 16;
      st128(*(const LAS u32x4*)(pr), rW, tb, O_XO, 16);
      if (wv >= 6) st128(*(const LAS u32x4*)(pr + 256), rW, tb + 16u * 256u, O_XO, 16);
      SIG(P_L1);
      if (bx == 0 && wv == 5) __builtin_amdgcn_raw_buffer_store_b64(u32x2{(unsigned)epn, (unsigned)(epn >> 32)}, rW, lane == 0 ? 0u : OOB, O_EP, 16);
    }
    FENCE();
    PRIO(0);
    FENCE();
  }

  float sthr = PTHR, scmx = CMAX;
  unsigned scsh = CSH;
  asm volatile("" : "+s"(sthr), "+s"(scsh), "+s"(scmx));
  unsigned xcw = (unsigned)(L_XCH + gm * 5120 + gk * 2048) + l16, xcr = (unsigned)(L_XCH + gm * 5120 + 2048 - gk * 2048) + l16, xcf = (unsigned)(L_CTL + gp * 4);
  asm volatile("" : "+v"(xcw), "+v"(xcr), "+v"(xcf));
  unsigned long long gkm;
  asm volatile("s_cmp_lg_u32 %1, 0\n\ts_cselect_b64 %0, -1, 0" : "=s"(gkm) : "s"(gk) : "scc");
  static_assert(KL >= 3 && KG <= 54 - 2 * KL, "the weights are read in front of the signal");
#define EXCHANGE(ph)                                                                        \
  do {                                                                                      \
    f32x4 e0, e1, e2;                                                                       \
    unsigned spins = 0;                                                                     \
    for (;;) {                                                                              \
      const unsigned fl = *(volatile LAS unsigned*)(lds + xcf);                                                           \
      e0 = *(volatile LAS f32x4*)(lds + xcr);                                               \
      e1 = *(volatile LAS f32x4*)(lds + xcr + 1024);                                        \
      e2 = *(volatile LAS f32x4*)(lds + xcr + 2048);                                        \
      if (__builtin_amdgcn_readfirstlane(fl) >= (ph)) break;                                \
      SPIN_STEP();                                                                          \
      if (spins > SPIN_CAP) break;                                                          \
    }                                                                                       \
    asm volatile("s_nop 3" : "+v"(acc[3]), "+v"(acc[4]));        \
    STG(12, e[i] = (i < 4 ? acc[3][i & 3] + e0[i & 3] : i < 8 ? acc[4][i & 3] + e1[i & 3] : acc[2][i & 3] + e2[i & 3]); PIN(e[i])); \
  } while (0)
#define KLOOP(CD, PB, BLK, VA, AOFF, XG, XS, ph)                                            \
  asm volatile("s_nop 1" : "+v"(A[0]));                                                     \
  sfor<0, 50>([&](auto M_) __attribute__((always_inline)) {                                 \
    constexpr int m = decltype(M_)::v, i = kl_i(m), j = kl_j(m), f = m / 4 + 1, c = m % 4;  \
    mf<i == 0 && (CD || !HFI || j < 2)>(acc[j], __builtin_bit_cast(u32x4, __builtin_shufflevector(Bh[2 * m], Bh[2 * m + 1], 0, 1, 2, 3, 4, 5, 6, 7)), A[i]); \
    SB();                                                                                   \
    constexpr int n0_ = m < KG ? (m * (100 - 2 * (PB))) / KG : 100, n1_ = m < KG ? ((m + 1) * (100 - 2 * (PB))) / KG : 100; \
    if constexpr (n1_ > n0_) RD_BH(2 * (PB) + n0_, BLK);                                    \
    if constexpr (n1_ > n0_ + 1) RD_BH(2 * (PB) + n0_ + 1, BLK);                            \
    static_assert(n1_ <= n0_ + 2 && (f > 9 || kl_i(m + 1) < f), "order");                   \
    if constexpr (f < 10) {                                                                 \
      if constexpr (c == 0) { m_ = CD ? amin2(A[f]) : __builtin_amdgcn_fmed3f(__builtin_fabsf(__uint_as_float(A[f][0])), __builtin_fabsf(__uint_as_float(A[f][2])), scmx); PIN(m_); } \
      if constexpr (c == 1) CMP_BAD(CD, bm, m_);                                            \
      if constexpr (c == 2 && CD) A[f][0] -= scsh;                                          \
      if constexpr (c == 3 && CD) { A[f][2] -= scsh; PIN(A[f]); }                           \
      if constexpr (c == 3) RETRY(CD, A[f], bm, VA, sa[f / 5], AOFF + (f % 5) * 2048, PHI); \
    }                                                                                       \
    if constexpr (m % 5 == 2 || m % 5 == 4) { XS(2 * (m / 5) + (m % 5 == 4)); }             \
    XG(m);                                                                                  \
                                \
    if constexpr (m == 51 - 2 * KL) { asm volatile("s_nop 3" : "+v"(acc[0])); *(volatile LAS f32x4*)(lds + xcw) = acc[0]; } \
    if constexpr (m == 52 - 2 * KL) *(volatile LAS f32x4*)(lds + xcw + 1024) = acc[1];      \
    if constexpr (m == 53 - 2 * KL) asm volatile("s_mov_b64 exec, %2\n\tds_write_b128 %0, %1 offset:2048\n\ts_mov_b64 exec, -1" :: "v"(xcw), "v"(acc[2]), "s"(gkm) : "memory"); \
    if constexpr (m == 54 - 2 * KL) SIG(ph);                                                \
    if constexpr (TSHI == 20 && m % 5 == 0) TX_((CD ? 30 : 44) + m / 5);                    \
    SB();                                                                                   \
  })
  {
    f32x4 acc[5];
    s16x4 Bh[100];
    u32x4 A[10];
    float m_, e[12];
    unsigned long long bm;
    unsigned sa[2] = {ROT ? (unsigned)(nt & 1) * 10240u : 0u, ROT ? 10240u - (unsigned)(nt & 1) * 10240u : 10240u};
    asm volatile("" : "+s"(sa[0]), "+s"(sa[1]));
    const unsigned va = (unsigned)(row >> 4) * 20480u + gk * 1024 + l16;
#define LD_A1(x) A[x] = __builtin_amdgcn_raw_buffer_load_b128(rW, va, sa[(x) / 5] + (O_HP + ((x) % 5) * 2048), 16)
    unsigned bb[3][2];
#pragma unroll
    for (int c = 0; c < 6; ++c) {
      bb[c >> 1][c & 1] = (unsigned)gk * 5120u + ((unsigned)lane >> 2) * 160u + ((unsigned)lane & 3u) * 8u + sa[c & 1] * 5u + (c < 2 ? 96u - 96u * gk : c < 4 ? 64u : 96u * gk);
      PIN(bb[c >> 1][c & 1]);
    }
#define RD_BH(h, BLK) Bh[h] = __builtin_amdgcn_ds_read_tr16_b64_v4i16((LAS s16x4*)(lds + bb[kl_j((h) / 2) < 2 ? 0 : kl_j((h) / 2) == 2 ? 1 : 2][kl_i((h) / 2) / 5] + (BLK) + (kl_i((h) / 2) % 5) * 10240 + (kl_j((h) / 2) % 3 == 1 ? 32 : 0) + ((h) & 1) * 2560))
#define BLK1 0
    FENCE();
#pragma unroll
    for (int n = 0; n < 2 * PBE; ++n) RD_BH(n, BLK1);
    FENCE();
    if (DLY1) __builtin_amdgcn_s_sleep(DLY1);
    PRIO(3);
    TS_(10);
#pragma unroll
    for (int i = 0; i < NA1; ++i) { LD_A1(i); SB(); }
    PRIO(1);
#pragma unroll
    for (int n = 2 * PBE; n < 2 * PB1; ++n) RD_BH(n, BLK1);
    FENCE();
#pragma unroll
    for (int i = NA1; i < NAF; ++i) { LD_A1(i); SB(); }
    u32x2 s3v;
    FENCE();
    if (kh) PRIO(PH1); else PRIO(PHI);
    STAMP(11);
    const int sh = (nt >> 1) == 0 ? 1 : (nt >> 1) == 1 ? -1 : (nt >> 1) == 2 ? 2 : -2;
    const int rr = row + sh, rw = rr & (S_ - 1);
    unsigned keep = rr == rw ? 0xffffffffu : 0u;
    unsigned oh1 = ofr - (unsigned)(row >> 4) * 20480u - (unsigned)l15 * 16u + (unsigned)(rw >> 4) * TS + (unsigned)(rw & 15) * 16u + O_H1;
    asm volatile("" : "+v"(keep), "+v"(oh1));
    asm volatile("" : "+s"(w3s[2]), "+s"(w3s[3]));
    if constexpr (TSHI == 21) { TX_(29); sfor<0, 10>([&](auto I_) __attribute__((always_inline)) { PIN(A[decltype(I_)::v]); TX_(30 + decltype(I_)::v); __builtin_amdgcn_s_sleep(1); }); }
    WAIT_ALL(P_L1);
    SB();
    CHECK_A(true, A[0], 0, NAF, LD_A1);
    SB();
#define XG1(m) do { if constexpr ((m) % 2 == 0 && NAF + (m) / 2 < 10) LD_A1(NAF + (m) / 2); if constexpr ((m) == 13) s3v = __builtin_amdgcn_raw_buffer_load_b64(rW, (unsigned)row * 8u, O_ST3, 16); if constexpr ((m) == 4) TS_(11); } while (0)
#define XSL(u, s0, n, f0) do { if constexpr ((u) >= (s0) && (((u) - (s0) + 1) * (n)) / (20 - (s0)) > (((u) - (s0)) * (n)) / (20 - (s0))) LD_W3U((f0) + (((u) - (s0)) * (n)) / (20 - (s0))); } while (0)
#define XS1(u) XSL(u, SL1, NL1, NWH)
    KLOOP(true, PB1, BLK1, va, O_HP, XG1, XS1, P_XF1);
    TS_(12);
    if (__builtin_expect(bad1(s3v[0]), 0)) {
      unsigned spins = 0;
      do {
        __builtin_amdgcn_s_sleep(RSLEEP);
        s3v = __builtin_amdgcn_raw_buffer_load_b64(rW, (unsigned)row * 8u, O_ST3, 16);
        SPIN_STEP();
      } while (bad1(s3v[0]) && spins < RETRY_CAP);
    }
    const float ld = __uint_as_float(s3v[1]), lr = __builtin_amdgcn_rcpf(ld);
    float gca = lr * GC1, gcb = lr * lr * lr * GC2;
    asm volatile("" : "+v"(gca), "+v"(gcb));
    EXCHANGE((unsigned)P_XF1);
    TS_(13);
    {
      unsigned pk[6];
      if (gk == 0) {
        gelu_s<12>(e, gca, gcb, ld, pk);
        __builtin_amdgcn_raw_buffer_store_b64(u32x2{pk[4] & keep, pk[5] & keep}, rW, oh1 + 1024, 0, 16);
      } else {
        gelu_s<8>(e, gca, gcb, ld, pk);
      }
      st128(widen(pk[0] & keep, pk[1] & keep, pk[2] & keep, pk[3] & keep), rW, oh1 + (unsigned)(g & 1) * 504u, 0, 16);
    }
    FENCE();
    TS_(14);
    PRIO(0);
    LD_W3R(NWH + NL1, NWH + NL1 + NP0);
    FENCE();
    WAIT_ALL(P_XF1);
#pragma unroll
    for (int i = 0; i < 10; ++i) *(LAS u32x4*)(lds + 20480 + wv * 5120 + (i / 5) * 40960 + (i % 5) * 1024 + l16) = WS2[i];
    SIG(P_G1);
    STAMP(13);
    FENCE();
    LD_W3R(NWH + NL1 + NP0, NWH + NL1 + NP1);
    FENCE();
    const unsigned va2 = (unsigned)(row >> 4) * TS + gk * 1024 + l16;
    u32x2 hpv[3];
#define LD_A2(x) A[x] = __builtin_amdgcn_raw_buffer_load_b128(rW, va2, sa[(x) / 5] + (O_H1 + ((x) % 5) * 2048), 16)
    u32x4 hp4;
    const unsigned ofw = ofr + (unsigned)(g & 1) * 504u;
    hp4 = __builtin_amdgcn_raw_buffer_load_b128(rW, ofw, O_HP, 16);
    hpv[2] = __builtin_amdgcn_raw_buffer_load_b64(rW, gk ? OOB : ofr + 1024, O_HP, 16);
    FENCE();
    WAIT_ALL(P_G1);
    if (DLY2) __builtin_amdgcn_s_sleep(DLY2);
    PRIO(3);
    TS_(15);
#pragma unroll
    for (int i = 0; i < NA1; ++i) { LD_A2(i); SB(); }
    PRIO(1);
#define BLK2 20480
#pragma unroll
    for (int n = 0; n < 2 * PB2; ++n) RD_BH(n, BLK2);
    FENCE();
#pragma unroll
    for (int i = NA1; i < NAF; ++i) { LD_A2(i); SB(); }
    FENCE();
    if (kh) PRIO(PH2); else PRIO(PHI);
    float hf[12];
    { const u32x4 n_ = widen(hp4[0], hp4[1], hp4[2], hp4[3]); hpv[0] = u32x2{n_[0], n_[1]}; hpv[1] = u32x2{n_[2], n_[3]}; }
#pragma unroll
    for (int j = 0; j < 3; ++j) { hpv[j][0] -= CSH; PIN(hpv[j]); }
    STG(12, hf[i] = ((i & 1) ? hi2f(hpv[i >> 2][(i >> 1) & 1]) : lo2f(hpv[i >> 2][(i >> 1) & 1])) + __uint_as_float(s3v[0]); PIN(hf[i]));
    acc[3] = f32x4{hf[0], hf[1], hf[2], hf[3]}; acc[4] = f32x4{hf[4], hf[5], hf[6], hf[7]};
    acc[2] = gk ? f32x4{0.f, 0.f, 0.f, 0.f} : f32x4{hf[8], hf[9], hf[10], hf[11]};
    PIN(acc[2]); PIN(acc[3]); PIN(acc[4]);
    STAMP(14);
    if constexpr (TSHI == 21) { TX_(43); sfor<0, 10>([&](auto I_) __attribute__((always_inline)) { PIN(A[decltype(I_)::v]); TX_(44 + decltype(I_)::v); __builtin_amdgcn_s_sleep(1); }); }
    SB();
    CHECK_A(false, A[0], 0, NAF, LD_A2);
    SB();
#define XG2(m) do { if constexpr ((m) % 2 == 0 && NAF + (m) / 2 < 10) LD_A2(NAF + (m) / 2); if constexpr ((m) == 4) TS_(16); } while (0)
#define XS2(u) XSL(u, SL2, NL2, NWH + NL1 + NP1)
    KLOOP(false, PB2, BLK2, va2, O_H1, XG2, XS2, P_XF2);
    TS_(17);
    EXCHANGE((unsigned)P_XF2);
    TS_(18);
    unsigned xk[6];
    {
      if (!HFI) STG(12, e[i] += hf[i]; PIN(e[i]));
      STG(3, e[4 * i + 1] = plainv(e[4 * i + 1]); PIN(e[4 * i + 1]));
      STG(6, xk[i] = pack2(e[2 * i], e[2 * i + 1]); PIN(xk[i]));
      st128(widen(xk[0], xk[1], xk[2], xk[3]), rW, ofw, O_X1, 16);
      if (gk == 0) __builtin_amdgcn_raw_buffer_store_b64(u32x2{xk[4], xk[5]}, rW, ofr + 1024, O_X1, 16);
    }
    FENCE();
    TS_(19);
    PRIO(1);
    SIG(P_G2);
    float v[12];
    float s = 0.f;
#pragma unroll
    for (int j = 0; j < 3; ++j) {
      v[4 * j] = lo2f(xk[2 * j]); v[4 * j + 1] = hi2f(xk[2 * j]); v[4 * j + 2] = lo2f(xk[2 * j + 1]); v[4 * j + 3] = hi2f(xk[2 * j + 1]);
      if (j < 2 || gk == 0) s += (v[4 * j] + v[4 * j + 1]) + (v[4 * j + 2] + v[4 * j + 3]);
    }
    s = swap_add32(swap_add16(s));
    const float mean = s * (gk ? (1.f / 32.f) : (1.f / 48.f));
    float m2 = 0.f;
#pragma unroll
    for (int i = 0; i < 12; ++i) {
      const float dd = v[i] - mean;
      if (i < 8 || gk == 0) m2 += dd * dd;
    }
    m2 = swap_add32(swap_add16(m2));
    {
      u32x2 st = {canon(__float_as_uint(mean)), __float_as_uint(m2)};
      __builtin_amdgcn_raw_buffer_store_b64(st, rW, g == 0 ? (unsigned)(((row >> 4) * 16 + nt * 2 + gk) * 16 + l15) * 8u : OOB, O_ST4, 16);
    }
    FENCE();
    PRIO(0);
    FENCE();
    STAMP(16);
  }
  {
    constexpr int NR3 = 40 - NWH - NL1 - NP1 - NL2;
#define NOW4D(...) __VA_ARGS__
#define LD_W4(f) W4[f] = __builtin_amdgcn_raw_buffer_load_b128(rW, l16, w4a + (f) * 1024, 16)
    const unsigned va4 = O_H + (unsigned)(m0 >> 4) * TS + (unsigned)k4 * 1024u;
#define A4OFF(x) (((x) / NS) * TS + ((x) % NS) * 1024)
#define LD_A4(x) A4[(x) % AW4] = __builtin_amdgcn_raw_buffer_load_b128(rW, l16, va4 + A4OFF(x), 16)
    const int lane0 = lane;
#define LANES() int lane = lane0; PIN(lane); const int l15 = lane & 15, g = lane >> 4; const unsigned l16 = (unsigned)lane * 16u; \
    const unsigned fa = (unsigned)L_CTL + ((unsigned)lane & 7u) * 4u
#define FLG(a) *(volatile LAS unsigned*)(lds + (a))
    auto g3 = [&](auto KH_, u32x4 (&W4)[35], u32x4 (&A4)[AW4], u32x2 (&x1v)[3]) __attribute__((always_inline)) {
      constexpr int KH = decltype(KH_)::v, NS = 7, NW0 = KH ? 6 : AW4;
      LANES();
      const unsigned hw0 = (unsigned)(m0 >> 4) * TS + (unsigned)l15 * 16u + (unsigned)(g >> 1) * 256u + O_H;
      const unsigned csp = (unsigned)(L_CS3 + wv * 128 + g * 16), f1 = (unsigned)L_CTL + 16u + ((unsigned)lane & 3u) * 4u;
      u32x2 stv[4];
#define LD_ST() _Pragma("unroll") for (int a = 0; a < 4; ++a) stv[a] = __builtin_amdgcn_raw_buffer_load_b64(rW, (unsigned)((((m0 >> 4) + mg) * 16 + 4 * g) * 16 + l15) * 8u, O_ST4 + a * 128, 16)
#define ST_BAD() bad1(max(max(stv[0][0], stv[1][0]), max(stv[2][0], stv[3][0])))
      const u32x4 ones = {0x3f803f80u, 0x3f803f80u, 0x3f803f80u, 0x3f803f80u};
      f32x4 c3a = {0.f, 0.f, 0.f, 0.f}, c3b = {0.f, 0.f, 0.f, 0.f};
#define CS3R(a, b) do { _Pragma("unroll") for (int f_ = (a); f_ < (b); f_ += 2) { c3a = mfma16(W3[f_], ones, c3a); if (!KH || f_ < 20) c3b = mfma16(W3[f_ + 1], ones, c3b); } } while (0)
      TS_(20);
      PRIO(PHI);
      LD_W3R(40 - NR3, 40);
      FENCE();
      const unsigned so = O_X1 + (unsigned)mt * 81920u + (unsigned)wv * 10240u, d0 = (unsigned)(KH ? L_XB : L_XA) + (unsigned)mg * 10240u;
      CS3R(0, KH ? KC3B : KC3);
      asm volatile("" : "+v"(c3a), "+v"(c3b));
      FENCE();
      TS_(21);
      WAIT_2(KH ? P_XF2 : P_G2, KH ? P_G2 : P_XF2);
      if (KH ? DLY3B : DLY3) __builtin_amdgcn_s_sleep(KH ? DLY3B : DLY3);
      PIN(W3[38]); PIN(W3[39]);
      if (KH) {
#pragma unroll
        for (int f = 0; f < 10; ++f) W4[f] = W3[21 + 2 * f];
      }
      FENCE();
      PRIO(3);
      TS_(22);
#define STG_DMA() do { dma<4, 1>(l16, dW, so, d0); dma<4, 1>(l16, dW, so + 4096, d0 + 4096); dma<1, 1>(l16, dW, so + 8192, d0 + 8192); dma<1, 1>(l16, dW, so + 9216, d0 + 9216); } while (0)
      if (KH) {
        asm volatile("buffer_load_dwordx2 %0, %4, %5, %6 offen sc1\n\tbuffer_load_dwordx2 %1, %4, %5, %6 offen offset:128 sc1\n\t"
                     "buffer_load_dwordx2 %2, %4, %5, %6 offen offset:256 sc1\n\tbuffer_load_dwordx2 %3, %4, %5, %6 offen offset:384 sc1"
                     : "=&v"(stv[0]), "=&v"(stv[1]), "=&v"(stv[2]), "=&v"(stv[3]) : "v"((unsigned)((((m0 >> 4) + mg) * 16 + 4 * g) * 16 + l15) * 8u), "s"(dW), "s"(O_ST4) : "memory");
      }
      STG_DMA();
      FENCE();
      TS_(23);
      PRIO(PHI);
      CS3R(KH ? KC3B : KC3, 40);
      if (l15 == 0) { *(LAS f32x4*)(lds + csp) = c3a; *(LAS f32x4*)(lds + csp + 64) = c3b; }
      FENCE();
      if (KH) {
        asm volatile("s_waitcnt vmcnt(10)" : "+v"(stv[0]), "+v"(stv[1]), "+v"(stv[2]), "+v"(stv[3]));
        if (__builtin_expect(ST_BAD(), 0)) {
          unsigned spins = 0;
          do {
            __builtin_amdgcn_s_sleep(RSLEEP);
            LD_ST();
            SPIN_STEP();
          } while (ST_BAD() && spins < RETRY_CAP);
        }
        const float m0_ = __uint_as_float(stv[0][0]), q0 = __uint_as_float(stv[0][1]), m1 = __uint_as_float(stv[1][0]), q1 = __uint_as_float(stv[1][1]);
        const float m2_ = __uint_as_float(stv[2][0]), q2 = __uint_as_float(stv[2][1]), m3 = __uint_as_float(stv[3][0]), q3 = __uint_as_float(stv[3][1]);
        float sm = 48.f * (m0_ + m2_) + 32.f * (m1 + m3);
        sm = swap_add32(swap_add16(sm));
        const float mean = sm * (1.f / (float)D_);
        const float d0_ = m0_ - mean, d1 = m1 - mean, d2 = m2_ - mean, d3 = m3 - mean;
        float qs = (q0 + q1) + (q2 + q3) + 48.f * (d0_ * d0_ + d2 * d2) + 32.f * (d1 * d1 + d3 * d3);
        qs = swap_add32(swap_add16(qs));
        const float var = rbf(qs * (1.f / (float)D_));
        const float den = rbf(__builtin_amdgcn_sqrtf(rbf(var + 1e-5f))), r = __builtin_amdgcn_rcpf(den);
        float rc1 = r * GC1;
        PIN(rc1);
        if (g == 0) *(volatile LAS f32x4*)(lds + L_ST4 + (mg * 16 + l15) * 16) = f32x4{rbf(mean), rc1, r * r * r * GC2, den};
        SIG(P_SF);
      }
      FENCE();
      TS_(24);
      VMW(0);
      TS_(25);
      {
        unsigned spins = 0;
        for (;;) {
          u32x4 a[10];
          float mx = 0.f;
#pragma unroll
          for (int i = 0; i < 10; ++i) a[i] = *(volatile LAS u32x4*)(lds + d0 + i * 1024 + l16);
#pragma unroll
          for (int i = 0; i < 10; ++i) mx = amax3(mx, a[i]);
          if (__builtin_expect(__builtin_amdgcn_ballot_w64(mx >= PTHR) == 0ull, 1)) break;
          SPIN_STEP();
          if (spins > RETRY_CAP) break;
          PRIO(0);
          __builtin_amdgcn_s_sleep(RSLEEP);
          STG_DMA();
          PRIO(PHI);
          asm volatile("s_waitcnt vmcnt(0)" ::: "memory");
        }
        SIG(P_SA);
      }
      TS_(26);
      STAMP(17);
      FENCE();
      constexpr int NP = KH ? NPH : NPK, F0 = KH ? 10 : 0, NEQ = NP + 6, EQ0 = KH ? EC0 : EA0, EQ1 = KH ? EC1 : EA1, NL0 = F0 + NEQ, NLT = (KH ? 30 : 35) - NL0;
      const unsigned pks = (unsigned)(KH ? L_PB + NPK * 1024 + mg * (NPH * 1024) : wv < 3 ? wv * (NPK * 1024) : L_PB);
      constexpr auto ecnt = [](int q) { return q <= EQ0 ? 0 : q >= EQ1 ? NEQ : ((q - EQ0) * NEQ + EQ1 - EQ0 - 1) / (EQ1 - EQ0); };
#define ESLOT(q) sfor<ecnt(q), ecnt((q) + 1)>([&](auto U_) __attribute__((always_inline)) { constexpr int u = decltype(U_)::v; \
        if constexpr (u < NP) { NOW4D(dma<1, 1>(l16, dW, w4a + (F0 + 6 + u) * 1024, pks + u * 1024)); } else LD_W4(F0 + u - NP); })
      static_assert(3 * NPK <= 32 && NPK + 4 * NPH <= 36 && MV0 + NP <= 20 && NLT <= 19 && NLT >= MV0, "w4");
      sfor<0, 2>([&](auto P_) __attribute__((always_inline)) {
        constexpr int p = decltype(P_)::v, NV = KH ? 12 : 16;
        TS_(28 + 8 * p + 0);
        if (p == 0) WAIT_2(P_SA, P_G2); else WAIT_HALF(1, P_SA);
        f32x4 acc[4];
        PRIO(KH ? (p ? PK1 : PK2) : PK0);
        TS_(28 + 8 * p + 1);
        u32x4 Ar[AD3][2];
        unsigned ab[2] = {l16 + (unsigned)((p ? L_XB : L_XA) + (KH ? hb * 30720 : 0)), l16 + (unsigned)((p ? L_XB : L_XA) + 20480 - (KH ? hb * 10240 : 0))};
        const unsigned sp = (unsigned)(L_ST4 + p * 512 + l15 * 16), fp = (unsigned)(L_CTL + (wv ^ 2) * 4), xs = (unsigned)(32768 + p * 4096) + l16;
        PIN(ab[0]);
        if (KH) PIN(ab[1]);
        f32x4 cs[3], st[2], rx;
        unsigned sf_, sx_;
#define RD_A3(s, h) Ar[(s) % AD3][h] = *(const LAS u32x4*)(lds + (KH ? ab[h] : ab[0] + (h) * 20480) + (s) * 1024)
#pragma unroll
        for (int s = 0; s < AD3 - 1; ++s) { RD_A3(s, 0); RD_A3(s, 1); }
        SB();
        sfor<0, 20>([&](auto S_) __attribute__((always_inline)) {
          constexpr int s = decltype(S_)::v;
          constexpr bool rd = s + AD3 - 1 < 20, T2 = !KH || s < 10;
          mf<s == 0>(acc[0], W3[2 * s], Ar[s % AD3][0]);
          SB();
          if constexpr (rd) RD_A3(s + AD3 - 1, 0);
          else if constexpr (s == 18) cs[0] = *(const LAS f32x4*)(lds + csp);
          else st[0] = *(const volatile LAS f32x4*)(lds + sp + (KH ? hb * 256 : 0));
          SB();
          mf<s == 0>(acc[1], W3[2 * s], Ar[s % AD3][1]);
          SB();
          if constexpr (rd) RD_A3(s + AD3 - 1, 1);
          else if constexpr (s == 18) cs[1] = *(const LAS f32x4*)(lds + csp + 64);
          else st[1] = *(const volatile LAS f32x4*)(lds + sp + 256 - (KH ? hb * 256 : 0));
          SB();
          if constexpr (T2) { mf<s == 0>(acc[2], W3[2 * s + 1], Ar[s % AD3][0]); SB(); }
          if constexpr (p == 0 && s == 15) sf_ = FLG(f1);
          if constexpr (p == 0 && s == 18) NEED_AT(sf_, f1, P_SF);
          if constexpr (p == 0) ESLOT(s);
          if constexpr (p == 1 && s >= 1 && s <= NLT) LD_W4(NL0 + s - 1);
          if constexpr (p == 1 && s == MV0 - 1) VMW(MV0 + 1);
          SB();
          if constexpr (T2) { mf<s == 0>(acc[3], W3[2 * s + 1], Ar[s % AD3][1]); SB(); }
          if constexpr (KH && s + AD3 == 10) { ab[0] -= hb * 20480; ab[1] -= hb * 20480; PIN(ab[0]); PIN(ab[1]); }
          if constexpr (KH && s == 12) { *(volatile LAS f32x4*)(lds + xs + mg * 1024) = acc[3]; SIG(p ? P_S1 : P_S0); }
          if constexpr (KH && s == 14) sx_ = FLG(fp);
          if constexpr (KH && s == 17) { NEED_AT(sx_, fp, p ? P_S1 : P_S0); rx = *(const volatile LAS f32x4*)(lds + xs + (mg ^ 2) * 1024); cs[2] = *(const volatile LAS f32x4*)(lds + csp + (hb ? -192 : 320)); }
          if constexpr (p == 1 && s >= MV0 && s < MV0 + NP) W4[F0 + 6 + s - MV0] = *(const LAS u32x4*)(lds + pks + l16 + (s - MV0) * 1024);
          if constexpr (p == 1 && s >= 1 && s < 4 - KH) {
            static_assert(H0S >= 4 && H0S < 12, "x1");
            const int tu = KH == 0 ? (s == 3 ? 3 : s - 1) : wv < 6 ? 2 : s == 1 ? wv & 1 : wv - 4, ju = KH == 0 ? wv : wv < 6 ? 2 * wv - 6 - s : 4;
            x1v[s - 1] = *(const LAS u32x2*)(lds + (tu < 2 ? L_XA : L_XB - 40960) + tu * 20480 + (nt * 10 + 2 * ju) * 256 + (g >> 1) * 256 + (g & 1) * 8 + l15 * 16);
          }
          if constexpr (p == 1 && s == H0S) SIG(P_H0);
          if constexpr (s == 9) TS_(28 + 8 * p + 2);
          SB();
        });
        TS_(28 + 8 * p + 3);
        PRIO(KH && p ? PE1 : PE0);
        SIG(p ? P_KB : P_KA);
        if constexpr (KH) asm volatile("s_nop 3" : "+v"(acc[0]), "+v"(acc[1]), "+v"(acc[2])); else asm volatile("s_nop 3" : "+v"(acc[0]), "+v"(acc[1]), "+v"(acc[2]), "+v"(acc[3]));
        STAMP(18 + 2 * p);
        {
          float e[NV], x[NV];
          unsigned pk[NV / 2];
#define SQ(i) st[((i) >> 2) & 1]
#define EPS(q) do { if constexpr (p == 0) { ESLOT(20 + (q)); } if constexpr (p == 1 && (q) == HG - 1) sf_ = FLG(fa); \
                    if constexpr (p == 1 && (q) == HG) { NEED_AT(sf_, fa, P_H0); _Pragma("unroll") for (int x = 0; x < NW0; ++x) { LD_A4(x); } } SB(); } while (0)
          if constexpr (KH) STG(4, x[i] = __builtin_fmaf(-st[0][0], cs[2][i], rx[i]); PIN(x[i]));
          STG(NV, e[i] = __builtin_fmaf(-SQ(i)[0], cs[i >> 3][i & 3], acc[i >> 2][i & 3]); PIN(e[i]));
          if constexpr (KH) STG(4, e[8 + i] += x[i]; PIN(e[8 + i]));
          TS_(28 + 8 * p + 4);
          EPS(0);
          STG(NV, x[i] = e[i] * e[i]; PIN(x[i]));
          EPS(1);
          STG(NV, x[i] = __builtin_fmaf(x[i], SQ(i)[2], SQ(i)[1]); PIN(x[i]));
          EPS(2);
          STG(NV, x[i] *= e[i]; PIN(x[i]));
          EPS(3);
          STG(NV, x[i] = __builtin_amdgcn_exp2f(x[i]); PIN(x[i]));
          TS_(28 + 8 * p + 5);
          EPS(4);
          STG(NV, x[i] = __builtin_fmaf(x[i], SQ(i)[3], SQ(i)[3]); PIN(x[i]));
          EPS(5);
          STG(NV, x[i] = __builtin_amdgcn_rcpf(x[i]); PIN(x[i]));
          EPS(6);
          STG(NV, e[i] *= x[i]; PIN(e[i]));
          TS_(28 + 8 * p + 6);
          EPS(7);
          STG(NV / 4, e[4 * i + 1] = codedv(e[4 * i + 1]); PIN(e[4 * i + 1]));
          STG(NV / 2, pk[i] = pack2(e[2 * i], e[2 * i + 1]); PIN(pk[i]));
          const unsigned hw1 = hw0 + (unsigned)((g & 1) ^ (KH ? hb : 0)) * TS;
          st128(widen(pk[0], pk[1], pk[2], pk[3]), rW, hw1, (unsigned)(sb3 + jl) * 512u + 2 * p * TS, 16);
          if constexpr (KH) __builtin_amdgcn_raw_buffer_store_b64(u32x2{pk[4], pk[5]}, rW, hw0 + (unsigned)(g & 1) * 8u, two ? (unsigned)(sb3 + jm) * 512u + (unsigned)(2 * p + hb) * TS : OOB, 16);
          else st128(widen(pk[4], pk[5], pk[6], pk[7]), rW, hw1, (unsigned)(sb3 + jm) * 512u + 2 * p * TS, 16);
          FENCE();
          EPS(8);
        }
        TS_(28 + 8 * p + 7);
        STAMP(19 + 2 * p);
      });
    };
#define PB(t) (81920 - (t) * 40960)
    const __amdgpu_buffer_rsrc_t rO = __builtin_amdgcn_make_buffer_rsrc((void*)out, 0, SD2, RS3);
    auto g4 = [&](auto KD_, u32x4 (&W4)[35], u32x4 (&A4)[AW4], u32x2 (&x1v)[3]) __attribute__((always_inline)) {
      constexpr int KD = decltype(KD_)::v, NS = KD == 1 ? 6 : 7, PR = KD ? P41 : P40;
      constexpr int GS = NS + (KD == 0 ? GSA : KD == 1 ? GSB : GSC);
      constexpr auto nreq = [](int x) { int n = AW4; for (int y = 0; y <= x; ++y) { int m = y + AW4 < 4 * NS ? y + AW4 : 4 * NS; if (y < GS && m > 2 * NS) m = 2 * NS; n = m < n + 3 ? (m > n ? m : n) : n + 3; } return n; };
      LANES();
      TS_(44);
      if constexpr (KD) {
#pragma unroll
        for (int x = 6; x < AW4; ++x) { LD_A4(x); }
      }
      if constexpr (KD == 2) {
#pragma unroll
        for (int f = 30; f < 35; ++f) { LD_W4(f); }
      }
      FENCE();
      PRIO(PR);
      u32x2 xv[2];
      unsigned fl_, fh_, fu_, pu_;
      f32x4 acc[2][5], rs[4], gq;
      float u_[4], y_[4], m4_;
      unsigned long long bm4;
      unsigned pw = (unsigned)(wv * 5120) + l16;
      PIN(pw);
      const unsigned fo0 = (unsigned)((m0 >> 4) * 20480 + ((n0 + 4 * g) >> 3) * 256 + (g & 1) * 8 + l15 * 16), oo0 = (unsigned)((m0 + l15) * D_ + n0 + 4 * g) * 2u;
#define U_LD(r, t, j) xv[r] = __builtin_amdgcn_raw_buffer_load_b64(rW, fo0, O_XO + (unsigned)((t) * 20480 + (j) * 512), 16)
#define U_PC(q, t, j, y)                                                                               \
  do {                                                                                                  \
    if constexpr ((q) == 0) { fu_ = FLG(fa); pu_ = (unsigned)(PB(t) + (j) * 1024) + l16; PIN(pu_); }    \
    if constexpr ((q) == 1) { gq = *(const LAS f32x4*)(lds + L_GB + ((j) * 16 + 4 * g) * 4); }          \
    if constexpr ((q) >= 1 && (q) < 5) { constexpr int i = (q) - 1; u_[i] = (i & 1) ? hi2f(x1v[y][i >> 1]) : lo2f(x1v[y][i >> 1]); PIN(u_[i]); } \
    if constexpr ((q) == 5) NEED_AT(fu_, fa, P_P0 + 1 + (t));                                           \
    if constexpr ((q) >= 6 && (q) < 10) rs[(q) - 6] = *(const volatile LAS f32x4*)(lds + pu_ + ((q) - 6) * 5120); \
    if constexpr ((q) >= 11 && (q) < 27) { constexpr int w = ((q) - 11) >> 1, h = (((q) - 11) & 1) * 2;  \
      u_[h] += rs[w & 3][h]; PIN(u_[h]); u_[h + 1] += rs[w & 3][h + 1]; PIN(u_[h + 1]);                 \
      if constexpr (h == 2 && w < 4) rs[w] = *(const volatile LAS f32x4*)(lds + pu_ + (4 + w) * 5120); } \
    if constexpr ((q) >= 6 && (q) < 10) { constexpr int i = (q) - 6; y_[i] = (i & 1) ? hi2f(xv[0][i >> 1]) : lo2f(xv[0][i >> 1]); PIN(y_[i]); } \
    if constexpr ((q) == 27 || (q) == 28) { constexpr int h = ((q) - 27) * 2; u_[h] = __builtin_fmaf(u_[h], gq[h], y_[h]); PIN(u_[h]); u_[h + 1] = __builtin_fmaf(u_[h + 1], gq[h + 1], y_[h + 1]); PIN(u_[h + 1]); } \
    if constexpr ((q) == 29) __builtin_amdgcn_raw_buffer_store_b64(u32x2{pack2(u_[0], u_[1]), pack2(u_[2], u_[3])}, rO, oo0, (unsigned)((t) * (16 * D_ * 2) + (j) * 32), 0); \
  } while (0)
      {
        unsigned long long b0;
        const float m0_ = amin2(A4[0]);
        CMP_BAD(1, b0, m0_);
        A4[0][0] -= scsh; A4[0][2] -= scsh;
        RETRY_C(A4[0], b0, l16, va4, 0, PR);
        asm volatile("s_nop 0" : "+v"(A4[0]));
      }
      SB();
      const int tc = KD == 0 ? 3 : KD == 1 ? 2 : wv - 4, jc = KD == 0 ? wv : KD == 1 ? 2 * wv - 8 : 4;
      sfor<0, 20 * NS>([&](auto M_) __attribute__((always_inline)) {
        constexpr int m = decltype(M_)::v, x = m / 5, j = m % 5, t = x / NS, s = x % NS, r0 = nreq(x - 2), r1 = nreq(x - 1);
        constexpr bool nx = x + 1 < 4 * NS;
        u32x4& an = A4[(x + 1) % AW4];
        mf<s == 0>(acc[t & 1][j], W4[5 * s + j], A4[x % AW4]);
        SB();
        if constexpr (j == 0) {
          if constexpr (x == GS + 1) NEED_AT(fh_, fa, P_H1);
          if constexpr (r0 < r1) { LD_A4(r0); }
        }
        if constexpr (j == 1 && nx) { m4_ = amin2(an); PIN(m4_); }
        if constexpr (j == 2 && nx) { CMP_BAD(1, bm4, m4_); an[0] -= scsh; }
        if constexpr (j == 3 && nx) { an[2] -= scsh; PIN(an); }
        if constexpr (j == 3 && r0 + 1 < r1) { LD_A4(r0 + 1); }
        if constexpr (j == 4) {
          if constexpr (nx) RETRY_C(an, bm4, l16, va4, A4OFF(x + 1), PR);
          if constexpr (r0 + 2 < r1) { LD_A4(r0 + 2); }
          if constexpr (x == GS) fh_ = FLG(fa);
          if constexpr (x == H1S) SIG(P_H1);
          if constexpr (s == 0) TS_(45 + 4 * t);
          if constexpr (s == 3) TS_(45 + 4 * t + 1);
          if constexpr (s == NS - 1) TS_(45 + 4 * t + 2);
          static_assert(GS + 1 < 2 * NS, "order");
          if constexpr (KD == 0 && t == 1 && s == 0) U_LD(0, 0, wv);
          if constexpr (KD == 0 && t == 2 && s == 3) U_LD(0, 1, wv);
          if constexpr (KD == 2 && t == 2 && s == 3) U_LD(0, wv & 1, 4);
          if constexpr (KD == 1 && t == 3 && s == 3) U_LD(0, 2, jc + 1);
          if constexpr (t == 2 && s == 5) U_LD(1, tc, jc);
          if constexpr (t == 3 && s == NS - 2) fl_ = FLG(L_CTL);
        }
        constexpr bool un = (KD == 0 && t >= 2) || (KD == 2 && t == 3);
        constexpr int wq = 5 * s + j, w0 = un ? W0U : 4;
        if constexpr (t > 0 && wq >= w0 && wq < w0 + 5 * WSP && (wq - w0) % WSP == 0) *(volatile LAS f32x4*)(lds + PB(t - 1) + pw + ((wq - w0) / WSP) * 1024) = acc[(t - 1) & 1][(wq - w0) / WSP];
        if constexpr (t > 0 && wq == w0 + 1 + 4 * WSP) SIG(P_P0 + t);
        static_assert(W0U + 1 + 4 * WSP < 35 && U0 + 30 <= 35, "unit");
        if constexpr (un && KD == 0 && wq >= U0) U_PC(wq - U0, t - 2, wv, t - 2);
        if constexpr (un && KD == 2 && wq >= U0) U_PC(wq - U0, wv & 1, 4, 0);
        SB();
      });
      asm volatile("s_nop 3" : "+v"(acc[1][0]), "+v"(acc[1][1]), "+v"(acc[1][2]), "+v"(acc[1][3]), "+v"(acc[1][4]));
#pragma unroll
      for (int j = 0; j < 4; ++j) *(volatile LAS f32x4*)(lds + L_PB + wv * 4096 + l16 + j * 1024) = acc[1][j];
      NEED_AT(fl_, (unsigned)L_CTL, P_P0 + 3);
      *(volatile LAS f32x4*)(lds + PB(0) + pw) = acc[1][4];
      SIG(P_P0 + 4);
      TS_(60);
#define UNIT(r, y, t, j, base, str)                                                                        \
  do {                                                                                                  \
    unsigned pq = (unsigned)(base) + l16, oq = oo0 + (unsigned)((t) * (16 * D_ * 2) + (j) * 32);        \
    f32x4 gq_ = *(const LAS f32x4*)(lds + L_GB + ((j) * 16 + 4 * g) * 4);                               \
    f32x2 xa = up2(xv[r][0]), xb = up2(xv[r][1]), ya = up2(x1v[y][0]), yb = up2(x1v[y][1]);             \
    asm volatile("" : "+v"(xa), "+v"(xb), "+v"(ya), "+v"(yb), "+v"(gq_), "+v"(pq), "+v"(oq));           \
    f32x4 r8[8];                                                                                        \
    unsigned spins = 0;                                                                                 \
    for (;;) {                                                         \
      const unsigned fl = FLG(fa);                                                                      \
      _Pragma("unroll") for (int w = 0; w < 8; ++w) r8[w] = *(const volatile LAS f32x4*)(lds + pq + w * (str)); \
      if (__builtin_amdgcn_ballot_w64(fl < (unsigned)(P_P0 + 1 + (t))) == 0ull) break;                  \
      SPIN_STEP();                                                                                      \
      if (spins > SPIN_CAP) break;                                                                      \
    }                                                                                                   \
    const f32x4 sm = ((r8[0] + r8[1]) + (r8[2] + r8[3])) + ((r8[4] + r8[5]) + (r8[6] + r8[7]));         \
    const f32x2 a_ = FMA2(f32x2{sm[0], sm[1]} + ya, (f32x2{gq_[0], gq_[1]}), xa), b_ = FMA2(f32x2{sm[2], sm[3]} + yb, (f32x2{gq_[2], gq_[3]}), xb); \
    __builtin_amdgcn_raw_buffer_store_b64(u32x2{pack2(a_[0], a_[1]), pack2(b_[0], b_[1])}, rO, oq, 0, 0); \
    FENCE();                                                                                            \
  } while (0)
      if constexpr (KD == 0) UNIT(1, 2, 3, wv, L_PB + wv * 1024, 4096);
      else UNIT(1, 1, tc, jc, tc == 3 ? PB(0) : PB(2) + jc * 1024, 5120);
      if constexpr (KD == 1) UNIT(0, 0, 2, jc + 1, PB(2) + jc * 1024 + 1024, 5120);
    };
#define KILL(V, N) _Pragma("unroll") for (int f_ = 0; f_ < (N); ++f_) asm volatile("" : "=v"(V[f_]))
    int arm = wv >> 1;
    if (arm < 2) {
      u32x4 W4[35], A4[AW4];
      u32x2 x1v[3];
      g3(IC<0>{}, W4, A4, x1v);
      g4(IC<0>{}, W4, A4, x1v);
      KILL(W3, 40 - NR3);
    }
    asm volatile("" : "+s"(arm));
    if (arm >= 2) {
      u32x4 W4[35], A4[AW4];
      u32x2 x1v[3];
      g3(IC<1>{}, W4, A4, x1v);
      if (arm == 3) {
        g4(IC<2>{}, W4, A4, x1v);
        KILL(W4, 30);
        KILL(A4, 6);
      }
      asm volatile("" : "+s"(arm));
      if (arm == 2) g4(IC<1>{}, W4, A4, x1v);
    }
    STAMP(27);
    CLK(31);
  }
}

extern "C" void kernel_launch(void* const* d_in, const int* in_sizes, int n_in, void* d_out, int out_size,
                              void* d_ws, size_t ws_size, hipStream_t stream) {
  const bf16* A_q = (const bf16*)d_in[0];
  const bf16* A_k = (const bf16*)d_in[1];
  const bf16* A_v = (const bf16*)d_in[2];
  const bf16* A_shift_k = (const bf16*)d_in[3];
  const bf16* A_ssm_k = (const bf16*)d_in[4];
  const bf16* A_ssm_D = (const bf16*)d_in[5];
  const bf16* w1 = (const bf16*)d_in[7];
  const bf16* w2 = (const bf16*)d_in[8];
  const bf16* w3 = (const bf16*)d_in[9];
  const bf16* w4 = (const bf16*)d_in[10];
  const bf16* c = (const bf16*)d_in[11];
  const bf16* w_mod = (const bf16*)d_in[12];
  char* ws = (char*)d_ws;
  (void)hipFuncSetAttribute((const void*)k_main, hipFuncAttributeMaxDynamicSharedMemorySize, L_TOT);
  k_main<<<256, 512, L_TOT, stream>>>(A_k, A_v, ws, A_shift_k, A_ssm_k, w_mod, c, w3, w4, A_q, w1, w2, A_ssm_D, (bf16*)d_out);
}
```

```cpp
#define RTE 7
#define FZ 2
#include <hip/hip_runtime.h>
#include <hip/hip_bf16.h>
#include <stdint.h>

typedef __hip_bfloat16 bf16;
typedef __attribute__((ext_vector_type(8))) short bf16x8;
typedef __attribute__((ext_vector_type(4))) short s16x4;
typedef __attribute__((ext_vector_type(4))) float f32x4;
typedef __attribute__((ext_vector_type(2))) float f32x2;
typedef __attribute__((ext_vector_type(4))) unsigned u32x4;
typedef __attribute__((ext_vector_type(2))) unsigned u32x2;
#define LAS __attribute__((address_space(3)))

static constexpr int S_ = 2048;
static constexpr int D_ = 640;
static constexpr int F_ = 1728;
static constexpr unsigned SENT = 0xffffffffu;
static constexpr int RS3 = 0x00020000;
static constexpr unsigned OOB = 0x40000000u;

static constexpr unsigned SD2 = S_ * D_ * 2;
static constexpr unsigned DF2 = D_ * F_ * 2;
static constexpr unsigned O_WT3 = 0, O_WT4 = DF2;
static constexpr unsigned O_EP = 2 * DF2;
static constexpr unsigned O_MOD = O_EP + 256;
static constexpr unsigned O_MP = O_MOD + 16384;
static constexpr unsigned O_FL1 = O_MP + 131072;
static constexpr unsigned O_F2 = O_FL1;
static constexpr unsigned O_KVT = O_FL1 + 4096;
static constexpr unsigned O_HP = O_KVT, O_X1 = O_KVT;
static constexpr unsigned TS = 57344;
static constexpr unsigned O_H = O_KVT + 384 * 8192;
static constexpr unsigned O_Z = O_H, O_H1 = O_H + 20480;
static constexpr unsigned O_ST3 = O_H + 128 * TS;
static constexpr unsigned O_ST4 = O_ST3 + S_ * 8;
static constexpr unsigned O_XO = O_ST4 + S_ * 16 * 8;
static constexpr unsigned O_W12 = O_XO + SD2;
static constexpr unsigned WS_USED = O_W12 + 256 * 5120;
#define KVROW(c) ((c) < 384u ? O_KVT + (c) * 8192u : O_H + 40960u + (((c) - 384u) >> 1) * TS + ((c) & 1u) * 8192u)
static constexpr unsigned CODED = 0x7f7f7f7fu;
static constexpr unsigned CSH = 98u << 23;
static constexpr float PTHR = 0x1p48f, PMAX = 0x1p47f, CMUL = 0x1p98f, CMAX = 0x1p127f;
__device__ __forceinline__ float plainv(float x) { return __builtin_amdgcn_fmed3f(x, -PMAX, PMAX); }
__device__ __forceinline__ float codedv(float x) { return __builtin_copysignf(__builtin_amdgcn_fmed3f(__builtin_fabsf(x * CMUL), PTHR, CMAX), x); }

__device__ __forceinline__ float rbf(float x) { return __bfloat162float(__float2bfloat16(x)); }
__device__ __forceinline__ float lo2f(unsigned w) { return __uint_as_float(w << 16); }
__device__ __forceinline__ float hi2f(unsigned w) { return __uint_as_float(w & 0xffff0000u); }
typedef __bf16 bf16v2 __attribute__((ext_vector_type(2)));
__device__ __forceinline__ unsigned pack2(float a, float b) {
  bf16v2 r = __builtin_convertvector(f32x2{a, b}, bf16v2);
  return __builtin_bit_cast(unsigned, r);
}

static constexpr float GC1 = -2.f * 0.7978845608028654f * 1.4426950408889634f;
static constexpr float GC2 = GC1 * 0.044715f;
__device__ __forceinline__ float row16_sum(float v) {
  v += __builtin_bit_cast(float, __builtin_amdgcn_update_dpp(0, __builtin_bit_cast(int, v), 0xB1, 0xf, 0xf, true));
  v += __builtin_bit_cast(float, __builtin_amdgcn_update_dpp(0, __builtin_bit_cast(int, v), 0x4E, 0xf, 0xf, true));
  v += __builtin_bit_cast(float, __builtin_amdgcn_update_dpp(0, __builtin_bit_cast(int, v), 0x141, 0xf, 0xf, true));
  v += __builtin_bit_cast(float, __builtin_amdgcn_update_dpp(0, __builtin_bit_cast(int, v), 0x140, 0xf, 0xf, true));
  return v;
}
typedef volatile LAS unsigned* ldsw_t;
static constexpr int CV_STR = 4160;
static constexpr int CV_U = 8 * CV_STR;
static constexpr int CV_SLOT = CV_U + 4608;
static constexpr int SL_A = 46592, SL_C = SL_A + CV_SLOT, SL_B = SL_C + CV_SLOT;
static constexpr int L_KV = 0;
static constexpr int L_RT = L_KV + 24576;
static constexpr int L_MP = L_RT + 20480;
static constexpr int L_XCH = 139264;
static constexpr int L_XA = 81920, L_XB = 40960;
static constexpr int L_PB = 122880;
static constexpr int L_CTL = 160256;
static constexpr int L_MOD = 103424;
static constexpr int L_GB = L_CTL + 64;
static constexpr int L_CS3 = L_CTL + 384;
static constexpr int L_ST4 = L_CTL + 1408;
static constexpr int L_STAMP = L_CTL + 2432;
static constexpr int L_TOT = 163840;
static_assert(L_MP + 1024 <= SL_A && SL_B + CV_SLOT <= L_CTL && L_MOD >= SL_C + 16384 && L_MOD >= 102400 && L_MOD + 7680 <= SL_C + CV_U, "lds");
static constexpr int YROW = 1296;
#define ZCE 0
enum { P_KV = 1, P_K, P_B, P_M, P_W, P_B2, P_U, P_C2, P_A2,
  P_RT, P_RO, P_GA, P_ZC,
  P_F2, P_TH, P_X, P_X2, P_T, P_Z, P_RP, P_Z2, P_L1, P_XF1, P_G1, P_XF2, P_G2, P_SF, P_SA, P_S0, P_KA, P_H0, P_S1, P_KB, P_H1, P_P0 };
static constexpr unsigned SPIN_CAP = 1u << 21;
static constexpr unsigned RETRY_CAP = 1u << 15;

__device__ __forceinline__ float swap_add16(float v) {
  const unsigned u = __float_as_uint(v);
  auto r = __builtin_amdgcn_permlane16_swap(u, u, false, false);
  return __uint_as_float((unsigned)r[0]) + __uint_as_float((unsigned)r[1]);
}
__device__ __forceinline__ float swap_add32(float v) {
  const unsigned u = __float_as_uint(v);
  auto r = __builtin_amdgcn_permlane32_swap(u, u, false, false);
  return __uint_as_float((unsigned)r[0]) + __uint_as_float((unsigned)r[1]);
}
__device__ __forceinline__ float wave_sum2(float v) { return swap_add32(swap_add16(row16_sum(v))); }
__device__ __forceinline__ f32x2 up2(unsigned u) { return f32x2{lo2f(u), hi2f(u)}; }
__device__ __forceinline__ f32x2 rbf2(f32x2 v) { return up2(pack2(v[0], v[1])); }
#define FMA2 __builtin_elementwise_fma
__device__ __forceinline__ f32x2 rf2(f32x2 v, f32x2 cp, f32x2 cn) { return FMA2(v, cn, FMA2(v, cp, v)); }
__device__ __forceinline__ void ln5(const f32x2* in, unsigned* out) {
  const f32x2 s2 = ((in[0] + in[1]) + (in[2] + in[3])) + in[4];
  const float mean = wave_sum2(s2[0] + s2[1]) * (1.f / (float)D_);
  f32x2 d[5];
#pragma unroll
  for (int j = 0; j < 5; ++j) d[j] = in[j] - mean;
  const f32x2 q2 = (d[0] * d[0] + d[1] * d[1]) + (d[2] * d[2] + d[3] * d[3]) + d[4] * d[4];
  const float m = rbf(mean);
#pragma unroll
  for (int j = 0; j < 5; ++j) d[j] = rbf2(in[j] - m);
  const float q = wave_sum2(q2[0] + q2[1]);
  const float var = rbf(q * (1.f / (float)D_));
  const float den = rbf(__builtin_amdgcn_sqrtf(rbf(var + 1e-5f)));
  const float rinv = __builtin_amdgcn_rcpf(den);
#pragma unroll
  for (int j = 0; j < 5; ++j) { const f32x2 o = d[j] * rinv; out[j] = pack2(o[0], o[1]); }
}
#define PIN(x) asm volatile("" : "+v"(x))
#define STG(N, stmt) do { _Pragma("unroll") for (int i = 0; i < N; ++i) { stmt; } __builtin_amdgcn_sched_barrier(0); } while (0)
template <int N>
__device__ __forceinline__ void wave_sums(float* v) {
  STG(N, v[i] += __builtin_bit_cast(float, __builtin_amdgcn_update_dpp(0, __builtin_bit_cast(int, v[i]), 0xB1, 0xf, 0xf, true)); PIN(v[i]));
  STG(N, v[i] += __builtin_bit_cast(float, __builtin_amdgcn_update_dpp(0, __builtin_bit_cast(int, v[i]), 0x4E, 0xf, 0xf, true)); PIN(v[i]));
  STG(N, v[i] += __builtin_bit_cast(float, __builtin_amdgcn_update_dpp(0, __builtin_bit_cast(int, v[i]), 0x141, 0xf, 0xf, true)); PIN(v[i]));
  STG(N, v[i] += __builtin_bit_cast(float, __builtin_amdgcn_update_dpp(0, __builtin_bit_cast(int, v[i]), 0x140, 0xf, 0xf, true)); PIN(v[i]));
  unsigned a[N], b[N];
  STG(N, a[i] = __float_as_uint(v[i]); b[i] = a[i]; PIN(b[i]));
  STG(N, { auto r = __builtin_amdgcn_permlane16_swap(a[i], b[i], false, false); a[i] = r[0]; b[i] = r[1]; PIN(a[i]); PIN(b[i]); });
  STG(N, v[i] = __uint_as_float(a[i]) + __uint_as_float(b[i]); PIN(v[i]));
  STG(N, a[i] = __float_as_uint(v[i]); b[i] = a[i]; PIN(b[i]));
  STG(N, { auto r = __builtin_amdgcn_permlane32_swap(a[i], b[i], false, false); a[i] = r[0]; b[i] = r[1]; PIN(a[i]); PIN(b[i]); });
  STG(N, v[i] = __uint_as_float(a[i]) + __uint_as_float(b[i]); PIN(v[i]));
}
template <int N>
__device__ __forceinline__ void gelu_s(float* e, float ga_, float gb2, float gd, unsigned* pk) {
  float x[N];
  STG(N, x[i] = e[i] * e[i]; PIN(x[i]));
  STG(N, x[i] = __builtin_fmaf(x[i], gb2, ga_); PIN(x[i]));
  STG(N, x[i] *= e[i]; PIN(x[i]));
  STG(N, x[i] = __builtin_amdgcn_exp2f(x[i]); PIN(x[i]));
  STG(N, x[i] = __builtin_fmaf(x[i], gd, gd); PIN(x[i]));
  STG(N, x[i] = __builtin_amdgcn_rcpf(x[i]); PIN(x[i]));
  STG(N, e[i] *= x[i]; PIN(e[i]));
  STG(N / 4, e[4 * i + 1] = plainv(e[4 * i + 1]); PIN(e[4 * i + 1]));
  STG(N / 2, pk[i] = pack2(e[2 * i], e[2 * i + 1]); PIN(pk[i]));
}
__device__ __forceinline__ f32x4 mfma16(u32x4 a, u32x4 b, f32x4 c) {
  return __builtin_amdgcn_mfma_f32_16x16x32_bf16(__builtin_bit_cast(bf16x8, a), __builtin_bit_cast(bf16x8, b), c, 0, 0, 0);
}
template <bool FIRST> __device__ __forceinline__ void mf(f32x4& c, const u32x4& a, const u32x4& b) {
  if (FIRST) asm volatile("v_mfma_f32_16x16x32_bf16 %0, %1, %2, 0" : "=v"(c) : "v"(a), "v"(b));
  else asm volatile("v_mfma_f32_16x16x32_bf16 %0, %1, %2, %0" : "+v"(c) : "v"(a), "v"(b));
}
__device__ __forceinline__ unsigned canon(unsigned x) { return x < 0xfffffffeu ? x : 0xfffffffeu; }
__device__ __forceinline__ bool bad1(unsigned a) { return __builtin_amdgcn_ballot_w64(a == SENT) != 0ull; }
__device__ __forceinline__ float amax2(const u32x4& a) { return __builtin_amdgcn_fmed3f(__builtin_fabsf(__uint_as_float(a[0])), __builtin_fabsf(__uint_as_float(a[2])), CMAX); }
__device__ __forceinline__ float amin2(const u32x4& a) { return __builtin_amdgcn_fmed3f(__builtin_fabsf(__uint_as_float(a[0])), __builtin_fabsf(__uint_as_float(a[2])), 0.f); }
__device__ __forceinline__ float amax3(float m, const u32x4& a) { float r; asm("v_max3_f32 %0, %1, |%2|, |%3|" : "=v"(r) : "v"(m), "v"(a[0]), "v"(a[2])); return r; }
template <bool CD> __device__ __forceinline__ bool any_bad4(u32x4& a, float thr, unsigned csh, float cmx) {
  const float m = __builtin_amdgcn_fmed3f(__builtin_fabsf(__uint_as_float(a[0])), __builtin_fabsf(__uint_as_float(a[2])), CD ? 0.f : cmx);
  if (CD) { a[0] -= csh; a[2] -= csh; }
  return __builtin_amdgcn_ballot_w64(CD ? m < thr : m >= thr) != 0ull;
}
__device__ __forceinline__ u32x4 widen(unsigned x0, unsigned x1, unsigned y0, unsigned y1) {
  const auto p0 = __builtin_amdgcn_permlane16_swap(x0, y0, false, false);
  const auto p1 = __builtin_amdgcn_permlane16_swap(x1, y1, false, false);
  return u32x4{(unsigned)p0[0], (unsigned)p1[0], (unsigned)p0[1], (unsigned)p1[1]};
}
#define DMA1(M) asm volatile("s_mov_b32 m0, %3\n\ts_nop 0\n\t" "buffer_load_dwordx4 %0, %1, %2 offen " M "lds" :: "v"(voff), "s"(rs), "s"(soff), "s"(dst) : "memory")
#define DMA4(M) asm volatile("s_mov_b32 m0, %3\n\ts_nop 0\n\t" \
                   "buffer_load_dwordx4 %0, %1, %2 offen " M "lds\n\tbuffer_load_dwordx4 %0, %1, %2 offen offset:1024 " M "lds\n\t" \
                   "buffer_load_dwordx4 %0, %1, %2 offen offset:2048 " M "lds\n\tbuffer_load_dwordx4 %0, %1, %2 offen offset:3072 " M "lds" \
                   :: "v"(voff), "s"(rs), "s"(soff), "s"(dst) : "memory")
template <int N, int AUX>
__device__ __forceinline__ void dma(unsigned voff, u32x4 rs, unsigned soff, unsigned dst) {
  if (N == 4) { if (AUX == 1) DMA4("sc1 "); else if (AUX == 2) DMA4("nt "); else DMA4(""); }
  else { if (AUX == 1) DMA1("sc1 "); else if (AUX == 2) DMA1("nt "); else DMA1(""); }
}
__device__ __forceinline__ void st128(u32x4 v, __amdgpu_buffer_rsrc_t r, unsigned voff, unsigned base, int) {
  __builtin_amdgcn_raw_buffer_store_b128(v, r, voff + base, 0, 16);
}
__device__ __forceinline__ u32x4 mk_rs(const void* p, unsigned bytes) {
  const unsigned long long a = (unsigned long long)p;
  return u32x4{(unsigned)a, (unsigned)(a >> 32) & 0xffffu, bytes, (unsigned)RS3};
}

#define USLEEP 2
#define DLY0 8
#define DLY1 3
#define DLY2 0
#define DLY3 2
#define NWH 8
#define ZG 8
#define ZSLEEP 2
#define AW4 8
#define AD3 3
#define RSLEEP 1
#define PHI 2
#define CPW 4
#define CPG 128u
#define W12 0
#define WDL 0
#define WSLEEP 2
#define LINW 0
#define NZ2 5
#define NS2 5
#define RS0 6
#define DLY3B 0
#define MV0 7
#define H0S 6
#define H1S 3
#define PK0 2
#define PE0 1
#define PK1 2
#define PE1 1
#define P40 1
#define P41 2
#define GSA 2
#define GSB 1
#define GSC 2
#define EA0 4
#define EA1 28
#define EC0 4
#define EC1 22
#define PB1 26
#define PB2 20
#define NL1 14
#define NP1 2
#define NL2 12
#define ROT 1
#define NA1 3
#define NP0 0
#define SL1 6
#define SL2 6
#define PH1 2
#define PH2 2
#define KG 42
#define KC3 12
#define PK2 2
#define KC3B 40
#define NPK 10
#define HG 4
#define WSP 3
#define PR1 3
#define HPZ 8
#define WS3 1
#define RB1 0
#define HFI 1
#define GZ 0
#define GZP 0
#define RPH 0
#define NGB 5
#define NS1 0
static_assert(!GZ || ZCE, "order of the progress words");
#define GSLEEP 4
#define TR 0
#define MGE 0
#define TZ 0
#define KE 0
#define TU1 200
#define TU2 260
#define TU 290
#define KL 5
#define PBE 18
#define NAF 6
#define W0U 4
#define U0 5
#define NPH 6
#define NGA 5
#define NR0 2
#define NR1 5
#define KVR 0
static_assert((NWH + NL1 + NP1) % 2 == 0 && NL2 % 2 == 0 && NWH + NL1 + NP1 + NL2 <= 40, "w3 schedule");
#define SPIN_STEP() do { ++spins; asm volatile("" : "+s"(spins)); } while (0)
#define SB() __builtin_amdgcn_sched_barrier(0)
#define FENCE() do { asm volatile("" ::: "memory"); __builtin_amdgcn_sched_barrier(0); } while (0)
#define LGKM0() do { __builtin_amdgcn_sched_barrier(0); asm volatile("s_waitcnt lgkmcnt(0)" ::: "memory"); } while (0)
#define BARRIER() do { LGKM0(); __builtin_amdgcn_s_barrier(); asm volatile("" ::: "memory"); } while (0)
#define VM0() __builtin_amdgcn_s_waitcnt(0x0F70)
#define PRIO(n) __builtin_amdgcn_s_setprio(n)
#define CHECK_A(CD, AV, i, hi, LD)                                                          \
  do {                                                                                      \
    if (__builtin_expect(any_bad4<CD>(AV, sthr, scsh, scmx), 0)) {                          \
      unsigned spins = 0;                                                                   \
      PRIO(0);                                                                              \
      do {                                                                                  \
        __builtin_amdgcn_s_sleep(RSLEEP);                                                   \
        _Pragma("unroll") for (int y_ = (i); y_ < (hi); ++y_) { LD(y_); }                   \
        SPIN_STEP();                                                                        \
      } while (any_bad4<CD>(AV, sthr, scsh, scmx) && spins < RETRY_CAP);                    \
      PRIO(PHI);                                                                            \
      VM0();                                                                                \
    }                                                                                       \
  } while (0)

#define ASM_MACROS \
  ".macro regno op\n.set r_, 0\n.set st_, 0\n.irpc c, \"\\op\"\n.ifc \\c, :\n.set st_, 2\n.endif\n.if st_ == 1\n.set r_, r_ * 10 + \\c\n.endif\n.ifc \\c, [\n.set st_, 1\n.endif\n.endr\n.endm\n" \
  ".macro retry n, cd, a, so, ct, t, vo, rs, sb, im, th, k, pr\n"                                   \
  "s_cbranch_vccnz Lc\\n\nLb\\n:\n.subsection 1\nLc\\n:\nregno \\a\n"              \
  "s_setprio 0\ns_add_u32 \\so, \\sb, \\im\ns_mov_b32 \\ct, 0\n"                                        \
  "Ll\\n:\ns_sleep 1\nbuffer_load_dwordx4 \\a, \\vo, \\rs, \\so offen sc1\ns_add_u32 \\ct, \\ct, 1\ns_waitcnt vmcnt(0)\n" \
  ".if \\cd\nv_med3_f32 \\t, |v[r_]|, |v[r_+2]|, 0\nv_cmp_gt_f32 vcc, \\th, \\t\n.else\nv_med3_f32 \\t, |v[r_]|, |v[r_+2]|, \\k\nv_cmp_le_f32 vcc, \\th, \\t\n.endif\n" \
  "s_cbranch_vccz Ld\\n\ns_cmp_lt_u32 \\ct, 0x8000\ns_cbranch_scc1 Ll\\n\n"                             \
  "Ld\\n:\n.if \\cd\nv_subrev_u32 v[r_], \\k, v[r_]\nv_subrev_u32 v[r_+2], \\k, v[r_+2]\n.endif\ns_setprio \\pr\ns_nop 1\ns_branch Lb\\n\n.subsection 0\n.endm\n" \
  ".macro need n, v, ct, ad, k\n"                                                                       \
  "v_cmp_gt_u32 vcc, \\k, \\v\ns_cbranch_vccnz Lc\\n\nLb\\n:\n.subsection 1\nLc\\n:\ns_mov_b32 \\ct, 0\n" \
  "Ll\\n:\nds_read_b32 \\v, \\ad\ns_waitcnt lgkmcnt(0)\nv_cmp_gt_u32 vcc, \\k, \\v\ns_cbranch_vccz Lb\\n\n" \
  "s_sleep 1\ns_add_u32 \\ct, \\ct, 1\ns_cmp_lt_u32 \\ct, 0x200000\ns_cbranch_scc1 Ll\\n\ns_branch Lb\\n\n.subsection 0\n.endm\n"
#define RETRY(CD, AV, BM, VOFF, SBASE, IMM, PR)                                                         \
  do {                                                                                                  \
    unsigned so_, ct_; float t_; register unsigned long long vc_ asm("vcc") = (BM);                                                                     \
    asm volatile("retry %=, %5, \"%0\", %1, %2, %3, %6, \"%7\", %8, %9, %10, %11, %12"                   \
                 : "+v"(AV), "=&s"(so_), "=&s"(ct_), "=&v"(t_), "+s"(vc_)                            \
                 : "i"((int)(CD)), "v"(VOFF), "s"(dW), "s"(SBASE), "i"(IMM), "s"(sthr), "s"((CD) ? scsh : __float_as_uint(scmx)), "i"(PR) : "scc", "memory"); \
  } while (0)
#define CMP_BAD(CD, BM, M) do { register unsigned long long vc_ asm("vcc");                             \
    if (CD) asm volatile("v_cmp_gt_f32 vcc, %1, %2" : "=s"(vc_) : "s"(sthr), "v"(M)); else asm volatile("v_cmp_le_f32 vcc, %1, %2" : "=s"(vc_) : "s"(sthr), "v"(M)); \
    BM = vc_; } while (0)
#define RETRY_C(AV, BM, VOFF, SBASE, IMM, PR) RETRY(1, AV, BM, VOFF, SBASE, IMM, PR)
#define NEED_AT(V, ADDR, K) do { unsigned ct_; asm volatile("need %=, %0, %1, %2, %3" : "+v"(V), "=&s"(ct_) : "v"(ADDR), "s"(K) : "vcc", "scc", "memory"); } while (0)

#define STAMP(i)
#define CLK(i)
#define KATTR
#define TS_(i)
#define TX_(i)
#define TSHI 62
__device__ __forceinline__ u32x4 rd_tr(const LAS char* p) {
  const s16x4 lo = __builtin_amdgcn_ds_read_tr16_b64_v4i16((LAS s16x4*)p);
  const s16x4 hi = __builtin_amdgcn_ds_read_tr16_b64_v4i16((LAS s16x4*)(p + 2560));
  const bf16x8 o = {lo[0], lo[1], lo[2], lo[3], hi[0], hi[1], hi[2], hi[3]};
  return __builtin_bit_cast(u32x4, o);
}
__device__ __forceinline__ float dpp_add(float v, int ctrl) {
  return v + __builtin_bit_cast(float, ctrl == 0 ? __builtin_amdgcn_update_dpp(0, __builtin_bit_cast(int, v), 0x128, 0xf, 0xf, true)
                                    : ctrl == 1 ? __builtin_amdgcn_update_dpp(0, __builtin_bit_cast(int, v), 0x124, 0xf, 0xf, true)
                                                : __builtin_amdgcn_update_dpp(0, __builtin_bit_cast(int, v), 0x4E, 0xf, 0xf, true));
}

template <int V> struct IC { static constexpr int v = V; };
template <int I, int N, class F> __device__ __forceinline__ void sfor(F&& f) { if constexpr (I < N) { f(IC<I>{}); sfor<I + 1, N>(f); } }
constexpr int cv_i(int m) { int i = 0; while (m >= 8 - i) { m -= 8 - i; ++i; } return i; }
constexpr int cv_d(int m) { int i = 0; while (m >= 8 - i) { m -= 8 - i; ++i; } return 7 - i - m; }
constexpr int kl_i(int m) { return m < 50 - 5 * KL ? m / 5 : m < 50 - 2 * KL ? 10 - KL + (m - 50 + 5 * KL) / 3 : 10 - KL + (m - 50 + 2 * KL) / 2; }
constexpr int kl_j(int m) { return m < 50 - 5 * KL ? m % 5 : m < 50 - 2 * KL ? (m - 50 + 5 * KL) % 3 : 3 + (m - 50 + 2 * KL) % 2; }
constexpr int cl_t(int m) { int t = 0; while (m > t) { m -= t + 1; ++t; } return t; }
constexpr int cl_i(int m) { int t = 0; while (m > t) { m -= t + 1; ++t; } return m; }
constexpr bool cv_free(int m, int k) {
  return k == 0 ? (m >= 64 && m < 111 && m != 65 && m != 69 && m != 72 && m != 75 && m != 77 && m != 94 && m != 95 && m != 104 && m != 110 && (m < 72 + RS0 || m >= 88 + RS0))
                : (m >= 123 && m != 126 && m != 131 && m != 132 && m != 139 && m != 140);
}
constexpr int cv_slot(int m, int k) { if (!cv_free(m, k)) return -1; int n = 0; for (int y = 0; y < m; ++y) n += cv_free(y, k); return n; }
static_assert(cv_slot(109, 0) >= 15 && cv_slot(143, 1) >= 15, "slots");
constexpr int cc_i(int J, int c) { for (int i = 0; i < 8; ++i) for (int t = 3; t >= 0; --t) if (i <= ((J >> (4 * t)) & 15) && c-- == 0) return i; return 0; }
constexpr int cc_t(int J, int c) { for (int i = 0; i < 8; ++i) for (int t = 3; t >= 0; --t) if (i <= ((J >> (4 * t)) & 15) && c-- == 0) return t; return 0; }
#define KWAIT
__global__ __launch_bounds__(512, 2) KATTR void k_main(const bf16* kin_, const bf16* vin_, char* ws_, const bf16* shift_k_, const bf16* ssm_k_,
                                                 const bf16* w_mod_, const bf16* cin_, const bf16* w3_,
                                                 const bf16* w4_, const bf16* q_, const bf16* w1_, const bf16* w2_,
                                                 const bf16* ssm_D_, bf16* out_) {
  typedef __attribute__((ext_vector_type(8))) unsigned long long u64x8;
  typedef __attribute__((ext_vector_type(4))) unsigned long long u64x4;
  typedef __attribute__((ext_vector_type(2))) unsigned long long u64x2;
  u64x8 ka;
  u64x4 kb;
  u64x2 kc;
  int tid_ = threadIdx.x, bx_ = blockIdx.x;
  unsigned long long pc0;
  unsigned code_bytes;
  asm volatile("s_load_dwordx16 %0, %6, 0x0\n\ts_load_dwordx8 %1, %6, 0x40\n\ts_load_dwordx4 %2, %6, 0x60\n\ts_getpc_b64 %3\n"
               "Lpc0:\n\ts_mov_b32 %4, Lcold0 - Lpc0\n" KWAIT ".subsection 1\nLcold0:\n.subsection 0"
               : "=&s"(ka), "=&s"(kb), "=&s"(kc), "=&s"(pc0), "=&s"(code_bytes), "+s"(bx_) : "s"(__builtin_amdgcn_kernarg_segment_ptr()));
  asm volatile(ASM_MACROS ".subsection 2\n.fill 256, 4, 0xbf800000\n.subsection 0" : "+v"(tid_));
  extern __shared__ __attribute__((aligned(16))) char lds_[];
  LAS char* lds = (LAS char*)lds_;
  ldsw_t LW = (ldsw_t)(lds + L_CTL);
  const int tid = tid_ & 511, lane = tid & 63;
  const int wv = __builtin_amdgcn_readfirstlane(tid >> 6) & 7;
  const int bx = bx_ & 255;
  const int mt = (bx & 7) * 4 + (bx >> 6), nt = (bx >> 3) & 7;
  const int n0 = nt * 80, m0 = mt * 64;
  const int l15 = lane & 15, g = lane >> 4;
  const int mg = wv & 3, kh = wv >> 2;
  const int gm = (wv >> 2) * 2 + (wv & 1), gk = ((wv >> 1) ^ (wv >> 2)) & 1, gp = wv ^ 2;
  const unsigned l16 = (unsigned)lane * 16u;
  TS_(0);
  STAMP(0);
  CLK(30);
  unsigned cp0 = 0;
  if (wv == CPW) {
    asm volatile("buffer_load_dword %0, %1, %2, 0 offen" : "=v"(cp0) : "v"((unsigned)((bx >> 3) + 32 * lane) * CPG), "s"(mk_rs((const void*)pc0, code_bytes)) : "memory");
  }
  FENCE();
  const int tok0 = 32 * ((bx & 7) * 8 + (bx >> 5)), ch0 = 160 * ((bx >> 3) & 3);
  unsigned ko[3], vo[3];
#pragma unroll
  for (int i = 0; i < 3; ++i) {
    const unsigned s = (unsigned)((mg + 4 * i) * 64 + lane);
    const unsigned gr = __umul24(s, 800u) >> 16, w = s - __umul24(82u, gr), rr = __umul24(w, 205u) >> 12;
    unsigned o = (unsigned)(tok0 * (D_ * 2) + ch0 * 2) + __umul24(gr, 4u * D_ * 2) + __umul24(rr, D_ * 2 - 320) + w * 16u;
    asm volatile("" : "+v"(o));
    ko[i] = (w < 80u && gr < 9u) ? o - 4u * D_ * 2 : OOB;
    vo[i] = (w < 80u && gr < 8u) ? o : OOB;
    asm volatile("" : "+v"(ko[i]), "+v"(vo[i]));
  }
  FENCE();
  asm volatile("s_waitcnt lgkmcnt(0)" : "+s"(ka), "+s"(kb), "+s"(kc));
  const bf16* kin = (const bf16*)ka[0];
  const bf16* vin = (const bf16*)ka[1];
  char* ws = (char*)ka[2];
  const bf16* shift_k = (const bf16*)ka[3];
  const bf16* ssm_k = (const bf16*)ka[4];
  const bf16* w_mod = (const bf16*)ka[5];
  const bf16* cin = (const bf16*)ka[6];
  const bf16* w3 = (const bf16*)ka[7];
  const bf16* w4 = (const bf16*)kb[0];
  const bf16* q = (const bf16*)kb[1];
  const bf16* w1 = (const bf16*)kb[2];
  const bf16* w2 = (const bf16*)kb[3];
  const bf16* ssm_D = (const bf16*)kc[0];
  bf16* out = (bf16*)kc[1];
  const __amdgpu_buffer_rsrc_t rW = __builtin_amdgcn_make_buffer_rsrc((void*)ws, 0, WS_USED, RS3);
  const u32x4 dW = mk_rs(ws, WS_USED);
  u32x2 ep;
  asm volatile("buffer_load_dwordx2 %0, off, %1, %2 sc1" : "=v"(ep) : "s"(dW), "s"(O_EP) : "memory");
  FENCE();
  if (kh == 0) {
    const u32x4 dK = mk_rs(kin, SD2), dV = mk_rs(vin, SD2);
#pragma unroll
    for (int i = 0; i < 3; ++i) dma<1, 0>(ko[i], dK, 0u, (unsigned)(L_KV + (mg + 4 * i) * 1024));
#pragma unroll
    for (int i = 0; i < 3; ++i) dma<1, 0>(vo[i], dV, 0u, (unsigned)(L_KV + 12288 + (mg + 4 * i) * 1024));
  }
  u32x4 sk[3];
  if (kh == 0) {
    asm volatile("buffer_load_dwordx4 %0, %3, %5, %6 offen\n\tbuffer_load_dwordx4 %1, %3, %5, %6 offen offset:16\n\tbuffer_load_dwordx4 %2, %4, %5, %7 offen"
                 : "=&v"(sk[0]), "=&v"(sk[1]), "=&v"(sk[2])
                 : "v"(((unsigned)lane >> 3) * 32u), "v"((unsigned)g * 16u), "s"(mk_rs(shift_k, D_ * 8)), "s"(ch0 * 8 + mg * 256), "s"(ch0 * 8 + 1024 + mg * 64) : "memory");
  }
  FENCE();
  if (tid >= 504) LW[tid - 504] = 0u;
  if (tid >= 256 && tid < 352) *(LAS u32x4*)(lds + SL_A + ((tid - 256) >> 5) * CV_SLOT + CV_U + (tid & 31) * 16) = u32x4{0u, 0u, 0u, 0u};
  if (tid >= 384 && tid < 408) *(LAS u32x4*)(lds + SL_A + ((tid - 384) >> 3) * CV_SLOT + (tid & 7) * CV_STR + 257 * 16) = u32x4{0u, 0u, 0u, 0u};
  STAMP(29);
  if constexpr (TSHI == 23) TX_(30);
  BARRIER();

  const unsigned psig = (unsigned)L_CTL + (unsigned)wv * 4u;
#define SIG(k) do { unsigned long long sv_; asm volatile("s_mov_b64 %0, exec\n\ts_mov_b64 exec, 1\n\tds_write_b32 %1, %2\n\ts_mov_b64 exec, %0" : "=&s"(sv_) : "v"(psig), "v"((unsigned)(k)) : "memory"); } while (0)
#define WAIT_AT(addr, k)                                                                                \
  do {                                                                                                  \
    unsigned tv_, tc_;                                                                                  \
    asm volatile("s_mov_b32 %1, 0\n"                                                                    \
                 "L%=_a:\n\tds_read_b32 %0, %2\n\ts_waitcnt lgkmcnt(0)\n\tv_cmp_gt_u32 vcc, %3, %0\n\ts_cbranch_vccz L%=_b\n\t"    \
                 "s_sleep 1\n\ts_add_u32 %1, %1, 1\n\ts_cmp_lt_u32 %1, 0x200000\n\ts_cbranch_scc1 L%=_a\n"            \
                 "L%=_b:" : "=&v"(tv_), "=&s"(tc_) : "v"(addr), "i"(k) : "vcc", "scc", "memory");       \
  } while (0)
#define WAIT_2(k0, k1)                                                                                 \
  do {                                                                                                  \
    unsigned tv_, tc_;                                                                                  \
    asm volatile("s_mov_b32 %1, 0\n"                                                                    \
                 "L%=_a:\n\tds_read_b32 %0, %2\n\ts_waitcnt lgkmcnt(0)\n\tv_cmp_gt_u32 vcc, %3, %0\n\ts_cbranch_vccz L%=_b\n\t"    \
                 "s_sleep 1\n\ts_add_u32 %1, %1, 1\n\ts_cmp_lt_u32 %1, 0x200000\n\ts_cbranch_scc1 L%=_a\n"            \
                 "L%=_b:" : "=&v"(tv_), "=&s"(tc_) : "v"((unsigned)L_CTL + ((unsigned)lane & 7u) * 4u), "v"((lane & 4) ? (unsigned)(k1) : (unsigned)(k0)) : "vcc", "scc", "memory"); \
  } while (0)
#define WAIT_ALL(k) WAIT_AT((unsigned)L_CTL + ((unsigned)lane & 7u) * 4u, k)
#define WAIT_HALF(h, k) WAIT_AT((unsigned)L_CTL + ((unsigned)lane & 3u) * 4u + (unsigned)(h) * 16u, k)

  const int sb3 = nt < 6 ? 14 * nt : 84 + 12 * (nt - 6);
  const bool two = kh == 0 || nt < 6;
  const int jl = kh ? 4 + wv : 2 * wv, jm = kh ? 12 + (wv & 1) : 2 * wv + 1, hb = wv > 5 ? 1 : 0;
  const int k4 = 7 * wv - (wv > 5 ? 2 : wv > 4 ? 1 : 0);
  const unsigned w4a = O_WT4 + (unsigned)(nt * 54 + k4) * 5120u;
  unsigned w3s[4] = {O_WT3 + (unsigned)(sb3 + jl) * 20480u + (unsigned)hb * 10240u, two ? O_WT3 + (unsigned)(sb3 + jm) * 20480u + (unsigned)hb * 10240u : OOB,
                     O_WT3 + (unsigned)(sb3 + jl) * 20480u - (unsigned)hb * 10240u, kh ? w4a - 10240u : O_WT3 + (unsigned)(sb3 + jm) * 20480u};
  f32x2 cp = {65536.f, 65536.f}, cn = {-65536.f, -65536.f};
  asm volatile("" : "+s"(cp), "+s"(cn));
  u32x4 W3[40];
#define LD_W3U(f) W3[f] = __builtin_amdgcn_raw_buffer_load_b128(rW, l16 + ((f) < 8 ? ((f) >> 1) * 1024 : 0), w3s[((f) & 1) + ((f) >= 20 ? 2 : 0)] + ((f) < 8 ? 0 : ((f) >> 1) * 1024), 16)
#define LD_W3R(a, b) do { _Pragma("unroll") for (int f_ = (a); f_ < (b); ++f_) LD_W3U(f_); } while (0)
#define VMW(n) asm volatile("s_waitcnt vmcnt(%0)" :: "i"(n) : "memory")
  const __amdgpu_buffer_rsrc_t rW1 = __builtin_amdgcn_make_buffer_rsrc((void*)w1, 0, D_ * D_ * 2, RS3);
  const __amdgpu_buffer_rsrc_t rW2 = __builtin_amdgcn_make_buffer_rsrc((void*)w2, 0, D_ * D_ * 2, RS3);
  const u32x4 dW1 = mk_rs(w1, D_ * D_ * 2), dW2 = mk_rs(w2, D_ * D_ * 2);
#define W12_G(gw) unsigned gw[5]; { unsigned ln_ = (unsigned)lane; asm volatile("" : "+v"(ln_)); \
    _Pragma("unroll") for (int j_ = 0; j_ < 5; ++j_) { const unsigned p_ = ln_ + 64u * j_, r_ = __umul24(p_, 6554u) >> 16; \
      gw[j_] = __umul24((r_ & 12u) * 2u + (r_ >> 4) * 4u + (r_ & 3u), D_ * 2) + (p_ - __umul24(r_, 10u)) * 16u + (unsigned)n0 * 2u; } }

  const int s0 = m0 + nt * 8;
  const int srow = s0 + wv;
  unsigned qw[5], hw[5];
  unsigned long long epn;
  unsigned tA, tB;
  u32x4 WS2[10];
#define LD_WS2U(a, b) _Pragma("unroll") for (int y_ = (a); y_ < (b); ++y_) asm volatile("buffer_load_dwordx4 %0, %1, %2, %3 offen ; ws2" : "=v"(WS2[y_]) : "v"(gw[y_ % 5]), "s"(dW2), "s"((wv + 8 * (y_ / 5)) * (32 * D_ * 2)) : "memory")
#define LD_WS2(a, b) _Pragma("unroll") for (int y_ = (a); y_ < (b); ++y_) WS2[y_] = __builtin_amdgcn_raw_buffer_load_b128(rW2, gw[y_ % 5], (wv + 8 * (y_ / 5)) * (32 * D_ * 2), 0)
  unsigned ry = (unsigned)(L_XCH + wv * YROW) + l16, rz = (unsigned)(L_XCH + wv * YROW + 1024 + 4 * lane);
  f32x2 xq[5], ha[5], ca[5], sb[5];
  float mc, mb;
  const int row = m0 + 16 * gm + l15;
  const int jb = 3 * gk;
  const unsigned ofr = (unsigned)(row >> 4) * 20480u + (unsigned)((n0 + 16 * jb + 4 * g) >> 3) * 256u + (unsigned)(g & 1) * 8u + (unsigned)l15 * 16u;
  {
    const int pi = (bx & 7) + 8 * (bx >> 4), half = (bx >> 3) & 1;
    const int chA = 2 * bx, chC = 512 + pi;
#define RD_MOD(v) do { const u32x4 b_ = *(const LAS u32x4*)(lds + L_MOD + (v) * (D_ * 2) + l16); mw[v][0] = b_[0]; mw[v][1] = b_[1]; mw[v][2] = b_[2]; mw[v][3] = b_[3]; \
                       mw[v][4] = *(const LAS unsigned*)(lds + L_MOD + (v) * (D_ * 2) + 1024 + lane * 4); } while (0)
#define LN1()                                                                                           \
  do {                                                                                                  \
    f32x2 xq_[5];                                                                                       \
    _Pragma("unroll") for (int j = 0; j < 5; ++j) xq_[j] = up2(qw[j]);                                  \
    ln5(xq_, hw);                                                                                       \
    _Pragma("unroll") for (int j = 0; j < 5; ++j) asm volatile("" : "+v"(hw[j]));                       \
  } while (0)
#define R_PRE() do {                                                                                    \
      unsigned mw[5][5];                                                                                \
      _Pragma("unroll") for (int v = 0; v < 5; ++v) RD_MOD(v);                                          \
      _Pragma("unroll") for (int j = 0; j < 5; ++j) {                                                   \
        xq[j] = up2(qw[j]);                                                                             \
        ha[j] = FMA2(up2(hw[j]), up2(mw[1][j]), up2(mw[0][j])) * up2(mw[2][j]); ca[j] = up2(mw[4][j]); sb[j] = up2(mw[3][j]); \
        asm volatile("" : "+v"(xq[j]), "+v"(ha[j]), "+v"(ca[j]), "+v"(sb[j]));                          \
      }                                                                                                 \
      const f32x2 c2_ = ((ca[0] + ca[1]) + (ca[2] + ca[3])) + ca[4], b2_ = ((sb[0] + sb[1]) + (sb[2] + sb[3])) + sb[4]; \
      mc = wave_sum2(c2_[0] + c2_[1]) * (1.f / (float)D_); mb = wave_sum2(b2_[0] + b2_[1]) * (1.f / (float)D_); \
      asm volatile("" : "+v"(mc), "+v"(mb));                                                            \
    } while (0)
    unsigned dv2;
#define GET_D() const unsigned dAB = __builtin_amdgcn_readlane(dv2, 0), dC_ = __builtin_amdgcn_readlane(dv2, 1), dC = (chC & 1) ? dC_ & 0xffff0000u : dC_ << 16
#define REQ_Q() do {                                                                                    \
      const __amdgpu_buffer_rsrc_t rQ = __builtin_amdgcn_make_buffer_rsrc((void*)q, 0, SD2, RS3);       \
      const u32x4 a = __builtin_amdgcn_raw_buffer_load_b128(rQ, l16, srow * (D_ * 2), 0);               \
      qw[0] = a[0]; qw[1] = a[1]; qw[2] = a[2]; qw[3] = a[3];                                           \
      qw[4] = __builtin_amdgcn_raw_buffer_load_b32(rQ, (unsigned)lane * 4u, srow * (D_ * 2) + 1024, 0); \
    } while (0)
#define REQ_D() dv2 = __builtin_amdgcn_raw_buffer_load_b32(__builtin_amdgcn_make_buffer_rsrc((void*)ssm_D, 0, D_ * 2, RS3), (unsigned)(lane ? (chC & ~1) : chA) * 2u, 0, 0)
#define TAGS()                                                                                          \
    {                                                                                                   \
      unsigned e0_, e1_;                                                                                \
      asm volatile("v_readfirstlane_b32 %0, %2\n\tv_readfirstlane_b32 %1, %3" : "=s"(e0_), "=s"(e1_) : "v"(ep[0]), "v"(ep[1])); \
      unsigned long long h = (unsigned long long)e1_ << 32 | e0_;                                       \
      epn = h + 1ull;                                                                                   \
      h += 0x9E3779B97F4A7C15ull;                                                                       \
      h = (h ^ (h >> 30)) * 0xBF58476D1CE4E5B9ull;                                                      \
      h = (h ^ (h >> 27)) * 0x94D049BB133111EBull;                                                      \
      h ^= h >> 31;                                                                                     \
      tA = (unsigned)h; tB = (unsigned)(h >> 32);                                                       \
    }
#define BUILD(SL, CC)                                                                                   \
  do {                                                                                                  \
    const u32x4 klo = *(const LAS u32x4*)(lds + (SL) + CV_U + 512 + 4064 - 16 * (CC)), khi = *(const LAS u32x4*)(lds + (SL) + CV_U + 512 + 4080 - 16 * (CC)); \
    const unsigned w[8] = {klo[0], klo[1], klo[2], klo[3], khi[0], khi[1], khi[2], khi[3]};             \
    _Pragma("unroll") for (int m = 0; m < 8; ++m) {                                                     \
      u32x4 o;                                                                                          \
      const int a = m >> 1;                                                                             \
      _Pragma("unroll") for (int t = 0; t < 4; ++t) {                                                   \
        if ((m & 1) == 0) o[t] = (w[a - t + 4] & 0xffffu) | (w[a - t + 3] & 0xffff0000u);               \
        else o[t] = (w[a - t + 4] >> 16) | (w[a - t + 4] << 16);                                        \
      }                                                                                                 \
      *(LAS u32x4*)(lds + (SL) + m * CV_STR + ((CC) + 1) * 16) = o;                                     \
    }                                                                                                   \
  } while (0)
#define ZCH(c) ((c) * 128u + ((c) / 160u) * (TS - 20480u))
#define ZOFF(JA, JB, CH) ((unsigned)((32 * ((g & 1) ? (JB) : (JA)) + 2 * l15 + (g >> 1)) >> 3) * (4u * TS) + ZCH((unsigned)(CH)) + (unsigned)((2 * l15 + (g >> 1)) & 7) * 16u)
#define Z2OFF(w) (O_Z + (unsigned)(4 * (w) + (l15 >> 2)) * (4u * TS) + ZCH((unsigned)chA) + (unsigned)((l15 & 3) * 64 + g * 16))
    const unsigned a00 = (unsigned)((l15 & 7) * CV_STR + (2032 - 8 * (l15 >> 3) + 8 * g) * 2) - 128u * mg;
    const unsigned b00 = (unsigned)(CV_U + 480 + 32 * l15 + 16 * g) - 128u * mg;
#define CV_RA(AV, SL, rd) _Pragma("unroll") for (int i = 0; i < 8; ++i) AV[8 * (rd) + i] = *(const LAS u32x4*)(lds + (SL) + a00 - 64 * (rd) - 512 * i)
#define CV_RB(BV, SL, rd) _Pragma("unroll") for (int i = 7; i >= 0; --i) BV[8 * (rd) + i] = *(const LAS u32x4*)(lds + (SL) + b00 + 512 * i - 64 * (rd))
    const int zja = mg ? (half ? 5 : 4) : (half ? 1 : 0), zjb = mg ? (half ? 6 : 7) : (half ? 2 : 3);
    const int t8 = tid & 255;
#define W_DMA(blk, j) dma<1, 0>(gw[j], (blk) < 20 ? dW1 : dW2, (unsigned)((blk) < 20 ? (blk) : (blk) - 4) * (32 * D_ * 2), (unsigned)((blk) * 5120 + (j) * 1024))
    if (__builtin_expect(kh != 0, 1)) {
      {
        const u32x4 dS = mk_rs(ssm_k, SD2);
        dma<1, 0>(l16, dS, (unsigned)(chC * (S_ * 2) + mg * 1024), (unsigned)(SL_C + CV_U + 512 + mg * 1024));
        dma<1, 0>(l16, dS, (unsigned)(chA * (S_ * 2) + mg * 1024), (unsigned)(SL_A + CV_U + 512 + mg * 1024));
        dma<1, 0>(l16, dS, (unsigned)((chA + 1) * (S_ * 2) + mg * 1024), (unsigned)(SL_B + CV_U + 512 + mg * 1024));
      }
      unsigned cpf = __builtin_amdgcn_raw_buffer_load_b32(rW, OOB, 0, 0);
      FENCE();
#define FILL_Z() do {                                                                                   \
      st128(u32x4{CODED, CODED, CODED, CODED}, rW, Z2OFF(mg), 0, 16);                                   \
      st128(u32x4{CODED, CODED, CODED, CODED}, rW, Z2OFF(wv), 0, 16);                                   \
      if (mg < 2) st128(u32x4{CODED, CODED, CODED, CODED}, rW, ZOFF(zja, zjb, chC), O_Z, 16);            \
    } while (0)
      unsigned fpa[3], fpv;
#pragma unroll
      for (int f_ = 0; f_ < 3; ++f_) {
        const unsigned o = (unsigned)(f_ * 4096 + t8 * 16);
        fpa[f_] = o < 10240u ? O_H1 + (unsigned)(bx >> 1) * TS + (bx & 1) * 10240u + o : o < 11264u ? O_ST4 + (unsigned)bx * 1024u + (o - 10240u)
                : (o < 11392u && (bx & 1)) ? O_ST3 + (unsigned)(bx >> 1) * 128u + (o - 11264u) : OOB;
        if (f_ == 2) fpv = o < 10240u ? CODED : SENT;
      }
#define FILL_P(a, b) _Pragma("unroll") for (int f_ = (a); f_ < (b); ++f_) { const unsigned fv = f_ < 2 ? CODED : fpv; st128(u32x4{fv, fv, fv, fv}, rW, fpa[f_], 0, 16); }
      u32x4 WS[10];
#define LD_WS(a, b) _Pragma("unroll") for (int x = (a); x < (b); ++x)                                   \
      WS[x] = __builtin_amdgcn_raw_buffer_load_b128(rW1, gw[x % 5], (9 + mg + 4 * (x / 5)) * (32 * D_ * 2), 0)
#define LD_WD(a, b) _Pragma("unroll") for (int x = (a); x < (b); ++x) {                                 \
      if (x < 10) W_DMA(mg + 4 * (x / 5), x % 5);                                                       \
      else if (x == 10) dma<1, 0>(mg == 0 ? gw[0] : mg == 1 ? gw[1] : mg == 2 ? gw[2] : gw[3], dW1, 8u * (32 * D_ * 2), (unsigned)(8 * 5120 + mg * 1024)); \
      else if (mg == 0) W_DMA(8, 4);                                                                    \
    }
      FENCE();
      u32x4 wm[5];
      unsigned short cv;
      {
        const __amdgpu_buffer_rsrc_t rM = __builtin_amdgcn_make_buffer_rsrc((void*)w_mod, 0, D_ * 6 * D_ * 2, RS3);
        const __amdgpu_buffer_rsrc_t rC = __builtin_amdgcn_make_buffer_rsrc((void*)cin, 0, D_ * 2, RS3);
        const unsigned o = bx < 240 ? (unsigned)(t8 >> 3) * (6 * D_ * 2) + (unsigned)(t8 & 7) * 16u : OOB;
        cv = __builtin_amdgcn_raw_buffer_load_b16(rC, (unsigned)((t8 >> 3) + 32 * min(lane & 7, 4)) * 2u, (bx & 3) * 320, 0);
#pragma unroll
        for (int i = 0; i < 5; ++i) wm[i] = __builtin_amdgcn_raw_buffer_load_b128(rM, o, ((bx & 3) * 160 + i * 32) * (6 * D_ * 2) + (bx >> 2) * 128, 0);
      }
      FENCE();
      W12_G(gw);
#pragma unroll
      for (int j = 0; j < 5; ++j) PIN(gw[j]);
      const int job = bx * 4 + mg;
      const bool j3 = job < 540, jok = job < 972;
      const int jks = j3 ? job % 20 : (job - 540) % 54, jcg = j3 ? job / 20 : (job - 540) / 54;
      const unsigned rtl = (unsigned)(L_RT + mg * 5120);
      u32x4 rt[5];
      unsigned rta[5];
      {
        const unsigned v3 = ((unsigned)lane >> 3) * (F_ * 2) + ((unsigned)lane & 7u) * 16u + (unsigned)jks * (32 * F_ * 2) + (unsigned)jcg * 128u;
        const unsigned s4 = (unsigned)jks * (32 * D_ * 2) + (unsigned)(jcg - nt) * 160u;
#pragma unroll
        for (int i = 0; i < 5; ++i) { rta[i] = j3 ? (i < 4 ? v3 + i * (8 * F_ * 2) : OOB) : gw[i] + s4;
          PIN(rta[i]); }
      }
#define RT_LD() do {                                                                                    \
      const __amdgpu_buffer_rsrc_t rS = __builtin_amdgcn_make_buffer_rsrc((void*)(j3 ? w3 : w4), 0, jok ? DF2 : 0, RS3); \
      _Pragma("unroll") for (int i = 0; i < 4; ++i) rt[i] = __builtin_amdgcn_raw_buffer_load_b128(rS, rta[i], 0, 0); \
      if (!j3) rt[4] = __builtin_amdgcn_raw_buffer_load_b128(rS, rta[4], 0, 0);                         \
      FENCE();                                                                                          \
    } while (0)
      FENCE();
      STAMP(12);
      asm volatile("" : "+v"(cpf));
      asm volatile("" :: "v"(cp0));
      SIG(P_K);
      if constexpr (TSHI == 23) TX_(31);
      FENCE();
      TAGS();
      {
        float a[8];
#pragma unroll
        for (int e = 0; e < 8; ++e) a[e] = 0.f;
        const float c_ = lo2f(cv);
        const int sci = __float_as_int(rbf(c_ * __builtin_amdgcn_rcpf(1.f + __builtin_amdgcn_exp2f(c_ * -1.4426950408889634f))));
#pragma unroll
        for (int i = 0; i < 5; ++i) {
          const float sc = __int_as_float(__builtin_amdgcn_ds_bpermute(((lane & 56) + i) * 4, sci));
#pragma unroll
          for (int e = 0; e < 4; ++e) { a[2 * e] += sc * lo2f(wm[i][e]); a[2 * e + 1] += sc * hi2f(wm[i][e]); }
        }
#pragma unroll
        for (int e = 0; e < 4; ++e) {
          const auto r = __builtin_amdgcn_permlane32_swap(__float_as_uint(a[e]), __float_as_uint(a[e + 4]), false, false);
          a[e] = __uint_as_float((unsigned)r[0]) + __uint_as_float((unsigned)r[1]);
        }
#pragma unroll
        for (int e = 0; e < 2; ++e) {
          const auto r = __builtin_amdgcn_permlane16_swap(__float_as_uint(a[e]), __float_as_uint(a[e + 2]), false, false);
          a[e] = dpp_add(__uint_as_float((unsigned)r[0]) + __uint_as_float((unsigned)r[1]), 0);
        }
        if ((lane & 8) == 0) *(LAS f32x2*)(lds + L_MP + mg * 256 + (lane & 7) * 32 + g * 8) = f32x2{a[0], a[1]};
        SIG(P_M);
        TS_(52);
        if constexpr (TSHI == 23) TX_(32);
        FENCE();
        if (wv == 7) {
          WAIT_HALF(1, P_M);
          f32x2 s = *(const LAS f32x2*)(lds + L_MP + (lane & 31) * 8);
#pragma unroll
          for (int w = 1; w < 4; ++w) s += *(const LAS f32x2*)(lds + L_MP + w * 256 + (lane & 31) * 8);
          st128(u32x4{__float_as_uint(s[0]), tA, __float_as_uint(s[1]), tB}, rW, lane < 32 ? l16 : OOB, O_MP + (unsigned)bx * 512u, 16);
        }
        FENCE();
      }
      STAMP(2);
      {
      WAIT_HALF(1, P_K);
      BUILD(SL_A, t8);
      FENCE();
      FILL_Z();
      FENCE();
      BUILD(SL_B, t8);
      SIG(P_B2);
      if constexpr (TSHI == 23) TX_(33);
      FENCE();
      STAMP(1);
      }
      WAIT_HALF(1, P_B2);
      REQ_Q();
      REQ_D();
      FENCE();
      FENCE();
#define RT_TR() do \
      { \
        _Pragma("unroll") \
        for (int i = 0; i < 5; ++i) *(LAS u32x4*)(lds + rtl + i * 1024 + l16) = rt[i]; \
        const LAS char* a0 = lds + rtl + (j3 ? (8 * g + (l15 >> 2)) * 128 : (lane >> 2) * 160) + (lane & 3) * 8; \
        const LAS char* a1 = a0 + (j3 ? 512 : 2560); \
        _Pragma("unroll") \
        for (int jn = 0; jn < 5; ++jn) { \
          const s16x4 lo = __builtin_amdgcn_ds_read_tr16_b64_v4i16((LAS s16x4*)(a0 + jn * 32)); \
          const s16x4 hi = __builtin_amdgcn_ds_read_tr16_b64_v4i16((LAS s16x4*)(a1 + jn * 32)); \
          rt[jn] = __builtin_bit_cast(u32x4, bf16x8{lo[0], lo[1], lo[2], lo[3], hi[0], hi[1], hi[2], hi[3]}); \
        } \
      } while (0)
#define RT_OUT() do {                                                                                   \
        const unsigned dst = j3 ? O_WT3 + (unsigned)(jcg * 80 + jks) * 1024u : O_WT4 + (unsigned)(jcg * 54 + jks) * 5120u, dstr = j3 ? 20480u : 1024u; \
        _Pragma("unroll") for (int jn = 0; jn < 4; ++jn) st128(rt[jn], rW, jok ? l16 : OOB, dst + jn * dstr, 16); \
        if (W12) st128(rt[4], rW, (jok && !j3) ? l16 : OOB, dst + 4096u, 16);                           \
        else if (!j3) st128(rt[4], rW, jok ? l16 : OOB, dst + 4096u, 16);                               \
        FENCE();                                                                                        \
      } while (0)
      VMW(3);
      SIG(P_U);
      if (wv == 6) { WAIT_HALF(1, P_U); __builtin_amdgcn_raw_buffer_store_b64(u32x2{tA, tB}, rW, lane == 0 ? (unsigned)bx * 8u : OOB, O_FL1, 16); }
      if constexpr (TSHI == 23) TX_(43);
      const bool mfin = wv == 7 && bx < 240 && lane < 32;
      const unsigned po = mfin ? ((unsigned)lane >> 3) * 512u + ((unsigned)lane & 7u) * 16u : OOB;
      u32x4 mp4;
#define LD_P() mp4 = __builtin_amdgcn_raw_buffer_load_b128(rW, po, O_MP + (unsigned)(bx & ~3) * 512u + (unsigned)(bx & 3) * 128u, 16)
#define MOD_L2() do {                                                                                   \
      if (wv == 7) {                                                                                    \
        unsigned spins = 0;                                                                             \
        while (__builtin_amdgcn_ballot_w64(mfin && (mp4[1] != tA || mp4[3] != tB)) != 0ull) {           \
          SPIN_STEP();                                                                                  \
          if (spins > RETRY_CAP) break;                                                                 \
          __builtin_amdgcn_s_sleep(USLEEP);                                                             \
          LD_P();                                                                                       \
        }                                                                                               \
        const float one = ((bx >= 40 && bx < 80) || (bx >= 160 && bx < 200)) ? 1.f : 0.f;               \
        const float s0_ = swap_add16(dpp_add(__uint_as_float(mp4[0]), 0)), s1_ = swap_add16(dpp_add(__uint_as_float(mp4[2]), 0)); \
        const unsigned d = pack2(one + rbf(s0_), one + rbf(s1_));                                       \
        const unsigned dn = (unsigned)__builtin_amdgcn_update_dpp(0, (int)d, 0xB1, 0xf, 0xf, true);     \
        st128(u32x4{d, tA, dn, tB}, rW, (lane < 8 && (lane & 1) == 0) ? (unsigned)lane * 8u : OOB, O_MOD + (unsigned)bx * 64u, 16); \
      }                                                                                                 \
      FENCE();                                                                                          \
    } while (0)
      if (wv == 7) LD_P();
      FENCE();
      STAMP(3);
      FENCE();
      asm volatile("" : "+v"(fpa[0]), "+v"(fpa[1]), "+v"(fpa[2]), "+v"(fpv));
#define RT_GO() RT_LD()
      LN1();
      GET_D();
      if constexpr (TSHI == 23) TX_(34);
      MOD_L2();
      if constexpr (TSHI == 23) TX_(35);
      TS_(27);
      if constexpr (TSHI == 23) TX_(36);
      STAMP(22);
      WAIT_ALL(P_U);
      RT_LD();
      TS_(4);
      STAMP(28);
      FILL_P(0, 3);
      FENCE();
      RT_TR();
      SIG(P_RT);
      TS_(48);
      RT_OUT();
      LD_WD(0, 5);
      WAIT_HALF(1, P_RT);
      LD_WD(5, NGA);
      FENCE();
      VMW(NGA + (GZ && GZP == 3 ? 5 : 0));
      SIG(P_GA);
      TS_(5);
      LD_WD(NGA, 12);
      FENCE();
      if (mg < 2) {
        u32x2 uc[2];
        f32x4 yc[8];
        float v[8];
        uc[0] = *(const LAS u32x2*)(lds + SL_C + CV_U + 512 + (256 * zja + 16 * l15 + 4 * g) * 2);
        uc[1] = *(const LAS u32x2*)(lds + SL_C + CV_U + 512 + (256 * zjb + 16 * l15 + 4 * g) * 2);
        unsigned zq = O_Z + ZOFF(zja, zjb, chC);
        PIN(zq);
        WAIT_HALF(0, P_C2);
#pragma unroll
        for (int c = 0; c < 8; ++c) yc[c] = *(const LAS f32x4*)(lds + SL_C + mg * 2048 + l16 + (c >> 1) * 4096 + (c & 1) * 1024);
        STG(8, v[i] = (i & 1) ? hi2f(uc[i >> 2][(i >> 1) & 1]) : lo2f(uc[i >> 2][(i >> 1) & 1]); PIN(v[i]));
        STG(8, v[i] = __builtin_fmaf(v[i], __uint_as_float(dC), yc[i >> 2][i & 3]); PIN(v[i]));
#pragma unroll
        for (int w = 1; w < 4; ++w) STG(8, v[i] += yc[2 * w + (i >> 2)][i & 3]; PIN(v[i]));
        st128(widen(pack2(v[0], plainv(v[1])), pack2(v[2], v[3]), pack2(v[4], plainv(v[5])), pack2(v[6], v[7])), rW, zq, 0, 16);
      }
      FENCE();
      SIG(P_ZC);
      FENCE();
      LD_WS(0, GZ ? NS1 : NS2);
      FENCE();
      if (HPZ) { WAIT_HALF(0, P_X2); __builtin_amdgcn_s_sleep(HPZ); }
      STAMP(15);
      LD_WS(NS2, 10);
      if (mg > 0) {
        WAIT_HALF(1, P_ZC);
#pragma unroll
        for (int j = 0; j < 5; ++j) W_DMA(16 + mg, j);
      }
      LD_WS2U(0, NZ2);
      FENCE();
      STAMP(5);
      TS_(6);
      WAIT_HALF(0, P_X2);
#pragma unroll
      for (int x = 0; x < 10; ++x) *(LAS u32x4*)(lds + (9 + mg + 4 * (x / 5)) * 5120 + (x % 5) * 1024 + l16) = WS[x];
      TS_(1);
      WAIT_HALF(0, P_T);
      R_PRE();
      VMW(NZ2);
      SIG(P_X);
      TS_(2);
      TS_(3);
      WAIT_HALF(0, P_Z);
      LD_WS2U(NZ2, 10);
      FENCE();
      WAIT_2(P_RP, P_X);
#pragma unroll
      for (int j = 0; j < 5; ++j) W_DMA(20 + mg, j);
      FENCE();
      TS_(7);
      STAMP(7);
    } else {
      PRIO(3);
      const unsigned rt0 = (unsigned)__builtin_amdgcn_s_memrealtime();
      unsigned kpa = (unsigned)L_KV + ((unsigned)lane & 7u) * 1312u + (unsigned)mg * 64u + ((unsigned)lane >> 3) * 8u;
      unsigned kpb = (unsigned)L_KV + ((unsigned)l15 >> 1) * 1312u + 256u + (unsigned)mg * 16u + (unsigned)g * 4u + ((unsigned)lane & 1u) * 640u, kpc = kpb + ((unsigned)lane & 1u) * 32u;
      const unsigned cha_ = (unsigned)ch0 + 32u * mg + ((unsigned)lane >> 3) * 4u, chb_ = (unsigned)ch0 + 128u + 8u * mg + 2u * g;
      unsigned koa = KVROW(cha_) + (unsigned)tok0 * 4u + ((unsigned)lane & 7u) * 16u, koa2 = KVROW(cha_ + 2u) + (unsigned)tok0 * 4u + ((unsigned)lane & 7u) * 16u;
      unsigned kob = KVROW(chb_) + (unsigned)tok0 * 4u + (unsigned)l15 * 8u;
      asm volatile("" : "+v"(kpa), "+v"(kpb), "+v"(kpc), "+v"(koa), "+v"(koa2), "+v"(kob));
      u32x4 ug[6];
      unsigned uo = (unsigned)mg * 2048u + l16;
      unsigned udone;
      asm volatile("s_mov_b32 %0, 0" : "=s"(udone));
      unsigned ubA = KVROW((unsigned)chA), ubC = KVROW((unsigned)chC), ubB = KVROW((unsigned)(chA + 1));
      asm volatile("" : "+s"(ubA), "+s"(ubB), "+s"(ubC));
      PIN(uo);
#pragma unroll
      for (int r = 0; r < 6; ++r) asm volatile("" : "=v"(ug[r]));
#define TRY_U(EL, T, CODE)                                                                               \
        asm volatile("s_cmp_lg_u32 %6, 0\n\ts_cbranch_scc1 Lu%=\n\ts_cmp_lt_u32 %[el], %[t]\n\ts_cbranch_scc1 Lu%=\n\t" \
                     "buffer_load_dwordx4 %2, %[vo], %[rs], %[c] offen sc1\n\tbuffer_load_dwordx4 %3, %[vo], %[rs], %[c] offen offset:1024 sc1\n\t" \
                     "buffer_load_dwordx4 %0, %[vo], %[rs], %[a] offen sc1\n\tbuffer_load_dwordx4 %1, %[vo], %[rs], %[a] offen offset:1024 sc1\n\t" \
                     "buffer_load_dwordx4 %4, %[vo], %[rs], %[b] offen sc1\n\tbuffer_load_dwordx4 %5, %[vo], %[rs], %[b] offen offset:1024 sc1\n\t" \
                     "s_mov_b32 %6, %[cd]\n"                                                            \
                     "Lu%=:" : "+v"(ug[0]), "+v"(ug[1]), "+v"(ug[2]), "+v"(ug[3]), "+v"(ug[4]), "+v"(ug[5]), "+s"(udone) \
                     : [el] "s"(EL), [t] "i"(T), [cd] "i"(CODE), [vo] "v"(uo), [rs] "s"(dW), [a] "s"(ubA), [b] "s"(ubB), [c] "s"(ubC) : "scc", "memory")
      VMW(9);
      TAGS();
      unsigned tAB = (lane & 1) ? tB : tA;
      PIN(tAB);
      asm volatile("s_waitcnt vmcnt(0)" : "+v"(sk[0]), "+v"(sk[1]), "+v"(sk[2]));
      const unsigned rt1 = (unsigned)__builtin_amdgcn_s_memrealtime();
      SIG(P_KV);
      FENCE();
      f32x2 wa[3][4];
#pragma unroll
      for (int c = 0; c < 3; ++c)
#pragma unroll
        for (int t = 0; t < 4; ++t) {
          const f32x2 w = (t & 1) ? f32x2{hi2f(sk[c][t >> 1]), hi2f(sk[c][2 + (t >> 1)])} : f32x2{lo2f(sk[c][t >> 1]), lo2f(sk[c][2 + (t >> 1)])};
          wa[c][t] = w;
          PIN(wa[c][t]);
        }
      asm volatile("" : "+v"(tAB));
      FENCE();
      TS_(1);
      STAMP(1);
      WAIT_HALF(0, P_KV);
      if (TU1) TRY_U(rt1 - rt0, TU1, 1);
      if constexpr (TSHI == 23) TX_(31);
      STAMP(12);
      {
        u32x2 kw[7], vw[4];
        unsigned kx[5], vx[2];
#pragma unroll
        for (int i = 0; i < 7; ++i) kw[i] = *(const LAS u32x2*)(lds + kpa + (i < 3 ? 320 + 320 * i : 1312 + 320 * (i - 3)));
#pragma unroll
        for (int i = 0; i < 5; ++i) kx[i] = *(const LAS unsigned*)(lds + ((i == 1 || i == 2) ? kpc : kpb) + (i < 3 ? 320 + 320 * i : 1312 + 320 * (i - 3)));
#pragma unroll
        for (int i = 0; i < 4; ++i) vw[i] = *(const LAS u32x2*)(lds + kpa + 12288 + 320 * i);
#pragma unroll
        for (int i = 0; i < 2; ++i) vx[i] = *(const LAS unsigned*)(lds + kpb + 12288 + 320 * i);
        __builtin_amdgcn_sched_barrier(0);
        f32x2 kf[19], a[10], p[10], t_[10];
#define KX(o, t) kf[((o) >> 2) * 7 + ((o) & 3) + (t)]
#define STR(lo, hi, stmt) do { _Pragma("unroll") for (int i = (lo); i < (hi); ++i) { stmt; } __builtin_amdgcn_sched_barrier(0); } while (0)
#define KV_GROUP(lo, hi) do {                                                                           \
        STR(((lo) >> 2) * 7, (hi) == 10 ? 19 : ((hi) >> 2) * 7, kf[i] = up2(i < 14 ? kw[i % 7][i / 7] : kx[i < 14 ? 0 : i - 14]); PIN(kf[i])); \
        {                                                                                               \
          STR(lo, hi, a[i] = KX(i, 0) * wa[i >> 2][3]; PIN(a[i]));                                      \
          _Pragma("unroll") for (int t = 1; t < 4; ++t) STR(lo, hi, a[i] = FMA2(KX(i, t), wa[i >> 2][3 - t], a[i]); PIN(a[i])); \
          STR(lo, hi, t_[i] = FMA2(a[i], cp, a[i]); PIN(t_[i]));                                        \
          STR(lo, hi, a[i] = FMA2(a[i], cn, t_[i]); PIN(a[i]));                                         \
        }                                                                                               \
        STR(lo, hi, p[i] = up2(i < 8 ? vw[i & 3][i >> 2] : vx[i & 1]); PIN(p[i]));                      \
        STR(lo, hi, a[i] *= p[i]; PIN(a[i]));                                                           \
        if constexpr (TSHI == 23) TX_(32 + (lo) / 2);                                                   \
        _Pragma("unroll") for (int c = (lo) >> 1; c < ((hi) < 8 ? (hi) : 8) >> 1; ++c) {                \
          const int o = (c >> 1) * 4, e = c & 1;                                                        \
          st128(u32x4{pack2(a[o][e], plainv(a[o + 1][e])), tA, pack2(a[o + 2][e], plainv(a[o + 3][e])), tB}, rW, c < 2 ? koa : koa2, e * 8192u, 16); \
        }                                                                                               \
        if ((hi) == 10) { _Pragma("unroll") for (int e = 0; e < 2; ++e) __builtin_amdgcn_raw_buffer_store_b64(u32x2{pack2(a[8][e], plainv(a[9][e])), tAB}, rW, kob, e * 8192, 16); } \
        __builtin_amdgcn_sched_barrier(0);                                                              \
      } while (0)
        KV_GROUP(0, 4);
        if constexpr (TSHI == 23) TX_(33);
        STAMP(5);
        KV_GROUP(4, 10);
        if constexpr (TSHI == 23) TX_(35);
      }
      FENCE();
      const unsigned rt2 = (unsigned)__builtin_amdgcn_s_memrealtime();
      REQ_Q();
      REQ_D();
      FENCE();
      if (TU2) TRY_U(rt2 - rt0, TU2, 2);
      STAMP(2);
      TS_(2);
      WAIT_HALF(1, P_K);
      BUILD(SL_C, t8);
      SIG(P_B);
      if constexpr (TSHI == 23) TX_(36);
      FENCE();
#define LD_U(r) ug[r] = __builtin_amdgcn_raw_buffer_load_b128(rW, uo + ((r) & 1) * 1024, (r) < 2 ? ubA : (r) < 4 ? ubC : ubB, 16)
      u32x4 A2[16], B2[16];
      WAIT_HALF(0, P_B);
      CV_RA(A2, SL_C, 0);
      CV_RA(A2, SL_C, 1);
      W12_G(gw);
#pragma unroll
      for (int j = 0; j < 5; ++j) PIN(gw[j]);
      unsigned zq = Z2OFF(wv);
      unsigned pa = a00 + SL_A - 3648, pb = b00 + SL_A - 64;
      unsigned xw = SL_A + mg * 8192 + l16, xc = SL_C + mg * 4096 + l16;
      asm volatile("" : "+v"(zq), "+v"(pa), "+v"(pb), "+v"(xw), "+v"(xc));
      FENCE();
      if (udone == 0u) while ((unsigned)__builtin_amdgcn_s_memrealtime() - rt0 < (unsigned)TU) __builtin_amdgcn_s_sleep(1);
      STAMP(3);
      TS_(3);
      TRY_U(rt0, 0, 3);
      FENCE();
      if constexpr (TSHI == 23) TX_(37);
      unsigned uw = (unsigned)(SL_A + CV_U + 512 + mg * 1024 + lane * 8);
      PIN(uw);
      WAIT_HALF(1, P_B2);
      asm volatile("s_cmp_eq_u32 %[d], 1\n\ts_cbranch_scc1 Lw%=\n\ts_waitcnt vmcnt(0)\nLw%=:\n\ts_waitcnt vmcnt(9)"
                   : "+v"(ug[0]), "+v"(ug[1]), "+v"(ug[2]), "+v"(ug[3]), "+v"(ug[4]), "+v"(ug[5]) : [d] "s"(udone) : "scc", "memory");
      if constexpr (TSHI == 23) { TX_(38); PIN(ug[5]); __builtin_amdgcn_s_sleep(1); TX_(39); }
      {
#define U_BAD(r) (ug[r][1] != tA || ug[r][3] != tB)
        if (__builtin_expect(__builtin_amdgcn_ballot_w64(U_BAD(0) || U_BAD(1) || U_BAD(2) || U_BAD(3) || U_BAD(4) || U_BAD(5)) != 0ull, 1)) {
          unsigned spins = 0;
          do {
            __builtin_amdgcn_s_sleep(USLEEP);
#pragma unroll
            for (int r = 0; r < 6; ++r) if (U_BAD(r)) LD_U(r);
            SPIN_STEP();
          } while (__builtin_amdgcn_ballot_w64(U_BAD(0) || U_BAD(1) || U_BAD(2) || U_BAD(3) || U_BAD(4) || U_BAD(5)) != 0ull && spins < RETRY_CAP);
          VM0();
        }
#pragma unroll
        for (int r = 2; r < 8; ++r) *(LAS u32x2*)(lds + uw + ((r % 6) >> 1) * CV_SLOT + (r & 1) * 512) = u32x2{ug[r % 6][0], ug[r % 6][2]};
      }
      SIG(P_U);
      TS_(56);
      if constexpr (TSHI == 23) TX_(40);
      STAMP(22);
      GET_D();
#if !(LINW & 12)
      LD_WS2(0, 10);
#endif
      FENCE();
      WAIT_HALF(0, P_U);
      CV_RB(B2, SL_C, 0);
      FENCE();
      STAMP(28);
      TS_(4);
      f32x4 acc[8], ac[4];
      u32x4 A[16], B[16];
#define RA(P, rd, i_) *(const LAS u32x4*)(lds + (P) + 3648 - 64 * (rd) - 512 * (i_))
#define RB(P, rd, i_) *(const LAS u32x4*)(lds + (P) + 64 - 64 * (rd) + 512 * (i_))
#define CONV_C(J, lo, hi)                                                                               \
      sfor<lo, hi>([&](auto C_) __attribute__((always_inline)) {                                         \
        constexpr int c = decltype(C_)::v, rd = c / 18, i = cc_i(J, c % 18), t = cc_t(J, c % 18);       \
        ac[t] = mfma16(A2[8 * rd + i], B2[8 * rd + (((J) >> (4 * t)) & 15) - i], (rd == 0 && i == 0) ? f32x4{0.f, 0.f, 0.f, 0.f} : ac[t]); \
        SB();                                                                                           \
        if constexpr (c < 8) B2[15 - c] = RB(pb + CV_SLOT, 1, 7 - c);                                   \
        if constexpr (c >= 18 && c < 34) { constexpr int x = (c - 18) >> 1; if constexpr ((c & 1) == 0) B[7 - x] = RB(pb, 0, 7 - x); else A[x] = RA(pa, 0, x); } \
        SB();                                                                                           \
      });
      if (half == 0) { CONV_C(0x7430, 0, 18) } else { CONV_C(0x6521, 0, 18) }
      SB();
      int half2 = half;
      asm volatile("" : "+s"(half2));
      if (half2 == 0) { CONV_C(0x7430, 18, 36) } else { CONV_C(0x6521, 18, 36) }
      u32x4 A3[16], B3[16];
      f32x4 ab_[8];
      unsigned pk_;
      float ud[16];
      u32x2 u2[4];
      f32x4 ya[8];
      unsigned ga_;
      unsigned pu = (unsigned)(SL_A + CV_U + 512 + (256 * wv + 16 * l15 + 4 * g) * 2), py = (unsigned)(SL_A + wv * 1024) + l16;
      const unsigned f0 = (unsigned)L_CTL + ((unsigned)lane & 3u) * 4u;
      sfor<0, 144>([&](auto M_) __attribute__((always_inline)) {
        constexpr int m = decltype(M_)::v, ch = m / 72, rd = (m / 36) & 1, r = m % 36, i = m < 108 ? cv_i(r) : cl_i(r), dd = m < 108 ? cv_d(r) : cl_t(r) - cl_i(r);
        if constexpr (ch == 0) acc[i + dd] = mfma16(A[8 * rd + i], B[8 * rd + dd], (rd == 0 && i == 0) ? f32x4{0.f, 0.f, 0.f, 0.f} : acc[i + dd]);
        else ab_[i + dd] = mfma16(A3[8 * rd + i], B3[8 * rd + dd], (rd == 0 && i == 0) ? f32x4{0.f, 0.f, 0.f, 0.f} : ab_[i + dd]);
        SB();
        if constexpr (m == 30) { pa += SL_B - SL_A; pb += SL_B - SL_A; asm volatile("" : "+v"(pa), "+v"(pb)); }
        if constexpr (m == 94) { xw += SL_B - SL_A; PIN(xw); }
        if constexpr (r >= RS0 && r < RS0 + 16 && m < 108) {
          constexpr int x = (r - RS0) >> 1;
          if constexpr (m < 36) { if constexpr ((r & 1) == 0) B[15 - x] = RB(pb, 1, 7 - x); else A[8 + x] = RA(pa, 1, x); }
          else if constexpr (m < 72) { if constexpr ((r & 1) == 0) B3[7 - x] = RB(pb, 0, 7 - x); else A3[x] = RA(pa, 0, x); }
          else { if constexpr ((r & 1) == 0) B3[15 - x] = RB(pb, 1, 7 - x); else A3[8 + x] = RA(pa, 1, x); }
        }
        if constexpr (m == 1 || m == 4 || m == 23 || m == 26) { constexpr int c = m == 1 ? 0 : m == 4 ? 1 : m == 23 ? 2 : 3; *(LAS f32x4*)(lds + xc + c * 1024) = ac[c]; }
        if constexpr (m == 28) SIG(P_C2);
        if constexpr (m == 38 || m == 104) pk_ = *(volatile LAS unsigned*)(lds + f0);
        if constexpr (m == 45) NEED_AT(pk_, f0, P_C2);
        if constexpr (m == 111) NEED_AT(pk_, f0, P_A2);
        constexpr int wa = m == 47 ? 0 : m == 54 ? 1 : m == 60 ? 2 : m == 65 ? 3 : m == 69 ? 4 : m == 72 ? 5 : m == 75 ? 6 : m == 77 ? 7 : -1;
        constexpr int wb = m == 113 ? 0 : m == 116 ? 1 : m == 119 ? 2 : m == 122 ? 3 : m == 126 ? 4 : m == 132 ? 5 : m == 139 ? 6 : -1;
        if constexpr (wa >= 0) *(LAS f32x4*)(lds + xw + wa * 1024) = acc[wa];
        if constexpr (wb >= 0) *(LAS f32x4*)(lds + xw + wb * 1024) = ab_[wb];
        if constexpr (m == 95) SIG(P_A2);
        if constexpr (m == 58) PIN(pu);
        if constexpr (m == 59 || m == 61) { constexpr int c = m - 59; u2[c] = *(const LAS u32x2*)(lds + pu + c * 1024); u2[c + 1] = *(const LAS u32x2*)(lds + pu + (SL_B - SL_A) + c * 1024); }
        constexpr int us = cv_slot(m, 0);
        if constexpr (us >= 0 && us < 16) {
#pragma unroll
          for (int o = 2 * us; o < 2 * us + 2; ++o) {
            const int v = (o >> 3) * 4 + (o & 3);
            if ((o & 4) == 0) ud[v] = (v & 1) ? hi2f(u2[v >> 2][(v >> 1) & 1]) : lo2f(u2[v >> 2][(v >> 1) & 1]);
            else ud[v] *= ((v >> 2) & 1) ? hi2f(dAB) : lo2f(dAB);
            PIN(ud[v]);
          }
        }
        if constexpr (m == 110) PIN(py);
        if constexpr (m == 112 || m == 114 || m == 115 || m == 117) { constexpr int c = m == 112 ? 0 : m == 114 ? 2 : m == 115 ? 4 : 6;
          ya[c] = *(const volatile LAS f32x4*)(lds + py + (c >> 2) * 4096 + (c & 3) * 8192); ya[c + 1] = *(const volatile LAS f32x4*)(lds + py + (c >> 2) * 4096 + (c & 3) * 8192 + 8192); }
        constexpr int zs = cv_slot(m, 1);
        if constexpr (zs >= 0 && zs < 16) {
#pragma unroll
          for (int o = 2 * zs; o < 2 * zs + 2; ++o) { const int tl = (o >> 2) & 1, e = o & 3; ud[tl * 8 + e] += ya[4 * tl + (o >> 3)][e]; PIN(ud[tl * 8 + e]); }
        }
        if constexpr (TSHI == 10 && (m % 18 == 0 || m == 143)) TX_(44 + (m + 1) / 18);
        if constexpr (m == 131) ga_ = *(volatile LAS unsigned*)(lds + f0 + 16);
        if constexpr (m == 140) { py += SL_B - SL_A; PIN(py); }
        SB();
      });
      *(LAS f32x4*)(lds + xw + 7 * 1024) = ab_[7];
      SIG(P_X);
      TS_(5);
      STAMP(4);
      u32x4 mg4[4];
#define LD_MG(i) mg4[i] = __builtin_amdgcn_raw_buffer_load_b128(rW, (unsigned)t8 * 16u, O_MOD + (i) * 4096, 16)
      u32x2 f1v;
#define LD_F1() f1v = __builtin_amdgcn_raw_buffer_load_b64(rW, (unsigned)t8 * 8u, O_FL1, 16)
      NEED_AT(ga_, f0 + 16, P_GA);
      {
        unsigned spins = 0;
        for (;;) {
          const unsigned fl = *(volatile LAS unsigned*)(lds + f0);
#pragma unroll
          for (int c = 0; c < 8; ++c) ya[c] = *(const volatile LAS f32x4*)(lds + py + (c >> 2) * 4096 + (c & 3) * 8192);
          if (__builtin_amdgcn_ballot_w64(fl < (unsigned)P_X) == 0ull) break;
          SPIN_STEP();
          if (spins > SPIN_CAP) break;
        }
#pragma unroll
        for (int tl = 0; tl < 2; ++tl) {
          const f32x4 yb = (ya[4 * tl] + ya[4 * tl + 1]) + (ya[4 * tl + 2] + ya[4 * tl + 3]);
          const f32x2 v2 = f32x2{yb[0], yb[1]} + f32x2{ud[8 * tl + 4], ud[8 * tl + 5]}, v3 = f32x2{yb[2], yb[3]} + f32x2{ud[8 * tl + 6], ud[8 * tl + 7]};
          st128(u32x4{pack2(ud[8 * tl], plainv(v2[0])), pack2(ud[8 * tl + 1], v2[1]), pack2(ud[8 * tl + 2], plainv(v3[0])), pack2(ud[8 * tl + 3], v3[1])}, rW, zq + tl * 64u * TS, 0, 16);
        }
      }
      FENCE();
      SIG(P_X2);
      TS_(6);
      LD_F1(); LD_MG(0); LD_MG(1); LD_MG(2); LD_MG(3);
      FENCE();
      LN1();
      STAMP(6);
      u32x4 za, zb, zc = {0u, 0u, 0u, 0u}, zd = {0u, 0u, 0u, 0u};
      const unsigned zo = (unsigned)mt * (4u * TS) + ZCH((unsigned)t8 & ~1u) + (unsigned)nt * 32u + ((unsigned)t8 & 1u) * 16u;
      const unsigned zo1 = (unsigned)mt * (4u * TS) + ZCH(256u + ((unsigned)t8 & ~1u)) + (unsigned)nt * 32u + ((unsigned)t8 & 1u) * 16u;
      const unsigned zo2 = (unsigned)mt * (4u * TS) + ZCH(2u * t8 + 128u) + (unsigned)nt * 16u;
      FENCE();
      {
#define MG_BAD(i) (mg4[i][1] != tA || mg4[i][3] != tB)
#define T_BAD() (__builtin_amdgcn_ballot_w64(f1v[0] != tA || f1v[1] != tB || MG_BAD(0) || MG_BAD(1) || MG_BAD(2) || MG_BAD(3)) != 0ull)
        if (__builtin_expect(T_BAD(), 0)) {
          unsigned spins = 0;
          do {
            __builtin_amdgcn_s_sleep(USLEEP);
            if (f1v[0] != tA || f1v[1] != tB) LD_F1();
#pragma unroll
            for (int i = 0; i < 4; ++i) if (MG_BAD(i)) LD_MG(i);
            SPIN_STEP();
          } while (T_BAD() && spins < RETRY_CAP);
          VM0();
        }
#pragma unroll
        for (int i = 0; i < 4; ++i) {
          const int n = (t8 + 256 * i) * 4;
          if (n < 6 * D_) *(LAS u32x2*)(lds + L_MOD + n * 2) = u32x2{mg4[i][0], mg4[i][2]};
          if (i == 3 && n >= 5 * D_ + n0 && n < 5 * D_ + n0 + 80)
            *(LAS f32x4*)(lds + L_GB + (n - 5 * D_ - n0) * 4) = f32x4{lo2f(mg4[i][0]), hi2f(mg4[i][0]), lo2f(mg4[i][2]), hi2f(mg4[i][2])};
        }
        SIG(P_T);
        if constexpr (TSHI == 23) TX_(41);
      }
      WAIT_HALF(0, P_T);
      if (DLY0) __builtin_amdgcn_s_sleep(DLY0);
      PRIO(3);
      STAMP(7);
      TS_(7);
      za = __builtin_amdgcn_raw_buffer_load_b128(rW, zo, O_Z, 16); zb = __builtin_amdgcn_raw_buffer_load_b128(rW, zo1, O_Z, 16);
      if (wv == 3) { zc = __builtin_amdgcn_raw_buffer_load_b128(rW, zo2, O_Z, 16); zd = __builtin_amdgcn_raw_buffer_load_b128(rW, zo2 + 16u * ZG, O_Z, 16); }
      FENCE();
      SIG(P_Z);
      R_PRE();
      SIG(P_RP);
      unsigned zw0 = (unsigned)(L_XCH + 4 * (t8 & 1) * YROW + 4 * (t8 >> 1)), zw1 = (unsigned)(L_XCH + 256 + 4 * t8);
      asm volatile("" : "+v"(zw0), "+v"(zw1), "+v"(ry), "+v"(rz));
      asm volatile("" : "+s"(w3s[0]), "+s"(w3s[1]));
      unsigned spins = 0;
      for (;;) {
        const bool ba = amax2(za) >= PTHR, bb_ = amax2(zb) >= PTHR, bc_ = amax2(zc) >= PTHR, bd = amax2(zd) >= PTHR;
        if (__builtin_amdgcn_ballot_w64(ba || bb_ || bc_ || bd) == 0ull) break;
        SPIN_STEP();
        if (spins > RETRY_CAP) break;
        __builtin_amdgcn_s_sleep(ZSLEEP);
        if (ba) za = __builtin_amdgcn_raw_buffer_load_b128(rW, zo, O_Z, 16);
        if (bb_) zb = __builtin_amdgcn_raw_buffer_load_b128(rW, zo1, O_Z, 16);
        if (bc_) zc = __builtin_amdgcn_raw_buffer_load_b128(rW, zo2, O_Z, 16);
        if (bd) zd = __builtin_amdgcn_raw_buffer_load_b128(rW, zo2 + 16u * ZG, O_Z, 16);
      }
#pragma unroll
      for (int i = 0; i < 4; ++i) {
        *(LAS unsigned*)(lds + zw0 + i * YROW) = za[i];
        *(LAS unsigned*)(lds + zw0 + i * YROW + 512) = zb[i];
      }
      if (wv == 3) {
#pragma unroll
        for (int i = 0; i < 4; ++i) {
          *(LAS unsigned*)(lds + zw1 + (2 * i) * YROW) = __builtin_amdgcn_perm(zd[i], zc[i], 0x05040100u);
          *(LAS unsigned*)(lds + zw1 + (2 * i + 1) * YROW) = __builtin_amdgcn_perm(zd[i], zc[i], 0x07060302u);
        }
      }
      VM0();
      SIG(P_Z2);
    }
  }
  {
    f32x2 hh[5], t_[5];
    STAMP(8);
    asm volatile("" : "+v"(ry), "+v"(rz));
    asm volatile("" : "+s"(w3s[0]), "+s"(w3s[1]));
    if (kh) PRIO(PR1); else PRIO(PHI);
    WAIT_HALF(0, P_Z2);
    STAMP(9);
    TS_(8);
    unsigned zw[5], xp[5], hp_[5];
    {
      const u32x4 a = *(LAS u32x4*)(lds + ry);
      zw[0] = a[0]; zw[1] = a[1]; zw[2] = a[2]; zw[3] = a[3];
      zw[4] = *(LAS unsigned*)(lds + rz);
    }
    FENCE();
#define RQ3(k) do { LD_W3R((k) == 0 ? 0 : (k) == 1 ? NR0 : NR1, (k) == 0 ? NR0 : (k) == 1 ? NR1 : NWH); FENCE(); } while (0)
    RQ3(0);
    FENCE();
    if constexpr (TSHI == 22) TX_(30);
    STG(5, hh[i] = up2(zw[i]); PIN(hh[i]));
    STG(5, xq[i] = FMA2(hh[i], ha[i], xq[i]); PIN(xq[i]));
    STG(5, t_[i] = FMA2(xq[i], cp, xq[i]); PIN(t_[i]));
    STG(5, xq[i] = FMA2(xq[i], cn, t_[i]); PIN(xq[i]));
    if constexpr (TSHI == 22) TX_(31);
    RQ3(1);
    if constexpr (TSHI == 22) TX_(32);
    float m3;
    {
      const f32x2 s2 = ((xq[0] + xq[1]) + (xq[2] + xq[3])) + xq[4];
      const f32x2 q2 = FMA2(xq[0], xq[0], xq[1] * xq[1]) + FMA2(xq[2], xq[2], FMA2(xq[3], xq[3], xq[4] * xq[4]));
      const f32x2 p2 = FMA2(xq[0], ca[0], xq[1] * ca[1]) + FMA2(xq[2], ca[2], FMA2(xq[3], ca[3], xq[4] * ca[4]));
      float sm3[3] = {s2[0] + s2[1], q2[0] + q2[1], p2[0] + p2[1]};
      wave_sums<3>(sm3);
      const float mean = sm3[0] * (1.f / (float)D_), qs = sm3[1], ps = sm3[2];
      const float m = rbf(mean);
      f32x2 d[5];
#pragma unroll
      for (int j = 0; j < 5; ++j) d[j] = (xq[j] - m) * ca[j];
      const float var = rbf(__builtin_fmaxf(__builtin_fmaf(-mean, mean, qs * (1.f / (float)D_)), 0.f));
      const float den = rbf(__builtin_amdgcn_sqrtf(rbf(var + 1e-5f)));
      const float rinv = __builtin_amdgcn_rcpf(den);
      m3 = rbf(__builtin_fmaf(__builtin_fmaf(-m, mc, ps * (1.f / (float)D_)), rinv, mb));
      __builtin_amdgcn_sched_barrier(0);
      if constexpr (TSHI == 22) TX_(33);
      RQ3(2);
      if constexpr (TSHI == 22) TX_(34);
      STG(5, hh[i] = FMA2(d[i], (f32x2{rinv, rinv}), sb[i]); PIN(hh[i]));
      STG(5, hh[i] -= m3; PIN(hh[i]));
    }
#pragma unroll
    for (int j = 0; j < 5; ++j) hp_[j] = pack2(hh[j][0], (j & 1) ? hh[j][1] : (j < 4 || (lane & 1) == 0) ? codedv(hh[j][1]) : hh[j][1]);
    *(LAS u32x4*)(lds + ry) = u32x4{hp_[0], hp_[1], hp_[2], hp_[3]};
    *(LAS unsigned*)(lds + rz) = hp_[4];
    BARRIER();
    {
      const unsigned tb = (unsigned)(s0 >> 4) * 20480u + (unsigned)(s0 & 15) * 16u + (unsigned)(tid >> 3) * 256u + (unsigned)(tid & 7) * 16u;
      const LAS char* pr = lds + L_XCH + (tid & 7) * YROW + (tid >> 3) * 16;
      const u32x4 v0 = *(const LAS u32x4*)(pr);
      const u32x4 v1 = *(const LAS u32x4*)(pr + (tid < 128 ? 1024 : 0));
      st128(v0, rW, tb, O_HP, 16);
      if (wv < 2) st128(v1, rW, tb + 64u * 256u, O_HP, 16);
    }
    FENCE();
    TS_(9);
    PRIO(1);
#pragma unroll
    for (int j = 0; j < 5; ++j) xp[j] = pack2(xq[j][0], xq[j][1]);
    *(LAS u32x4*)(lds + ry + 8 * YROW) = u32x4{xp[0], xp[1], xp[2], xp[3]};
    *(LAS unsigned*)(lds + rz + 8 * YROW) = xp[4];
    FENCE();
    STAMP(10);
    {
      const f32x2 s2 = ((hh[0] + hh[1]) + (hh[2] + hh[3])) + hh[4];
      const f32x2 q2 = FMA2(hh[0], hh[0], hh[1] * hh[1]) + FMA2(hh[2], hh[2], FMA2(hh[3], hh[3], hh[4] * hh[4]));
      const float mean = wave_sum2(s2[0] + s2[1]) * (1.f / (float)D_), qs = wave_sum2(q2[0] + q2[1]);
      const float var = rbf(__builtin_fmaxf(__builtin_fmaf(-mean, mean, qs * (1.f / (float)D_)), 0.f));
      const float den = rbf(__builtin_amdgcn_sqrtf(rbf(var + 1e-5f)));
      const u32x2 st = {canon(__float_as_uint(m3)), __float_as_uint(den)};
      __builtin_amdgcn_raw_buffer_store_b64(st, rW, lane == 0 ? (unsigned)srow * 8u : OOB, O_ST3, 16);
    }
    {
      BARRIER();
      const unsigned tb = (unsigned)(s0 >> 4) * 20480u + (unsigned)(s0 & 15) * 16u + (unsigned)(tid >> 3) * 256u + (unsigned)(tid & 7) * 16u;
      const LAS char* pr = lds + L_XCH + (8 + (tid & 7)) * YROW + (tid >> 3) * 16;
      st128(*(const LAS u32x4*)(pr), rW, tb, O_XO, 16);
      if (wv >= 6) st128(*(const LAS u32x4*)(pr + 256), rW, tb + 16u * 256u, O_XO, 16);
      SIG(P_L1);
      if (bx == 0 && wv == 5) __builtin_amdgcn_raw_buffer_store_b64(u32x2{(unsigned)epn, (unsigned)(epn >> 32)}, rW, lane == 0 ? 0u : OOB, O_EP, 16);
    }
    FENCE();
    PRIO(0);
    FENCE();
  }

  float sthr = PTHR, scmx = CMAX;
  unsigned scsh = CSH;
  asm volatile("" : "+s"(sthr), "+s"(scsh), "+s"(scmx));
  unsigned xcw = (unsigned)(L_XCH + gm * 5120 + gk * 2048) + l16, xcr = (unsigned)(L_XCH + gm * 5120 + 2048 - gk * 2048) + l16, xcf = (unsigned)(L_CTL + gp * 4);
  asm volatile("" : "+v"(xcw), "+v"(xcr), "+v"(xcf));
  unsigned long long gkm;
  asm volatile("s_cmp_lg_u32 %1, 0\n\ts_cselect_b64 %0, -1, 0" : "=s"(gkm) : "s"(gk) : "scc");
  static_assert(KL >= 3 && KG <= 54 - 2 * KL, "the weights are read in front of the signal");
#define EXCHANGE(ph)                                                                        \
  do {                                                                                      \
    f32x4 e0, e1, e2;                                                                       \
    unsigned spins = 0;                                                                     \
    for (;;) {                                                                              \
      const unsigned fl = *(volatile LAS unsigned*)(lds + xcf);                                                           \
      e0 = *(volatile LAS f32x4*)(lds + xcr);                                               \
      e1 = *(volatile LAS f32x4*)(lds + xcr + 1024);                                        \
      e2 = *(volatile LAS f32x4*)(lds + xcr + 2048);                                        \
      if (__builtin_amdgcn_readfirstlane(fl) >= (ph)) break;                                \
      SPIN_STEP();                                                                          \
      if (spins > SPIN_CAP) break;                                                          \
    }                                                                                       \
    asm volatile("s_nop 3" : "+v"(acc[3]), "+v"(acc[4]));        \
    STG(12, e[i] = (i < 4 ? acc[3][i & 3] + e0[i & 3] : i < 8 ? acc[4][i & 3] + e1[i & 3] : acc[2][i & 3] + e2[i & 3]); PIN(e[i])); \
  } while (0)
#define KLOOP(CD, PB, BLK, VA, AOFF, XG, XS, ph)                                            \
  asm volatile("s_nop 1" : "+v"(A[0]));                                                     \
  sfor<0, 50>([&](auto M_) __attribute__((always_inline)) {                                 \
    constexpr int m = decltype(M_)::v, i = kl_i(m), j = kl_j(m), f = m / 4 + 1, c = m % 4;  \
    mf<i == 0 && (CD || !HFI || j < 2)>(acc[j], __builtin_bit_cast(u32x4, __builtin_shufflevector(Bh[2 * m], Bh[2 * m + 1], 0, 1, 2, 3, 4, 5, 6, 7)), A[i]); \
    SB();                                                                                   \
    constexpr int n0_ = m < KG ? (m * (100 - 2 * (PB))) / KG : 100, n1_ = m < KG ? ((m + 1) * (100 - 2 * (PB))) / KG : 100; \
    if constexpr (n1_ > n0_) RD_BH(2 * (PB) + n0_, BLK);                                    \
    if constexpr (n1_ > n0_ + 1) RD_BH(2 * (PB) + n0_ + 1, BLK);                            \
    static_assert(n1_ <= n0_ + 2 && (f > 9 || kl_i(m + 1) < f), "order");                   \
    if constexpr (f < 10) {                                                                 \
      if constexpr (c == 0) { m_ = CD ? amin2(A[f]) : __builtin_amdgcn_fmed3f(__builtin_fabsf(__uint_as_float(A[f][0])), __builtin_fabsf(__uint_as_float(A[f][2])), scmx); PIN(m_); } \
      if constexpr (c == 1) CMP_BAD(CD, bm, m_);                                            \
      if constexpr (c == 2 && CD) A[f][0] -= scsh;                                          \
      if constexpr (c == 3 && CD) { A[f][2] -= scsh; PIN(A[f]); }                           \
      if constexpr (c == 3) RETRY(CD, A[f], bm, VA, sa[f / 5], AOFF + (f % 5) * 2048, PHI); \
    }                                                                                       \
    if constexpr (m % 5 == 2 || m % 5 == 4) { XS(2 * (m / 5) + (m % 5 == 4)); }             \
    XG(m);                                                                                  \
                                \
    if constexpr (m == 51 - 2 * KL) { asm volatile("s_nop 3" : "+v"(acc[0])); *(volatile LAS f32x4*)(lds + xcw) = acc[0]; } \
    if constexpr (m == 52 - 2 * KL) *(volatile LAS f32x4*)(lds + xcw + 1024) = acc[1];      \
    if constexpr (m == 53 - 2 * KL) asm volatile("s_mov_b64 exec, %2\n\tds_write_b128 %0, %1 offset:2048\n\ts_mov_b64 exec, -1" :: "v"(xcw), "v"(acc[2]), "s"(gkm) : "memory"); \
    if constexpr (m == 54 - 2 * KL) SIG(ph);                                                \
    if constexpr (TSHI == 20 && m % 5 == 0) TX_((CD ? 30 : 44) + m / 5);                    \
    SB();                                                                                   \
  })
  {
    f32x4 acc[5];
    s16x4 Bh[100];
    u32x4 A[10];
    float m_, e[12];
    unsigned long long bm;
    unsigned sa[2] = {ROT ? (unsigned)(nt & 1) * 10240u : 0u, ROT ? 10240u - (unsigned)(nt & 1) * 10240u : 10240u};
    asm volatile("" : "+s"(sa[0]), "+s"(sa[1]));
    const unsigned va = (unsigned)(row >> 4) * 20480u + gk * 1024 + l16;
#define LD_A1(x) A[x] = __builtin_amdgcn_raw_buffer_load_b128(rW, va, sa[(x) / 5] + (O_HP + ((x) % 5) * 2048), 16)
    unsigned bb[3][2];
#pragma unroll
    for (int c = 0; c < 6; ++c) {
      bb[c >> 1][c & 1] = (unsigned)gk * 5120u + ((unsigned)lane >> 2) * 160u + ((unsigned)lane & 3u) * 8u + sa[c & 1] * 5u + (c < 2 ? 96u - 96u * gk : c < 4 ? 64u : 96u * gk);
      PIN(bb[c >> 1][c & 1]);
    }
#define RD_BH(h, BLK) Bh[h] = __builtin_amdgcn_ds_read_tr16_b64_v4i16((LAS s16x4*)(lds + bb[kl_j((h) / 2) < 2 ? 0 : kl_j((h) / 2) == 2 ? 1 : 2][kl_i((h) / 2) / 5] + (BLK) + (kl_i((h) / 2) % 5) * 10240 + (kl_j((h) / 2) % 3 == 1 ? 32 : 0) + ((h) & 1) * 2560))
#define BLK1 0
    FENCE();
#pragma unroll
    for (int n = 0; n < 2 * PBE; ++n) RD_BH(n, BLK1);
    FENCE();
    if (DLY1) __builtin_amdgcn_s_sleep(DLY1);
    PRIO(3);
    TS_(10);
#pragma unroll
    for (int i = 0; i < NA1; ++i) { LD_A1(i); SB(); }
    PRIO(1);
#pragma unroll
    for (int n = 2 * PBE; n < 2 * PB1; ++n) RD_BH(n, BLK1);
    FENCE();
#pragma unroll
    for (int i = NA1; i < NAF; ++i) { LD_A1(i); SB(); }
    u32x2 s3v;
    FENCE();
    if (kh) PRIO(PH1); else PRIO(PHI);
    STAMP(11);
    const int sh = (nt >> 1) == 0 ? 1 : (nt >> 1) == 1 ? -1 : (nt >> 1) == 2 ? 2 : -2;
    const int rr = row + sh, rw = rr & (S_ - 1);
    unsigned keep = rr == rw ? 0xffffffffu : 0u;
    unsigned oh1 = ofr - (unsigned)(row >> 4) * 20480u - (unsigned)l15 * 16u + (unsigned)(rw >> 4) * TS + (unsigned)(rw & 15) * 16u + O_H1;
    asm volatile("" : "+v"(keep), "+v"(oh1));
    asm volatile("" : "+s"(w3s[2]), "+s"(w3s[3]));
    if constexpr (TSHI == 21) { TX_(29); sfor<0, 10>([&](auto I_) __attribute__((always_inline)) { PIN(A[decltype(I_)::v]); TX_(30 + decltype(I_)::v); __builtin_amdgcn_s_sleep(1); }); }
    WAIT_ALL(P_L1);
    SB();
    CHECK_A(true, A[0], 0, NAF, LD_A1);
    SB();
#define XG1(m) do { if constexpr ((m) % 2 == 0 && NAF + (m) / 2 < 10) LD_A1(NAF + (m) / 2); if constexpr ((m) == 13) s3v = __builtin_amdgcn_raw_buffer_load_b64(rW, (unsigned)row * 8u, O_ST3, 16); if constexpr ((m) == 4) TS_(11); } while (0)
#define XSL(u, s0, n, f0) do { if constexpr ((u) >= (s0) && (((u) - (s0) + 1) * (n)) / (20 - (s0)) > (((u) - (s0)) * (n)) / (20 - (s0))) LD_W3U((f0) + (((u) - (s0)) * (n)) / (20 - (s0))); } while (0)
#define XS1(u) XSL(u, SL1, NL1, NWH)
    KLOOP(true, PB1, BLK1, va, O_HP, XG1, XS1, P_XF1);
    TS_(12);
    if (__builtin_expect(bad1(s3v[0]), 0)) {
      unsigned spins = 0;
      do {
        __builtin_amdgcn_s_sleep(RSLEEP);
        s3v = __builtin_amdgcn_raw_buffer_load_b64(rW, (unsigned)row * 8u, O_ST3, 16);
        SPIN_STEP();
      } while (bad1(s3v[0]) && spins < RETRY_CAP);
    }
    const float ld = __uint_as_float(s3v[1]), lr = __builtin_amdgcn_rcpf(ld);
    float gca = lr * GC1, gcb = lr * lr * lr * GC2;
    asm volatile("" : "+v"(gca), "+v"(gcb));
    EXCHANGE((unsigned)P_XF1);
    TS_(13);
    {
      unsigned pk[6];
      if (gk == 0) {
        gelu_s<12>(e, gca, gcb, ld, pk);
        __builtin_amdgcn_raw_buffer_store_b64(u32x2{pk[4] & keep, pk[5] & keep}, rW, oh1 + 1024, 0, 16);
      } else {
        gelu_s<8>(e, gca, gcb, ld, pk);
      }
      st128(widen(pk[0] & keep, pk[1] & keep, pk[2] & keep, pk[3] & keep), rW, oh1 + (unsigned)(g & 1) * 504u, 0, 16);
    }
    FENCE();
    TS_(14);
    PRIO(0);
    LD_W3R(NWH + NL1, NWH + NL1 + NP0);
    FENCE();
    WAIT_ALL(P_XF1);
#pragma unroll
    for (int i = 0; i < 10; ++i) *(LAS u32x4*)(lds + 20480 + wv * 5120 + (i / 5) * 40960 + (i % 5) * 1024 + l16) = WS2[i];
    SIG(P_G1);
    STAMP(13);
    FENCE();
    LD_W3R(NWH + NL1 + NP0, NWH + NL1 + NP1);
    FENCE();
    const unsigned va2 = (unsigned)(row >> 4) * TS + gk * 1024 + l16;
    u32x2 hpv[3];
#define LD_A2(x) A[x] = __builtin_amdgcn_raw_buffer_load_b128(rW, va2, sa[(x) / 5] + (O_H1 + ((x) % 5) * 2048), 16)
    u32x4 hp4;
    const unsigned ofw = ofr + (unsigned)(g & 1) * 504u;
    hp4 = __builtin_amdgcn_raw_buffer_load_b128(rW, ofw, O_HP, 16);
    hpv[2] = __builtin_amdgcn_raw_buffer_load_b64(rW, gk ? OOB : ofr + 1024, O_HP, 16);
    FENCE();
    WAIT_ALL(P_G1);
    if (DLY2) __builtin_amdgcn_s_sleep(DLY2);
    PRIO(3);
    TS_(15);
#pragma unroll
    for (int i = 0; i < NA1; ++i) { LD_A2(i); SB(); }
    PRIO(1);
#define BLK2 20480
#pragma unroll
    for (int n = 0; n < 2 * PB2; ++n) RD_BH(n, BLK2);
    FENCE();
#pragma unroll
    for (int i = NA1; i < NAF; ++i) { LD_A2(i); SB(); }
    FENCE();
    if (kh) PRIO(PH2); else PRIO(PHI);
    float hf[12];
    { const u32x4 n_ = widen(hp4[0], hp4[1], hp4[2], hp4[3]); hpv[0] = u32x2{n_[0], n_[1]}; hpv[1] = u32x2{n_[2], n_[3]}; }
#pragma unroll
    for (int j = 0; j < 3; ++j) { hpv[j][0] -= CSH; PIN(hpv[j]); }
    STG(12, hf[i] = ((i & 1) ? hi2f(hpv[i >> 2][(i >> 1) & 1]) : lo2f(hpv[i >> 2][(i >> 1) & 1])) + __uint_as_float(s3v[0]); PIN(hf[i]));
    acc[3] = f32x4{hf[0], hf[1], hf[2], hf[3]}; acc[4] = f32x4{hf[4], hf[5], hf[6], hf[7]};
    acc[2] = gk ? f32x4{0.f, 0.f, 0.f, 0.f} : f32x4{hf[8], hf[9], hf[10], hf[11]};
    PIN(acc[2]); PIN(acc[3]); PIN(acc[4]);
    STAMP(14);
    if constexpr (TSHI == 21) { TX_(43); sfor<0, 10>([&](auto I_) __attribute__((always_inline)) { PIN(A[decltype(I_)::v]); TX_(44 + decltype(I_)::v); __builtin_amdgcn_s_sleep(1); }); }
    SB();
    CHECK_A(false, A[0], 0, NAF, LD_A2);
    SB();
#define XG2(m) do { if constexpr ((m) % 2 == 0 && NAF + (m) / 2 < 10) LD_A2(NAF + (m) / 2); if constexpr ((m) == 4) TS_(16); } while (0)
#define XS2(u) XSL(u, SL2, NL2, NWH + NL1 + NP1)
    KLOOP(false, PB2, BLK2, va2, O_H1, XG2, XS2, P_XF2);
    TS_(17);
    EXCHANGE((unsigned)P_XF2);
    TS_(18);
    unsigned xk[6];
    {
      if (!HFI) STG(12, e[i] += hf[i]; PIN(e[i]));
      STG(3, e[4 * i + 1] = plainv(e[4 * i + 1]); PIN(e[4 * i + 1]));
      STG(6, xk[i] = pack2(e[2 * i], e[2 * i + 1]); PIN(xk[i]));
      st128(widen(xk[0], xk[1], xk[2], xk[3]), rW, ofw, O_X1, 16);
      if (gk == 0) __builtin_amdgcn_raw_buffer_store_b64(u32x2{xk[4], xk[5]}, rW, ofr + 1024, O_X1, 16);
    }
    FENCE();
    TS_(19);
    PRIO(1);
    SIG(P_G2);
    float v[12];
    float s = 0.f;
#pragma unroll
    for (int j = 0; j < 3; ++j) {
      v[4 * j] = lo2f(xk[2 * j]); v[4 * j + 1] = hi2f(xk[2 * j]); v[4 * j + 2] = lo2f(xk[2 * j + 1]); v[4 * j + 3] = hi2f(xk[2 * j + 1]);
      if (j < 2 || gk == 0) s += (v[4 * j] + v[4 * j + 1]) + (v[4 * j + 2] + v[4 * j + 3]);
    }
    s = swap_add32(swap_add16(s));
    const float mean = s * (gk ? (1.f / 32.f) : (1.f / 48.f));
    float m2 = 0.f;
#pragma unroll
    for (int i = 0; i < 12; ++i) {
      const float dd = v[i] - mean;
      if (i < 8 || gk == 0) m2 += dd * dd;
    }
    m2 = swap_add32(swap_add16(m2));
    {
      u32x2 st = {canon(__float_as_uint(mean)), __float_as_uint(m2)};
      __builtin_amdgcn_raw_buffer_store_b64(st, rW, g == 0 ? (unsigned)(((row >> 4) * 16 + nt * 2 + gk) * 16 + l15) * 8u : OOB, O_ST4, 16);
    }
    FENCE();
    PRIO(0);
    FENCE();
    STAMP(16);
  }
  {
    constexpr int NR3 = 40 - NWH - NL1 - NP1 - NL2;
#define NOW4D(...) __VA_ARGS__
#define LD_W4(f) W4[f] = __builtin_amdgcn_raw_buffer_load_b128(rW, l16, w4a + (f) * 1024, 16)
    const unsigned va4 = O_H + (unsigned)(m0 >> 4) * TS + (unsigned)k4 * 1024u;
#define A4OFF(x) (((x) / NS) * TS + ((x) % NS) * 1024)
#define LD_A4(x) A4[(x) % AW4] = __builtin_amdgcn_raw_buffer_load_b128(rW, l16, va4 + A4OFF(x), 16)
    const int lane0 = lane;
#define LANES() int lane = lane0; PIN(lane); const int l15 = lane & 15, g = lane >> 4; const unsigned l16 = (unsigned)lane * 16u; \
    const unsigned fa = (unsigned)L_CTL + ((unsigned)lane & 7u) * 4u
#define FLG(a) *(volatile LAS unsigned*)(lds + (a))
    auto g3 = [&](auto KH_, u32x4 (&W4)[35], u32x4 (&A4)[AW4], u32x2 (&x1v)[3]) __attribute__((always_inline)) {
      constexpr int KH = decltype(KH_)::v, NS = 7, NW0 = KH ? 6 : AW4;
      LANES();
      const unsigned hw0 = (unsigned)(m0 >> 4) * TS + (unsigned)l15 * 16u + (unsigned)(g >> 1) * 256u + O_H;
      const unsigned csp = (unsigned)(L_CS3 + wv * 128 + g * 16), f1 = (unsigned)L_CTL + 16u + ((unsigned)lane & 3u) * 4u;
      u32x2 stv[4];
#define LD_ST() _Pragma("unroll") for (int a = 0; a < 4; ++a) stv[a] = __builtin_amdgcn_raw_buffer_load_b64(rW, (unsigned)((((m0 >> 4) + mg) * 16 + 4 * g) * 16 + l15) * 8u, O_ST4 + a * 128, 16)
#define ST_BAD() bad1(max(max(stv[0][0], stv[1][0]), max(stv[2][0], stv[3][0])))
      const u32x4 ones = {0x3f803f80u, 0x3f803f80u, 0x3f803f80u, 0x3f803f80u};
      f32x4 c3a = {0.f, 0.f, 0.f, 0.f}, c3b = {0.f, 0.f, 0.f, 0.f};
#define CS3R(a, b) do { _Pragma("unroll") for (int f_ = (a); f_ < (b); f_ += 2) { c3a = mfma16(W3[f_], ones, c3a); if (!KH || f_ < 20) c3b = mfma16(W3[f_ + 1], ones, c3b); } } while (0)
      TS_(20);
      PRIO(PHI);
      LD_W3R(40 - NR3, 40);
      FENCE();
      const unsigned so = O_X1 + (unsigned)mt * 81920u + (unsigned)wv * 10240u, d0 = (unsigned)(KH ? L_XB : L_XA) + (unsigned)mg * 10240u;
      CS3R(0, KH ? KC3B : KC3);
      asm volatile("" : "+v"(c3a), "+v"(c3b));
      FENCE();
      TS_(21);
      WAIT_2(KH ? P_XF2 : P_G2, KH ? P_G2 : P_XF2);
      if (KH ? DLY3B : DLY3) __builtin_amdgcn_s_sleep(KH ? DLY3B : DLY3);
      PIN(W3[38]); PIN(W3[39]);
      if (KH) {
#pragma unroll
        for (int f = 0; f < 10; ++f) W4[f] = W3[21 + 2 * f];
      }
      FENCE();
      PRIO(3);
      TS_(22);
#define STG_DMA() do { dma<4, 1>(l16, dW, so, d0); dma<4, 1>(l16, dW, so + 4096, d0 + 4096); dma<1, 1>(l16, dW, so + 8192, d0 + 8192); dma<1, 1>(l16, dW, so + 9216, d0 + 9216); } while (0)
      if (KH) {
        asm volatile("buffer_load_dwordx2 %0, %4, %5, %6 offen sc1\n\tbuffer_load_dwordx2 %1, %4, %5, %6 offen offset:128 sc1\n\t"
                     "buffer_load_dwordx2 %2, %4, %5, %6 offen offset:256 sc1\n\tbuffer_load_dwordx2 %3, %4, %5, %6 offen offset:384 sc1"
                     : "=&v"(stv[0]), "=&v"(stv[1]), "=&v"(stv[2]), "=&v"(stv[3]) : "v"((unsigned)((((m0 >> 4) + mg) * 16 + 4 * g) * 16 + l15) * 8u), "s"(dW), "s"(O_ST4) : "memory");
      }
      STG_DMA();
      FENCE();
      TS_(23);
      PRIO(PHI);
      CS3R(KH ? KC3B : KC3, 40);
      if (l15 == 0) { *(LAS f32x4*)(lds + csp) = c3a; *(LAS f32x4*)(lds + csp + 64) = c3b; }
      FENCE();
      if (KH) {
        asm volatile("s_waitcnt vmcnt(10)" : "+v"(stv[0]), "+v"(stv[1]), "+v"(stv[2]), "+v"(stv[3]));
        if (__builtin_expect(ST_BAD(), 0)) {
          unsigned spins = 0;
          do {
            __builtin_amdgcn_s_sleep(RSLEEP);
            LD_ST();
            SPIN_STEP();
          } while (ST_BAD() && spins < RETRY_CAP);
        }
        const float m0_ = __uint_as_float(stv[0][0]), q0 = __uint_as_float(stv[0][1]), m1 = __uint_as_float(stv[1][0]), q1 = __uint_as_float(stv[1][1]);
        const float m2_ = __uint_as_float(stv[2][0]), q2 = __uint_as_float(stv[2][1]), m3 = __uint_as_float(stv[3][0]), q3 = __uint_as_float(stv[3][1]);
        float sm = 48.f * (m0_ + m2_) + 32.f * (m1 + m3);
        sm = swap_add32(swap_add16(sm));
        const float mean = sm * (1.f / (float)D_);
        const float d0_ = m0_ - mean, d1 = m1 - mean, d2 = m2_ - mean, d3 = m3 - mean;
        float qs = (q0 + q1) + (q2 + q3) + 48.f * (d0_ * d0_ + d2 * d2) + 32.f * (d1 * d1 + d3 * d3);
        qs = swap_add32(swap_add16(qs));
        const float var = rbf(qs * (1.f / (float)D_));
        const float den = rbf(__builtin_amdgcn_sqrtf(rbf(var + 1e-5f))), r = __builtin_amdgcn_rcpf(den);
        float rc1 = r * GC1;
        PIN(rc1);
        if (g == 0) *(volatile LAS f32x4*)(lds + L_ST4 + (mg * 16 + l15) * 16) = f32x4{rbf(mean), rc1, r * r * r * GC2, den};
        SIG(P_SF);
      }
      FENCE();
      TS_(24);
      VMW(0);
      TS_(25);
      {
        unsigned spins = 0;
        for (;;) {
          u32x4 a[10];
          float mx = 0.f;
#pragma unroll
          for (int i = 0; i < 10; ++i) a[i] = *(volatile LAS u32x4*)(lds + d0 + i * 1024 + l16);
#pragma unroll
          for (int i = 0; i < 10; ++i) mx = amax3(mx, a[i]);
          if (__builtin_expect(__builtin_amdgcn_ballot_w64(mx >= PTHR) == 0ull, 1)) break;
          SPIN_STEP();
          if (spins > RETRY_CAP) break;
          PRIO(0);
          __builtin_amdgcn_s_sleep(RSLEEP);
          STG_DMA();
          PRIO(PHI);
          asm volatile("s_waitcnt vmcnt(0)" ::: "memory");
        }
        SIG(P_SA);
      }
      TS_(26);
      STAMP(17);
      FENCE();
      constexpr int NP = KH ? NPH : NPK, F0 = KH ? 10 : 0, NEQ = NP + 6, EQ0 = KH ? EC0 : EA0, EQ1 = KH ? EC1 : EA1, NL0 = F0 + NEQ, NLT = (KH ? 30 : 35) - NL0;
      const unsigned pks = (unsigned)(KH ? L_PB + NPK * 1024 + mg * (NPH * 1024) : wv < 3 ? wv * (NPK * 1024) : L_PB);
      constexpr auto ecnt = [](int q) { return q <= EQ0 ? 0 : q >= EQ1 ? NEQ : ((q - EQ0) * NEQ + EQ1 - EQ0 - 1) / (EQ1 - EQ0); };
#define ESLOT(q) sfor<ecnt(q), ecnt((q) + 1)>([&](auto U_) __attribute__((always_inline)) { constexpr int u = decltype(U_)::v; \
        if constexpr (u < NP) { NOW4D(dma<1, 1>(l16, dW, w4a + (F0 + 6 + u) * 1024, pks + u * 1024)); } else LD_W4(F0 + u - NP); })
      static_assert(3 * NPK <= 32 && NPK + 4 * NPH <= 36 && MV0 + NP <= 20 && NLT <= 19 && NLT >= MV0, "w4");
      sfor<0, 2>([&](auto P_) __attribute__((always_inline)) {
        constexpr int p = decltype(P_)::v, NV = KH ? 12 : 16;
        TS_(28 + 8 * p + 0);
        if (p == 0) WAIT_2(P_SA, P_G2); else WAIT_HALF(1, P_SA);
        f32x4 acc[4];
        PRIO(KH ? (p ? PK1 : PK2) : PK0);
        TS_(28 + 8 * p + 1);
        u32x4 Ar[AD3][2];
        unsigned ab[2] = {l16 + (unsigned)((p ? L_XB : L_XA) + (KH ? hb * 30720 : 0)), l16 + (unsigned)((p ? L_XB : L_XA) + 20480 - (KH ? hb * 10240 : 0))};
        const unsigned sp = (unsigned)(L_ST4 + p * 512 + l15 * 16), fp = (unsigned)(L_CTL + (wv ^ 2) * 4), xs = (unsigned)(32768 + p * 4096) + l16;
        PIN(ab[0]);
        if (KH) PIN(ab[1]);
        f32x4 cs[3], st[2], rx;
        unsigned sf_, sx_;
#define RD_A3(s, h) Ar[(s) % AD3][h] = *(const LAS u32x4*)(lds + (KH ? ab[h] : ab[0] + (h) * 20480) + (s) * 1024)
#pragma unroll
        for (int s = 0; s < AD3 - 1; ++s) { RD_A3(s, 0); RD_A3(s, 1); }
        SB();
        sfor<0, 20>([&](auto S_) __attribute__((always_inline)) {
          constexpr int s = decltype(S_)::v;
          constexpr bool rd = s + AD3 - 1 < 20, T2 = !KH || s < 10;
          mf<s == 0>(acc[0], W3[2 * s], Ar[s % AD3][0]);
          SB();
          if constexpr (rd) RD_A3(s + AD3 - 1, 0);
          else if constexpr (s == 18) cs[0] = *(const LAS f32x4*)(lds + csp);
          else st[0] = *(const volatile LAS f32x4*)(lds + sp + (KH ? hb * 256 : 0));
          SB();
          mf<s == 0>(acc[1], W3[2 * s], Ar[s % AD3][1]);
          SB();
          if constexpr (rd) RD_A3(s + AD3 - 1, 1);
          else if constexpr (s == 18) cs[1] = *(const LAS f32x4*)(lds + csp + 64);
          else st[1] = *(const volatile LAS f32x4*)(lds + sp + 256 - (KH ? hb * 256 : 0));
          SB();
          if constexpr (T2) { mf<s == 0>(acc[2], W3[2 * s + 1], Ar[s % AD3][0]); SB(); }
          if constexpr (p == 0 && s == 15) sf_ = FLG(f1);
          if constexpr (p == 0 && s == 18) NEED_AT(sf_, f1, P_SF);
          if constexpr (p == 0) ESLOT(s);
          if constexpr (p == 1 && s >= 1 && s <= NLT) LD_W4(NL0 + s - 1);
          if constexpr (p == 1 && s == MV0 - 1) VMW(MV0 + 1);
          SB();
          if constexpr (T2) { mf<s == 0>(acc[3], W3[2 * s + 1], Ar[s % AD3][1]); SB(); }
          if constexpr (KH && s + AD3 == 10) { ab[0] -= hb * 20480; ab[1] -= hb * 20480; PIN(ab[0]); PIN(ab[1]); }
          if constexpr (KH && s == 12) { *(volatile LAS f32x4*)(lds + xs + mg * 1024) = acc[3]; SIG(p ? P_S1 : P_S0); }
          if constexpr (KH && s == 14) sx_ = FLG(fp);
          if constexpr (KH && s == 17) { NEED_AT(sx_, fp, p ? P_S1 : P_S0); rx = *(const volatile LAS f32x4*)(lds + xs + (mg ^ 2) * 1024); cs[2] = *(const volatile LAS f32x4*)(lds + csp + (hb ? -192 : 320)); }
          if constexpr (p == 1 && s >= MV0 && s < MV0 + NP) W4[F0 + 6 + s - MV0] = *(const LAS u32x4*)(lds + pks + l16 + (s - MV0) * 1024);
          if constexpr (p == 1 && s >= 1 && s < 4 - KH) {
            static_assert(H0S >= 4 && H0S < 12, "x1");
            const int tu = KH == 0 ? (s == 3 ? 3 : s - 1) : wv < 6 ? 2 : s == 1 ? wv & 1 : wv - 4, ju = KH == 0 ? wv : wv < 6 ? 2 * wv - 6 - s : 4;
            x1v[s - 1] = *(const LAS u32x2*)(lds + (tu < 2 ? L_XA : L_XB - 40960) + tu * 20480 + (nt * 10 + 2 * ju) * 256 + (g >> 1) * 256 + (g & 1) * 8 + l15 * 16);
          }
          if constexpr (p == 1 && s == H0S) SIG(P_H0);
          if constexpr (s == 9) TS_(28 + 8 * p + 2);
          SB();
        });
        TS_(28 + 8 * p + 3);
        PRIO(KH && p ? PE1 : PE0);
        SIG(p ? P_KB : P_KA);
        if constexpr (KH) asm volatile("s_nop 3" : "+v"(acc[0]), "+v"(acc[1]), "+v"(acc[2])); else asm volatile("s_nop 3" : "+v"(acc[0]), "+v"(acc[1]), "+v"(acc[2]), "+v"(acc[3]));
        STAMP(18 + 2 * p);
        {
          float e[NV], x[NV];
          unsigned pk[NV / 2];
#define SQ(i) st[((i) >> 2) & 1]
#define EPS(q) do { if constexpr (p == 0) { ESLOT(20 + (q)); } if constexpr (p == 1 && (q) == HG - 1) sf_ = FLG(fa); \
                    if constexpr (p == 1 && (q) == HG) { NEED_AT(sf_, fa, P_H0); _Pragma("unroll") for (int x = 0; x < NW0; ++x) { LD_A4(x); } } SB(); } while (0)
          if constexpr (KH) STG(4, x[i] = __builtin_fmaf(-st[0][0], cs[2][i], rx[i]); PIN(x[i]));
          STG(NV, e[i] = __builtin_fmaf(-SQ(i)[0], cs[i >> 3][i & 3], acc[i >> 2][i & 3]); PIN(e[i]));
          if constexpr (KH) STG(4, e[8 + i] += x[i]; PIN(e[8 + i]));
          TS_(28 + 8 * p + 4);
          EPS(0);
          STG(NV, x[i] = e[i] * e[i]; PIN(x[i]));
          EPS(1);
          STG(NV, x[i] = __builtin_fmaf(x[i], SQ(i)[2], SQ(i)[1]); PIN(x[i]));
          EPS(2);
          STG(NV, x[i] *= e[i]; PIN(x[i]));
          EPS(3);
          STG(NV, x[i] = __builtin_amdgcn_exp2f(x[i]); PIN(x[i]));
          TS_(28 + 8 * p + 5);
          EPS(4);
          STG(NV, x[i] = __builtin_fmaf(x[i], SQ(i)[3], SQ(i)[3]); PIN(x[i]));
          EPS(5);
          STG(NV, x[i] = __builtin_amdgcn_rcpf(x[i]); PIN(x[i]));
          EPS(6);
          STG(NV, e[i] *= x[i]; PIN(e[i]));
          TS_(28 + 8 * p + 6);
          EPS(7);
          STG(NV / 4, e[4 * i + 1] = codedv(e[4 * i + 1]); PIN(e[4 * i + 1]));
          STG(NV / 2, pk[i] = pack2(e[2 * i], e[2 * i + 1]); PIN(pk[i]));
          const unsigned hw1 = hw0 + (unsigned)((g & 1) ^ (KH ? hb : 0)) * TS;
          st128(widen(pk[0], pk[1], pk[2], pk[3]), rW, hw1, (unsigned)(sb3 + jl) * 512u + 2 * p * TS, 16);
          if constexpr (KH) __builtin_amdgcn_raw_buffer_store_b64(u32x2{pk[4], pk[5]}, rW, hw0 + (unsigned)(g & 1) * 8u, two ? (unsigned)(sb3 + jm) * 512u + (unsigned)(2 * p + hb) * TS : OOB, 16);
          else st128(widen(pk[4], pk[5], pk[6], pk[7]), rW, hw1, (unsigned)(sb3 + jm) * 512u + 2 * p * TS, 16);
          FENCE();
          EPS(8);
        }
        TS_(28 + 8 * p + 7);
        STAMP(19 + 2 * p);
      });
    };
#define PB(t) (81920 - (t) * 40960)
    const __amdgpu_buffer_rsrc_t rO = __builtin_amdgcn_make_buffer_rsrc((void*)out, 0, SD2, RS3);
    auto g4 = [&](auto KD_, u32x4 (&W4)[35], u32x4 (&A4)[AW4], u32x2 (&x1v)[3]) __attribute__((always_inline)) {
      constexpr int KD = decltype(KD_)::v, NS = KD == 1 ? 6 : 7, PR = KD ? P41 : P40;
      constexpr int GS = NS + (KD == 0 ? GSA : KD == 1 ? GSB : GSC);
      constexpr auto nreq = [](int x) { int n = AW4; for (int y = 0; y <= x; ++y) { int m = y + AW4 < 4 * NS ? y + AW4 : 4 * NS; if (y < GS && m > 2 * NS) m = 2 * NS; n = m < n + 3 ? (m > n ? m : n) : n + 3; } return n; };
      LANES();
      TS_(44);
      if constexpr (KD) {
#pragma unroll
        for (int x = 6; x < AW4; ++x) { LD_A4(x); }
      }
      if constexpr (KD == 2) {
#pragma unroll
        for (int f = 30; f < 35; ++f) { LD_W4(f); }
      }
      FENCE();
      PRIO(PR);
      u32x2 xv[2];
      unsigned fl_, fh_, fu_, pu_;
      f32x4 acc[2][5], rs[4], gq;
      float u_[4], y_[4], m4_;
      unsigned long long bm4;
      unsigned pw = (unsigned)(wv * 5120) + l16;
      PIN(pw);
      const unsigned fo0 = (unsigned)((m0 >> 4) * 20480 + ((n0 + 4 * g) >> 3) * 256 + (g & 1) * 8 + l15 * 16), oo0 = (unsigned)((m0 + l15) * D_ + n0 + 4 * g) * 2u;
#define U_LD(r, t, j) xv[r] = __builtin_amdgcn_raw_buffer_load_b64(rW, fo0, O_XO + (unsigned)((t) * 20480 + (j) * 512), 16)
#define U_PC(q, t, j, y)                                                                               \
  do {                                                                                                  \
    if constexpr ((q) == 0) { fu_ = FLG(fa); pu_ = (unsigned)(PB(t) + (j) * 1024) + l16; PIN(pu_); }    \
    if constexpr ((q) == 1) { gq = *(const LAS f32x4*)(lds + L_GB + ((j) * 16 + 4 * g) * 4); }          \
    if constexpr ((q) >= 1 && (q) < 5) { constexpr int i = (q) - 1; u_[i] = (i & 1) ? hi2f(x1v[y][i >> 1]) : lo2f(x1v[y][i >> 1]); PIN(u_[i]); } \
    if constexpr ((q) == 5) NEED_AT(fu_, fa, P_P0 + 1 + (t));                                           \
    if constexpr ((q) >= 6 && (q) < 10) rs[(q) - 6] = *(const volatile LAS f32x4*)(lds + pu_ + ((q) - 6) * 5120); \
    if constexpr ((q) >= 11 && (q) < 27) { constexpr int w = ((q) - 11) >> 1, h = (((q) - 11) & 1) * 2;  \
      u_[h] += rs[w & 3][h]; PIN(u_[h]); u_[h + 1] += rs[w & 3][h + 1]; PIN(u_[h + 1]);                 \
      if constexpr (h == 2 && w < 4) rs[w] = *(const volatile LAS f32x4*)(lds + pu_ + (4 + w) * 5120); } \
    if constexpr ((q) >= 6 && (q) < 10) { constexpr int i = (q) - 6; y_[i] = (i & 1) ? hi2f(xv[0][i >> 1]) : lo2f(xv[0][i >> 1]); PIN(y_[i]); } \
    if constexpr ((q) == 27 || (q) == 28) { constexpr int h = ((q) - 27) * 2; u_[h] = __builtin_fmaf(u_[h], gq[h], y_[h]); PIN(u_[h]); u_[h + 1] = __builtin_fmaf(u_[h + 1], gq[h + 1], y_[h + 1]); PIN(u_[h + 1]); } \
    if constexpr ((q) == 29) __builtin_amdgcn_raw_buffer_store_b64(u32x2{pack2(u_[0], u_[1]), pack2(u_[2], u_[3])}, rO, oo0, (unsigned)((t) * (16 * D_ * 2) + (j) * 32), 0); \
  } while (0)
      {
        unsigned long long b0;
        const float m0_ = amin2(A4[0]);
        CMP_BAD(1, b0, m0_);
        A4[0][0] -= scsh; A4[0][2] -= scsh;
        RETRY_C(A4[0], b0, l16, va4, 0, PR);
        asm volatile("s_nop 0" : "+v"(A4[0]));
      }
      SB();
      const int tc = KD == 0 ? 3 : KD == 1 ? 2 : wv - 4, jc = KD == 0 ? wv : KD == 1 ? 2 * wv - 8 : 4;
      sfor<0, 20 * NS>([&](auto M_) __attribute__((always_inline)) {
        constexpr int m = decltype(M_)::v, x = m / 5, j = m % 5, t = x / NS, s = x % NS, r0 = nreq(x - 2), r1 = nreq(x - 1);
        constexpr bool nx = x + 1 < 4 * NS;
        u32x4& an = A4[(x + 1) % AW4];
        mf<s == 0>(acc[t & 1][j], W4[5 * s + j], A4[x % AW4]);
        SB();
        if constexpr (j == 0) {
          if constexpr (x == GS + 1) NEED_AT(fh_, fa, P_H1);
          if constexpr (r0 < r1) { LD_A4(r0); }
        }
        if constexpr (j == 1 && nx) { m4_ = amin2(an); PIN(m4_); }
        if constexpr (j == 2 && nx) { CMP_BAD(1, bm4, m4_); an[0] -= scsh; }
        if constexpr (j == 3 && nx) { an[2] -= scsh; PIN(an); }
        if constexpr (j == 3 && r0 + 1 < r1) { LD_A4(r0 + 1); }
        if constexpr (j == 4) {
          if constexpr (nx) RETRY_C(an, bm4, l16, va4, A4OFF(x + 1), PR);
          if constexpr (r0 + 2 < r1) { LD_A4(r0 + 2); }
          if constexpr (x == GS) fh_ = FLG(fa);
          if constexpr (x == H1S) SIG(P_H1);
          if constexpr (s == 0) TS_(45 + 4 * t);
          if constexpr (s == 3) TS_(45 + 4 * t + 1);
          if constexpr (s == NS - 1) TS_(45 + 4 * t + 2);
          static_assert(GS + 1 < 2 * NS, "order");
          if constexpr (KD == 0 && t == 1 && s == 0) U_LD(0, 0, wv);
          if constexpr (KD == 0 && t == 2 && s == 3) U_LD(0, 1, wv);
          if constexpr (KD == 2 && t == 2 && s == 3) U_LD(0, wv & 1, 4);
          if constexpr (KD == 1 && t == 3 && s == 3) U_LD(0, 2, jc + 1);
          if constexpr (t == 2 && s == 5) U_LD(1, tc, jc);
          if constexpr (t == 3 && s == NS - 2) fl_ = FLG(L_CTL);
        }
        constexpr bool un = (KD == 0 && t >= 2) || (KD == 2 && t == 3);
        constexpr int wq = 5 * s + j, w0 = un ? W0U : 4;
        if constexpr (t > 0 && wq >= w0 && wq < w0 + 5 * WSP && (wq - w0) % WSP == 0) *(volatile LAS f32x4*)(lds + PB(t - 1) + pw + ((wq - w0) / WSP) * 1024) = acc[(t - 1) & 1][(wq - w0) / WSP];
        if constexpr (t > 0 && wq == w0 + 1 + 4 * WSP) SIG(P_P0 + t);
        static_assert(W0U + 1 + 4 * WSP < 35 && U0 + 30 <= 35, "unit");
        if constexpr (un && KD == 0 && wq >= U0) U_PC(wq - U0, t - 2, wv, t - 2);
        if constexpr (un && KD == 2 && wq >= U0) U_PC(wq - U0, wv & 1, 4, 0);
        SB();
      });
      asm volatile("s_nop 3" : "+v"(acc[1][0]), "+v"(acc[1][1]), "+v"(acc[1][2]), "+v"(acc[1][3]), "+v"(acc[1][4]));
#pragma unroll
      for (int j = 0; j < 4; ++j) *(volatile LAS f32x4*)(lds + L_PB + wv * 4096 + l16 + j * 1024) = acc[1][j];
      NEED_AT(fl_, (unsigned)L_CTL, P_P0 + 3);
      *(volatile LAS f32x4*)(lds + PB(0) + pw) = acc[1][4];
      SIG(P_P0 + 4);
      TS_(60);
#define UNIT(r, y, t, j, base, str)                                                                        \
  do {                                                                                                  \
    unsigned pq = (unsigned)(base) + l16, oq = oo0 + (unsigned)((t) * (16 * D_ * 2) + (j) * 32);        \
    f32x4 gq_ = *(const LAS f32x4*)(lds + L_GB + ((j) * 16 + 4 * g) * 4);                               \
    f32x2 xa = up2(xv[r][0]), xb = up2(xv[r][1]), ya = up2(x1v[y][0]), yb = up2(x1v[y][1]);             \
    asm volatile("" : "+v"(xa), "+v"(xb), "+v"(ya), "+v"(yb), "+v"(gq_), "+v"(pq), "+v"(oq));           \
    f32x4 r8[8];                                                                                        \
    unsigned spins = 0;                                                                                 \
    for (;;) {                                                         \
      const unsigned fl = FLG(fa);                                                                      \
      _Pragma("unroll") for (int w = 0; w < 8; ++w) r8[w] = *(const volatile LAS f32x4*)(lds + pq + w * (str)); \
      if (__builtin_amdgcn_ballot_w64(fl < (unsigned)(P_P0 + 1 + (t))) == 0ull) break;                  \
      SPIN_STEP();                                                                                      \
      if (spins > SPIN_CAP) break;                                                                      \
    }                                                                                                   \
    const f32x4 sm = ((r8[0] + r8[1]) + (r8[2] + r8[3])) + ((r8[4] + r8[5]) + (r8[6] + r8[7]));         \
    const f32x2 a_ = FMA2(f32x2{sm[0], sm[1]} + ya, (f32x2{gq_[0], gq_[1]}), xa), b_ = FMA2(f32x2{sm[2], sm[3]} + yb, (f32x2{gq_[2], gq_[3]}), xb); \
    __builtin_amdgcn_raw_buffer_store_b64(u32x2{pack2(a_[0], a_[1]), pack2(b_[0], b_[1])}, rO, oq, 0, 0); \
    FENCE();                                                                                            \
  } while (0)
      if constexpr (KD == 0) UNIT(1, 2, 3, wv, L_PB + wv * 1024, 4096);
      else UNIT(1, 1, tc, jc, tc == 3 ? PB(0) : PB(2) + jc * 1024, 5120);
      if constexpr (KD == 1) UNIT(0, 0, 2, jc + 1, PB(2) + jc * 1024 + 1024, 5120);
    };
#define KILL(V, N) _Pragma("unroll") for (int f_ = 0; f_ < (N); ++f_) asm volatile("" : "=v"(V[f_]))
    int arm = wv >> 1;
    if (arm < 2) {
      u32x4 W4[35], A4[AW4];
      u32x2 x1v[3];
      g3(IC<0>{}, W4, A4, x1v);
      g4(IC<0>{}, W4, A4, x1v);
      KILL(W3, 40 - NR3);
    }
    asm volatile("" : "+s"(arm));
    if (arm >= 2) {
      u32x4 W4[35], A4[AW4];
      u32x2 x1v[3];
      g3(IC<1>{}, W4, A4, x1v);
      if (arm == 3) {
        g4(IC<2>{}, W4, A4, x1v);
        KILL(W4, 30);
        KILL(A4, 6);
      }
      asm volatile("" : "+s"(arm));
      if (arm == 2) g4(IC<1>{}, W4, A4, x1v);
    }
    STAMP(27);
    CLK(31);
  }
}

extern "C" void kernel_launch(void* const* d_in, const int* in_sizes, int n_in, void* d_out, int out_size,
                              void* d_ws, size_t ws_size, hipStream_t stream) {
  const bf16* A_q = (const bf16*)d_in[0];
  const bf16* A_k = (const bf16*)d_in[1];
  const bf16* A_v = (const bf16*)d_in[2];
  const bf16* A_shift_k = (const bf16*)d_in[3];
  const bf16* A_ssm_k = (const bf16*)d_in[4];
  const bf16* A_ssm_D = (const bf16*)d_in[5];
  const bf16* w1 = (const bf16*)d_in[7];
  const bf16* w2 = (const bf16*)d_in[8];
  const bf16* w3 = (const bf16*)d_in[9];
  const bf16* w4 = (const bf16*)d_in[10];
  const bf16* c = (const bf16*)d_in[11];
  const bf16* w_mod = (const bf16*)d_in[12];
  char* ws = (char*)d_ws;
  (void)hipFuncSetAttribute((const void*)k_main, hipFuncAttributeMaxDynamicSharedMemorySize, L_TOT);
  k_main<<<256, 512, L_TOT, stream>>>(A_k, A_v, ws, A_shift_k, A_ssm_k, w_mod, c, w3, w4, A_q, w1, w2, A_ssm_D, (bf16*)d_out);
}
```

```cpp
#define RTE 7
#define QE 2
#define RD 1
#define RSW 1
#define PF 1
#include <hip/hip_runtime.h>
#include <hip/hip_bf16.h>
#include <stdint.h>

typedef __hip_bfloat16 bf16;
typedef __attribute__((ext_vector_type(8))) short bf16x8;
typedef __attribute__((ext_vector_type(4))) short s16x4;
typedef __attribute__((ext_vector_type(4))) float f32x4;
typedef __attribute__((ext_vector_type(2))) float f32x2;
typedef __attribute__((ext_vector_type(4))) unsigned u32x4;
typedef __attribute__((ext_vector_type(2))) unsigned u32x2;
#define LAS __attribute__((address_space(3)))

static constexpr int S_ = 2048;
static constexpr int D_ = 640;
static constexpr int F_ = 1728;
static constexpr unsigned SENT = 0xffffffffu;
static constexpr int RS3 = 0x00020000;
static constexpr unsigned OOB = 0x40000000u;

static constexpr unsigned SD2 = S_ * D_ * 2;
static constexpr unsigned DF2 = D_ * F_ * 2;
static constexpr unsigned O_WT3 = 0, O_WT4 = DF2;
static constexpr unsigned O_EP = 2 * DF2;
static constexpr unsigned O_MOD = O_EP + 256;
static constexpr unsigned O_MP = O_MOD + 16384;
static constexpr unsigned O_FL1 = O_MP + 131072;
static constexpr unsigned O_F2 = O_FL1;
static constexpr unsigned O_KVT = O_FL1 + 4096;
static constexpr unsigned O_HP = O_KVT, O_X1 = O_KVT;
static constexpr unsigned TS = 57344;
static constexpr unsigned O_H = O_KVT + 384 * 8192;
static constexpr unsigned O_Z = O_H, O_H1 = O_H + 20480;
static constexpr unsigned O_ST3 = O_H + 128 * TS;
static constexpr unsigned O_ST4 = O_ST3 + S_ * 8;
static constexpr unsigned O_XO = O_ST4 + S_ * 16 * 8;
static constexpr unsigned O_W12 = O_XO + SD2;
static constexpr unsigned WS_USED = O_W12 + 256 * 5120;
#define KVROW(c) ((c) < 384u ? O_KVT + (c) * 8192u : O_H + 40960u + (((c) - 384u) >> 1) * TS + ((c) & 1u) * 8192u)
static constexpr unsigned CODED = 0x7f7f7f7fu;
static constexpr unsigned CSH = 98u << 23;
static constexpr float PTHR = 0x1p48f, PMAX = 0x1p47f, CMUL = 0x1p98f, CMAX = 0x1p127f;
__device__ __forceinline__ float plainv(float x) { return __builtin_amdgcn_fmed3f(x, -PMAX, PMAX); }
__device__ __forceinline__ float codedv(float x) { return __builtin_copysignf(__builtin_amdgcn_fmed3f(__builtin_fabsf(x * CMUL), PTHR, CMAX), x); }

__device__ __forceinline__ float rbf(float x) { return __bfloat162float(__float2bfloat16(x)); }
__device__ __forceinline__ float lo2f(unsigned w) { return __uint_as_float(w << 16); }
__device__ __forceinline__ float hi2f(unsigned w) { return __uint_as_float(w & 0xffff0000u); }
typedef __bf16 bf16v2 __attribute__((ext_vector_type(2)));
__device__ __forceinline__ unsigned pack2(float a, float b) {
  bf16v2 r = __builtin_convertvector(f32x2{a, b}, bf16v2);
  return __builtin_bit_cast(unsigned, r);
}

static constexpr float GC1 = -2.f * 0.7978845608028654f * 1.4426950408889634f;
static constexpr float GC2 = GC1 * 0.044715f;
__device__ __forceinline__ float row16_sum(float v) {
  v += __builtin_bit_cast(float, __builtin_amdgcn_update_dpp(0, __builtin_bit_cast(int, v), 0xB1, 0xf, 0xf, true));
  v += __builtin_bit_cast(float, __builtin_amdgcn_update_dpp(0, __builtin_bit_cast(int, v), 0x4E, 0xf, 0xf, true));
  v += __builtin_bit_cast(float, __builtin_amdgcn_update_dpp(0, __builtin_bit_cast(int, v), 0x141, 0xf, 0xf, true));
  v += __builtin_bit_cast(float, __builtin_amdgcn_update_dpp(0, __builtin_bit_cast(int, v), 0x140, 0xf, 0xf, true));
  return v;
}
typedef volatile LAS unsigned* ldsw_t;
static constexpr int CV_STR = 4160;
static constexpr int CV_U = 8 * CV_STR;
static constexpr int CV_SLOT = CV_U + 4608;
static constexpr int SL_A = 46592, SL_C = SL_A + CV_SLOT, SL_B = SL_C + CV_SLOT;
static constexpr int L_KV = 0;
static constexpr int L_RT = L_KV + 24576;
static constexpr int L_MP = L_RT + 20480;
static constexpr int L_XCH = 139264;
static constexpr int L_XA = 81920, L_XB = 40960;
static constexpr int L_PB = 122880;
static constexpr int L_CTL = 160256;
static constexpr int L_MOD = 103424;
static constexpr int L_GB = L_CTL + 64;
static constexpr int L_CS3 = L_CTL + 384;
static constexpr int L_ST4 = L_CTL + 1408;
static constexpr int L_STAMP = L_CTL + 2432;
static constexpr int L_TOT = 163840;
static_assert(L_MP + 1024 <= SL_A && SL_B + CV_SLOT <= L_CTL && L_MOD >= SL_C + 16384 && L_MOD >= 102400 && L_MOD + 7680 <= SL_C + CV_U, "lds");
static constexpr int YROW = 1296;
#define ZCE 0
enum { P_KV = 1, P_K, P_B, P_M, P_W, P_B2, P_M2, P_U, P_C2, P_A2,
  P_RT, P_RO, P_GA, P_ZC,
  P_F2, P_TH, P_X, P_X2, P_T, P_Z, P_RP, P_Z2, P_L1, P_XF1, P_G1, P_XF2, P_G2, P_SF, P_SA, P_S0, P_KA, P_H0, P_S1, P_KB, P_H1, P_P0 };
static constexpr unsigned SPIN_CAP = 1u << 21;
static constexpr unsigned RETRY_CAP = 1u << 15;

__device__ __forceinline__ float swap_add16(float v) {
  const unsigned u = __float_as_uint(v);
  auto r = __builtin_amdgcn_permlane16_swap(u, u, false, false);
  return __uint_as_float((unsigned)r[0]) + __uint_as_float((unsigned)r[1]);
}
__device__ __forceinline__ float swap_add32(float v) {
  const unsigned u = __float_as_uint(v);
  auto r = __builtin_amdgcn_permlane32_swap(u, u, false, false);
  return __uint_as_float((unsigned)r[0]) + __uint_as_float((unsigned)r[1]);
}
__device__ __forceinline__ float wave_sum2(float v) { return swap_add32(swap_add16(row16_sum(v))); }
__device__ __forceinline__ f32x2 up2(unsigned u) { return f32x2{lo2f(u), hi2f(u)}; }
__device__ __forceinline__ f32x2 rbf2(f32x2 v) { return up2(pack2(v[0], v[1])); }
#define FMA2 __builtin_elementwise_fma
__device__ __forceinline__ f32x2 rf2(f32x2 v, f32x2 cp, f32x2 cn) { return FMA2(v, cn, FMA2(v, cp, v)); }
__device__ __forceinline__ void ln5(const f32x2* in, unsigned* out) {
  const f32x2 s2 = ((in[0] + in[1]) + (in[2] + in[3])) + in[4];
  const float mean = wave_sum2(s2[0] + s2[1]) * (1.f / (float)D_);
  f32x2 d[5];
#pragma unroll
  for (int j = 0; j < 5; ++j) d[j] = in[j] - mean;
  const f32x2 q2 = (d[0] * d[0] + d[1] * d[1]) + (d[2] * d[2] + d[3] * d[3]) + d[4] * d[4];
  const float m = rbf(mean);
#pragma unroll
  for (int j = 0; j < 5; ++j) d[j] = rbf2(in[j] - m);
  const float q = wave_sum2(q2[0] + q2[1]);
  const float var = rbf(q * (1.f / (float)D_));
  const float den = rbf(__builtin_amdgcn_sqrtf(rbf(var + 1e-5f)));
  const float rinv = __builtin_amdgcn_rcpf(den);
#pragma unroll
  for (int j = 0; j < 5; ++j) { const f32x2 o = d[j] * rinv; out[j] = pack2(o[0], o[1]); }
}
#define PIN(x) asm volatile("" : "+v"(x))
#define STG(N, stmt) do { _Pragma("unroll") for (int i = 0; i < N; ++i) { stmt; } __builtin_amdgcn_sched_barrier(0); } while (0)
template <int N>
__device__ __forceinline__ void wave_sums(float* v) {
  STG(N, v[i] += __builtin_bit_cast(float, __builtin_amdgcn_update_dpp(0, __builtin_bit_cast(int, v[i]), 0xB1, 0xf, 0xf, true)); PIN(v[i]));
  STG(N, v[i] += __builtin_bit_cast(float, __builtin_amdgcn_update_dpp(0, __builtin_bit_cast(int, v[i]), 0x4E, 0xf, 0xf, true)); PIN(v[i]));
  STG(N, v[i] += __builtin_bit_cast(float, __builtin_amdgcn_update_dpp(0, __builtin_bit_cast(int, v[i]), 0x141, 0xf, 0xf, true)); PIN(v[i]));
  STG(N, v[i] += __builtin_bit_cast(float, __builtin_amdgcn_update_dpp(0, __builtin_bit_cast(int, v[i]), 0x140, 0xf, 0xf, true)); PIN(v[i]));
  unsigned a[N], b[N];
  STG(N, a[i] = __float_as_uint(v[i]); b[i] = a[i]; PIN(b[i]));
  STG(N, { auto r = __builtin_amdgcn_permlane16_swap(a[i], b[i], false, false); a[i] = r[0]; b[i] = r[1]; PIN(a[i]); PIN(b[i]); });
  STG(N, v[i] = __uint_as_float(a[i]) + __uint_as_float(b[i]); PIN(v[i]));
  STG(N, a[i] = __float_as_uint(v[i]); b[i] = a[i]; PIN(b[i]));
  STG(N, { auto r = __builtin_amdgcn_permlane32_swap(a[i], b[i], false, false); a[i] = r[0]; b[i] = r[1]; PIN(a[i]); PIN(b[i]); });
  STG(N, v[i] = __uint_as_float(a[i]) + __uint_as_float(b[i]); PIN(v[i]));
}
template <int N>
__device__ __forceinline__ void gelu_s(float* e, float ga_, float gb2, float gd, unsigned* pk) {
  float x[N];
  STG(N, x[i] = e[i] * e[i]; PIN(x[i]));
  STG(N, x[i] = __builtin_fmaf(x[i], gb2, ga_); PIN(x[i]));
  STG(N, x[i] *= e[i]; PIN(x[i]));
  STG(N, x[i] = __builtin_amdgcn_exp2f(x[i]); PIN(x[i]));
  STG(N, x[i] = __builtin_fmaf(x[i], gd, gd); PIN(x[i]));
  STG(N, x[i] = __builtin_amdgcn_rcpf(x[i]); PIN(x[i]));
  STG(N, e[i] *= x[i]; PIN(e[i]));
  STG(N / 4, e[4 * i + 1] = plainv(e[4 * i + 1]); PIN(e[4 * i + 1]));
  STG(N / 2, pk[i] = pack2(e[2 * i], e[2 * i + 1]); PIN(pk[i]));
}
__device__ __forceinline__ f32x4 mfma16(u32x4 a, u32x4 b, f32x4 c) {
  return __builtin_amdgcn_mfma_f32_16x16x32_bf16(__builtin_bit_cast(bf16x8, a), __builtin_bit_cast(bf16x8, b), c, 0, 0, 0);
}
template <bool FIRST> __device__ __forceinline__ void mf(f32x4& c, const u32x4& a, const u32x4& b) {
  if (FIRST) asm volatile("v_mfma_f32_16x16x32_bf16 %0, %1, %2, 0" : "=v"(c) : "v"(a), "v"(b));
  else asm volatile("v_mfma_f32_16x16x32_bf16 %0, %1, %2, %0" : "+v"(c) : "v"(a), "v"(b));
}
__device__ __forceinline__ unsigned canon(unsigned x) { return x < 0xfffffffeu ? x : 0xfffffffeu; }
__device__ __forceinline__ bool bad1(unsigned a) { return __builtin_amdgcn_ballot_w64(a == SENT) != 0ull; }
__device__ __forceinline__ float amax2(const u32x4& a) { return __builtin_amdgcn_fmed3f(__builtin_fabsf(__uint_as_float(a[0])), __builtin_fabsf(__uint_as_float(a[2])), CMAX); }
__device__ __forceinline__ float amin2(const u32x4& a) { return __builtin_amdgcn_fmed3f(__builtin_fabsf(__uint_as_float(a[0])), __builtin_fabsf(__uint_as_float(a[2])), 0.f); }
__device__ __forceinline__ float amax3(float m, const u32x4& a) { float r; asm("v_max3_f32 %0, %1, |%2|, |%3|" : "=v"(r) : "v"(m), "v"(a[0]), "v"(a[2])); return r; }
template <bool CD> __device__ __forceinline__ bool any_bad4(u32x4& a, float thr, unsigned csh, float cmx) {
  const float m = __builtin_amdgcn_fmed3f(__builtin_fabsf(__uint_as_float(a[0])), __builtin_fabsf(__uint_as_float(a[2])), CD ? 0.f : cmx);
  if (CD) { a[0] -= csh; a[2] -= csh; }
  return __builtin_amdgcn_ballot_w64(CD ? m < thr : m >= thr) != 0ull;
}
__device__ __forceinline__ u32x4 widen(unsigned x0, unsigned x1, unsigned y0, unsigned y1) {
  const auto p0 = __builtin_amdgcn_permlane16_swap(x0, y0, false, false);
  const auto p1 = __builtin_amdgcn_permlane16_swap(x1, y1, false, false);
  return u32x4{(unsigned)p0[0], (unsigned)p1[0], (unsigned)p0[1], (unsigned)p1[1]};
}
#define DMA1(M) asm volatile("s_mov_b32 m0, %3\n\ts_nop 0\n\t" "buffer_load_dwordx4 %0, %1, %2 offen " M "lds" :: "v"(voff), "s"(rs), "s"(soff), "s"(dst) : "memory")
#define DMA4(M) asm volatile("s_mov_b32 m0, %3\n\ts_nop 0\n\t" \
                   "buffer_load_dwordx4 %0, %1, %2 offen " M "lds\n\tbuffer_load_dwordx4 %0, %1, %2 offen offset:1024 " M "lds\n\t" \
                   "buffer_load_dwordx4 %0, %1, %2 offen offset:2048 " M "lds\n\tbuffer_load_dwordx4 %0, %1, %2 offen offset:3072 " M "lds" \
                   :: "v"(voff), "s"(rs), "s"(soff), "s"(dst) : "memory")
template <int N, int AUX>
__device__ __forceinline__ void dma(unsigned voff, u32x4 rs, unsigned soff, unsigned dst) {
  if (N == 4) { if (AUX == 1) DMA4("sc1 "); else if (AUX == 2) DMA4("nt "); else DMA4(""); }
  else { if (AUX == 1) DMA1("sc1 "); else if (AUX == 2) DMA1("nt "); else DMA1(""); }
}
__device__ __forceinline__ void st128(u32x4 v, __amdgpu_buffer_rsrc_t r, unsigned voff, unsigned base, int) {
  __builtin_amdgcn_raw_buffer_store_b128(v, r, voff + base, 0, 16);
}
__device__ __forceinline__ u32x4 mk_rs(const void* p, unsigned bytes) {
  const unsigned long long a = (unsigned long long)p;
  return u32x4{(unsigned)a, (unsigned)(a >> 32) & 0xffffu, bytes, (unsigned)RS3};
}

#define USLEEP 2
#define DLY0 8
#define DLY1 3
#define DLY2 0
#define DLY3 2
#define NWH 8
#define ZG 8
#define ZSLEEP 2
#define AW4 8
#define AD3 3
#define RSLEEP 1
#define PHI 2
#define CPW 4
#define CPG 128u
#define W12 0
#define ML 0
#define TW 0
#define LNL 0
#define F1L 0
#define UL 0
#define WI0 -1
#define WIS 8
#define WIE 0
#define HP 0
#define ZP 0
#define ZPN NGA
#define ZCG 0
#define WMD 0
#define WME 0
#define FZ 0
#define WDL 0
#define WSLEEP 2
#define LINW 0
#define NZ2 5
#define NS2 5
#define RS0 6
#define DLY3B 0
#define MV0 7
#define H0S 6
#define H1S 3
#define PK0 2
#define PE0 1
#define PK1 2
#define PE1 1
#define P40 1
#define P41 2
#define GSA 2
#define GSB 1
#define GSC 2
#define EA0 4
#define EA1 28
#define EC0 4
#define EC1 22
#define PB1 26
#define PB2 20
#define NL1 14
#define NP1 2
#define NL2 12
#define ROT 1
#define NA1 3
#define NP0 0
#define SL1 6
#define SL2 6
#define PH1 2
#define PH2 2
#define KG 42
#define KC3 12
#define PK2 2
#define KC3B 40
#define NPK 10
#define HG 4
#define WSP 3
#define PR1 3
#define HPZ 8
#define WS3 1
#define RB1 0
#define HFI 1
#define GZ 0
#define GZP 0
#define RPH 0
#define NGB 5
#define NS1 0
static_assert(!GZ || ZCE, "order of the progress words");
#define GSLEEP 4
#define TR 0
#define MGE 0
#define TZ 0
#define KE 0
#define TU1 200
#define TU2 260
#define TU 290
#define KL 5
#define PBE 18
#define NAF 6
#define W0U 4
#define U0 5
#define NPH 6
#define NGA 5
#define NR0 2
#define NR1 5
#define KVR 0
static_assert((NWH + NL1 + NP1) % 2 == 0 && NL2 % 2 == 0 && NWH + NL1 + NP1 + NL2 <= 40, "w3 schedule");
#define SPIN_STEP() do { ++spins; asm volatile("" : "+s"(spins)); } while (0)
#define SB() __builtin_amdgcn_sched_barrier(0)
#define FENCE() do { asm volatile("" ::: "memory"); __builtin_amdgcn_sched_barrier(0); } while (0)
#define LGKM0() do { __builtin_amdgcn_sched_barrier(0); asm volatile("s_waitcnt lgkmcnt(0)" ::: "memory"); } while (0)
#define BARRIER() do { LGKM0(); __builtin_amdgcn_s_barrier(); asm volatile("" ::: "memory"); } while (0)
#define VM0() __builtin_amdgcn_s_waitcnt(0x0F70)
#define PRIO(n) __builtin_amdgcn_s_setprio(n)
#define CHECK_A(CD, AV, i, hi, LD)                                                          \
  do {                                                                                      \
    if (__builtin_expect(any_bad4<CD>(AV, sthr, scsh, scmx), 0)) {                          \
      unsigned spins = 0;                                                                   \
      PRIO(0);                                                                              \
      do {                                                                                  \
        __builtin_amdgcn_s_sleep(RSLEEP);                                                   \
        _Pragma("unroll") for (int y_ = (i); y_ < (hi); ++y_) { LD(y_); }                   \
        SPIN_STEP();                                                                        \
      } while (any_bad4<CD>(AV, sthr, scsh, scmx) && spins < RETRY_CAP);                    \
      PRIO(PHI);                                                                            \
      VM0();                                                                                \
    }                                                                                       \
  } while (0)

#define ASM_MACROS \
  ".macro regno op\n.set r_, 0\n.set st_, 0\n.irpc c, \"\\op\"\n.ifc \\c, :\n.set st_, 2\n.endif\n.if st_ == 1\n.set r_, r_ * 10 + \\c\n.endif\n.ifc \\c, [\n.set st_, 1\n.endif\n.endr\n.endm\n" \
  ".macro retry n, cd, a, so, ct, t, vo, rs, sb, im, th, k, pr\n"                                   \
  "s_cbranch_vccnz Lc\\n\nLb\\n:\n.subsection 1\nLc\\n:\nregno \\a\n"              \
  "s_setprio 0\ns_add_u32 \\so, \\sb, \\im\ns_mov_b32 \\ct, 0\n"                                        \
  "Ll\\n:\ns_sleep 1\nbuffer_load_dwordx4 \\a, \\vo, \\rs, \\so offen sc1\ns_add_u32 \\ct, \\ct, 1\ns_waitcnt vmcnt(0)\n" \
  ".if \\cd\nv_med3_f32 \\t, |v[r_]|, |v[r_+2]|, 0\nv_cmp_gt_f32 vcc, \\th, \\t\n.else\nv_med3_f32 \\t, |v[r_]|, |v[r_+2]|, \\k\nv_cmp_le_f32 vcc, \\th, \\t\n.endif\n" \
  "s_cbranch_vccz Ld\\n\ns_cmp_lt_u32 \\ct, 0x8000\ns_cbranch_scc1 Ll\\n\n"                             \
  "Ld\\n:\n.if \\cd\nv_subrev_u32 v[r_], \\k, v[r_]\nv_subrev_u32 v[r_+2], \\k, v[r_+2]\n.endif\ns_setprio \\pr\ns_nop 1\ns_branch Lb\\n\n.subsection 0\n.endm\n" \
  ".macro need n, v, ct, ad, k\n"                                                                       \
  "v_cmp_gt_u32 vcc, \\k, \\v\ns_cbranch_vccnz Lc\\n\nLb\\n:\n.subsection 1\nLc\\n:\ns_mov_b32 \\ct, 0\n" \
  "Ll\\n:\nds_read_b32 \\v, \\ad\ns_waitcnt lgkmcnt(0)\nv_cmp_gt_u32 vcc, \\k, \\v\ns_cbranch_vccz Lb\\n\n" \
  "s_sleep 1\ns_add_u32 \\ct, \\ct, 1\ns_cmp_lt_u32 \\ct, 0x200000\ns_cbranch_scc1 Ll\\n\ns_branch Lb\\n\n.subsection 0\n.endm\n"
#define RETRY(CD, AV, BM, VOFF, SBASE, IMM, PR)                                                         \
  do {                                                                                                  \
    unsigned so_, ct_; float t_; register unsigned long long vc_ asm("vcc") = (BM);                                                                     \
    asm volatile("retry %=, %5, \"%0\", %1, %2, %3, %6, \"%7\", %8, %9, %10, %11, %12"                   \
                 : "+v"(AV), "=&s"(so_), "=&s"(ct_), "=&v"(t_), "+s"(vc_)                            \
                 : "i"((int)(CD)), "v"(VOFF), "s"(dW), "s"(SBASE), "i"(IMM), "s"(sthr), "s"((CD) ? scsh : __float_as_uint(scmx)), "i"(PR) : "scc", "memory"); \
  } while (0)
#define CMP_BAD(CD, BM, M) do { register unsigned long long vc_ asm("vcc");                             \
    if (CD) asm volatile("v_cmp_gt_f32 vcc, %1, %2" : "=s"(vc_) : "s"(sthr), "v"(M)); else asm volatile("v_cmp_le_f32 vcc, %1, %2" : "=s"(vc_) : "s"(sthr), "v"(M)); \
    BM = vc_; } while (0)
#define RETRY_C(AV, BM, VOFF, SBASE, IMM, PR) RETRY(1, AV, BM, VOFF, SBASE, IMM, PR)
#define NEED_AT(V, ADDR, K) do { unsigned ct_; asm volatile("need %=, %0, %1, %2, %3" : "+v"(V), "=&s"(ct_) : "v"(ADDR), "s"(K) : "vcc", "scc", "memory"); } while (0)

#define STAMP(i)
#define CLK(i)
#define KATTR
#define TS_(i)
#define TX_(i)
#define TSHI 62
__device__ __forceinline__ u32x4 rd_tr(const LAS char* p) {
  const s16x4 lo = __builtin_amdgcn_ds_read_tr16_b64_v4i16((LAS s16x4*)p);
  const s16x4 hi = __builtin_amdgcn_ds_read_tr16_b64_v4i16((LAS s16x4*)(p + 2560));
  const bf16x8 o = {lo[0], lo[1], lo[2], lo[3], hi[0], hi[1], hi[2], hi[3]};
  return __builtin_bit_cast(u32x4, o);
}
__device__ __forceinline__ float dpp_add(float v, int ctrl) {
  return v + __builtin_bit_cast(float, ctrl == 0 ? __builtin_amdgcn_update_dpp(0, __builtin_bit_cast(int, v), 0x128, 0xf, 0xf, true)
                                    : ctrl == 1 ? __builtin_amdgcn_update_dpp(0, __builtin_bit_cast(int, v), 0x124, 0xf, 0xf, true)
                                                : __builtin_amdgcn_update_dpp(0, __builtin_bit_cast(int, v), 0x4E, 0xf, 0xf, true));
}

template <int V> struct IC { static constexpr int v = V; };
template <int I, int N, class F> __device__ __forceinline__ void sfor(F&& f) { if constexpr (I < N) { f(IC<I>{}); sfor<I + 1, N>(f); } }
constexpr int cv_i(int m) { int i = 0; while (m >= 8 - i) { m -= 8 - i; ++i; } return i; }
constexpr int cv_d(int m) { int i = 0; while (m >= 8 - i) { m -= 8 - i; ++i; } return 7 - i - m; }
constexpr int kl_i(int m) { return m < 50 - 5 * KL ? m / 5 : m < 50 - 2 * KL ? 10 - KL + (m - 50 + 5 * KL) / 3 : 10 - KL + (m - 50 + 2 * KL) / 2; }
constexpr int kl_j(int m) { return m < 50 - 5 * KL ? m % 5 : m < 50 - 2 * KL ? (m - 50 + 5 * KL) % 3 : 3 + (m - 50 + 2 * KL) % 2; }
constexpr int cl_t(int m) { int t = 0; while (m > t) { m -= t + 1; ++t; } return t; }
constexpr int cl_i(int m) { int t = 0; while (m > t) { m -= t + 1; ++t; } return m; }
constexpr bool cv_free(int m, int k) {
  return k == 0 ? (m >= 64 && m < 111 && m != 65 && m != 69 && m != 72 && m != 75 && m != 77 && m != 94 && m != 95 && m != 104 && m != 110 && (m < 72 + RS0 || m >= 88 + RS0))
                : (m >= 123 && m != 126 && m != 131 && m != 132 && m != 139 && m != 140);
}
constexpr int cv_slot(int m, int k) { if (!cv_free(m, k)) return -1; int n = 0; for (int y = 0; y < m; ++y) n += cv_free(y, k); return n; }
static_assert(cv_slot(109, 0) >= 15 && cv_slot(143, 1) >= 15, "slots");
constexpr int cc_i(int J, int c) { for (int i = 0; i < 8; ++i) for (int t = 3; t >= 0; --t) if (i <= ((J >> (4 * t)) & 15) && c-- == 0) return i; return 0; }
constexpr int cc_t(int J, int c) { for (int i = 0; i < 8; ++i) for (int t = 3; t >= 0; --t) if (i <= ((J >> (4 * t)) & 15) && c-- == 0) return t; return 0; }
#define KWAIT
__global__ __launch_bounds__(512, 2) KATTR void k_main(const bf16* kin_, const bf16* vin_, char* ws_, const bf16* shift_k_, const bf16* ssm_k_,
                                                 const bf16* w_mod_, const bf16* cin_, const bf16* w3_,
                                                 const bf16* w4_, const bf16* q_, const bf16* w1_, const bf16* w2_,
                                                 const bf16* ssm_D_, bf16* out_) {
  typedef __attribute__((ext_vector_type(8))) unsigned long long u64x8;
  typedef __attribute__((ext_vector_type(4))) unsigned long long u64x4;
  typedef __attribute__((ext_vector_type(2))) unsigned long long u64x2;
  u64x8 ka;
  u64x4 kb;
  u64x2 kc;
  int tid_ = threadIdx.x, bx_ = blockIdx.x;
  unsigned long long pc0;
  unsigned code_bytes;
  asm volatile("s_load_dwordx16 %0, %6, 0x0\n\ts_load_dwordx8 %1, %6, 0x40\n\ts_load_dwordx4 %2, %6, 0x60\n\ts_getpc_b64 %3\n"
               "Lpc0:\n\ts_mov_b32 %4, Lcold0 - Lpc0\n" KWAIT ".subsection 1\nLcold0:\n.subsection 0"
               : "=&s"(ka), "=&s"(kb), "=&s"(kc), "=&s"(pc0), "=&s"(code_bytes), "+s"(bx_) : "s"(__builtin_amdgcn_kernarg_segment_ptr()));
  asm volatile(ASM_MACROS ".subsection 2\n.fill 256, 4, 0xbf800000\n.subsection 0" : "+v"(tid_));
  extern __shared__ __attribute__((aligned(16))) char lds_[];
  LAS char* lds = (LAS char*)lds_;
  ldsw_t LW = (ldsw_t)(lds + L_CTL);
  const int tid = tid_ & 511, lane = tid & 63;
  const int wv = __builtin_amdgcn_readfirstlane(tid >> 6) & 7;
  const int bx = bx_ & 255;
  const int mt = (bx & 7) * 4 + (bx >> 6), nt = (bx >> 3) & 7;
  const int n0 = nt * 80, m0 = mt * 64;
  const int l15 = lane & 15, g = lane >> 4;
  const int mg = wv & 3, kh = wv >> 2;
  const int gm = (wv >> 2) * 2 + (wv & 1), gk = ((wv >> 1) ^ (wv >> 2)) & 1, gp = wv ^ 2;
  const unsigned l16 = (unsigned)lane * 16u;
  TS_(0);
  STAMP(0);
  CLK(30);
  unsigned cp0 = 0;
  if (wv == CPW) {
    asm volatile("buffer_load_dword %0, %1, %2, 0 offen" : "=v"(cp0) : "v"((unsigned)((bx >> 3) + 32 * lane) * CPG), "s"(mk_rs((const void*)pc0, code_bytes)) : "memory");
  }
  FENCE();
  const int tok0 = 32 * ((bx & 7) * 8 + (bx >> 5)), ch0 = 160 * ((bx >> 3) & 3);
  unsigned ko[3], vo[3];
#pragma unroll
  for (int i = 0; i < 3; ++i) {
    const unsigned s = (unsigned)((mg + 4 * i) * 64 + lane);
    const unsigned gr = __umul24(s, 800u) >> 16, w = s - __umul24(82u, gr), rr = __umul24(w, 205u) >> 12;
    unsigned o = (unsigned)(tok0 * (D_ * 2) + ch0 * 2) + __umul24(gr, 4u * D_ * 2) + __umul24(rr, D_ * 2 - 320) + w * 16u;
    asm volatile("" : "+v"(o));
    ko[i] = (w < 80u && gr < 9u) ? o - 4u * D_ * 2 : OOB;
    vo[i] = (w < 80u && gr < 8u) ? o : OOB;
    asm volatile("" : "+v"(ko[i]), "+v"(vo[i]));
  }
  FENCE();
  asm volatile("s_waitcnt lgkmcnt(0)" : "+s"(ka), "+s"(kb), "+s"(kc));
  const bf16* kin = (const bf16*)ka[0];
  const bf16* vin = (const bf16*)ka[1];
  char* ws = (char*)ka[2];
  const bf16* shift_k = (const bf16*)ka[3];
  const bf16* ssm_k = (const bf16*)ka[4];
  const bf16* w_mod = (const bf16*)ka[5];
  const bf16* cin = (const bf16*)ka[6];
  const bf16* w3 = (const bf16*)ka[7];
  const bf16* w4 = (const bf16*)kb[0];
  const bf16* q = (const bf16*)kb[1];
  const bf16* w1 = (const bf16*)kb[2];
  const bf16* w2 = (const bf16*)kb[3];
  const bf16* ssm_D = (const bf16*)kc[0];
  bf16* out = (bf16*)kc[1];
  const __amdgpu_buffer_rsrc_t rW = __builtin_amdgcn_make_buffer_rsrc((void*)ws, 0, WS_USED, RS3);
  const u32x4 dW = mk_rs(ws, WS_USED);
  u32x2 ep;
  asm volatile("buffer_load_dwordx2 %0, off, %1, %2 sc1" : "=v"(ep) : "s"(dW), "s"(O_EP) : "memory");
  FENCE();
  if (kh == 0) {
    const u32x4 dK = mk_rs(kin, SD2), dV = mk_rs(vin, SD2);
#pragma unroll
    for (int i = 0; i < 3; ++i) dma<1, 0>(ko[i], dK, 0u, (unsigned)(L_KV + (mg + 4 * i) * 1024));
#pragma unroll
    for (int i = 0; i < 3; ++i) dma<1, 0>(vo[i], dV, 0u, (unsigned)(L_KV + 12288 + (mg + 4 * i) * 1024));
  }
  u32x4 sk[3];
  if (kh == 0) {
    asm volatile("buffer_load_dwordx4 %0, %3, %5, %6 offen\n\tbuffer_load_dwordx4 %1, %3, %5, %6 offen offset:16\n\tbuffer_load_dwordx4 %2, %4, %5, %7 offen"
                 : "=&v"(sk[0]), "=&v"(sk[1]), "=&v"(sk[2])
                 : "v"(((unsigned)lane >> 3) * 32u), "v"((unsigned)g * 16u), "s"(mk_rs(shift_k, D_ * 8)), "s"(ch0 * 8 + mg * 256), "s"(ch0 * 8 + 1024 + mg * 64) : "memory");
  }
  FENCE();
  if (tid >= 504) LW[tid - 504] = 0u;
  if (tid >= 256 && tid < 352) *(LAS u32x4*)(lds + SL_A + ((tid - 256) >> 5) * CV_SLOT + CV_U + (tid & 31) * 16) = u32x4{0u, 0u, 0u, 0u};
  if (tid >= 384 && tid < 408) *(LAS u32x4*)(lds + SL_A + ((tid - 384) >> 3) * CV_SLOT + (tid & 7) * CV_STR + 257 * 16) = u32x4{0u, 0u, 0u, 0u};
  STAMP(29);
  if constexpr (TSHI == 23) TX_(30);
  BARRIER();

  const unsigned psig = (unsigned)L_CTL + (unsigned)wv * 4u;
#define SIG(k) do { unsigned long long sv_; asm volatile("s_mov_b64 %0, exec\n\ts_mov_b64 exec, 1\n\tds_write_b32 %1, %2\n\ts_mov_b64 exec, %0" : "=&s"(sv_) : "v"(psig), "v"((unsigned)(k)) : "memory"); } while (0)
#define WAIT_AT(addr, k)                                                                                \
  do {                                                                                                  \
    unsigned tv_, tc_;                                                                                  \
    asm volatile("s_mov_b32 %1, 0\n"                                                                    \
                 "L%=_a:\n\tds_read_b32 %0, %2\n\ts_waitcnt lgkmcnt(0)\n\tv_cmp_gt_u32 vcc, %3, %0\n\ts_cbranch_vccz L%=_b\n\t"    \
                 "s_sleep 1\n\ts_add_u32 %1, %1, 1\n\ts_cmp_lt_u32 %1, 0x200000\n\ts_cbranch_scc1 L%=_a\n"            \
                 "L%=_b:" : "=&v"(tv_), "=&s"(tc_) : "v"(addr), "i"(k) : "vcc", "scc", "memory");       \
  } while (0)
#define WAIT_2(k0, k1)                                                                                 \
  do {                                                                                                  \
    unsigned tv_, tc_;                                                                                  \
    asm volatile("s_mov_b32 %1, 0\n"                                                                    \
                 "L%=_a:\n\tds_read_b32 %0, %2\n\ts_waitcnt lgkmcnt(0)\n\tv_cmp_gt_u32 vcc, %3, %0\n\ts_cbranch_vccz L%=_b\n\t"    \
                 "s_sleep 1\n\ts_add_u32 %1, %1, 1\n\ts_cmp_lt_u32 %1, 0x200000\n\ts_cbranch_scc1 L%=_a\n"            \
                 "L%=_b:" : "=&v"(tv_), "=&s"(tc_) : "v"((unsigned)L_CTL + ((unsigned)lane & 7u) * 4u), "v"((lane & 4) ? (unsigned)(k1) : (unsigned)(k0)) : "vcc", "scc", "memory"); \
  } while (0)
#define WAIT_ALL(k) WAIT_AT((unsigned)L_CTL + ((unsigned)lane & 7u) * 4u, k)
#define WAIT_HALF(h, k) WAIT_AT((unsigned)L_CTL + ((unsigned)lane & 3u) * 4u + (unsigned)(h) * 16u, k)

  const int sb3 = nt < 6 ? 14 * nt : 84 + 12 * (nt - 6);
  const bool two = kh == 0 || nt < 6;
  const int jl = kh ? 4 + wv : 2 * wv, jm = kh ? 12 + (wv & 1) : 2 * wv + 1, hb = wv > 5 ? 1 : 0;
  const int k4 = 7 * wv - (wv > 5 ? 2 : wv > 4 ? 1 : 0);
  const unsigned w4a = O_WT4 + (unsigned)(nt * 54 + k4) * 5120u;
  unsigned w3s[4] = {O_WT3 + (unsigned)(sb3 + jl) * 20480u + (unsigned)hb * 10240u, two ? O_WT3 + (unsigned)(sb3 + jm) * 20480u + (unsigned)hb * 10240u : OOB,
                     O_WT3 + (unsigned)(sb3 + jl) * 20480u - (unsigned)hb * 10240u, kh ? w4a - 10240u : O_WT3 + (unsigned)(sb3 + jm) * 20480u};
  f32x2 cp = {65536.f, 65536.f}, cn = {-65536.f, -65536.f};
  asm volatile("" : "+s"(cp), "+s"(cn));
  u32x4 W3[40];
#define LD_W3U(f) W3[f] = __builtin_amdgcn_raw_buffer_load_b128(rW, l16 + ((f) < 8 ? ((f) >> 1) * 1024 : 0), w3s[((f) & 1) + ((f) >= 20 ? 2 : 0)] + ((f) < 8 ? 0 : ((f) >> 1) * 1024), 16)
#define LD_W3R(a, b) do { _Pragma("unroll") for (int f_ = (a); f_ < (b); ++f_) LD_W3U(f_); } while (0)
#define VMW(n) asm volatile("s_waitcnt vmcnt(%0)" :: "i"(n) : "memory")
  const __amdgpu_buffer_rsrc_t rW1 = __builtin_amdgcn_make_buffer_rsrc((void*)w1, 0, D_ * D_ * 2, RS3);
  const __amdgpu_buffer_rsrc_t rW2 = __builtin_amdgcn_make_buffer_rsrc((void*)w2, 0, D_ * D_ * 2, RS3);
  const u32x4 dW1 = mk_rs(w1, D_ * D_ * 2), dW2 = mk_rs(w2, D_ * D_ * 2);
#define W12_G(gw) unsigned gw[5]; { unsigned ln_ = (unsigned)lane; asm volatile("" : "+v"(ln_)); \
    _Pragma("unroll") for (int j_ = 0; j_ < 5; ++j_) { const unsigned p_ = ln_ + 64u * j_, r_ = __umul24(p_, 6554u) >> 16; \
      gw[j_] = __umul24((r_ & 12u) * 2u + (r_ >> 4) * 4u + (r_ & 3u), D_ * 2) + (p_ - __umul24(r_, 10u)) * 16u + (unsigned)n0 * 2u; } }

  const int s0 = m0 + nt * 8;
  const int srow = s0 + wv;
  unsigned qw[5], hw[5];
  unsigned long long epn;
  unsigned tA, tB;
  u32x4 WS2[10];
#define LD_WS2U(a, b) _Pragma("unroll") for (int y_ = (a); y_ < (b); ++y_) asm volatile("buffer_load_dwordx4 %0, %1, %2, %3 offen ; ws2" : "=v"(WS2[y_]) : "v"(gw[y_ % 5]), "s"(dW2), "s"((wv + 8 * (y_ / 5)) * (32 * D_ * 2)) : "memory")
#define LD_WS2(a, b) _Pragma("unroll") for (int y_ = (a); y_ < (b); ++y_) WS2[y_] = __builtin_amdgcn_raw_buffer_load_b128(rW2, gw[y_ % 5], (wv + 8 * (y_ / 5)) * (32 * D_ * 2), 0)
  unsigned ry = (unsigned)(L_XCH + wv * YROW) + l16, rz = (unsigned)(L_XCH + wv * YROW + 1024 + 4 * lane);
  f32x2 xq[5], ha[5], ca[5], sb[5];
  float mc, mb;
  const int row = m0 + 16 * gm + l15;
  const int jb = 3 * gk;
  const unsigned ofr = (unsigned)(row >> 4) * 20480u + (unsigned)((n0 + 16 * jb + 4 * g) >> 3) * 256u + (unsigned)(g & 1) * 8u + (unsigned)l15 * 16u;
  {
    const int pi = (bx & 7) + 8 * (bx >> 4), half = (bx >> 3) & 1;
    const int chA = 2 * bx, chC = 512 + pi;
#define RD_MOD(v) do { const u32x4 b_ = *(const LAS u32x4*)(lds + L_MOD + (v) * (D_ * 2) + l16); mw[v][0] = b_[0]; mw[v][1] = b_[1]; mw[v][2] = b_[2]; mw[v][3] = b_[3]; \
                       mw[v][4] = *(const LAS unsigned*)(lds + L_MOD + (v) * (D_ * 2) + 1024 + lane * 4); } while (0)
#define LN1()                                                                                           \
  do {                                                                                                  \
    f32x2 xq_[5];                                                                                       \
    _Pragma("unroll") for (int j = 0; j < 5; ++j) xq_[j] = up2(qw[j]);                                  \
    ln5(xq_, hw);                                                                                       \
    _Pragma("unroll") for (int j = 0; j < 5; ++j) asm volatile("" : "+v"(hw[j]));                       \
  } while (0)
#define R_PRE() do {                                                                                    \
      unsigned mw[5][5];                                                                                \
      _Pragma("unroll") for (int v = 0; v < 5; ++v) RD_MOD(v);                                          \
      _Pragma("unroll") for (int j = 0; j < 5; ++j) {                                                   \
        xq[j] = up2(qw[j]);                                                                             \
        ha[j] = FMA2(up2(hw[j]), up2(mw[1][j]), up2(mw[0][j])) * up2(mw[2][j]); ca[j] = up2(mw[4][j]); sb[j] = up2(mw[3][j]); \
        asm volatile("" : "+v"(xq[j]), "+v"(ha[j]), "+v"(ca[j]), "+v"(sb[j]));                          \
      }                                                                                                 \
      const f32x2 c2_ = ((ca[0] + ca[1]) + (ca[2] + ca[3])) + ca[4], b2_ = ((sb[0] + sb[1]) + (sb[2] + sb[3])) + sb[4]; \
      mc = wave_sum2(c2_[0] + c2_[1]) * (1.f / (float)D_); mb = wave_sum2(b2_[0] + b2_[1]) * (1.f / (float)D_); \
      asm volatile("" : "+v"(mc), "+v"(mb));                                                            \
    } while (0)
    unsigned dv2;
#define GET_D() const unsigned dAB = __builtin_amdgcn_readlane(dv2, 0), dC_ = __builtin_amdgcn_readlane(dv2, 1), dC = (chC & 1) ? dC_ & 0xffff0000u : dC_ << 16
#define REQ_Q() do {                                                                                    \
      const __amdgpu_buffer_rsrc_t rQ = __builtin_amdgcn_make_buffer_rsrc((void*)q, 0, SD2, RS3);       \
      const u32x4 a = __builtin_amdgcn_raw_buffer_load_b128(rQ, l16, srow * (D_ * 2), 0);               \
      qw[0] = a[0]; qw[1] = a[1]; qw[2] = a[2]; qw[3] = a[3];                                           \
      qw[4] = __builtin_amdgcn_raw_buffer_load_b32(rQ, (unsigned)lane * 4u, srow * (D_ * 2) + 1024, 0); \
    } while (0)
#define REQ_D() dv2 = __builtin_amdgcn_raw_buffer_load_b32(__builtin_amdgcn_make_buffer_rsrc((void*)ssm_D, 0, D_ * 2, RS3), (unsigned)(lane ? (chC & ~1) : chA) * 2u, 0, 0)
#define TAGS()                                                                                          \
    {                                                                                                   \
      unsigned e0_, e1_;                                                                                \
      asm volatile("v_readfirstlane_b32 %0, %2\n\tv_readfirstlane_b32 %1, %3" : "=s"(e0_), "=s"(e1_) : "v"(ep[0]), "v"(ep[1])); \
      unsigned long long h = (unsigned long long)e1_ << 32 | e0_;                                       \
      epn = h + 1ull;                                                                                   \
      h += 0x9E3779B97F4A7C15ull;                                                                       \
      h = (h ^ (h >> 30)) * 0xBF58476D1CE4E5B9ull;                                                      \
      h = (h ^ (h >> 27)) * 0x94D049BB133111EBull;                                                      \
      h ^= h >> 31;                                                                                     \
      tA = (unsigned)h; tB = (unsigned)(h >> 32);                                                       \
    }
#define BUILD(SL, CC)                                                                                   \
  do {                                                                                                  \
    const u32x4 klo = *(const LAS u32x4*)(lds + (SL) + CV_U + 512 + 4064 - 16 * (CC)), khi = *(const LAS u32x4*)(lds + (SL) + CV_U + 512 + 4080 - 16 * (CC)); \
    const unsigned w[8] = {klo[0], klo[1], klo[2], klo[3], khi[0], khi[1], khi[2], khi[3]};             \
    _Pragma("unroll") for (int m = 0; m < 8; ++m) {                                                     \
      u32x4 o;                                                                                          \
      const int a = m >> 1;                                                                             \
      _Pragma("unroll") for (int t = 0; t < 4; ++t) {                                                   \
        if ((m & 1) == 0) o[t] = (w[a - t + 4] & 0xffffu) | (w[a - t + 3] & 0xffff0000u);               \
        else o[t] = (w[a - t + 4] >> 16) | (w[a - t + 4] << 16);                                        \
      }                                                                                                 \
      *(LAS u32x4*)(lds + (SL) + m * CV_STR + ((CC) + 1) * 16) = o;                                     \
    }                                                                                                   \
  } while (0)
#define ZCH(c) ((c) * 128u + ((c) / 160u) * (TS - 20480u))
#define ZOFF(JA, JB, CH) ((unsigned)((32 * ((g & 1) ? (JB) : (JA)) + 2 * l15 + (g >> 1)) >> 3) * (4u * TS) + ZCH((unsigned)(CH)) + (unsigned)((2 * l15 + (g >> 1)) & 7) * 16u)
#define Z2OFF(w) (O_Z + (unsigned)(4 * (w) + (l15 >> 2)) * (4u * TS) + ZCH((unsigned)chA) + (unsigned)((l15 & 3) * 64 + g * 16))
    const unsigned a00 = (unsigned)((l15 & 7) * CV_STR + (2032 - 8 * (l15 >> 3) + 8 * g) * 2) - 128u * mg;
    const unsigned b00 = (unsigned)(CV_U + 480 + 32 * l15 + 16 * g) - 128u * mg;
#define CV_RA(AV, SL, rd) _Pragma("unroll") for (int i = 0; i < 8; ++i) AV[8 * (rd) + i] = *(const LAS u32x4*)(lds + (SL) + a00 - 64 * (rd) - 512 * i)
#define CV_RB(BV, SL, rd) _Pragma("unroll") for (int i = 7; i >= 0; --i) BV[8 * (rd) + i] = *(const LAS u32x4*)(lds + (SL) + b00 + 512 * i - 64 * (rd))
    const int zja = mg ? (half ? 5 : 4) : (half ? 1 : 0), zjb = mg ? (half ? 6 : 7) : (half ? 2 : 3);
    const int t8 = tid & 255;
#define W_DMA(blk, j) dma<1, 0>(gw[j], (blk) < 20 ? dW1 : dW2, (unsigned)((blk) < 20 ? (blk) : (blk) - 4) * (32 * D_ * 2), (unsigned)((blk) * 5120 + (j) * 1024))
    if (__builtin_expect(kh != 0, 1)) {
      {
        const u32x4 dS = mk_rs(ssm_k, SD2);
        dma<1, 0>(l16, dS, (unsigned)(chC * (S_ * 2) + mg * 1024), (unsigned)(SL_C + CV_U + 512 + mg * 1024));
        dma<1, 0>(l16, dS, (unsigned)(chA * (S_ * 2) + mg * 1024), (unsigned)(SL_A + CV_U + 512 + mg * 1024));
        dma<1, 0>(l16, dS, (unsigned)((chA + 1) * (S_ * 2) + mg * 1024), (unsigned)(SL_B + CV_U + 512 + mg * 1024));
      }
#define PF_W() do { const unsigned o_ = lane < 50 ? (unsigned)(bx * 4 + mg) * 800u + l16 : OOB;        \
        dma<1, 0>(o_, dW1, 0u, (unsigned)(L_RT + mg * 5120)); dma<1, 0>(o_, dW2, 0u, (unsigned)(L_RT + mg * 5120 + 1024)); FENCE(); } while (0)
      if (PF == 2) PF_W();
      unsigned cpf = __builtin_amdgcn_raw_buffer_load_b32(rW, OOB, 0, 0);
      FENCE();
#define FILL_Z() do {                                                                                   \
      st128(u32x4{CODED, CODED, CODED, CODED}, rW, Z2OFF(mg), 0, 16);                                   \
      st128(u32x4{CODED, CODED, CODED, CODED}, rW, Z2OFF(wv), 0, 16);                                   \
      if (mg < 2) st128(u32x4{CODED, CODED, CODED, CODED}, rW, ZOFF(zja, zjb, chC), O_Z, 16);            \
    } while (0)
      unsigned fpa[3], fpv;
#pragma unroll
      for (int f_ = 0; f_ < 3; ++f_) {
        const unsigned o = (unsigned)(f_ * 4096 + t8 * 16);
        fpa[f_] = o < 10240u ? O_H1 + (unsigned)(bx >> 1) * TS + (bx & 1) * 10240u + o : o < 11264u ? O_ST4 + (unsigned)bx * 1024u + (o - 10240u)
                : (o < 11392u && (bx & 1)) ? O_ST3 + (unsigned)(bx >> 1) * 128u + (o - 11264u) : OOB;
        if (f_ == 2) fpv = o < 10240u ? CODED : SENT;
      }
#define FILL_P(a, b) _Pragma("unroll") for (int f_ = (a); f_ < (b); ++f_) { const unsigned fv = f_ < 2 ? CODED : fpv; st128(u32x4{fv, fv, fv, fv}, rW, fpa[f_], 0, 16); }
      u32x4 WS[10];
#define LD_WS(a, b) _Pragma("unroll") for (int x = (a); x < (b); ++x)                                   \
      WS[x] = __builtin_amdgcn_raw_buffer_load_b128(rW1, gw[x % 5], (9 + mg + 4 * (x / 5)) * (32 * D_ * 2), 0)
#define LD_WD(a, b) _Pragma("unroll") for (int x = (a); x < (b); ++x) {                                 \
      if (x < 10) W_DMA(mg + 4 * (x / 5), x % 5);                                                       \
      else if (x == 10) dma<1, 0>(mg == 0 ? gw[0] : mg == 1 ? gw[1] : mg == 2 ? gw[2] : gw[3], dW1, 8u * (32 * D_ * 2), (unsigned)(8 * 5120 + mg * 1024)); \
      else if (mg == 0) W_DMA(8, 4);                                                                    \
    }
      FILL_Z();
      FENCE();
      u32x4 wm[5];
      unsigned short cv;
      const __amdgpu_buffer_rsrc_t rM = __builtin_amdgcn_make_buffer_rsrc((void*)w_mod, 0, D_ * 6 * D_ * 2, RS3);
      unsigned wmo = bx < 240 ? (unsigned)(t8 >> 3) * (6 * D_ * 2) + (unsigned)(t8 & 7) * 16u : OOB;
#define LD_WM(a, b) do { _Pragma("unroll") for (int i = (a); i < (b); ++i) wm[i] = __builtin_amdgcn_raw_buffer_load_b128(rM, wmo, ((bx & 3) * 160 + i * 32) * (6 * D_ * 2) + (bx >> 2) * 128, 0); FENCE(); } while (0)
      {
        const __amdgpu_buffer_rsrc_t rC = __builtin_amdgcn_make_buffer_rsrc((void*)cin, 0, D_ * 2, RS3);
        cv = __builtin_amdgcn_raw_buffer_load_b16(rC, (unsigned)((t8 >> 3) + 32 * min(lane & 7, 4)) * 2u, (bx & 3) * 320, 0);
        LD_WM(0, WMD ? WME : 5);
      }
      FENCE();
      if (WMD) PIN(wmo);
      if (QE == 3) { REQ_Q(); REQ_D(); FENCE(); }
      W12_G(gw);
#pragma unroll
      for (int j = 0; j < 5; ++j) PIN(gw[j]);
      const int job = bx * 4 + mg;
      const bool j3 = job < 540, jok = job < 972;
      const int jks = j3 ? job % 20 : (job - 540) % 54, jcg = j3 ? job / 20 : (job - 540) / 54;
      const unsigned rtl = (unsigned)(L_RT + mg * 5120);
      u32x4 rt[5];
      unsigned rta[5];
      {
        const unsigned v3 = ((unsigned)lane >> 3) * (F_ * 2) + (((unsigned)lane & 7u) * 16u ^ (RSW ? ((unsigned)lane & 24u) * 4u : 0u)) + (unsigned)jks * (32 * F_ * 2) + (unsigned)jcg * 128u;
        const unsigned s4 = (unsigned)jks * (32 * D_ * 2) + (unsigned)(jcg - nt) * 160u;
#pragma unroll
        for (int i = 0; i < 5; ++i) { rta[i] = j3 ? (i < 4 ? (v3 ^ (RSW ? (i & 1) * 32u : 0u)) + i * (8 * F_ * 2) : OOB) : gw[i] + s4;
          PIN(rta[i]); }
      }
#define RT_LD() do {                                                                                    \
      const u32x4 dS = mk_rs(j3 ? w3 : w4, jok ? DF2 : 0);                                              \
      _Pragma("unroll") for (int i = 0; i < 4; ++i) dma<1, 0>(rta[i], dS, 0u, rtl + i * 1024);          \
      if (!j3) dma<1, 0>(rta[4], dS, 0u, rtl + 4096);                                                   \
      FENCE();                                                                                          \
    } while (0)
      FENCE();
      STAMP(12);
      asm volatile("" : "+v"(cpf));
      asm volatile("" :: "v"(cp0));
      SIG(P_K);
      if constexpr (TSHI == 23) TX_(31);
      FENCE();
      TAGS();
      auto mod_p = [&]() __attribute__((always_inline)) {
        float a[8];
#pragma unroll
        for (int e = 0; e < 8; ++e) a[e] = 0.f;
        const float c_ = lo2f(cv);
        const int sci = __float_as_int(rbf(c_ * __builtin_amdgcn_rcpf(1.f + __builtin_amdgcn_exp2f(c_ * -1.4426950408889634f))));
#pragma unroll
        for (int i = 0; i < 5; ++i) {
          const float sc = __int_as_float(__builtin_amdgcn_ds_bpermute(((lane & 56) + i) * 4, sci));
#pragma unroll
          for (int e = 0; e < 4; ++e) { a[2 * e] += sc * lo2f(wm[i][e]); a[2 * e + 1] += sc * hi2f(wm[i][e]); }
        }
#pragma unroll
        for (int e = 0; e < 4; ++e) {
          const auto r = __builtin_amdgcn_permlane32_swap(__float_as_uint(a[e]), __float_as_uint(a[e + 4]), false, false);
          a[e] = __uint_as_float((unsigned)r[0]) + __uint_as_float((unsigned)r[1]);
        }
#pragma unroll
        for (int e = 0; e < 2; ++e) {
          const auto r = __builtin_amdgcn_permlane16_swap(__float_as_uint(a[e]), __float_as_uint(a[e + 2]), false, false);
          a[e] = dpp_add(__uint_as_float((unsigned)r[0]) + __uint_as_float((unsigned)r[1]), 0);
        }
        if ((lane & 8) == 0) *(LAS f32x2*)(lds + L_MP + mg * 256 + (lane & 7) * 32 + g * 8) = f32x2{a[0], a[1]};
        SIG(WMD ? P_M2 : P_M);
        TS_(52);
        if constexpr (TSHI == 23) TX_(32);
        FENCE();
        if (QE == 2) { REQ_Q(); REQ_D(); FENCE(); }
        if (PF == 1) PF_W();
        if (wv == 7) {
          WAIT_HALF(1, WMD ? P_M2 : P_M);
          f32x2 s = *(const LAS f32x2*)(lds + L_MP + (lane & 31) * 8);
#pragma unroll
          for (int w = 1; w < 4; ++w) s += *(const LAS f32x2*)(lds + L_MP + w * 256 + (lane & 31) * 8);
          st128(u32x4{__float_as_uint(s[0]), tA, __float_as_uint(s[1]), tB}, rW, lane < 32 ? l16 : OOB, O_MP + (unsigned)bx * 512u, 16);
          __builtin_amdgcn_raw_buffer_store_b64(u32x2{tA, tB}, rW, lane == 0 ? (unsigned)bx * 8u : OOB, O_FL1, 16);
        }
        FENCE();
      };
      if (!WMD) mod_p();
      if (WMD == 1) WAIT_HALF(0, P_KV);
      if (WMD == 1 || WMD == 2) LD_WM(WME, 5);
      STAMP(2);
      {
      WAIT_HALF(1, P_K);
      BUILD(SL_A, t8);
      if (QE == 4) { FENCE(); REQ_Q(); REQ_D(); FENCE(); }
      if (WMD == 4) { FENCE(); LD_WM(WME, 5); }
      BUILD(SL_B, t8);
      SIG(P_B2);
      if constexpr (TSHI == 23) TX_(33);
      FENCE();
      STAMP(1);
      }
      if (WMD == 3) LD_WM(WME, 5);
      if (QE == 1) { REQ_Q(); REQ_D(); FENCE(); }
      if (PF == 3) PF_W();
      if (WMD) mod_p();
      if (!QE) { WAIT_HALF(1, P_B2); REQ_Q(); REQ_D(); }
      FENCE();
      FENCE();
      unsigned tra[10], rto[5];
      {
        const unsigned b3 = rtl + (unsigned)(8 * g + (l15 >> 2)) * 128u + (unsigned)((l15 >> 2) ^ (g & 1)) * 32u + ((unsigned)lane & 3u) * 8u, b4 = rtl + ((unsigned)lane >> 2) * 160u + ((unsigned)lane & 3u) * 8u;
        const unsigned dst = j3 ? O_WT3 + (unsigned)(jcg * 80 + jks) * 1024u : O_WT4 + (unsigned)(jcg * 54 + jks) * 5120u, dstr = j3 ? 20480u : 1024u;
#pragma unroll
        for (int jn = 0; jn < 5; ++jn) {
          tra[jn] = j3 ? b3 ^ (unsigned)(jn & 3) * 32u : b4 + jn * 32u;
          tra[5 + jn] = tra[jn] + (j3 ? 512u : 2560u);
          rto[jn] = (jok && !(j3 && jn == 4)) ? l16 + dst + jn * dstr : OOB;
          PIN(tra[jn]); PIN(tra[5 + jn]); PIN(rto[jn]);
        }
      }
#define RT_TR() do {                                                                                    \
        _Pragma("unroll") for (int jn = 0; jn < 5; ++jn) {                                              \
          const s16x4 lo = __builtin_amdgcn_ds_read_tr16_b64_v4i16((LAS s16x4*)tra[jn]);        \
          const s16x4 hi = __builtin_amdgcn_ds_read_tr16_b64_v4i16((LAS s16x4*)tra[5 + jn]);    \
          rt[jn] = __builtin_bit_cast(u32x4, bf16x8{lo[0], lo[1], lo[2], lo[3], hi[0], hi[1], hi[2], hi[3]}); \
          SB();                                                                                         \
        }                                                                                               \
      } while (0)
#define RT_OUT() do {                                                                                   \
        _Pragma("unroll") for (int jn = 0; jn < 4; ++jn) st128(rt[jn], rW, rto[jn], 0, 16);             \
        if (!j3) st128(rt[4], rW, rto[4], 0, 16);                                                       \
        FENCE();                                                                                        \
      } while (0)
      SIG(P_U);
      const bool mfin = wv == 7 && bx < 240 && lane < 32;
      const unsigned po = mfin ? ((unsigned)lane >> 3) * 512u + ((unsigned)lane & 7u) * 16u : OOB;
      u32x4 mp4;
#define LD_P() mp4 = __builtin_amdgcn_raw_buffer_load_b128(rW, po, O_MP + (unsigned)(bx & ~3) * 512u + (unsigned)(bx & 3) * 128u, 16)
#define MOD_BAD() (__builtin_amdgcn_ballot_w64(mfin && (mp4[1] != tA || mp4[3] != tB)) != 0ull)
#define MOD_FIN() do {                                                                                  \
        const float one = ((bx >= 40 && bx < 80) || (bx >= 160 && bx < 200)) ? 1.f : 0.f;               \
        const float s0_ = swap_add16(dpp_add(__uint_as_float(mp4[0]), 0)), s1_ = swap_add16(dpp_add(__uint_as_float(mp4[2]), 0)); \
        const unsigned d = pack2(one + rbf(s0_), one + rbf(s1_));                                       \
        const unsigned dn = (unsigned)__builtin_amdgcn_update_dpp(0, (int)d, 0xB1, 0xf, 0xf, true);     \
        st128(u32x4{d, tA, dn, tB}, rW, (lane < 8 && (lane & 1) == 0) ? (unsigned)lane * 8u : OOB, O_MOD + (unsigned)bx * 64u, 16); \
      } while (0)
#define MOD_L2(c) do {                                                                                  \
      if (c) {                                                                                          \
        unsigned spins = 0;                                                                             \
        while (MOD_BAD()) {                                                                             \
          SPIN_STEP();                                                                                  \
          if (spins > RETRY_CAP) break;                                                                 \
          __builtin_amdgcn_s_sleep(USLEEP);                                                             \
          LD_P();                                                                                       \
        }                                                                                               \
        MOD_FIN();                                                                                      \
      }                                                                                                 \
      FENCE();                                                                                          \
    } while (0)
      if (!ML && wv == 7) LD_P();
      FENCE();
      STAMP(3);
      FENCE();
      asm volatile("" : "+v"(fpa[0]), "+v"(fpa[1]), "+v"(fpa[2]), "+v"(fpv));
#define RT_GO() RT_LD()
      LN1();
      GET_D();
      if constexpr (TSHI == 23) TX_(34);
      if (!ML) MOD_L2(wv == 7);
      if constexpr (TSHI == 23) TX_(35);
      TS_(27);
      if constexpr (TSHI == 23) TX_(36);
      STAMP(22);
      if (HP & 4) PRIO(3);
      WAIT_ALL(P_U);
      if (ML && wv == 7) LD_P();
      FENCE();
      RT_LD();
      TS_(4);
      STAMP(28);
      FILL_P(0, 3);
      FENCE();
      if constexpr (TSHI == 23) { TX_(49); VMW(3); TX_(50); }
      if (HP & 3) PRIO(3);
      if (F1L || RD) VMW(3);
      RT_TR();
      SIG(P_RT);
      TS_(48);
      if constexpr (TSHI == 23) TX_(48);
      RT_OUT();
      if (HP & 1) PRIO(0);
      if constexpr (TSHI == 23) TX_(51);
      bool mlate = false;
      if (ML && wv == 7) { mlate = MOD_BAD(); if (!mlate) MOD_FIN(); }
      FENCE();
      LD_WD(0, 5);
      if constexpr (TSHI == 23) TX_(52);
      WAIT_HALF(1, P_RT);
      LD_WD(5, NGA);
      FENCE();
      if constexpr (TSHI == 23) TX_(53);
      VMW(NGA + F1L + (GZ && GZP == 3 ? 5 : 0));
      SIG(P_GA);
      if (HP & 2) PRIO(0);
      if constexpr (TSHI != 23) TS_(5);
      if (ML) MOD_L2(__builtin_expect(mlate, 0));
      if constexpr (TSHI == 23) TX_(47);
      FENCE();
      LD_WD(NGA, ZPN);
      if (ZP) {
        const unsigned fl = *(volatile LAS unsigned*)(lds + L_CTL + (lane & 3) * 4);
        if (__builtin_amdgcn_ballot_w64(fl >= (unsigned)(ZP == 1 ? P_X : P_A2)) != 0ull) WAIT_HALF(0, P_X2);
        FENCE();
      }
      LD_WD(ZPN, 12);
      FENCE();
      if (mg < 2) {
        u32x2 uc[2];
        f32x4 yc[8];
        float v[8];
        uc[0] = *(const LAS u32x2*)(lds + SL_C + CV_U + 512 + (256 * zja + 16 * l15 + 4 * g) * 2);
        uc[1] = *(const LAS u32x2*)(lds + SL_C + CV_U + 512 + (256 * zjb + 16 * l15 + 4 * g) * 2);
        unsigned zq = O_Z + ZOFF(zja, zjb, chC);
        PIN(zq);
        WAIT_HALF(0, P_C2);
#pragma unroll
        for (int c = 0; c < 8; ++c) yc[c] = *(const LAS f32x4*)(lds + SL_C + mg * 2048 + l16 + (c >> 1) * 4096 + (c & 1) * 1024);
        STG(8, v[i] = (i & 1) ? hi2f(uc[i >> 2][(i >> 1) & 1]) : lo2f(uc[i >> 2][(i >> 1) & 1]); PIN(v[i]));
        STG(8, v[i] = __builtin_fmaf(v[i], __uint_as_float(dC), yc[i >> 2][i & 3]); PIN(v[i]));
#pragma unroll
        for (int w = 1; w < 4; ++w) STG(8, v[i] += yc[2 * w + (i >> 2)][i & 3]; PIN(v[i]));
        st128(widen(pack2(v[0], plainv(v[1])), pack2(v[2], v[3]), pack2(v[4], plainv(v[5])), pack2(v[6], v[7])), rW, zq, 0, 16);
      }
      FENCE();
      SIG(P_ZC);
      FENCE();
      LD_WS(0, GZ ? NS1 : NS2);
      FENCE();
      if (HPZ) { WAIT_HALF(0, P_X2); __builtin_amdgcn_s_sleep(HPZ); }
      STAMP(15);
      LD_WS(NS2, 10);
      if (mg > 0) {
        WAIT_HALF(1, P_ZC);
#pragma unroll
        for (int j = 0; j < 5; ++j) W_DMA(16 + mg, j);
      }
      LD_WS2U(0, NZ2);
      FENCE();
      STAMP(5);
      TS_(6);
      WAIT_HALF(0, P_X2);
#pragma unroll
      for (int x = 0; x < 10; ++x) *(LAS u32x4*)(lds + (9 + mg + 4 * (x / 5)) * 5120 + (x % 5) * 1024 + l16) = WS[x];
      TS_(1);
      WAIT_HALF(0, P_T);
      R_PRE();
      VMW(NZ2);
      SIG(P_X);
      TS_(2);
      TS_(3);
      WAIT_HALF(0, P_Z);
      LD_WS2U(NZ2, 10);
      FENCE();
      WAIT_2(P_RP, P_X);
#pragma unroll
      for (int j = 0; j < 5; ++j) W_DMA(20 + mg, j);
      FENCE();
      TS_(7);
      STAMP(7);
    } else {
      PRIO(3);
      const unsigned rt0 = (unsigned)__builtin_amdgcn_s_memrealtime();
      unsigned kpa = (unsigned)L_KV + ((unsigned)lane & 7u) * 1312u + (unsigned)mg * 64u + ((unsigned)lane >> 3) * 8u;
      unsigned kpb = (unsigned)L_KV + ((unsigned)l15 >> 1) * 1312u + 256u + (unsigned)mg * 16u + (unsigned)g * 4u + ((unsigned)lane & 1u) * 640u, kpc = kpb + ((unsigned)lane & 1u) * 32u;
      const unsigned cha_ = (unsigned)ch0 + 32u * mg + ((unsigned)lane >> 3) * 4u, chb_ = (unsigned)ch0 + 128u + 8u * mg + 2u * g;
      unsigned koa = KVROW(cha_) + (unsigned)tok0 * 4u + ((unsigned)lane & 7u) * 16u, koa2 = KVROW(cha_ + 2u) + (unsigned)tok0 * 4u + ((unsigned)lane & 7u) * 16u;
      unsigned kob = KVROW(chb_) + (unsigned)tok0 * 4u + (unsigned)l15 * 8u;
      asm volatile("" : "+v"(kpa), "+v"(kpb), "+v"(kpc), "+v"(koa), "+v"(koa2), "+v"(kob));
      u32x4 ug[6];
      unsigned uo = (unsigned)mg * 2048u + l16;
      unsigned udone;
      asm volatile("s_mov_b32 %0, 0" : "=s"(udone));
      unsigned ubA = KVROW((unsigned)chA), ubC = KVROW((unsigned)chC), ubB = KVROW((unsigned)(chA + 1));
      asm volatile("" : "+s"(ubA), "+s"(ubB), "+s"(ubC));
      PIN(uo);
#pragma unroll
      for (int r = 0; r < 6; ++r) asm volatile("" : "=v"(ug[r]));
#define TRY_U(EL, T, CODE)                                                                               \
        asm volatile("s_cmp_lg_u32 %6, 0\n\ts_cbranch_scc1 Lu%=\n\ts_cmp_lt_u32 %[el], %[t]\n\ts_cbranch_scc1 Lu%=\n\t" \
                     "buffer_load_dwordx4 %2, %[vo], %[rs], %[c] offen sc1\n\tbuffer_load_dwordx4 %3, %[vo], %[rs], %[c] offen offset:1024 sc1\n\t" \
                     "buffer_load_dwordx4 %0, %[vo], %[rs], %[a] offen sc1\n\tbuffer_load_dwordx4 %1, %[vo], %[rs], %[a] offen offset:1024 sc1\n\t" \
                     "buffer_load_dwordx4 %4, %[vo], %[rs], %[b] offen sc1\n\tbuffer_load_dwordx4 %5, %[vo], %[rs], %[b] offen offset:1024 sc1\n\t" \
                     "s_mov_b32 %6, %[cd]\n"                                                            \
                     "Lu%=:" : "+v"(ug[0]), "+v"(ug[1]), "+v"(ug[2]), "+v"(ug[3]), "+v"(ug[4]), "+v"(ug[5]), "+s"(udone) \
                     : [el] "s"(EL), [t] "i"(T), [cd] "i"(CODE), [vo] "v"(uo), [rs] "s"(dW), [a] "s"(ubA), [b] "s"(ubB), [c] "s"(ubC) : "scc", "memory")
      VMW(9);
      TAGS();
      unsigned tAB = (lane & 1) ? tB : tA;
      PIN(tAB);
      asm volatile("s_waitcnt vmcnt(0)" : "+v"(sk[0]), "+v"(sk[1]), "+v"(sk[2]));
      const unsigned rt1 = (unsigned)__builtin_amdgcn_s_memrealtime();
      SIG(P_KV);
      FENCE();
      f32x2 wa[3][4];
#pragma unroll
      for (int c = 0; c < 3; ++c)
#pragma unroll
        for (int t = 0; t < 4; ++t) {
          const f32x2 w = (t & 1) ? f32x2{hi2f(sk[c][t >> 1]), hi2f(sk[c][2 + (t >> 1)])} : f32x2{lo2f(sk[c][t >> 1]), lo2f(sk[c][2 + (t >> 1)])};
          wa[c][t] = w;
          PIN(wa[c][t]);
        }
      asm volatile("" : "+v"(tAB));
      FENCE();
      TS_(1);
      STAMP(1);
      WAIT_HALF(0, P_KV);
      if (TU1) TRY_U(rt1 - rt0, TU1, 1);
      if constexpr (TSHI == 23) TX_(31);
      STAMP(12);
      {
        u32x2 kw[7], vw[4];
        unsigned kx[5], vx[2];
#pragma unroll
        for (int i = 0; i < 7; ++i) kw[i] = *(const LAS u32x2*)(lds + kpa + (i < 3 ? 320 + 320 * i : 1312 + 320 * (i - 3)));
#pragma unroll
        for (int i = 0; i < 5; ++i) kx[i] = *(const LAS unsigned*)(lds + ((i == 1 || i == 2) ? kpc : kpb) + (i < 3 ? 320 + 320 * i : 1312 + 320 * (i - 3)));
#pragma unroll
        for (int i = 0; i < 4; ++i) vw[i] = *(const LAS u32x2*)(lds + kpa + 12288 + 320 * i);
#pragma unroll
        for (int i = 0; i < 2; ++i) vx[i] = *(const LAS unsigned*)(lds + kpb + 12288 + 320 * i);
        __builtin_amdgcn_sched_barrier(0);
        f32x2 kf[19], a[10], p[10], t_[10];
#define KX(o, t) kf[((o) >> 2) * 7 + ((o) & 3) + (t)]
#define STR(lo, hi, stmt) do { _Pragma("unroll") for (int i = (lo); i < (hi); ++i) { stmt; } __builtin_amdgcn_sched_barrier(0); } while (0)
#define KV_GROUP(lo, hi) do {                                                                           \
        STR(((lo) >> 2) * 7, (hi) == 10 ? 19 : ((hi) >> 2) * 7, kf[i] = up2(i < 14 ? kw[i % 7][i / 7] : kx[i < 14 ? 0 : i - 14]); PIN(kf[i])); \
        {                                                                                               \
          STR(lo, hi, a[i] = KX(i, 0) * wa[i >> 2][3]; PIN(a[i]));                                      \
          _Pragma("unroll") for (int t = 1; t < 4; ++t) STR(lo, hi, a[i] = FMA2(KX(i, t), wa[i >> 2][3 - t], a[i]); PIN(a[i])); \
          STR(lo, hi, t_[i] = FMA2(a[i], cp, a[i]); PIN(t_[i]));                                        \
          STR(lo, hi, a[i] = FMA2(a[i], cn, t_[i]); PIN(a[i]));                                         \
        }                                                                                               \
        STR(lo, hi, p[i] = up2(i < 8 ? vw[i & 3][i >> 2] : vx[i & 1]); PIN(p[i]));                      \
        STR(lo, hi, a[i] *= p[i]; PIN(a[i]));                                                           \
        if constexpr (TSHI == 23) TX_(32 + (lo) / 2);                                                   \
        _Pragma("unroll") for (int c = (lo) >> 1; c < ((hi) < 8 ? (hi) : 8) >> 1; ++c) {                \
          const int o = (c >> 1) * 4, e = c & 1;                                                        \
          st128(u32x4{pack2(a[o][e], plainv(a[o + 1][e])), tA, pack2(a[o + 2][e], plainv(a[o + 3][e])), tB}, rW, c < 2 ? koa : koa2, e * 8192u, 16); \
        }                                                                                               \
        if ((hi) == 10) { _Pragma("unroll") for (int e = 0; e < 2; ++e) __builtin_amdgcn_raw_buffer_store_b64(u32x2{pack2(a[8][e], plainv(a[9][e])), tAB}, rW, kob, e * 8192, 16); } \
        __builtin_amdgcn_sched_barrier(0);                                                              \
      } while (0)
        KV_GROUP(0, 4);
        if constexpr (TSHI == 23) TX_(33);
        STAMP(5);
        KV_GROUP(4, 10);
        if constexpr (TSHI == 23) TX_(35);
      }
      FENCE();
      const unsigned rt2 = (unsigned)__builtin_amdgcn_s_memrealtime();
      REQ_Q();
      REQ_D();
      FENCE();
      if (TU2) TRY_U(rt2 - rt0, TU2, 2);
      STAMP(2);
      TS_(2);
      WAIT_HALF(1, P_K);
      BUILD(SL_C, t8);
      SIG(P_B);
      if constexpr (TSHI == 23) TX_(36);
      FENCE();
#define LD_U(r) ug[r] = __builtin_amdgcn_raw_buffer_load_b128(rW, uo + ((r) & 1) * 1024, (r) < 2 ? ubA : (r) < 4 ? ubC : ubB, 16)
      u32x4 A2[16], B2[16];
      WAIT_HALF(0, P_B);
      CV_RA(A2, SL_C, 0);
      CV_RA(A2, SL_C, 1);
      W12_G(gw);
#pragma unroll
      for (int j = 0; j < 5; ++j) PIN(gw[j]);
      unsigned zq = Z2OFF(wv);
      unsigned pa = a00 + SL_A - 3648, pb = b00 + SL_A - 64;
      unsigned xw = SL_A + mg * 8192 + l16, xc = SL_C + mg * 4096 + l16;
      asm volatile("" : "+v"(zq), "+v"(pa), "+v"(pb), "+v"(xw), "+v"(xc));
      FENCE();
      if (udone == 0u) while ((unsigned)__builtin_amdgcn_s_memrealtime() - rt0 < (unsigned)TU) __builtin_amdgcn_s_sleep(1);
      STAMP(3);
      TS_(3);
      TRY_U(rt0, 0, 3);
      FENCE();
      if constexpr (TSHI == 23) TX_(37);
      unsigned uw = (unsigned)(SL_A + CV_U + 512 + mg * 1024 + lane * 8);
      PIN(uw);
      WAIT_HALF(1, P_B2);
      asm volatile("s_cmp_eq_u32 %[d], 1\n\ts_cbranch_scc1 Lw%=\n\ts_waitcnt vmcnt(0)\nLw%=:\n\ts_waitcnt vmcnt(9)"
                   : "+v"(ug[0]), "+v"(ug[1]), "+v"(ug[2]), "+v"(ug[3]), "+v"(ug[4]), "+v"(ug[5]) : [d] "s"(udone) : "scc", "memory");
      if constexpr (TSHI == 23) { TX_(38); PIN(ug[5]); __builtin_amdgcn_s_sleep(1); TX_(39); }
      {
#define U_BAD(r) (ug[r][1] != tA || ug[r][3] != tB)
        if (__builtin_expect(__builtin_amdgcn_ballot_w64(U_BAD(0) || U_BAD(1) || U_BAD(2) || U_BAD(3) || U_BAD(4) || U_BAD(5)) != 0ull, 1)) {
          unsigned spins = 0;
          do {
            __builtin_amdgcn_s_sleep(USLEEP);
#pragma unroll
            for (int r = 0; r < 6; ++r) if (U_BAD(r)) LD_U(r);
            SPIN_STEP();
          } while (__builtin_amdgcn_ballot_w64(U_BAD(0) || U_BAD(1) || U_BAD(2) || U_BAD(3) || U_BAD(4) || U_BAD(5)) != 0ull && spins < RETRY_CAP);
          VM0();
        }
#pragma unroll
        for (int r = 2; r < 8; ++r) *(LAS u32x2*)(lds + uw + ((r % 6) >> 1) * CV_SLOT + (r & 1) * 512) = u32x2{ug[r % 6][0], ug[r % 6][2]};
      }
      SIG(P_U);
      TS_(56);
      if constexpr (TSHI == 23) TX_(40);
      STAMP(22);
      GET_D();
#if !(LINW & 12)
      if (WI0 < 0) { LD_WS2(0, 10); } else { LD_WS2(0, WIE); }
#endif
#define WI_AT(x) do { if constexpr (WI0 >= 0 && (x) >= WI0 && ((x) - WI0) % WIS == 0 && ((x) - WI0) / WIS + WIE < 10) { constexpr int y0_ = ((x) - WI0) / WIS + WIE; LD_WS2(y0_, y0_ + 1); } } while (0)
      FENCE();
      WAIT_HALF(0, P_U);
      CV_RB(B2, SL_C, 0);
      FENCE();
      STAMP(28);
      TS_(4);
      f32x4 acc[8], ac[4];
      u32x4 A[16], B[16];
#define RA(P, rd, i_) *(const LAS u32x4*)(lds + (P) + 3648 - 64 * (rd) - 512 * (i_))
#define RB(P, rd, i_) *(const LAS u32x4*)(lds + (P) + 64 - 64 * (rd) + 512 * (i_))
#define CONV_C(J, lo, hi)                                                                               \
      sfor<lo, hi>([&](auto C_) __attribute__((always_inline)) {                                         \
        constexpr int c = decltype(C_)::v, rd = c / 18, i = cc_i(J, c % 18), t = cc_t(J, c % 18);       \
        ac[t] = mfma16(A2[8 * rd + i], B2[8 * rd + (((J) >> (4 * t)) & 15) - i], (rd == 0 && i == 0) ? f32x4{0.f, 0.f, 0.f, 0.f} : ac[t]); \
        SB();                                                                                           \
        WI_AT(c);                                                                                       \
        if constexpr (c < 8) B2[15 - c] = RB(pb + CV_SLOT, 1, 7 - c);                                   \
        if constexpr (c >= 18 && c < 34) { constexpr int x = (c - 18) >> 1; if constexpr ((c & 1) == 0) B[7 - x] = RB(pb, 0, 7 - x); else A[x] = RA(pa, 0, x); } \
        SB();                                                                                           \
      });
      if (half == 0) { CONV_C(0x7430, 0, 18) } else { CONV_C(0x6521, 0, 18) }
      SB();
      int half2 = half;
      asm volatile("" : "+s"(half2));
      if (half2 == 0) { CONV_C(0x7430, 18, 36) } else { CONV_C(0x6521, 18, 36) }
      u32x4 A3[16], B3[16];
      f32x4 ab_[8];
      unsigned pk_;
      float ud[16];
      u32x2 u2[4];
      f32x4 ya[8];
      unsigned ga_;
      unsigned pu = (unsigned)(SL_A + CV_U + 512 + (256 * wv + 16 * l15 + 4 * g) * 2), py = (unsigned)(SL_A + wv * 1024) + l16;
      const unsigned f0 = (unsigned)L_CTL + ((unsigned)lane & 3u) * 4u;
      sfor<0, 144>([&](auto M_) __attribute__((always_inline)) {
        constexpr int m = decltype(M_)::v, ch = m / 72, rd = (m / 36) & 1, r = m % 36, i = m < 108 ? cv_i(r) : cl_i(r), dd = m < 108 ? cv_d(r) : cl_t(r) - cl_i(r);
        if constexpr (ch == 0) acc[i + dd] = mfma16(A[8 * rd + i], B[8 * rd + dd], (rd == 0 && i == 0) ? f32x4{0.f, 0.f, 0.f, 0.f} : acc[i + dd]);
        else ab_[i + dd] = mfma16(A3[8 * rd + i], B3[8 * rd + dd], (rd == 0 && i == 0) ? f32x4{0.f, 0.f, 0.f, 0.f} : ab_[i + dd]);
        SB();
        WI_AT(36 + m);
        if constexpr (m == 30) { pa += SL_B - SL_A; pb += SL_B - SL_A; asm volatile("" : "+v"(pa), "+v"(pb)); }
        if constexpr (m == 94) { xw += SL_B - SL_A; PIN(xw); }
        if constexpr (r >= RS0 && r < RS0 + 16 && m < 108) {
          constexpr int x = (r - RS0) >> 1;
          if constexpr (m < 36) { if constexpr ((r & 1) == 0) B[15 - x] = RB(pb, 1, 7 - x); else A[8 + x] = RA(pa, 1, x); }
          else if constexpr (m < 72) { if constexpr ((r & 1) == 0) B3[7 - x] = RB(pb, 0, 7 - x); else A3[x] = RA(pa, 0, x); }
          else { if constexpr ((r & 1) == 0) B3[15 - x] = RB(pb, 1, 7 - x); else A3[8 + x] = RA(pa, 1, x); }
        }
        if constexpr (m == 1 || m == 4 || m == 23 || m == 26) { constexpr int c = m == 1 ? 0 : m == 4 ? 1 : m == 23 ? 2 : 3; *(LAS f32x4*)(lds + xc + c * 1024) = ac[c]; }
        if constexpr (m == 28) SIG(P_C2);
        if constexpr (m == 38 || m == 104) pk_ = *(volatile LAS unsigned*)(lds + f0);
        if constexpr (m == 45) NEED_AT(pk_, f0, P_C2);
        if constexpr (m == 111) NEED_AT(pk_, f0, P_A2);
        constexpr int wa = m == 47 ? 0 : m == 54 ? 1 : m == 60 ? 2 : m == 65 ? 3 : m == 69 ? 4 : m == 72 ? 5 : m == 75 ? 6 : m == 77 ? 7 : -1;
        constexpr int wb = m == 113 ? 0 : m == 116 ? 1 : m == 119 ? 2 : m == 122 ? 3 : m == 126 ? 4 : m == 132 ? 5 : m == 139 ? 6 : -1;
        if constexpr (wa >= 0) *(LAS f32x4*)(lds + xw + wa * 1024) = acc[wa];
        if constexpr (wb >= 0) *(LAS f32x4*)(lds + xw + wb * 1024) = ab_[wb];
        if constexpr (m == 95) SIG(P_A2);
        if constexpr (m == 58) PIN(pu);
        if constexpr (m == 59 || m == 61) { constexpr int c = m - 59; u2[c] = *(const LAS u32x2*)(lds + pu + c * 1024); u2[c + 1] = *(const LAS u32x2*)(lds + pu + (SL_B - SL_A) + c * 1024); }
        constexpr int us = cv_slot(m, 0);
        if constexpr (us >= 0 && us < 16) {
#pragma unroll
          for (int o = 2 * us; o < 2 * us + 2; ++o) {
            const int v = (o >> 3) * 4 + (o & 3);
            if ((o & 4) == 0) ud[v] = (v & 1) ? hi2f(u2[v >> 2][(v >> 1) & 1]) : lo2f(u2[v >> 2][(v >> 1) & 1]);
            else ud[v] *= ((v >> 2) & 1) ? hi2f(dAB) : lo2f(dAB);
            PIN(ud[v]);
          }
        }
        if constexpr (m == 110) PIN(py);
        if constexpr (m == 112 || m == 114 || m == 115 || m == 117) { constexpr int c = m == 112 ? 0 : m == 114 ? 2 : m == 115 ? 4 : 6;
          ya[c] = *(const volatile LAS f32x4*)(lds + py + (c >> 2) * 4096 + (c & 3) * 8192); ya[c + 1] = *(const volatile LAS f32x4*)(lds + py + (c >> 2) * 4096 + (c & 3) * 8192 + 8192); }
        constexpr int zs = cv_slot(m, 1);
        if constexpr (zs >= 0 && zs < 16) {
#pragma unroll
          for (int o = 2 * zs; o < 2 * zs + 2; ++o) { const int tl = (o >> 2) & 1, e = o & 3; ud[tl * 8 + e] += ya[4 * tl + (o >> 3)][e]; PIN(ud[tl * 8 + e]); }
        }
        if constexpr (TSHI == 10 && (m % 18 == 0 || m == 143)) TX_(44 + (m + 1) / 18);
        if constexpr (m == 131) ga_ = *(volatile LAS unsigned*)(lds + f0 + 16);
        if constexpr (m == 140) { py += SL_B - SL_A; PIN(py); }
        SB();
      });
      *(LAS f32x4*)(lds + xw + 7 * 1024) = ab_[7];
      SIG(P_X);
      TS_(5);
      STAMP(4);
      u32x4 mg4[4];
#define LD_MG(i) mg4[i] = __builtin_amdgcn_raw_buffer_load_b128(rW, (unsigned)t8 * 16u, O_MOD + (i) * 4096, 16)
      u32x2 f1v;
#define LD_F1() f1v = __builtin_amdgcn_raw_buffer_load_b64(rW, (unsigned)t8 * 8u, O_FL1, 16)
      NEED_AT(ga_, f0 + 16, P_GA);
      {
        unsigned spins = 0;
        for (;;) {
          const unsigned fl = *(volatile LAS unsigned*)(lds + f0);
#pragma unroll
          for (int c = 0; c < 8; ++c) ya[c] = *(const volatile LAS f32x4*)(lds + py + (c >> 2) * 4096 + (c & 3) * 8192);
          if (__builtin_amdgcn_ballot_w64(fl < (unsigned)P_X) == 0ull) break;
          SPIN_STEP();
          if (spins > SPIN_CAP) break;
        }
#pragma unroll
        for (int tl = 0; tl < 2; ++tl) {
          const f32x4 yb = (ya[4 * tl] + ya[4 * tl + 1]) + (ya[4 * tl + 2] + ya[4 * tl + 3]);
          const f32x2 v2 = f32x2{yb[0], yb[1]} + f32x2{ud[8 * tl + 4], ud[8 * tl + 5]}, v3 = f32x2{yb[2], yb[3]} + f32x2{ud[8 * tl + 6], ud[8 * tl + 7]};
          st128(u32x4{pack2(ud[8 * tl], plainv(v2[0])), pack2(ud[8 * tl + 1], v2[1]), pack2(ud[8 * tl + 2], plainv(v3[0])), pack2(ud[8 * tl + 3], v3[1])}, rW, zq + tl * 64u * TS, 0, 16);
        }
      }
      FENCE();
      SIG(P_X2);
      TS_(6);
      LD_F1(); LD_MG(0); LD_MG(1); LD_MG(2); LD_MG(3);
      FENCE();
      LN1();
      STAMP(6);
      u32x4 za, zb, zc = {0u, 0u, 0u, 0u}, zd = {0u, 0u, 0u, 0u};
      const unsigned zo = (unsigned)mt * (4u * TS) + ZCH((unsigned)t8 & ~1u) + (unsigned)nt * 32u + ((unsigned)t8 & 1u) * 16u;
      const unsigned zo1 = (unsigned)mt * (4u * TS) + ZCH(256u + ((unsigned)t8 & ~1u)) + (unsigned)nt * 32u + ((unsigned)t8 & 1u) * 16u;
      const unsigned zo2 = (unsigned)mt * (4u * TS) + ZCH(2u * t8 + 128u) + (unsigned)nt * 16u;
      FENCE();
      {
#define MG_BAD(i) (mg4[i][1] != tA || mg4[i][3] != tB)
#define T_BAD() (__builtin_amdgcn_ballot_w64(f1v[0] != tA || f1v[1] != tB || MG_BAD(0) || MG_BAD(1) || MG_BAD(2) || MG_BAD(3)) != 0ull)
        if (__builtin_expect(T_BAD(), 0)) {
          unsigned spins = 0;
          do {
            __builtin_amdgcn_s_sleep(USLEEP);
            if (f1v[0] != tA || f1v[1] != tB) LD_F1();
#pragma unroll
            for (int i = 0; i < 4; ++i) if (MG_BAD(i)) LD_MG(i);
            SPIN_STEP();
          } while (T_BAD() && spins < RETRY_CAP);
          VM0();
        }
#pragma unroll
        for (int i = 0; i < 4; ++i) {
          const int n = (t8 + 256 * i) * 4;
          if (n < 6 * D_) *(LAS u32x2*)(lds + L_MOD + n * 2) = u32x2{mg4[i][0], mg4[i][2]};
          if (i == 3 && n >= 5 * D_ + n0 && n < 5 * D_ + n0 + 80)
            *(LAS f32x4*)(lds + L_GB + (n - 5 * D_ - n0) * 4) = f32x4{lo2f(mg4[i][0]), hi2f(mg4[i][0]), lo2f(mg4[i][2]), hi2f(mg4[i][2])};
        }
        SIG(P_T);
        if constexpr (TSHI == 23) TX_(41);
      }
      WAIT_HALF(0, P_T);
      if (DLY0) __builtin_amdgcn_s_sleep(DLY0);
      PRIO(3);
      STAMP(7);
      TS_(7);
      za = __builtin_amdgcn_raw_buffer_load_b128(rW, zo, O_Z, 16); zb = __builtin_amdgcn_raw_buffer_load_b128(rW, zo1, O_Z, 16);
      if (wv == 3) { zc = __builtin_amdgcn_raw_buffer_load_b128(rW, zo2, O_Z, 16); zd = __builtin_amdgcn_raw_buffer_load_b128(rW, zo2 + 16u * ZG, O_Z, 16); }
      FENCE();
      SIG(P_Z);
      R_PRE();
      SIG(P_RP);
      unsigned zw0 = (unsigned)(L_XCH + 4 * (t8 & 1) * YROW + 4 * (t8 >> 1)), zw1 = (unsigned)(L_XCH + 256 + 4 * t8);
      asm volatile("" : "+v"(zw0), "+v"(zw1), "+v"(ry), "+v"(rz));
      asm volatile("" : "+s"(w3s[0]), "+s"(w3s[1]));
      unsigned spins = 0;
      for (;;) {
        const bool ba = amax2(za) >= PTHR, bb_ = amax2(zb) >= PTHR, bc_ = amax2(zc) >= PTHR, bd = amax2(zd) >= PTHR;
        if (__builtin_amdgcn_ballot_w64(ba || bb_ || bc_ || bd) == 0ull) break;
        SPIN_STEP();
        if (spins > RETRY_CAP) break;
        __builtin_amdgcn_s_sleep(ZSLEEP);
        if (ba) za = __builtin_amdgcn_raw_buffer_load_b128(rW, zo, O_Z, 16);
        if (bb_) zb = __builtin_amdgcn_raw_buffer_load_b128(rW, zo1, O_Z, 16);
        if (bc_) zc = __builtin_amdgcn_raw_buffer_load_b128(rW, zo2, O_Z, 16);
        if (bd) zd = __builtin_amdgcn_raw_buffer_load_b128(rW, zo2 + 16u * ZG, O_Z, 16);
      }
#pragma unroll
      for (int i = 0; i < 4; ++i) {
        *(LAS unsigned*)(lds + zw0 + i * YROW) = za[i];
        *(LAS unsigned*)(lds + zw0 + i * YROW + 512) = zb[i];
      }
      if (wv == 3) {
#pragma unroll
        for (int i = 0; i < 4; ++i) {
          *(LAS unsigned*)(lds + zw1 + (2 * i) * YROW) = __builtin_amdgcn_perm(zd[i], zc[i], 0x05040100u);
          *(LAS unsigned*)(lds + zw1 + (2 * i + 1) * YROW) = __builtin_amdgcn_perm(zd[i], zc[i], 0x07060302u);
        }
      }
      VM0();
      SIG(P_Z2);
    }
  }
  {
    f32x2 hh[5], t_[5];
    STAMP(8);
    asm volatile("" : "+v"(ry), "+v"(rz));
    asm volatile("" : "+s"(w3s[0]), "+s"(w3s[1]));
    if (kh) PRIO(PR1); else PRIO(PHI);
    WAIT_HALF(0, P_Z2);
    STAMP(9);
    TS_(8);
    unsigned zw[5], xp[5], hp_[5];
    {
      const u32x4 a = *(LAS u32x4*)(lds + ry);
      zw[0] = a[0]; zw[1] = a[1]; zw[2] = a[2]; zw[3] = a[3];
      zw[4] = *(LAS unsigned*)(lds + rz);
    }
    FENCE();
#define RQ3(k) do { LD_W3R((k) == 0 ? 0 : (k) == 1 ? NR0 : NR1, (k) == 0 ? NR0 : (k) == 1 ? NR1 : NWH); FENCE(); } while (0)
    RQ3(0);
    FENCE();
    if constexpr (TSHI == 22) TX_(30);
    STG(5, hh[i] = up2(zw[i]); PIN(hh[i]));
    STG(5, xq[i] = FMA2(hh[i], ha[i], xq[i]); PIN(xq[i]));
    STG(5, t_[i] = FMA2(xq[i], cp, xq[i]); PIN(t_[i]));
    STG(5, xq[i] = FMA2(xq[i], cn, t_[i]); PIN(xq[i]));
    if constexpr (TSHI == 22) TX_(31);
    RQ3(1);
    if constexpr (TSHI == 22) TX_(32);
    float m3;
    {
      const f32x2 s2 = ((xq[0] + xq[1]) + (xq[2] + xq[3])) + xq[4];
      const f32x2 q2 = FMA2(xq[0], xq[0], xq[1] * xq[1]) + FMA2(xq[2], xq[2], FMA2(xq[3], xq[3], xq[4] * xq[4]));
      const f32x2 p2 = FMA2(xq[0], ca[0], xq[1] * ca[1]) + FMA2(xq[2], ca[2], FMA2(xq[3], ca[3], xq[4] * ca[4]));
      float sm3[3] = {s2[0] + s2[1], q2[0] + q2[1], p2[0] + p2[1]};
      wave_sums<3>(sm3);
      const float mean = sm3[0] * (1.f / (float)D_), qs = sm3[1], ps = sm3[2];
      const float m = rbf(mean);
      f32x2 d[5];
#pragma unroll
      for (int j = 0; j < 5; ++j) d[j] = (xq[j] - m) * ca[j];
      const float var = rbf(__builtin_fmaxf(__builtin_fmaf(-mean, mean, qs * (1.f / (float)D_)), 0.f));
      const float den = rbf(__builtin_amdgcn_sqrtf(rbf(var + 1e-5f)));
      const float rinv = __builtin_amdgcn_rcpf(den);
      m3 = rbf(__builtin_fmaf(__builtin_fmaf(-m, mc, ps * (1.f / (float)D_)), rinv, mb));
      __builtin_amdgcn_sched_barrier(0);
      if constexpr (TSHI == 22) TX_(33);
      RQ3(2);
      if constexpr (TSHI == 22) TX_(34);
      STG(5, hh[i] = FMA2(d[i], (f32x2{rinv, rinv}), sb[i]); PIN(hh[i]));
      STG(5, hh[i] -= m3; PIN(hh[i]));
    }
#pragma unroll
    for (int j = 0; j < 5; ++j) hp_[j] = pack2(hh[j][0], (j & 1) ? hh[j][1] : (j < 4 || (lane & 1) == 0) ? codedv(hh[j][1]) : hh[j][1]);
    *(LAS u32x4*)(lds + ry) = u32x4{hp_[0], hp_[1], hp_[2], hp_[3]};
    *(LAS unsigned*)(lds + rz) = hp_[4];
    BARRIER();
    {
      const unsigned tb = (unsigned)(s0 >> 4) * 20480u + (unsigned)(s0 & 15) * 16u + (unsigned)(tid >> 3) * 256u + (unsigned)(tid & 7) * 16u;
      const LAS char* pr = lds + L_XCH + (tid & 7) * YROW + (tid >> 3) * 16;
      const u32x4 v0 = *(const LAS u32x4*)(pr);
      const u32x4 v1 = *(const LAS u32x4*)(pr + (tid < 128 ? 1024 : 0));
      st128(v0, rW, tb, O_HP, 16);
      if (wv < 2) st128(v1, rW, tb + 64u * 256u, O_HP, 16);
    }
    FENCE();
    TS_(9);
    PRIO(1);
#pragma unroll
    for (int j = 0; j < 5; ++j) xp[j] = pack2(xq[j][0], xq[j][1]);
    *(LAS u32x4*)(lds + ry + 8 * YROW) = u32x4{xp[0], xp[1], xp[2], xp[3]};
    *(LAS unsigned*)(lds + rz + 8 * YROW) = xp[4];
    FENCE();
    STAMP(10);
    {
      const f32x2 s2 = ((hh[0] + hh[1]) + (hh[2] + hh[3])) + hh[4];
      const f32x2 q2 = FMA2(hh[0], hh[0], hh[1] * hh[1]) + FMA2(hh[2], hh[2], FMA2(hh[3], hh[3], hh[4] * hh[4]));
      const float mean = wave_sum2(s2[0] + s2[1]) * (1.f / (float)D_), qs = wave_sum2(q2[0] + q2[1]);
      const float var = rbf(__builtin_fmaxf(__builtin_fmaf(-mean, mean, qs * (1.f / (float)D_)), 0.f));
      const float den = rbf(__builtin_amdgcn_sqrtf(rbf(var + 1e-5f)));
      const u32x2 st = {canon(__float_as_uint(m3)), __float_as_uint(den)};
      __builtin_amdgcn_raw_buffer_store_b64(st, rW, lane == 0 ? (unsigned)srow * 8u : OOB, O_ST3, 16);
    }
    {
      BARRIER();
      const unsigned tb = (unsigned)(s0 >> 4) * 20480u + (unsigned)(s0 & 15) * 16u + (unsigned)(tid >> 3) * 256u + (unsigned)(tid & 7) * 16u;
      const LAS char* pr = lds + L_XCH + (8 + (tid & 7)) * YROW + (tid >> 3) * 16;
      st128(*(const LAS u32x4*)(pr), rW, tb, O_XO, 16);
      if (wv >= 6) st128(*(const LAS u32x4*)(pr + 256), rW, tb + 16u * 256u, O_XO, 16);
      SIG(P_L1);
      if (bx == 0 && wv == 5) __builtin_amdgcn_raw_buffer_store_b64(u32x2{(unsigned)epn, (unsigned)(epn >> 32)}, rW, lane == 0 ? 0u : OOB, O_EP, 16);
    }
    FENCE();
    PRIO(0);
    FENCE();
  }

  float sthr = PTHR, scmx = CMAX;
  unsigned scsh = CSH;
  asm volatile("" : "+s"(sthr), "+s"(scsh), "+s"(scmx));
  unsigned xcw = (unsigned)(L_XCH + gm * 5120 + gk * 2048) + l16, xcr = (unsigned)(L_XCH + gm * 5120 + 2048 - gk * 2048) + l16, xcf = (unsigned)(L_CTL + gp * 4);
  asm volatile("" : "+v"(xcw), "+v"(xcr), "+v"(xcf));
  unsigned long long gkm;
  asm volatile("s_cmp_lg_u32 %1, 0\n\ts_cselect_b64 %0, -1, 0" : "=s"(gkm) : "s"(gk) : "scc");
  static_assert(KL >= 3 && KG <= 54 - 2 * KL, "the weights are read in front of the signal");
#define EXCHANGE(ph)                                                                        \
  do {                                                                                      \
    f32x4 e0, e1, e2;                                                                       \
    unsigned spins = 0;                                                                     \
    for (;;) {                                                                              \
      const unsigned fl = *(volatile LAS unsigned*)(lds + xcf);                                                           \
      e0 = *(volatile LAS f32x4*)(lds + xcr);                                               \
      e1 = *(volatile LAS f32x4*)(lds + xcr + 1024);                                        \
      e2 = *(volatile LAS f32x4*)(lds + xcr + 2048);                                        \
      if (__builtin_amdgcn_readfirstlane(fl) >= (ph)) break;                                \
      SPIN_STEP();                                                                          \
      if (spins > SPIN_CAP) break;                                                          \
    }                                                                                       \
    asm volatile("s_nop 3" : "+v"(acc[3]), "+v"(acc[4]));        \
    STG(12, e[i] = (i < 4 ? acc[3][i & 3] + e0[i & 3] : i < 8 ? acc[4][i & 3] + e1[i & 3] : acc[2][i & 3] + e2[i & 3]); PIN(e[i])); \
  } while (0)
#define KLOOP(CD, PB, BLK, VA, AOFF, XG, XS, ph)                                            \
  asm volatile("s_nop 1" : "+v"(A[0]));                                                     \
  sfor<0, 50>([&](auto M_) __attribute__((always_inline)) {                                 \
    constexpr int m = decltype(M_)::v, i = kl_i(m), j = kl_j(m), f = m / 4 + 1, c = m % 4;  \
    mf<i == 0 && (CD || !HFI || j < 2)>(acc[j], __builtin_bit_cast(u32x4, __builtin_shufflevector(Bh[2 * m], Bh[2 * m + 1], 0, 1, 2, 3, 4, 5, 6, 7)), A[i]); \
    SB();                                                                                   \
    constexpr int n0_ = m < KG ? (m * (100 - 2 * (PB))) / KG : 100, n1_ = m < KG ? ((m + 1) * (100 - 2 * (PB))) / KG : 100; \
    if constexpr (n1_ > n0_) RD_BH(2 * (PB) + n0_, BLK);                                    \
    if constexpr (n1_ > n0_ + 1) RD_BH(2 * (PB) + n0_ + 1, BLK);                            \
    static_assert(n1_ <= n0_ + 2 && (f > 9 || kl_i(m + 1) < f), "order");                   \
    if constexpr (f < 10) {                                                                 \
      if constexpr (c == 0) { m_ = CD ? amin2(A[f]) : __builtin_amdgcn_fmed3f(__builtin_fabsf(__uint_as_float(A[f][0])), __builtin_fabsf(__uint_as_float(A[f][2])), scmx); PIN(m_); } \
      if constexpr (c == 1) CMP_BAD(CD, bm, m_);                                            \
      if constexpr (c == 2 && CD) A[f][0] -= scsh;                                          \
      if constexpr (c == 3 && CD) { A[f][2] -= scsh; PIN(A[f]); }                           \
      if constexpr (c == 3) RETRY(CD, A[f], bm, VA, sa[f / 5], AOFF + (f % 5) * 2048, PHI); \
    }                                                                                       \
    if constexpr (m % 5 == 2 || m % 5 == 4) { XS(2 * (m / 5) + (m % 5 == 4)); }             \
    XG(m);                                                                                  \
                                \
    if constexpr (m == 51 - 2 * KL) { asm volatile("s_nop 3" : "+v"(acc[0])); *(volatile LAS f32x4*)(lds + xcw) = acc[0]; } \
    if constexpr (m == 52 - 2 * KL) *(volatile LAS f32x4*)(lds + xcw + 1024) = acc[1];      \
    if constexpr (m == 53 - 2 * KL) asm volatile("s_mov_b64 exec, %2\n\tds_write_b128 %0, %1 offset:2048\n\ts_mov_b64 exec, -1" :: "v"(xcw), "v"(acc[2]), "s"(gkm) : "memory"); \
    if constexpr (m == 54 - 2 * KL) SIG(ph);                                                \
    if constexpr (TSHI == 20 && m % 5 == 0) TX_((CD ? 30 : 44) + m / 5);                    \
    SB();                                                                                   \
  })
  {
    f32x4 acc[5];
    s16x4 Bh[100];
    u32x4 A[10];
    float m_, e[12];
    unsigned long long bm;
    unsigned sa[2] = {ROT ? (unsigned)(nt & 1) * 10240u : 0u, ROT ? 10240u - (unsigned)(nt & 1) * 10240u : 10240u};
    asm volatile("" : "+s"(sa[0]), "+s"(sa[1]));
    const unsigned va = (unsigned)(row >> 4) * 20480u + gk * 1024 + l16;
#define LD_A1(x) A[x] = __builtin_amdgcn_raw_buffer_load_b128(rW, va, sa[(x) / 5] + (O_HP + ((x) % 5) * 2048), 16)
    unsigned bb[3][2];
#pragma unroll
    for (int c = 0; c < 6; ++c) {
      bb[c >> 1][c & 1] = (unsigned)gk * 5120u + ((unsigned)lane >> 2) * 160u + ((unsigned)lane & 3u) * 8u + sa[c & 1] * 5u + (c < 2 ? 96u - 96u * gk : c < 4 ? 64u : 96u * gk);
      PIN(bb[c >> 1][c & 1]);
    }
#define RD_BH(h, BLK) Bh[h] = __builtin_amdgcn_ds_read_tr16_b64_v4i16((LAS s16x4*)(lds + bb[kl_j((h) / 2) < 2 ? 0 : kl_j((h) / 2) == 2 ? 1 : 2][kl_i((h) / 2) / 5] + (BLK) + (kl_i((h) / 2) % 5) * 10240 + (kl_j((h) / 2) % 3 == 1 ? 32 : 0) + ((h) & 1) * 2560))
#define BLK1 0
    FENCE();
#pragma unroll
    for (int n = 0; n < 2 * PBE; ++n) RD_BH(n, BLK1);
    FENCE();
    if (DLY1) __builtin_amdgcn_s_sleep(DLY1);
    PRIO(3);
    TS_(10);
#pragma unroll
    for (int i = 0; i < NA1; ++i) { LD_A1(i); SB(); }
    PRIO(1);
#pragma unroll
    for (int n = 2 * PBE; n < 2 * PB1; ++n) RD_BH(n, BLK1);
    FENCE();
#pragma unroll
    for (int i = NA1; i < NAF; ++i) { LD_A1(i); SB(); }
    u32x2 s3v;
    FENCE();
    if (kh) PRIO(PH1); else PRIO(PHI);
    STAMP(11);
    const int sh = (nt >> 1) == 0 ? 1 : (nt >> 1) == 1 ? -1 : (nt >> 1) == 2 ? 2 : -2;
    const int rr = row + sh, rw = rr & (S_ - 1);
    unsigned keep = rr == rw ? 0xffffffffu : 0u;
    unsigned oh1 = ofr - (unsigned)(row >> 4) * 20480u - (unsigned)l15 * 16u + (unsigned)(rw >> 4) * TS + (unsigned)(rw & 15) * 16u + O_H1;
    asm volatile("" : "+v"(keep), "+v"(oh1));
    asm volatile("" : "+s"(w3s[2]), "+s"(w3s[3]));
    if constexpr (TSHI == 21) { TX_(29); sfor<0, 10>([&](auto I_) __attribute__((always_inline)) { PIN(A[decltype(I_)::v]); TX_(30 + decltype(I_)::v); __builtin_amdgcn_s_sleep(1); }); }
    WAIT_ALL(P_L1);
    SB();
    CHECK_A(true, A[0], 0, NAF, LD_A1);
    SB();
#define XG1(m) do { if constexpr ((m) % 2 == 0 && NAF + (m) / 2 < 10) LD_A1(NAF + (m) / 2); if constexpr ((m) == 13) s3v = __builtin_amdgcn_raw_buffer_load_b64(rW, (unsigned)row * 8u, O_ST3, 16); if constexpr ((m) == 4) TS_(11); } while (0)
#define XSL(u, s0, n, f0) do { if constexpr ((u) >= (s0) && (((u) - (s0) + 1) * (n)) / (20 - (s0)) > (((u) - (s0)) * (n)) / (20 - (s0))) LD_W3U((f0) + (((u) - (s0)) * (n)) / (20 - (s0))); } while (0)
#define XS1(u) XSL(u, SL1, NL1, NWH)
    KLOOP(true, PB1, BLK1, va, O_HP, XG1, XS1, P_XF1);
    TS_(12);
    if (__builtin_expect(bad1(s3v[0]), 0)) {
      unsigned spins = 0;
      do {
        __builtin_amdgcn_s_sleep(RSLEEP);
        s3v = __builtin_amdgcn_raw_buffer_load_b64(rW, (unsigned)row * 8u, O_ST3, 16);
        SPIN_STEP();
      } while (bad1(s3v[0]) && spins < RETRY_CAP);
    }
    const float ld = __uint_as_float(s3v[1]), lr = __builtin_amdgcn_rcpf(ld);
    float gca = lr * GC1, gcb = lr * lr * lr * GC2;
    asm volatile("" : "+v"(gca), "+v"(gcb));
    EXCHANGE((unsigned)P_XF1);
    TS_(13);
    {
      unsigned pk[6];
      if (gk == 0) {
        gelu_s<12>(e, gca, gcb, ld, pk);
        __builtin_amdgcn_raw_buffer_store_b64(u32x2{pk[4] & keep, pk[5] & keep}, rW, oh1 + 1024, 0, 16);
      } else {
        gelu_s<8>(e, gca, gcb, ld, pk);
      }
      st128(widen(pk[0] & keep, pk[1] & keep, pk[2] & keep, pk[3] & keep), rW, oh1 + (unsigned)(g & 1) * 504u, 0, 16);
    }
    FENCE();
    TS_(14);
    PRIO(0);
    LD_W3R(NWH + NL1, NWH + NL1 + NP0);
    FENCE();
    WAIT_ALL(P_XF1);
#pragma unroll
    for (int i = 0; i < 10; ++i) *(LAS u32x4*)(lds + 20480 + wv * 5120 + (i / 5) * 40960 + (i % 5) * 1024 + l16) = WS2[i];
    SIG(P_G1);
    STAMP(13);
    FENCE();
    LD_W3R(NWH + NL1 + NP0, NWH + NL1 + NP1);
    FENCE();
    const unsigned va2 = (unsigned)(row >> 4) * TS + gk * 1024 + l16;
    u32x2 hpv[3];
#define LD_A2(x) A[x] = __builtin_amdgcn_raw_buffer_load_b128(rW, va2, sa[(x) / 5] + (O_H1 + ((x) % 5) * 2048), 16)
    u32x4 hp4;
    const unsigned ofw = ofr + (unsigned)(g & 1) * 504u;
    hp4 = __builtin_amdgcn_raw_buffer_load_b128(rW, ofw, O_HP, 16);
    hpv[2] = __builtin_amdgcn_raw_buffer_load_b64(rW, gk ? OOB : ofr + 1024, O_HP, 16);
    FENCE();
    WAIT_ALL(P_G1);
    if (DLY2) __builtin_amdgcn_s_sleep(DLY2);
    PRIO(3);
    TS_(15);
#pragma unroll
    for (int i = 0; i < NA1; ++i) { LD_A2(i); SB(); }
    PRIO(1);
#define BLK2 20480
#pragma unroll
    for (int n = 0; n < 2 * PB2; ++n) RD_BH(n, BLK2);
    FENCE();
#pragma unroll
    for (int i = NA1; i < NAF; ++i) { LD_A2(i); SB(); }
    FENCE();
    if (kh) PRIO(PH2); else PRIO(PHI);
    float hf[12];
    { const u32x4 n_ = widen(hp4[0], hp4[1], hp4[2], hp4[3]); hpv[0] = u32x2{n_[0], n_[1]}; hpv[1] = u32x2{n_[2], n_[3]}; }
#pragma unroll
    for (int j = 0; j < 3; ++j) { hpv[j][0] -= CSH; PIN(hpv[j]); }
    STG(12, hf[i] = ((i & 1) ? hi2f(hpv[i >> 2][(i >> 1) & 1]) : lo2f(hpv[i >> 2][(i >> 1) & 1])) + __uint_as_float(s3v[0]); PIN(hf[i]));
    acc[3] = f32x4{hf[0], hf[1], hf[2], hf[3]}; acc[4] = f32x4{hf[4], hf[5], hf[6], hf[7]};
    acc[2] = gk ? f32x4{0.f, 0.f, 0.f, 0.f} : f32x4{hf[8], hf[9], hf[10], hf[11]};
    PIN(acc[2]); PIN(acc[3]); PIN(acc[4]);
    STAMP(14);
    if constexpr (TSHI == 21) { TX_(43); sfor<0, 10>([&](auto I_) __attribute__((always_inline)) { PIN(A[decltype(I_)::v]); TX_(44 + decltype(I_)::v); __builtin_amdgcn_s_sleep(1); }); }
    SB();
    CHECK_A(false, A[0], 0, NAF, LD_A2);
    SB();
#define XG2(m) do { if constexpr ((m) % 2 == 0 && NAF + (m) / 2 < 10) LD_A2(NAF + (m) / 2); if constexpr ((m) == 4) TS_(16); } while (0)
#define XS2(u) XSL(u, SL2, NL2, NWH + NL1 + NP1)
    KLOOP(false, PB2, BLK2, va2, O_H1, XG2, XS2, P_XF2);
    TS_(17);
    EXCHANGE((unsigned)P_XF2);
    TS_(18);
    unsigned xk[6];
    {
      if (!HFI) STG(12, e[i] += hf[i]; PIN(e[i]));
      STG(3, e[4 * i + 1] = plainv(e[4 * i + 1]); PIN(e[4 * i + 1]));
      STG(6, xk[i] = pack2(e[2 * i], e[2 * i + 1]); PIN(xk[i]));
      st128(widen(xk[0], xk[1], xk[2], xk[3]), rW, ofw, O_X1, 16);
      if (gk == 0) __builtin_amdgcn_raw_buffer_store_b64(u32x2{xk[4], xk[5]}, rW, ofr + 1024, O_X1, 16);
    }
    FENCE();
    TS_(19);
    PRIO(1);
    SIG(P_G2);
    float v[12];
    float s = 0.f;
#pragma unroll
    for (int j = 0; j < 3; ++j) {
      v[4 * j] = lo2f(xk[2 * j]); v[4 * j + 1] = hi2f(xk[2 * j]); v[4 * j + 2] = lo2f(xk[2 * j + 1]); v[4 * j + 3] = hi2f(xk[2 * j + 1]);
      if (j < 2 || gk == 0) s += (v[4 * j] + v[4 * j + 1]) + (v[4 * j + 2] + v[4 * j + 3]);
    }
    s = swap_add32(swap_add16(s));
    const float mean = s * (gk ? (1.f / 32.f) : (1.f / 48.f));
    float m2 = 0.f;
#pragma unroll
    for (int i = 0; i < 12; ++i) {
      const float dd = v[i] - mean;
      if (i < 8 || gk == 0) m2 += dd * dd;
    }
    m2 = swap_add32(swap_add16(m2));
    {
      u32x2 st = {canon(__float_as_uint(mean)), __float_as_uint(m2)};
      __builtin_amdgcn_raw_buffer_store_b64(st, rW, g == 0 ? (unsigned)(((row >> 4) * 16 + nt * 2 + gk) * 16 + l15) * 8u : OOB, O_ST4, 16);
    }
    FENCE();
    PRIO(0);
    FENCE();
    STAMP(16);
  }
  {
    constexpr int NR3 = 40 - NWH - NL1 - NP1 - NL2;
#define NOW4D(...) __VA_ARGS__
#define LD_W4(f) W4[f] = __builtin_amdgcn_raw_buffer_load_b128(rW, l16, w4a + (f) * 1024, 16)
    const unsigned va4 = O_H + (unsigned)(m0 >> 4) * TS + (unsigned)k4 * 1024u;
#define A4OFF(x) (((x) / NS) * TS + ((x) % NS) * 1024)
#define LD_A4(x) A4[(x) % AW4] = __builtin_amdgcn_raw_buffer_load_b128(rW, l16, va4 + A4OFF(x), 16)
    const int lane0 = lane;
#define LANES() int lane = lane0; PIN(lane); const int l15 = lane & 15, g = lane >> 4; const unsigned l16 = (unsigned)lane * 16u; \
    const unsigned fa = (unsigned)L_CTL + ((unsigned)lane & 7u) * 4u
#define FLG(a) *(volatile LAS unsigned*)(lds + (a))
    auto g3 = [&](auto KH_, u32x4 (&W4)[35], u32x4 (&A4)[AW4], u32x2 (&x1v)[3]) __attribute__((always_inline)) {
      constexpr int KH = decltype(KH_)::v, NS = 7, NW0 = KH ? 6 : AW4;
      LANES();
      const unsigned hw0 = (unsigned)(m0 >> 4) * TS + (unsigned)l15 * 16u + (unsigned)(g >> 1) * 256u + O_H;
      const unsigned csp = (unsigned)(L_CS3 + wv * 128 + g * 16), f1 = (unsigned)L_CTL + 16u + ((unsigned)lane & 3u) * 4u;
      u32x2 stv[4];
#define LD_ST() _Pragma("unroll") for (int a = 0; a < 4; ++a) stv[a] = __builtin_amdgcn_raw_buffer_load_b64(rW, (unsigned)((((m0 >> 4) + mg) * 16 + 4 * g) * 16 + l15) * 8u, O_ST4 + a * 128, 16)
#define ST_BAD() bad1(max(max(stv[0][0], stv[1][0]), max(stv[2][0], stv[3][0])))
      const u32x4 ones = {0x3f803f80u, 0x3f803f80u, 0x3f803f80u, 0x3f803f80u};
      f32x4 c3a = {0.f, 0.f, 0.f, 0.f}, c3b = {0.f, 0.f, 0.f, 0.f};
#define CS3R(a, b) do { _Pragma("unroll") for (int f_ = (a); f_ < (b); f_ += 2) { c3a = mfma16(W3[f_], ones, c3a); if (!KH || f_ < 20) c3b = mfma16(W3[f_ + 1], ones, c3b); } } while (0)
      TS_(20);
      PRIO(PHI);
      LD_W3R(40 - NR3, 40);
      FENCE();
      const unsigned so = O_X1 + (unsigned)mt * 81920u + (unsigned)wv * 10240u, d0 = (unsigned)(KH ? L_XB : L_XA) + (unsigned)mg * 10240u;
      CS3R(0, KH ? KC3B : KC3);
      asm volatile("" : "+v"(c3a), "+v"(c3b));
      FENCE();
      TS_(21);
      WAIT_2(KH ? P_XF2 : P_G2, KH ? P_G2 : P_XF2);
      if (KH ? DLY3B : DLY3) __builtin_amdgcn_s_sleep(KH ? DLY3B : DLY3);
      PIN(W3[38]); PIN(W3[39]);
      if (KH) {
#pragma unroll
        for (int f = 0; f < 10; ++f) W4[f] = W3[21 + 2 * f];
      }
      FENCE();
      PRIO(3);
      TS_(22);
#define STG_DMA() do { dma<4, 1>(l16, dW, so, d0); dma<4, 1>(l16, dW, so + 4096, d0 + 4096); dma<1, 1>(l16, dW, so + 8192, d0 + 8192); dma<1, 1>(l16, dW, so + 9216, d0 + 9216); } while (0)
      if (KH) {
        asm volatile("buffer_load_dwordx2 %0, %4, %5, %6 offen sc1\n\tbuffer_load_dwordx2 %1, %4, %5, %6 offen offset:128 sc1\n\t"
                     "buffer_load_dwordx2 %2, %4, %5, %6 offen offset:256 sc1\n\tbuffer_load_dwordx2 %3, %4, %5, %6 offen offset:384 sc1"
                     : "=&v"(stv[0]), "=&v"(stv[1]), "=&v"(stv[2]), "=&v"(stv[3]) : "v"((unsigned)((((m0 >> 4) + mg) * 16 + 4 * g) * 16 + l15) * 8u), "s"(dW), "s"(O_ST4) : "memory");
      }
      STG_DMA();
      FENCE();
      TS_(23);
      PRIO(PHI);
      CS3R(KH ? KC3B : KC3, 40);
      if (l15 == 0) { *(LAS f32x4*)(lds + csp) = c3a; *(LAS f32x4*)(lds + csp + 64) = c3b; }
      FENCE();
      if (KH) {
        asm volatile("s_waitcnt vmcnt(10)" : "+v"(stv[0]), "+v"(stv[1]), "+v"(stv[2]), "+v"(stv[3]));
        if (__builtin_expect(ST_BAD(), 0)) {
          unsigned spins = 0;
          do {
            __builtin_amdgcn_s_sleep(RSLEEP);
            LD_ST();
            SPIN_STEP();
          } while (ST_BAD() && spins < RETRY_CAP);
        }
        const float m0_ = __uint_as_float(stv[0][0]), q0 = __uint_as_float(stv[0][1]), m1 = __uint_as_float(stv[1][0]), q1 = __uint_as_float(stv[1][1]);
        const float m2_ = __uint_as_float(stv[2][0]), q2 = __uint_as_float(stv[2][1]), m3 = __uint_as_float(stv[3][0]), q3 = __uint_as_float(stv[3][1]);
        float sm = 48.f * (m0_ + m2_) + 32.f * (m1 + m3);
        sm = swap_add32(swap_add16(sm));
        const float mean = sm * (1.f / (float)D_);
        const float d0_ = m0_ - mean, d1 = m1 - mean, d2 = m2_ - mean, d3 = m3 - mean;
        float qs = (q0 + q1) + (q2 + q3) + 48.f * (d0_ * d0_ + d2 * d2) + 32.f * (d1 * d1 + d3 * d3);
        qs = swap_add32(swap_add16(qs));
        const float var = rbf(qs * (1.f / (float)D_));
        const float den = rbf(__builtin_amdgcn_sqrtf(rbf(var + 1e-5f))), r = __builtin_amdgcn_rcpf(den);
        float rc1 = r * GC1;
        PIN(rc1);
        if (g == 0) *(volatile LAS f32x4*)(lds + L_ST4 + (mg * 16 + l15) * 16) = f32x4{rbf(mean), rc1, r * r * r * GC2, den};
        SIG(P_SF);
      }
      FENCE();
      TS_(24);
      VMW(0);
      TS_(25);
      {
        unsigned spins = 0;
        for (;;) {
          u32x4 a[10];
          float mx = 0.f;
#pragma unroll
          for (int i = 0; i < 10; ++i) a[i] = *(volatile LAS u32x4*)(lds + d0 + i * 1024 + l16);
#pragma unroll
          for (int i = 0; i < 10; ++i) mx = amax3(mx, a[i]);
          if (__builtin_expect(__builtin_amdgcn_ballot_w64(mx >= PTHR) == 0ull, 1)) break;
          SPIN_STEP();
          if (spins > RETRY_CAP) break;
          PRIO(0);
          __builtin_amdgcn_s_sleep(RSLEEP);
          STG_DMA();
          PRIO(PHI);
          asm volatile("s_waitcnt vmcnt(0)" ::: "memory");
        }
        SIG(P_SA);
      }
      TS_(26);
      STAMP(17);
      FENCE();
      constexpr int NP = KH ? NPH : NPK, F0 = KH ? 10 : 0, NEQ = NP + 6, EQ0 = KH ? EC0 : EA0, EQ1 = KH ? EC1 : EA1, NL0 = F0 + NEQ, NLT = (KH ? 30 : 35) - NL0;
      const unsigned pks = (unsigned)(KH ? L_PB + NPK * 1024 + mg * (NPH * 1024) : wv < 3 ? wv * (NPK * 1024) : L_PB);
      constexpr auto ecnt = [](int q) { return q <= EQ0 ? 0 : q >= EQ1 ? NEQ : ((q - EQ0) * NEQ + EQ1 - EQ0 - 1) / (EQ1 - EQ0); };
#define ESLOT(q) sfor<ecnt(q), ecnt((q) + 1)>([&](auto U_) __attribute__((always_inline)) { constexpr int u = decltype(U_)::v; \
        if constexpr (u < NP) { NOW4D(dma<1, 1>(l16, dW, w4a + (F0 + 6 + u) * 1024, pks + u * 1024)); } else LD_W4(F0 + u - NP); })
      static_assert(3 * NPK <= 32 && NPK + 4 * NPH <= 36 && MV0 + NP <= 20 && NLT <= 19 && NLT >= MV0, "w4");
      sfor<0, 2>([&](auto P_) __attribute__((always_inline)) {
        constexpr int p = decltype(P_)::v, NV = KH ? 12 : 16;
        TS_(28 + 8 * p + 0);
        if (p == 0) WAIT_2(P_SA, P_G2); else WAIT_HALF(1, P_SA);
        f32x4 acc[4];
        PRIO(KH ? (p ? PK1 : PK2) : PK0);
        TS_(28 + 8 * p + 1);
        u32x4 Ar[AD3][2];
        unsigned ab[2] = {l16 + (unsigned)((p ? L_XB : L_XA) + (KH ? hb * 30720 : 0)), l16 + (unsigned)((p ? L_XB : L_XA) + 20480 - (KH ? hb * 10240 : 0))};
        const unsigned sp = (unsigned)(L_ST4 + p * 512 + l15 * 16), fp = (unsigned)(L_CTL + (wv ^ 2) * 4), xs = (unsigned)(32768 + p * 4096) + l16;
        PIN(ab[0]);
        if (KH) PIN(ab[1]);
        f32x4 cs[3], st[2], rx;
        unsigned sf_, sx_;
#define RD_A3(s, h) Ar[(s) % AD3][h] = *(const LAS u32x4*)(lds + (KH ? ab[h] : ab[0] + (h) * 20480) + (s) * 1024)
#pragma unroll
        for (int s = 0; s < AD3 - 1; ++s) { RD_A3(s, 0); RD_A3(s, 1); }
        SB();
        sfor<0, 20>([&](auto S_) __attribute__((always_inline)) {
          constexpr int s = decltype(S_)::v;
          constexpr bool rd = s + AD3 - 1 < 20, T2 = !KH || s < 10;
          mf<s == 0>(acc[0], W3[2 * s], Ar[s % AD3][0]);
          SB();
          if constexpr (rd) RD_A3(s + AD3 - 1, 0);
          else if constexpr (s == 18) cs[0] = *(const LAS f32x4*)(lds + csp);
          else st[0] = *(const volatile LAS f32x4*)(lds + sp + (KH ? hb * 256 : 0));
          SB();
          mf<s == 0>(acc[1], W3[2 * s], Ar[s % AD3][1]);
          SB();
          if constexpr (rd) RD_A3(s + AD3 - 1, 1);
          else if constexpr (s == 18) cs[1] = *(const LAS f32x4*)(lds + csp + 64);
          else st[1] = *(const volatile LAS f32x4*)(lds + sp + 256 - (KH ? hb * 256 : 0));
          SB();
          if constexpr (T2) { mf<s == 0>(acc[2], W3[2 * s + 1], Ar[s % AD3][0]); SB(); }
          if constexpr (p == 0 && s == 15) sf_ = FLG(f1);
          if constexpr (p == 0 && s == 18) NEED_AT(sf_, f1, P_SF);
          if constexpr (p == 0) ESLOT(s);
          if constexpr (p == 1 && s >= 1 && s <= NLT) LD_W4(NL0 + s - 1);
          if constexpr (p == 1 && s == MV0 - 1) VMW(MV0 + 1);
          SB();
          if constexpr (T2) { mf<s == 0>(acc[3], W3[2 * s + 1], Ar[s % AD3][1]); SB(); }
          if constexpr (KH && s + AD3 == 10) { ab[0] -= hb * 20480; ab[1] -= hb * 20480; PIN(ab[0]); PIN(ab[1]); }
          if constexpr (KH && s == 12) { *(volatile LAS f32x4*)(lds + xs + mg * 1024) = acc[3]; SIG(p ? P_S1 : P_S0); }
          if constexpr (KH && s == 14) sx_ = FLG(fp);
          if constexpr (KH && s == 17) { NEED_AT(sx_, fp, p ? P_S1 : P_S0); rx = *(const volatile LAS f32x4*)(lds + xs + (mg ^ 2) * 1024); cs[2] = *(const volatile LAS f32x4*)(lds + csp + (hb ? -192 : 320)); }
          if constexpr (p == 1 && s >= MV0 && s < MV0 + NP) W4[F0 + 6 + s - MV0] = *(const LAS u32x4*)(lds + pks + l16 + (s - MV0) * 1024);
          if constexpr (p == 1 && s >= 1 && s < 4 - KH) {
            static_assert(H0S >= 4 && H0S < 12, "x1");
            const int tu = KH == 0 ? (s == 3 ? 3 : s - 1) : wv < 6 ? 2 : s == 1 ? wv & 1 : wv - 4, ju = KH == 0 ? wv : wv < 6 ? 2 * wv - 6 - s : 4;
            x1v[s - 1] = *(const LAS u32x2*)(lds + (tu < 2 ? L_XA : L_XB - 40960) + tu * 20480 + (nt * 10 + 2 * ju) * 256 + (g >> 1) * 256 + (g & 1) * 8 + l15 * 16);
          }
          if constexpr (p == 1 && s == H0S) SIG(P_H0);
          if constexpr (s == 9) TS_(28 + 8 * p + 2);
          SB();
        });
        TS_(28 + 8 * p + 3);
        PRIO(KH && p ? PE1 : PE0);
        SIG(p ? P_KB : P_KA);
        if constexpr (KH) asm volatile("s_nop 3" : "+v"(acc[0]), "+v"(acc[1]), "+v"(acc[2])); else asm volatile("s_nop 3" : "+v"(acc[0]), "+v"(acc[1]), "+v"(acc[2]), "+v"(acc[3]));
        STAMP(18 + 2 * p);
        {
          float e[NV], x[NV];
          unsigned pk[NV / 2];
#define SQ(i) st[((i) >> 2) & 1]
#define EPS(q) do { if constexpr (p == 0) { ESLOT(20 + (q)); } if constexpr (p == 1 && (q) == HG - 1) sf_ = FLG(fa); \
                    if constexpr (p == 1 && (q) == HG) { NEED_AT(sf_, fa, P_H0); _Pragma("unroll") for (int x = 0; x < NW0; ++x) { LD_A4(x); } } SB(); } while (0)
          if constexpr (KH) STG(4, x[i] = __builtin_fmaf(-st[0][0], cs[2][i], rx[i]); PIN(x[i]));
          STG(NV, e[i] = __builtin_fmaf(-SQ(i)[0], cs[i >> 3][i & 3], acc[i >> 2][i & 3]); PIN(e[i]));
          if constexpr (KH) STG(4, e[8 + i] += x[i]; PIN(e[8 + i]));
          TS_(28 + 8 * p + 4);
          EPS(0);
          STG(NV, x[i] = e[i] * e[i]; PIN(x[i]));
          EPS(1);
          STG(NV, x[i] = __builtin_fmaf(x[i], SQ(i)[2], SQ(i)[1]); PIN(x[i]));
          EPS(2);
          STG(NV, x[i] *= e[i]; PIN(x[i]));
          EPS(3);
          STG(NV, x[i] = __builtin_amdgcn_exp2f(x[i]); PIN(x[i]));
          TS_(28 + 8 * p + 5);
          EPS(4);
          STG(NV, x[i] = __builtin_fmaf(x[i], SQ(i)[3], SQ(i)[3]); PIN(x[i]));
          EPS(5);
          STG(NV, x[i] = __builtin_amdgcn_rcpf(x[i]); PIN(x[i]));
          EPS(6);
          STG(NV, e[i] *= x[i]; PIN(e[i]));
          TS_(28 + 8 * p + 6);
          EPS(7);
          STG(NV / 4, e[4 * i + 1] = codedv(e[4 * i + 1]); PIN(e[4 * i + 1]));
          STG(NV / 2, pk[i] = pack2(e[2 * i], e[2 * i + 1]); PIN(pk[i]));
          const unsigned hw1 = hw0 + (unsigned)((g & 1) ^ (KH ? hb : 0)) * TS;
          st128(widen(pk[0], pk[1], pk[2], pk[3]), rW, hw1, (unsigned)(sb3 + jl) * 512u + 2 * p * TS, 16);
          if constexpr (KH) __builtin_amdgcn_raw_buffer_store_b64(u32x2{pk[4], pk[5]}, rW, hw0 + (unsigned)(g & 1) * 8u, two ? (unsigned)(sb3 + jm) * 512u + (unsigned)(2 * p + hb) * TS : OOB, 16);
          else st128(widen(pk[4], pk[5], pk[6], pk[7]), rW, hw1, (unsigned)(sb3 + jm) * 512u + 2 * p * TS, 16);
          FENCE();
          EPS(8);
        }
        TS_(28 + 8 * p + 7);
        STAMP(19 + 2 * p);
      });
    };
#define PB(t) (81920 - (t) * 40960)
    const __amdgpu_buffer_rsrc_t rO = __builtin_amdgcn_make_buffer_rsrc((void*)out, 0, SD2, RS3);
    auto g4 = [&](auto KD_, u32x4 (&W4)[35], u32x4 (&A4)[AW4], u32x2 (&x1v)[3]) __attribute__((always_inline)) {
      constexpr int KD = decltype(KD_)::v, NS = KD == 1 ? 6 : 7, PR = KD ? P41 : P40;
      constexpr int GS = NS + (KD == 0 ? GSA : KD == 1 ? GSB : GSC);
      constexpr auto nreq = [](int x) { int n = AW4; for (int y = 0; y <= x; ++y) { int m = y + AW4 < 4 * NS ? y + AW4 : 4 * NS; if (y < GS && m > 2 * NS) m = 2 * NS; n = m < n + 3 ? (m > n ? m : n) : n + 3; } return n; };
      LANES();
      TS_(44);
      if constexpr (KD) {
#pragma unroll
        for (int x = 6; x < AW4; ++x) { LD_A4(x); }
      }
      if constexpr (KD == 2) {
#pragma unroll
        for (int f = 30; f < 35; ++f) { LD_W4(f); }
      }
      FENCE();
      PRIO(PR);
      u32x2 xv[2];
      unsigned fl_, fh_, fu_, pu_;
      f32x4 acc[2][5], rs[4], gq;
      float u_[4], y_[4], m4_;
      unsigned long long bm4;
      unsigned pw = (unsigned)(wv * 5120) + l16;
      PIN(pw);
      const unsigned fo0 = (unsigned)((m0 >> 4) * 20480 + ((n0 + 4 * g) >> 3) * 256 + (g & 1) * 8 + l15 * 16), oo0 = (unsigned)((m0 + l15) * D_ + n0 + 4 * g) * 2u;
#define U_LD(r, t, j) xv[r] = __builtin_amdgcn_raw_buffer_load_b64(rW, fo0, O_XO + (unsigned)((t) * 20480 + (j) * 512), 16)
#define U_PC(q, t, j, y)                                                                               \
  do {                                                                                                  \
    if constexpr ((q) == 0) { fu_ = FLG(fa); pu_ = (unsigned)(PB(t) + (j) * 1024) + l16; PIN(pu_); }    \
    if constexpr ((q) == 1) { gq = *(const LAS f32x4*)(lds + L_GB + ((j) * 16 + 4 * g) * 4); }          \
    if constexpr ((q) >= 1 && (q) < 5) { constexpr int i = (q) - 1; u_[i] = (i & 1) ? hi2f(x1v[y][i >> 1]) : lo2f(x1v[y][i >> 1]); PIN(u_[i]); } \
    if constexpr ((q) == 5) NEED_AT(fu_, fa, P_P0 + 1 + (t));                                           \
    if constexpr ((q) >= 6 && (q) < 10) rs[(q) - 6] = *(const volatile LAS f32x4*)(lds + pu_ + ((q) - 6) * 5120); \
    if constexpr ((q) >= 11 && (q) < 27) { constexpr int w = ((q) - 11) >> 1, h = (((q) - 11) & 1) * 2;  \
      u_[h] += rs[w & 3][h]; PIN(u_[h]); u_[h + 1] += rs[w & 3][h + 1]; PIN(u_[h + 1]);                 \
      if constexpr (h == 2 && w < 4) rs[w] = *(const volatile LAS f32x4*)(lds + pu_ + (4 + w) * 5120); } \
    if constexpr ((q) >= 6 && (q) < 10) { constexpr int i = (q) - 6; y_[i] = (i & 1) ? hi2f(xv[0][i >> 1]) : lo2f(xv[0][i >> 1]); PIN(y_[i]); } \
    if constexpr ((q) == 27 || (q) == 28) { constexpr int h = ((q) - 27) * 2; u_[h] = __builtin_fmaf(u_[h], gq[h], y_[h]); PIN(u_[h]); u_[h + 1] = __builtin_fmaf(u_[h + 1], gq[h + 1], y_[h + 1]); PIN(u_[h + 1]); } \
    if constexpr ((q) == 29) __builtin_amdgcn_raw_buffer_store_b64(u32x2{pack2(u_[0], u_[1]), pack2(u_[2], u_[3])}, rO, oo0, (unsigned)((t) * (16 * D_ * 2) + (j) * 32), 0); \
  } while (0)
      {
        unsigned long long b0;
        const float m0_ = amin2(A4[0]);
        CMP_BAD(1, b0, m0_);
        A4[0][0] -= scsh; A4[0][2] -= scsh;
        RETRY_C(A4[0], b0, l16, va4, 0, PR);
        asm volatile("s_nop 0" : "+v"(A4[0]));
      }
      SB();
      const int tc = KD == 0 ? 3 : KD == 1 ? 2 : wv - 4, jc = KD == 0 ? wv : KD == 1 ? 2 * wv - 8 : 4;
      sfor<0, 20 * NS>([&](auto M_) __attribute__((always_inline)) {
        constexpr int m = decltype(M_)::v, x = m / 5, j = m % 5, t = x / NS, s = x % NS, r0 = nreq(x - 2), r1 = nreq(x - 1);
        constexpr bool nx = x + 1 < 4 * NS;
        u32x4& an = A4[(x + 1) % AW4];
        mf<s == 0>(acc[t & 1][j], W4[5 * s + j], A4[x % AW4]);
        SB();
        if constexpr (j == 0) {
          if constexpr (x == GS + 1) NEED_AT(fh_, fa, P_H1);
          if constexpr (r0 < r1) { LD_A4(r0); }
        }
        if constexpr (j == 1 && nx) { m4_ = amin2(an); PIN(m4_); }
        if constexpr (j == 2 && nx) { CMP_BAD(1, bm4, m4_); an[0] -= scsh; }
        if constexpr (j == 3 && nx) { an[2] -= scsh; PIN(an); }
        if constexpr (j == 3 && r0 + 1 < r1) { LD_A4(r0 + 1); }
        if constexpr (j == 4) {
          if constexpr (nx) RETRY_C(an, bm4, l16, va4, A4OFF(x + 1), PR);
          if constexpr (r0 + 2 < r1) { LD_A4(r0 + 2); }
          if constexpr (x == GS) fh_ = FLG(fa);
          if constexpr (x == H1S) SIG(P_H1);
          if constexpr (s == 0) TS_(45 + 4 * t);
          if constexpr (s == 3) TS_(45 + 4 * t + 1);
          if constexpr (s == NS - 1) TS_(45 + 4 * t + 2);
          static_assert(GS + 1 < 2 * NS, "order");
          if constexpr (KD == 0 && t == 1 && s == 0) U_LD(0, 0, wv);
          if constexpr (KD == 0 && t == 2 && s == 3) U_LD(0, 1, wv);
          if constexpr (KD == 2 && t == 2 && s == 3) U_LD(0, wv & 1, 4);
          if constexpr (KD == 1 && t == 3 && s == 3) U_LD(0, 2, jc + 1);
          if constexpr (t == 2 && s == 5) U_LD(1, tc, jc);
          if constexpr (t == 3 && s == NS - 2) fl_ = FLG(L_CTL);
        }
        constexpr bool un = (KD == 0 && t >= 2) || (KD == 2 && t == 3);
        constexpr int wq = 5 * s + j, w0 = un ? W0U : 4;
        if constexpr (t > 0 && wq >= w0 && wq < w0 + 5 * WSP && (wq - w0) % WSP == 0) *(volatile LAS f32x4*)(lds + PB(t - 1) + pw + ((wq - w0) / WSP) * 1024) = acc[(t - 1) & 1][(wq - w0) / WSP];
        if constexpr (t > 0 && wq == w0 + 1 + 4 * WSP) SIG(P_P0 + t);
        static_assert(W0U + 1 + 4 * WSP < 35 && U0 + 30 <= 35, "unit");
        if constexpr (un && KD == 0 && wq >= U0) U_PC(wq - U0, t - 2, wv, t - 2);
        if constexpr (un && KD == 2 && wq >= U0) U_PC(wq - U0, wv & 1, 4, 0);
        SB();
      });
      asm volatile("s_nop 3" : "+v"(acc[1][0]), "+v"(acc[1][1]), "+v"(acc[1][2]), "+v"(acc[1][3]), "+v"(acc[1][4]));
#pragma unroll
      for (int j = 0; j < 4; ++j) *(volatile LAS f32x4*)(lds + L_PB + wv * 4096 + l16 + j * 1024) = acc[1][j];
      NEED_AT(fl_, (unsigned)L_CTL, P_P0 + 3);
      *(volatile LAS f32x4*)(lds + PB(0) + pw) = acc[1][4];
      SIG(P_P0 + 4);
      TS_(60);
#define UNIT(r, y, t, j, base, str)                                                                        \
  do {                                                                                                  \
    unsigned pq = (unsigned)(base) + l16, oq = oo0 + (unsigned)((t) * (16 * D_ * 2) + (j) * 32);        \
    f32x4 gq_ = *(const LAS f32x4*)(lds + L_GB + ((j) * 16 + 4 * g) * 4);                               \
    f32x2 xa = up2(xv[r][0]), xb = up2(xv[r][1]), ya = up2(x1v[y][0]), yb = up2(x1v[y][1]);             \
    asm volatile("" : "+v"(xa), "+v"(xb), "+v"(ya), "+v"(yb), "+v"(gq_), "+v"(pq), "+v"(oq));           \
    f32x4 r8[8];                                                                                        \
    unsigned spins = 0;                                                                                 \
    for (;;) {                                                         \
      const unsigned fl = FLG(fa);                                                                      \
      _Pragma("unroll") for (int w = 0; w < 8; ++w) r8[w] = *(const volatile LAS f32x4*)(lds + pq + w * (str)); \
      if (__builtin_amdgcn_ballot_w64(fl < (unsigned)(P_P0 + 1 + (t))) == 0ull) break;                  \
      SPIN_STEP();                                                                                      \
      if (spins > SPIN_CAP) break;                                                                      \
    }                                                                                                   \
    const f32x4 sm = ((r8[0] + r8[1]) + (r8[2] + r8[3])) + ((r8[4] + r8[5]) + (r8[6] + r8[7]));         \
    const f32x2 a_ = FMA2(f32x2{sm[0], sm[1]} + ya, (f32x2{gq_[0], gq_[1]}), xa), b_ = FMA2(f32x2{sm[2], sm[3]} + yb, (f32x2{gq_[2], gq_[3]}), xb); \
    __builtin_amdgcn_raw_buffer_store_b64(u32x2{pack2(a_[0], a_[1]), pack2(b_[0], b_[1])}, rO, oq, 0, 0); \
    FENCE();                                                                                            \
  } while (0)
      if constexpr (KD == 0) UNIT(1, 2, 3, wv, L_PB + wv * 1024, 4096);
      else UNIT(1, 1, tc, jc, tc == 3 ? PB(0) : PB(2) + jc * 1024, 5120);
      if constexpr (KD == 1) UNIT(0, 0, 2, jc + 1, PB(2) + jc * 1024 + 1024, 5120);
    };
#define KILL(V, N) _Pragma("unroll") for (int f_ = 0; f_ < (N); ++f_) asm volatile("" : "=v"(V[f_]))
    int arm = wv >> 1;
    if (arm < 2) {
      u32x4 W4[35], A4[AW4];
      u32x2 x1v[3];
      g3(IC<0>{}, W4, A4, x1v);
      g4(IC<0>{}, W4, A4, x1v);
      KILL(W3, 40 - NR3);
    }
    asm volatile("" : "+s"(arm));
    if (arm >= 2) {
      u32x4 W4[35], A4[AW4];
      u32x2 x1v[3];
      g3(IC<1>{}, W4, A4, x1v);
      if (arm == 3) {
        g4(IC<2>{}, W4, A4, x1v);
        KILL(W4, 30);
        KILL(A4, 6);
      }
      asm volatile("" : "+s"(arm));
      if (arm == 2) g4(IC<1>{}, W4, A4, x1v);
    }
    STAMP(27);
    CLK(31);
  }
}

extern "C" void kernel_launch(void* const* d_in, const int* in_sizes, int n_in, void* d_out, int out_size,
                              void* d_ws, size_t ws_size, hipStream_t stream) {
  const bf16* A_q = (const bf16*)d_in[0];
  const bf16* A_k = (const bf16*)d_in[1];
  const bf16* A_v = (const bf16*)d_in[2];
  const bf16* A_shift_k = (const bf16*)d_in[3];
  const bf16* A_ssm_k = (const bf16*)d_in[4];
  const bf16* A_ssm_D = (const bf16*)d_in[5];
  const bf16* w1 = (const bf16*)d_in[7];
  const bf16* w2 = (const bf16*)d_in[8];
  const bf16* w3 = (const bf16*)d_in[9];
  const bf16* w4 = (const bf16*)d_in[10];
  const bf16* c = (const bf16*)d_in[11];
  const bf16* w_mod = (const bf16*)d_in[12];
  char* ws = (char*)d_ws;
  (void)hipFuncSetAttribute((const void*)k_main, hipFuncAttributeMaxDynamicSharedMemorySize, L_TOT);
  k_main<<<256, 512, L_TOT, stream>>>(A_k, A_v, ws, A_shift_k, A_ssm_k, w_mod, c, w3, w4, A_q, w1, w2, A_ssm_D, (bf16*)d_out);
}
```

```cpp
#define RTE 7
#define QE 2
#define RD 1
#define RSW 1
#define JM 1
#define SLN 1
#define PF 3
#include <hip/hip_runtime.h>
#include <hip/hip_bf16.h>
#include <stdint.h>

typedef __hip_bfloat16 bf16;
typedef __attribute__((ext_vector_type(8))) short bf16x8;
typedef __attribute__((ext_vector_type(4))) short s16x4;
typedef __attribute__((ext_vector_type(4))) float f32x4;
typedef __attribute__((ext_vector_type(2))) float f32x2;
typedef __attribute__((ext_vector_type(4))) unsigned u32x4;
typedef __attribute__((ext_vector_type(2))) unsigned u32x2;
#define LAS __attribute__((address_space(3)))

static constexpr int S_ = 2048;
static constexpr int D_ = 640;
static constexpr int F_ = 1728;
static constexpr unsigned SENT = 0xffffffffu;
static constexpr int RS3 = 0x00020000;
static constexpr unsigned OOB = 0x40000000u;

static constexpr unsigned SD2 = S_ * D_ * 2;
static constexpr unsigned DF2 = D_ * F_ * 2;
static constexpr unsigned O_WT3 = 0, O_WT4 = DF2;
static constexpr unsigned O_EP = 2 * DF2;
static constexpr unsigned O_MOD = O_EP + 256;
static constexpr unsigned O_MP = O_MOD + 16384;
static constexpr unsigned O_FL1 = O_MP + 131072;
static constexpr unsigned O_F2 = O_FL1;
static constexpr unsigned O_KVT = O_FL1 + 4096;
static constexpr unsigned O_HP = O_KVT, O_X1 = O_KVT;
static constexpr unsigned TS = 57344;
static constexpr unsigned O_H = O_KVT + 384 * 8192;
static constexpr unsigned O_Z = O_H, O_H1 = O_H + 20480;
static constexpr unsigned O_ST3 = O_H + 128 * TS;
static constexpr unsigned O_ST4 = O_ST3 + S_ * 8;
static constexpr unsigned O_XO = O_ST4 + S_ * 16 * 8;
static constexpr unsigned O_W12 = O_XO + SD2;
static constexpr unsigned WS_USED = O_W12 + 256 * 5120;
#define KVROW(c) ((c) < 384u ? O_KVT + (c) * 8192u : O_H + 40960u + (((c) - 384u) >> 1) * TS + ((c) & 1u) * 8192u)
static constexpr unsigned CODED = 0x7f7f7f7fu;
static constexpr unsigned CSH = 98u << 23;
static constexpr float PTHR = 0x1p48f, PMAX = 0x1p47f, CMUL = 0x1p98f, CMAX = 0x1p127f;
__device__ __forceinline__ float plainv(float x) { return __builtin_amdgcn_fmed3f(x, -PMAX, PMAX); }
__device__ __forceinline__ float codedv(float x) { return __builtin_copysignf(__builtin_amdgcn_fmed3f(__builtin_fabsf(x * CMUL), PTHR, CMAX), x); }

__device__ __forceinline__ float rbf(float x) { return __bfloat162float(__float2bfloat16(x)); }
__device__ __forceinline__ float lo2f(unsigned w) { return __uint_as_float(w << 16); }
__device__ __forceinline__ float hi2f(unsigned w) { return __uint_as_float(w & 0xffff0000u); }
typedef __bf16 bf16v2 __attribute__((ext_vector_type(2)));
__device__ __forceinline__ unsigned pack2(float a, float b) {
  bf16v2 r = __builtin_convertvector(f32x2{a, b}, bf16v2);
  return __builtin_bit_cast(unsigned, r);
}

static constexpr float GC1 = -2.f * 0.7978845608028654f * 1.4426950408889634f;
static constexpr float GC2 = GC1 * 0.044715f;
__device__ __forceinline__ float row16_sum(float v) {
  v += __builtin_bit_cast(float, __builtin_amdgcn_update_dpp(0, __builtin_bit_cast(int, v), 0xB1, 0xf, 0xf, true));
  v += __builtin_bit_cast(float, __builtin_amdgcn_update_dpp(0, __builtin_bit_cast(int, v), 0x4E, 0xf, 0xf, true));
  v += __builtin_bit_cast(float, __builtin_amdgcn_update_dpp(0, __builtin_bit_cast(int, v), 0x141, 0xf, 0xf, true));
  v += __builtin_bit_cast(float, __builtin_amdgcn_update_dpp(0, __builtin_bit_cast(int, v), 0x140, 0xf, 0xf, true));
  return v;
}
typedef volatile LAS unsigned* ldsw_t;
static constexpr int CV_STR = 4160;
static constexpr int CV_U = 8 * CV_STR;
static constexpr int CV_SLOT = CV_U + 4608;
static constexpr int SL_A = 46592, SL_C = SL_A + CV_SLOT, SL_B = SL_C + CV_SLOT;
static constexpr int L_KV = 0;
static constexpr int L_RT = L_KV + 24576;
static constexpr int L_MP = L_RT + 20480;
static constexpr int L_XCH = 139264;
static constexpr int L_XA = 81920, L_XB = 40960;
static constexpr int L_PB = 122880;
static constexpr int L_CTL = 160256;
static constexpr int L_MOD = 103424;
static constexpr int L_GB = L_CTL + 64;
static constexpr int L_CS3 = L_CTL + 384;
static constexpr int L_ST4 = L_CTL + 1408;
static constexpr int L_STAMP = L_CTL + 2432;
static constexpr int L_TOT = 163840;
static_assert(L_MP + 1024 <= SL_A && SL_B + CV_SLOT <= L_CTL && L_MOD >= SL_C + 16384 && L_MOD >= 102400 && L_MOD + 7680 <= SL_C + CV_U, "lds");
static constexpr int YROW = 1296;
#define ZCE 0
enum { P_KV = 1, P_K, P_B, P_M, P_W, P_B2, P_M2, P_U, P_MD, P_C2, P_A2,
  P_RT, P_RO, P_GA, P_ZC,
  P_F2, P_TH, P_X, P_X2, P_T, P_Z, P_RP, P_Z2, P_L1, P_XF1, P_G1, P_XF2, P_G2, P_SF, P_SA, P_S0, P_KA, P_H0, P_S1, P_KB, P_H1, P_P0 };
static constexpr unsigned SPIN_CAP = 1u << 21;
static constexpr unsigned RETRY_CAP = 1u << 15;

__device__ __forceinline__ float swap_add16(float v) {
  const unsigned u = __float_as_uint(v);
  auto r = __builtin_amdgcn_permlane16_swap(u, u, false, false);
  return __uint_as_float((unsigned)r[0]) + __uint_as_float((unsigned)r[1]);
}
__device__ __forceinline__ float swap_add32(float v) {
  const unsigned u = __float_as_uint(v);
  auto r = __builtin_amdgcn_permlane32_swap(u, u, false, false);
  return __uint_as_float((unsigned)r[0]) + __uint_as_float((unsigned)r[1]);
}
__device__ __forceinline__ float wave_sum2(float v) { return swap_add32(swap_add16(row16_sum(v))); }
__device__ __forceinline__ f32x2 up2(unsigned u) { return f32x2{lo2f(u), hi2f(u)}; }
__device__ __forceinline__ f32x2 rbf2(f32x2 v) { return up2(pack2(v[0], v[1])); }
#define FMA2 __builtin_elementwise_fma
__device__ __forceinline__ f32x2 rf2(f32x2 v, f32x2 cp, f32x2 cn) { return FMA2(v, cn, FMA2(v, cp, v)); }
__device__ __forceinline__ void ln5(const f32x2* in, unsigned* out) {
  const f32x2 s2 = ((in[0] + in[1]) + (in[2] + in[3])) + in[4];
  const float mean = wave_sum2(s2[0] + s2[1]) * (1.f / (float)D_);
  f32x2 d[5];
#pragma unroll
  for (int j = 0; j < 5; ++j) d[j] = in[j] - mean;
  const f32x2 q2 = (d[0] * d[0] + d[1] * d[1]) + (d[2] * d[2] + d[3] * d[3]) + d[4] * d[4];
  const float m = rbf(mean);
#pragma unroll
  for (int j = 0; j < 5; ++j) d[j] = rbf2(in[j] - m);
  const float q = wave_sum2(q2[0] + q2[1]);
  const float var = rbf(q * (1.f / (float)D_));
  const float den = rbf(__builtin_amdgcn_sqrtf(rbf(var + 1e-5f)));
  const float rinv = __builtin_amdgcn_rcpf(den);
#pragma unroll
  for (int j = 0; j < 5; ++j) { const f32x2 o = d[j] * rinv; out[j] = pack2(o[0], o[1]); }
}
#define PIN(x) asm volatile("" : "+v"(x))
#define STG(N, stmt) do { _Pragma("unroll") for (int i = 0; i < N; ++i) { stmt; } __builtin_amdgcn_sched_barrier(0); } while (0)
__device__ __forceinline__ void ln5s(const unsigned* in, unsigned* out) {
  float x[10], d[10], t[4];
  STG(10, x[i] = (i & 1) ? hi2f(in[i >> 1]) : lo2f(in[i >> 1]); PIN(x[i]));
  STG(4, t[i] = x[(i & 1) + (i >> 1) * 4] + x[(i & 1) + (i >> 1) * 4 + 2]; PIN(t[i]));
  STG(2, t[i] += t[2 + i]; PIN(t[i]));
  STG(2, t[i] += x[8 + i]; PIN(t[i]));
  const float mean = wave_sum2(t[0] + t[1]) * (1.f / (float)D_);
  STG(10, d[i] = x[i] - mean; PIN(d[i]));
  STG(4, t[i] = d[(i & 1) + (i >> 1) * 4] * d[(i & 1) + (i >> 1) * 4]; PIN(t[i]));
  STG(4, t[i] = __builtin_fmaf(d[(i & 1) + (i >> 1) * 4 + 2], d[(i & 1) + (i >> 1) * 4 + 2], t[i]); PIN(t[i]));
  STG(2, t[i] += t[2 + i]; PIN(t[i]));
  STG(2, t[i] = __builtin_fmaf(d[8 + i], d[8 + i], t[i]); PIN(t[i]));
  const float m = rbf(mean);
  STG(10, d[i] = x[i] - m; PIN(d[i]));
  unsigned p[5];
  STG(5, p[i] = pack2(d[2 * i], d[2 * i + 1]); PIN(p[i]));
  const float q = wave_sum2(t[0] + t[1]);
  const float var = rbf(q * (1.f / (float)D_));
  const float den = rbf(__builtin_amdgcn_sqrtf(rbf(var + 1e-5f)));
  const float rinv = __builtin_amdgcn_rcpf(den);
  STG(10, d[i] = ((i & 1) ? hi2f(p[i >> 1]) : lo2f(p[i >> 1])) * rinv; PIN(d[i]));
  STG(5, out[i] = pack2(d[2 * i], d[2 * i + 1]); PIN(out[i]));
}
template <int N>
__device__ __forceinline__ void wave_sums(float* v) {
  STG(N, v[i] += __builtin_bit_cast(float, __builtin_amdgcn_update_dpp(0, __builtin_bit_cast(int, v[i]), 0xB1, 0xf, 0xf, true)); PIN(v[i]));
  STG(N, v[i] += __builtin_bit_cast(float, __builtin_amdgcn_update_dpp(0, __builtin_bit_cast(int, v[i]), 0x4E, 0xf, 0xf, true)); PIN(v[i]));
  STG(N, v[i] += __builtin_bit_cast(float, __builtin_amdgcn_update_dpp(0, __builtin_bit_cast(int, v[i]), 0x141, 0xf, 0xf, true)); PIN(v[i]));
  STG(N, v[i] += __builtin_bit_cast(float, __builtin_amdgcn_update_dpp(0, __builtin_bit_cast(int, v[i]), 0x140, 0xf, 0xf, true)); PIN(v[i]));
  unsigned a[N], b[N];
  STG(N, a[i] = __float_as_uint(v[i]); b[i] = a[i]; PIN(b[i]));
  STG(N, { auto r = __builtin_amdgcn_permlane16_swap(a[i], b[i], false, false); a[i] = r[0]; b[i] = r[1]; PIN(a[i]); PIN(b[i]); });
  STG(N, v[i] = __uint_as_float(a[i]) + __uint_as_float(b[i]); PIN(v[i]));
  STG(N, a[i] = __float_as_uint(v[i]); b[i] = a[i]; PIN(b[i]));
  STG(N, { auto r = __builtin_amdgcn_permlane32_swap(a[i], b[i], false, false); a[i] = r[0]; b[i] = r[1]; PIN(a[i]); PIN(b[i]); });
  STG(N, v[i] = __uint_as_float(a[i]) + __uint_as_float(b[i]); PIN(v[i]));
}
template <int N>
__device__ __forceinline__ void gelu_s(float* e, float ga_, float gb2, float gd, unsigned* pk) {
  float x[N];
  STG(N, x[i] = e[i] * e[i]; PIN(x[i]));
  STG(N, x[i] = __builtin_fmaf(x[i], gb2, ga_); PIN(x[i]));
  STG(N, x[i] *= e[i]; PIN(x[i]));
  STG(N, x[i] = __builtin_amdgcn_exp2f(x[i]); PIN(x[i]));
  STG(N, x[i] = __builtin_fmaf(x[i], gd, gd); PIN(x[i]));
  STG(N, x[i] = __builtin_amdgcn_rcpf(x[i]); PIN(x[i]));
  STG(N, e[i] *= x[i]; PIN(e[i]));
  STG(N / 4, e[4 * i + 1] = plainv(e[4 * i + 1]); PIN(e[4 * i + 1]));
  STG(N / 2, pk[i] = pack2(e[2 * i], e[2 * i + 1]); PIN(pk[i]));
}
__device__ __forceinline__ f32x4 mfma16(u32x4 a, u32x4 b, f32x4 c) {
  return __builtin_amdgcn_mfma_f32_16x16x32_bf16(__builtin_bit_cast(bf16x8, a), __builtin_bit_cast(bf16x8, b), c, 0, 0, 0);
}
template <bool FIRST> __device__ __forceinline__ void mf(f32x4& c, const u32x4& a, const u32x4& b) {
  if (FIRST) asm volatile("v_mfma_f32_16x16x32_bf16 %0, %1, %2, 0" : "=v"(c) : "v"(a), "v"(b));
  else asm volatile("v_mfma_f32_16x16x32_bf16 %0, %1, %2, %0" : "+v"(c) : "v"(a), "v"(b));
}
__device__ __forceinline__ unsigned canon(unsigned x) { return x < 0xfffffffeu ? x : 0xfffffffeu; }
__device__ __forceinline__ bool bad1(unsigned a) { return __builtin_amdgcn_ballot_w64(a == SENT) != 0ull; }
__device__ __forceinline__ float amax2(const u32x4& a) { return __builtin_amdgcn_fmed3f(__builtin_fabsf(__uint_as_float(a[0])), __builtin_fabsf(__uint_as_float(a[2])), CMAX); }
__device__ __forceinline__ float amin2(const u32x4& a) { return __builtin_amdgcn_fmed3f(__builtin_fabsf(__uint_as_float(a[0])), __builtin_fabsf(__uint_as_float(a[2])), 0.f); }
__device__ __forceinline__ float amax3(float m, const u32x4& a) { float r; asm("v_max3_f32 %0, %1, |%2|, |%3|" : "=v"(r) : "v"(m), "v"(a[0]), "v"(a[2])); return r; }
template <bool CD> __device__ __forceinline__ bool any_bad4(u32x4& a, float thr, unsigned csh, float cmx) {
  const float m = __builtin_amdgcn_fmed3f(__builtin_fabsf(__uint_as_float(a[0])), __builtin_fabsf(__uint_as_float(a[2])), CD ? 0.f : cmx);
  if (CD) { a[0] -= csh; a[2] -= csh; }
  return __builtin_amdgcn_ballot_w64(CD ? m < thr : m >= thr) != 0ull;
}
__device__ __forceinline__ u32x4 widen(unsigned x0, unsigned x1, unsigned y0, unsigned y1) {
  const auto p0 = __builtin_amdgcn_permlane16_swap(x0, y0, false, false);
  const auto p1 = __builtin_amdgcn_permlane16_swap(x1, y1, false, false);
  return u32x4{(unsigned)p0[0], (unsigned)p1[0], (unsigned)p0[1], (unsigned)p1[1]};
}
#define DMA1(M) asm volatile("s_mov_b32 m0, %3\n\ts_nop 0\n\t" "buffer_load_dwordx4 %0, %1, %2 offen " M "lds" :: "v"(voff), "s"(rs), "s"(soff), "s"(dst) : "memory")
#define DMA4(M) asm volatile("s_mov_b32 m0, %3\n\ts_nop 0\n\t" \
                   "buffer_load_dwordx4 %0, %1, %2 offen " M "lds\n\tbuffer_load_dwordx4 %0, %1, %2 offen offset:1024 " M "lds\n\t" \
                   "buffer_load_dwordx4 %0, %1, %2 offen offset:2048 " M "lds\n\tbuffer_load_dwordx4 %0, %1, %2 offen offset:3072 " M "lds" \
                   :: "v"(voff), "s"(rs), "s"(soff), "s"(dst) : "memory")
template <int N, int AUX>
__device__ __forceinline__ void dma(unsigned voff, u32x4 rs, unsigned soff, unsigned dst) {
  if (N == 4) { if (AUX == 1) DMA4("sc1 "); else if (AUX == 2) DMA4("nt "); else DMA4(""); }
  else { if (AUX == 1) DMA1("sc1 "); else if (AUX == 2) DMA1("nt "); else DMA1(""); }
}
__device__ __forceinline__ void st128(u32x4 v, __amdgpu_buffer_rsrc_t r, unsigned voff, unsigned base, int) {
  __builtin_amdgcn_raw_buffer_store_b128(v, r, voff + base, 0, 16);
}
__device__ __forceinline__ u32x4 mk_rs(const void* p, unsigned bytes) {
  const unsigned long long a = (unsigned long long)p;
  return u32x4{(unsigned)a, (unsigned)(a >> 32) & 0xffffu, bytes, (unsigned)RS3};
}

#define USLEEP 2
#define DLY0 8
#define DLY1 3
#define DLY2 0
#define DLY3 2
#define NWH 8
#define ZG 8
#define ZSLEEP 2
#define AW4 8
#define AD3 3
#define RSLEEP 1
#define PHI 2
#define CPW 4
#define CPG 128u
#define W12 0
#define ML 0
#define TW 0
#define LNL 0
#define F1L 0
#define UL 0
#define WI0 -1
#define WIS 8
#define WIE 0
#define HP 0
#define ZP 0
#define ZPN NGA
#define ZCG 0
#define WE 0
#define WEN 10
#define QG 0
#define PFP 0
#define LNX 2
#define MB 0
#define R7 0
#define WMD 0
#define WME 0
#define FZ 0
#define WDL 0
#define WSLEEP 2
#define LINW 0
#define NZ2 5
#define NS2 5
#define RS0 6
#define DLY3B 0
#define MV0 7
#define H0S 6
#define H1S 3
#define PK0 2
#define PE0 1
#define PK1 2
#define PE1 1
#define P40 1
#define P41 2
#define GSA 2
#define GSB 1
#define GSC 2
#define EA0 4
#define EA1 28
#define EC0 4
#define EC1 22
#define PB1 26
#define PB2 20
#define NL1 14
#define NP1 2
#define NL2 12
#define ROT 1
#define NA1 3
#define NP0 0
#define SL1 6
#define SL2 6
#define PH1 2
#define PH2 2
#define KG 42
#define KC3 12
#define PK2 2
#define KC3B 40
#define NPK 10
#define HG 4
#define WSP 3
#define PR1 3
#define HPZ 8
#define WS3 1
#define RB1 0
#define HFI 1
#define GZ 0
#define GZP 0
#define RPH 0
#define NGB 5
#define NS1 0
static_assert(!GZ || ZCE, "order of the progress words");
#define GSLEEP 4
#define TR 0
#define MGE 0
#define TZ 0
#define KE 0
#define TU1 200
#define TU2 260
#define TU 290
#define KL 5
#define PBE 18
#define NAF 6
#define W0U 4
#define U0 5
#define NPH 6
#define NGA 5
#define NR0 2
#define NR1 5
#define KVR 0
static_assert((NWH + NL1 + NP1) % 2 == 0 && NL2 % 2 == 0 && NWH + NL1 + NP1 + NL2 <= 40, "w3 schedule");
#define SPIN_STEP() do { ++spins; asm volatile("" : "+s"(spins)); } while (0)
#define SB() __builtin_amdgcn_sched_barrier(0)
#define FENCE() do { asm volatile("" ::: "memory"); __builtin_amdgcn_sched_barrier(0); } while (0)
#define LGKM0() do { __builtin_amdgcn_sched_barrier(0); asm volatile("s_waitcnt lgkmcnt(0)" ::: "memory"); } while (0)
#define BARRIER() do { LGKM0(); __builtin_amdgcn_s_barrier(); asm volatile("" ::: "memory"); } while (0)
#define VM0() __builtin_amdgcn_s_waitcnt(0x0F70)
#define PRIO(n) __builtin_amdgcn_s_setprio(n)
#define CHECK_A(CD, AV, i, hi, LD)                                                          \
  do {                                                                                      \
    if (__builtin_expect(any_bad4<CD>(AV, sthr, scsh, scmx), 0)) {                          \
      unsigned spins = 0;                                                                   \
      PRIO(0);                                                                              \
      do {                                                                                  \
        __builtin_amdgcn_s_sleep(RSLEEP);                                                   \
        _Pragma("unroll") for (int y_ = (i); y_ < (hi); ++y_) { LD(y_); }                   \
        SPIN_STEP();                                                                        \
      } while (any_bad4<CD>(AV, sthr, scsh, scmx) && spins < RETRY_CAP);                    \
      PRIO(PHI);                                                                            \
      VM0();                                                                                \
    }                                                                                       \
  } while (0)

#define ASM_MACROS \
  ".macro regno op\n.set r_, 0\n.set st_, 0\n.irpc c, \"\\op\"\n.ifc \\c, :\n.set st_, 2\n.endif\n.if st_ == 1\n.set r_, r_ * 10 + \\c\n.endif\n.ifc \\c, [\n.set st_, 1\n.endif\n.endr\n.endm\n" \
  ".macro retry n, cd, a, so, ct, t, vo, rs, sb, im, th, k, pr\n"                                   \
  "s_cbranch_vccnz Lc\\n\nLb\\n:\n.subsection 1\nLc\\n:\nregno \\a\n"              \
  "s_setprio 0\ns_add_u32 \\so, \\sb, \\im\ns_mov_b32 \\ct, 0\n"                                        \
  "Ll\\n:\ns_sleep 1\nbuffer_load_dwordx4 \\a, \\vo, \\rs, \\so offen sc1\ns_add_u32 \\ct, \\ct, 1\ns_waitcnt vmcnt(0)\n" \
  ".if \\cd\nv_med3_f32 \\t, |v[r_]|, |v[r_+2]|, 0\nv_cmp_gt_f32 vcc, \\th, \\t\n.else\nv_med3_f32 \\t, |v[r_]|, |v[r_+2]|, \\k\nv_cmp_le_f32 vcc, \\th, \\t\n.endif\n" \
  "s_cbranch_vccz Ld\\n\ns_cmp_lt_u32 \\ct, 0x8000\ns_cbranch_scc1 Ll\\n\n"                             \
  "Ld\\n:\n.if \\cd\nv_subrev_u32 v[r_], \\k, v[r_]\nv_subrev_u32 v[r_+2], \\k, v[r_+2]\n.endif\ns_setprio \\pr\ns_nop 1\ns_branch Lb\\n\n.subsection 0\n.endm\n" \
  ".macro need n, v, ct, ad, k\n"                                                                       \
  "v_cmp_gt_u32 vcc, \\k, \\v\ns_cbranch_vccnz Lc\\n\nLb\\n:\n.subsection 1\nLc\\n:\ns_mov_b32 \\ct, 0\n" \
  "Ll\\n:\nds_read_b32 \\v, \\ad\ns_waitcnt lgkmcnt(0)\nv_cmp_gt_u32 vcc, \\k, \\v\ns_cbranch_vccz Lb\\n\n" \
  "s_sleep 1\ns_add_u32 \\ct, \\ct, 1\ns_cmp_lt_u32 \\ct, 0x200000\ns_cbranch_scc1 Ll\\n\ns_branch Lb\\n\n.subsection 0\n.endm\n"
#define RETRY(CD, AV, BM, VOFF, SBASE, IMM, PR)                                                         \
  do {                                                                                                  \
    unsigned so_, ct_; float t_; register unsigned long long vc_ asm("vcc") = (BM);                                                                     \
    asm volatile("retry %=, %5, \"%0\", %1, %2, %3, %6, \"%7\", %8, %9, %10, %11, %12"                   \
                 : "+v"(AV), "=&s"(so_), "=&s"(ct_), "=&v"(t_), "+s"(vc_)                            \
                 : "i"((int)(CD)), "v"(VOFF), "s"(dW), "s"(SBASE), "i"(IMM), "s"(sthr), "s"((CD) ? scsh : __float_as_uint(scmx)), "i"(PR) : "scc", "memory"); \
  } while (0)
#define CMP_BAD(CD, BM, M) do { register unsigned long long vc_ asm("vcc");                             \
    if (CD) asm volatile("v_cmp_gt_f32 vcc, %1, %2" : "=s"(vc_) : "s"(sthr), "v"(M)); else asm volatile("v_cmp_le_f32 vcc, %1, %2" : "=s"(vc_) : "s"(sthr), "v"(M)); \
    BM = vc_; } while (0)
#define RETRY_C(AV, BM, VOFF, SBASE, IMM, PR) RETRY(1, AV, BM, VOFF, SBASE, IMM, PR)
#define NEED_AT(V, ADDR, K) do { unsigned ct_; asm volatile("need %=, %0, %1, %2, %3" : "+v"(V), "=&s"(ct_) : "v"(ADDR), "s"(K) : "vcc", "scc", "memory"); } while (0)

#define STAMP(i)
#define CLK(i)
#define KATTR
#define TS_(i)
#define TX_(i)
#define TSHI 62
__device__ __forceinline__ u32x4 rd_tr(const LAS char* p) {
  const s16x4 lo = __builtin_amdgcn_ds_read_tr16_b64_v4i16((LAS s16x4*)p);
  const s16x4 hi = __builtin_amdgcn_ds_read_tr16_b64_v4i16((LAS s16x4*)(p + 2560));
  const bf16x8 o = {lo[0], lo[1], lo[2], lo[3], hi[0], hi[1], hi[2], hi[3]};
  return __builtin_bit_cast(u32x4, o);
}
__device__ __forceinline__ float dpp_add(float v, int ctrl) {
  return v + __builtin_bit_cast(float, ctrl == 0 ? __builtin_amdgcn_update_dpp(0, __builtin_bit_cast(int, v), 0x128, 0xf, 0xf, true)
                                    : ctrl == 1 ? __builtin_amdgcn_update_dpp(0, __builtin_bit_cast(int, v), 0x124, 0xf, 0xf, true)
                                                : __builtin_amdgcn_update_dpp(0, __builtin_bit_cast(int, v), 0x4E, 0xf, 0xf, true));
}

template <int V> struct IC { static constexpr int v = V; };
template <int I, int N, class F> __device__ __forceinline__ void sfor(F&& f) { if constexpr (I < N) { f(IC<I>{}); sfor<I + 1, N>(f); } }
constexpr int cv_i(int m) { int i = 0; while (m >= 8 - i) { m -= 8 - i; ++i; } return i; }
constexpr int cv_d(int m) { int i = 0; while (m >= 8 - i) { m -= 8 - i; ++i; } return 7 - i - m; }
constexpr int kl_i(int m) { return m < 50 - 5 * KL ? m / 5 : m < 50 - 2 * KL ? 10 - KL + (m - 50 + 5 * KL) / 3 : 10 - KL + (m - 50 + 2 * KL) / 2; }
constexpr int kl_j(int m) { return m < 50 - 5 * KL ? m % 5 : m < 50 - 2 * KL ? (m - 50 + 5 * KL) % 3 : 3 + (m - 50 + 2 * KL) % 2; }
constexpr int cl_t(int m) { int t = 0; while (m > t) { m -= t + 1; ++t; } return t; }
constexpr int cl_i(int m) { int t = 0; while (m > t) { m -= t + 1; ++t; } return m; }
constexpr bool cv_free(int m, int k) {
  return k == 0 ? (m >= 64 && m < 111 && m != 65 && m != 69 && m != 72 && m != 75 && m != 77 && m != 94 && m != 95 && m != 104 && m != 110 && (m < 72 + RS0 || m >= 88 + RS0))
                : (m >= 123 && m != 126 && m != 131 && m != 132 && m != 139 && m != 140);
}
constexpr int cv_slot(int m, int k) { if (!cv_free(m, k)) return -1; int n = 0; for (int y = 0; y < m; ++y) n += cv_free(y, k); return n; }
static_assert(cv_slot(109, 0) >= 15 && cv_slot(143, 1) >= 15, "slots");
constexpr int cc_i(int J, int c) { for (int i = 0; i < 8; ++i) for (int t = 3; t >= 0; --t) if (i <= ((J >> (4 * t)) & 15) && c-- == 0) return i; return 0; }
constexpr int cc_t(int J, int c) { for (int i = 0; i < 8; ++i) for (int t = 3; t >= 0; --t) if (i <= ((J >> (4 * t)) & 15) && c-- == 0) return t; return 0; }
#define KWAIT
__global__ __launch_bounds__(512, 2) KATTR void k_main(const bf16* kin_, const bf16* vin_, char* ws_, const bf16* shift_k_, const bf16* ssm_k_,
                                                 const bf16* w_mod_, const bf16* cin_, const bf16* w3_,
                                                 const bf16* w4_, const bf16* q_, const bf16* w1_, const bf16* w2_,
                                                 const bf16* ssm_D_, bf16* out_) {
  typedef __attribute__((ext_vector_type(8))) unsigned long long u64x8;
  typedef __attribute__((ext_vector_type(4))) unsigned long long u64x4;
  typedef __attribute__((ext_vector_type(2))) unsigned long long u64x2;
  u64x8 ka;
  u64x4 kb;
  u64x2 kc;
  int tid_ = threadIdx.x, bx_ = blockIdx.x;
  unsigned long long pc0;
  unsigned code_bytes;
  asm volatile("s_load_dwordx16 %0, %6, 0x0\n\ts_load_dwordx8 %1, %6, 0x40\n\ts_load_dwordx4 %2, %6, 0x60\n\ts_getpc_b64 %3\n"
               "Lpc0:\n\ts_mov_b32 %4, Lcold0 - Lpc0\n" KWAIT ".subsection 1\nLcold0:\n.subsection 0"
               : "=&s"(ka), "=&s"(kb), "=&s"(kc), "=&s"(pc0), "=&s"(code_bytes), "+s"(bx_) : "s"(__builtin_amdgcn_kernarg_segment_ptr()));
  asm volatile(ASM_MACROS ".subsection 2\n.fill 256, 4, 0xbf800000\n.subsection 0" : "+v"(tid_));
  extern __shared__ __attribute__((aligned(16))) char lds_[];
  LAS char* lds = (LAS char*)lds_;
  ldsw_t LW = (ldsw_t)(lds + L_CTL);
  const int tid = tid_ & 511, lane = tid & 63;
  const int wv = __builtin_amdgcn_readfirstlane(tid >> 6) & 7;
  const int bx = bx_ & 255;
  const int mt = (bx & 7) * 4 + (bx >> 6), nt = (bx >> 3) & 7;
  const int n0 = nt * 80, m0 = mt * 64;
  const int l15 = lane & 15, g = lane >> 4;
  const int mg = wv & 3, kh = wv >> 2;
  const int gm = (wv >> 2) * 2 + (wv & 1), gk = ((wv >> 1) ^ (wv >> 2)) & 1, gp = wv ^ 2;
  const unsigned l16 = (unsigned)lane * 16u;
  TS_(0);
  STAMP(0);
  CLK(30);
  unsigned cp0 = 0;
  if (wv == CPW) {
    asm volatile("buffer_load_dword %0, %1, %2, 0 offen" : "=v"(cp0) : "v"((unsigned)((bx >> 3) + 32 * lane) * CPG), "s"(mk_rs((const void*)pc0, code_bytes)) : "memory");
  }
  FENCE();
  const int tok0 = 32 * ((bx & 7) * 8 + (bx >> 5)), ch0 = 160 * ((bx >> 3) & 3);
  unsigned ko[3], vo[3];
#pragma unroll
  for (int i = 0; i < 3; ++i) {
    const unsigned s = (unsigned)((mg + 4 * i) * 64 + lane);
    const unsigned gr = __umul24(s, 800u) >> 16, w = s - __umul24(82u, gr), rr = __umul24(w, 205u) >> 12;
    unsigned o = (unsigned)(tok0 * (D_ * 2) + ch0 * 2) + __umul24(gr, 4u * D_ * 2) + __umul24(rr, D_ * 2 - 320) + w * 16u;
    asm volatile("" : "+v"(o));
    ko[i] = (w < 80u && gr < 9u) ? o - 4u * D_ * 2 : OOB;
    vo[i] = (w < 80u && gr < 8u) ? o : OOB;
    asm volatile("" : "+v"(ko[i]), "+v"(vo[i]));
  }
  FENCE();
  asm volatile("s_waitcnt lgkmcnt(0)" : "+s"(ka), "+s"(kb), "+s"(kc));
  const bf16* kin = (const bf16*)ka[0];
  const bf16* vin = (const bf16*)ka[1];
  char* ws = (char*)ka[2];
  const bf16* shift_k = (const bf16*)ka[3];
  const bf16* ssm_k = (const bf16*)ka[4];
  const bf16* w_mod = (const bf16*)ka[5];
  const bf16* cin = (const bf16*)ka[6];
  const bf16* w3 = (const bf16*)ka[7];
  const bf16* w4 = (const bf16*)kb[0];
  const bf16* q = (const bf16*)kb[1];
  const bf16* w1 = (const bf16*)kb[2];
  const bf16* w2 = (const bf16*)kb[3];
  const bf16* ssm_D = (const bf16*)kc[0];
  bf16* out = (bf16*)kc[1];
  const __amdgpu_buffer_rsrc_t rW = __builtin_amdgcn_make_buffer_rsrc((void*)ws, 0, WS_USED, RS3);
  const u32x4 dW = mk_rs(ws, WS_USED);
  u32x2 ep;
  asm volatile("buffer_load_dwordx2 %0, off, %1, %2 sc1" : "=v"(ep) : "s"(dW), "s"(O_EP) : "memory");
  FENCE();
  if (kh == 0) {
    const u32x4 dK = mk_rs(kin, SD2), dV = mk_rs(vin, SD2);
#pragma unroll
    for (int i = 0; i < 3; ++i) dma<1, 0>(ko[i], dK, 0u, (unsigned)(L_KV + (mg + 4 * i) * 1024));
#pragma unroll
    for (int i = 0; i < 3; ++i) dma<1, 0>(vo[i], dV, 0u, (unsigned)(L_KV + 12288 + (mg + 4 * i) * 1024));
  }
  u32x4 sk[3];
  if (kh == 0) {
    asm volatile("buffer_load_dwordx4 %0, %3, %5, %6 offen\n\tbuffer_load_dwordx4 %1, %3, %5, %6 offen offset:16\n\tbuffer_load_dwordx4 %2, %4, %5, %7 offen"
                 : "=&v"(sk[0]), "=&v"(sk[1]), "=&v"(sk[2])
                 : "v"(((unsigned)lane >> 3) * 32u), "v"((unsigned)g * 16u), "s"(mk_rs(shift_k, D_ * 8)), "s"(ch0 * 8 + mg * 256), "s"(ch0 * 8 + 1024 + mg * 64) : "memory");
  }
  FENCE();
  if (tid >= 496) LW[tid - 496] = 0u;
  if (tid >= 256 && tid < 352) *(LAS u32x4*)(lds + SL_A + ((tid - 256) >> 5) * CV_SLOT + CV_U + (tid & 31) * 16) = u32x4{0u, 0u, 0u, 0u};
  if (tid >= 384 && tid < 408) *(LAS u32x4*)(lds + SL_A + ((tid - 384) >> 3) * CV_SLOT + (tid & 7) * CV_STR + 257 * 16) = u32x4{0u, 0u, 0u, 0u};
  STAMP(29);
  if constexpr (TSHI == 23) TX_(30);
  BARRIER();

  const unsigned psig = (unsigned)L_CTL + (unsigned)wv * 4u;
#define SIG(k) do { unsigned long long sv_; asm volatile("s_mov_b64 %0, exec\n\ts_mov_b64 exec, 1\n\tds_write_b32 %1, %2\n\ts_mov_b64 exec, %0" : "=&s"(sv_) : "v"(psig), "v"((unsigned)(k)) : "memory"); } while (0)
#define WAIT_AT(addr, k)                                                                                \
  do {                                                                                                  \
    unsigned tv_, tc_;                                                                                  \
    asm volatile("s_mov_b32 %1, 0\n"                                                                    \
                 "L%=_a:\n\tds_read_b32 %0, %2\n\ts_waitcnt lgkmcnt(0)\n\tv_cmp_gt_u32 vcc, %3, %0\n\ts_cbranch_vccz L%=_b\n\t"    \
                 "s_sleep 1\n\ts_add_u32 %1, %1, 1\n\ts_cmp_lt_u32 %1, 0x200000\n\ts_cbranch_scc1 L%=_a\n"            \
                 "L%=_b:" : "=&v"(tv_), "=&s"(tc_) : "v"(addr), "i"(k) : "vcc", "scc", "memory");       \
  } while (0)
#define WAIT_2(k0, k1)                                                                                 \
  do {                                                                                                  \
    unsigned tv_, tc_;                                                                                  \
    asm volatile("s_mov_b32 %1, 0\n"                                                                    \
                 "L%=_a:\n\tds_read_b32 %0, %2\n\ts_waitcnt lgkmcnt(0)\n\tv_cmp_gt_u32 vcc, %3, %0\n\ts_cbranch_vccz L%=_b\n\t"    \
                 "s_sleep 1\n\ts_add_u32 %1, %1, 1\n\ts_cmp_lt_u32 %1, 0x200000\n\ts_cbranch_scc1 L%=_a\n"            \
                 "L%=_b:" : "=&v"(tv_), "=&s"(tc_) : "v"((unsigned)L_CTL + ((unsigned)lane & 7u) * 4u), "v"((lane & 4) ? (unsigned)(k1) : (unsigned)(k0)) : "vcc", "scc", "memory"); \
  } while (0)
#define WAIT_ALL(k) WAIT_AT((unsigned)L_CTL + ((unsigned)lane & 7u) * 4u, k)
#define WAIT_HALF(h, k) WAIT_AT((unsigned)L_CTL + ((unsigned)lane & 3u) * 4u + (unsigned)(h) * 16u, k)

  const int sb3 = nt < 6 ? 14 * nt : 84 + 12 * (nt - 6);
  const bool two = kh == 0 || nt < 6;
  const int jl = kh ? 4 + wv : 2 * wv, jm = kh ? 12 + (wv & 1) : 2 * wv + 1, hb = wv > 5 ? 1 : 0;
  const int k4 = 7 * wv - (wv > 5 ? 2 : wv > 4 ? 1 : 0);
  const unsigned w4a = O_WT4 + (unsigned)(nt * 54 + k4) * 5120u;
  unsigned w3s[4] = {O_WT3 + (unsigned)(sb3 + jl) * 20480u + (unsigned)hb * 10240u, two ? O_WT3 + (unsigned)(sb3 + jm) * 20480u + (unsigned)hb * 10240u : OOB,
                     O_WT3 + (unsigned)(sb3 + jl) * 20480u - (unsigned)hb * 10240u, kh ? w4a - 10240u : O_WT3 + (unsigned)(sb3 + jm) * 20480u};
  f32x2 cp = {65536.f, 65536.f}, cn = {-65536.f, -65536.f};
  asm volatile("" : "+s"(cp), "+s"(cn));
  u32x4 W3[40];
#define LD_W3U(f) W3[f] = __builtin_amdgcn_raw_buffer_load_b128(rW, l16 + ((f) < 8 ? ((f) >> 1) * 1024 : 0), w3s[((f) & 1) + ((f) >= 20 ? 2 : 0)] + ((f) < 8 ? 0 : ((f) >> 1) * 1024), 16)
#define LD_W3R(a, b) do { _Pragma("unroll") for (int f_ = (a); f_ < (b); ++f_) LD_W3U(f_); } while (0)
#define VMW(n) asm volatile("s_waitcnt vmcnt(%0)" :: "i"(n) : "memory")
  const __amdgpu_buffer_rsrc_t rW1 = __builtin_amdgcn_make_buffer_rsrc((void*)w1, 0, D_ * D_ * 2, RS3);
  const __amdgpu_buffer_rsrc_t rW2 = __builtin_amdgcn_make_buffer_rsrc((void*)w2, 0, D_ * D_ * 2, RS3);
  const u32x4 dW1 = mk_rs(w1, D_ * D_ * 2), dW2 = mk_rs(w2, D_ * D_ * 2);
#define W12_G(gw) unsigned gw[5]; { unsigned ln_ = (unsigned)lane; asm volatile("" : "+v"(ln_)); \
    _Pragma("unroll") for (int j_ = 0; j_ < 5; ++j_) { const unsigned p_ = ln_ + 64u * j_, r_ = __umul24(p_, 6554u) >> 16; \
      gw[j_] = __umul24((r_ & 12u) * 2u + (r_ >> 4) * 4u + (r_ & 3u), D_ * 2) + (p_ - __umul24(r_, 10u)) * 16u + (unsigned)n0 * 2u; } }

  const int s0 = m0 + nt * 8;
  const int srow = s0 + wv;
  unsigned qw[5], hw[5];
  unsigned long long epn;
  unsigned tA, tB;
  u32x4 WS2[10];
#define LD_WS2U(a, b) _Pragma("unroll") for (int y_ = (a); y_ < (b); ++y_) asm volatile("buffer_load_dwordx4 %0, %1, %2, %3 offen ; ws2" : "=v"(WS2[y_]) : "v"(gw[y_ % 5]), "s"(dW2), "s"((wv + 8 * (y_ / 5)) * (32 * D_ * 2)) : "memory")
#define LD_WS2(a, b) _Pragma("unroll") for (int y_ = (a); y_ < (b); ++y_) WS2[y_] = __builtin_amdgcn_raw_buffer_load_b128(rW2, gw[y_ % 5], (wv + 8 * (y_ / 5)) * (32 * D_ * 2), 0)
  unsigned ry = (unsigned)(L_XCH + wv * YROW) + l16, rz = (unsigned)(L_XCH + wv * YROW + 1024 + 4 * lane);
  f32x2 xq[5], ha[5], ca[5], sb[5];
  float mc, mb;
  const int row = m0 + 16 * gm + l15;
  const int jb = 3 * gk;
  const unsigned ofr = (unsigned)(row >> 4) * 20480u + (unsigned)((n0 + 16 * jb + 4 * g) >> 3) * 256u + (unsigned)(g & 1) * 8u + (unsigned)l15 * 16u;
  {
    const int pi = (bx & 7) + 8 * (bx >> 4), half = (bx >> 3) & 1;
    const int chA = 2 * bx, chC = 512 + pi;
#define RD_MOD(v) do { const u32x4 b_ = *(const LAS u32x4*)(lds + L_MOD + (v) * (D_ * 2) + l16); mw[v][0] = b_[0]; mw[v][1] = b_[1]; mw[v][2] = b_[2]; mw[v][3] = b_[3]; \
                       mw[v][4] = *(const LAS unsigned*)(lds + L_MOD + (v) * (D_ * 2) + 1024 + lane * 4); } while (0)
#define LN1()                                                                                           \
  do {                                                                                                  \
    f32x2 xq_[5];                                                                                       \
    _Pragma("unroll") for (int j = 0; j < 5; ++j) xq_[j] = up2(qw[j]);                                  \
    ln5(xq_, hw);                                                                                       \
    _Pragma("unroll") for (int j = 0; j < 5; ++j) asm volatile("" : "+v"(hw[j]));                       \
  } while (0)
#define LN1H() do { if (SLN) ln5s(qw, hw); else LN1(); } while (0)
#define R_PRE() do {                                                                                    \
      unsigned mw[5][5];                                                                                \
      _Pragma("unroll") for (int v = 0; v < 5; ++v) RD_MOD(v);                                          \
      _Pragma("unroll") for (int j = 0; j < 5; ++j) {                                                   \
        xq[j] = up2(qw[j]);                                                                             \
        ha[j] = FMA2(up2(hw[j]), up2(mw[1][j]), up2(mw[0][j])) * up2(mw[2][j]); ca[j] = up2(mw[4][j]); sb[j] = up2(mw[3][j]); \
        asm volatile("" : "+v"(xq[j]), "+v"(ha[j]), "+v"(ca[j]), "+v"(sb[j]));                          \
      }                                                                                                 \
      const f32x2 c2_ = ((ca[0] + ca[1]) + (ca[2] + ca[3])) + ca[4], b2_ = ((sb[0] + sb[1]) + (sb[2] + sb[3])) + sb[4]; \
      mc = wave_sum2(c2_[0] + c2_[1]) * (1.f / (float)D_); mb = wave_sum2(b2_[0] + b2_[1]) * (1.f / (float)D_); \
      asm volatile("" : "+v"(mc), "+v"(mb));                                                            \
    } while (0)
    unsigned dv2;
#define GET_D() const unsigned dAB = __builtin_amdgcn_readlane(dv2, 0), dC_ = __builtin_amdgcn_readlane(dv2, 1), dC = (chC & 1) ? dC_ & 0xffff0000u : dC_ << 16
#define REQ_Q() do {                                                                                    \
      const __amdgpu_buffer_rsrc_t rQ = __builtin_amdgcn_make_buffer_rsrc((void*)q, 0, SD2, RS3);       \
      const u32x4 a = __builtin_amdgcn_raw_buffer_load_b128(rQ, l16, srow * (D_ * 2), 0);               \
      qw[0] = a[0]; qw[1] = a[1]; qw[2] = a[2]; qw[3] = a[3];                                           \
      qw[4] = __builtin_amdgcn_raw_buffer_load_b32(rQ, (unsigned)lane * 4u, srow * (D_ * 2) + 1024, 0); \
    } while (0)
#define REQ_D() dv2 = __builtin_amdgcn_raw_buffer_load_b32(__builtin_amdgcn_make_buffer_rsrc((void*)ssm_D, 0, D_ * 2, RS3), (unsigned)(lane ? (chC & ~1) : chA) * 2u, 0, 0)
#define TAGS()                                                                                          \
    {                                                                                                   \
      unsigned e0_, e1_;                                                                                \
      asm volatile("v_readfirstlane_b32 %0, %2\n\tv_readfirstlane_b32 %1, %3" : "=s"(e0_), "=s"(e1_) : "v"(ep[0]), "v"(ep[1])); \
      unsigned long long h = (unsigned long long)e1_ << 32 | e0_;                                       \
      epn = h + 1ull;                                                                                   \
      h += 0x9E3779B97F4A7C15ull;                                                                       \
      h = (h ^ (h >> 30)) * 0xBF58476D1CE4E5B9ull;                                                      \
      h = (h ^ (h >> 27)) * 0x94D049BB133111EBull;                                                      \
      h ^= h >> 31;                                                                                     \
      tA = (unsigned)h; tB = (unsigned)(h >> 32);                                                       \
    }
#define BUILD(SL, CC)                                                                                   \
  do {                                                                                                  \
    const u32x4 klo = *(const LAS u32x4*)(lds + (SL) + CV_U + 512 + 4064 - 16 * (CC)), khi = *(const LAS u32x4*)(lds + (SL) + CV_U + 512 + 4080 - 16 * (CC)); \
    const unsigned w[8] = {klo[0], klo[1], klo[2], klo[3], khi[0], khi[1], khi[2], khi[3]};             \
    _Pragma("unroll") for (int m = 0; m < 8; ++m) {                                                     \
      u32x4 o;                                                                                          \
      const int a = m >> 1;                                                                             \
      _Pragma("unroll") for (int t = 0; t < 4; ++t) {                                                   \
        if ((m & 1) == 0) o[t] = (w[a - t + 4] & 0xffffu) | (w[a - t + 3] & 0xffff0000u);               \
        else o[t] = (w[a - t + 4] >> 16) | (w[a - t + 4] << 16);                                        \
      }                                                                                                 \
      *(LAS u32x4*)(lds + (SL) + m * CV_STR + ((CC) + 1) * 16) = o;                                     \
    }                                                                                                   \
  } while (0)
#define ZCH(c) ((c) * 128u + ((c) / 160u) * (TS - 20480u))
#define ZOFF(JA, JB, CH) ((unsigned)((32 * ((g & 1) ? (JB) : (JA)) + 2 * l15 + (g >> 1)) >> 3) * (4u * TS) + ZCH((unsigned)(CH)) + (unsigned)((2 * l15 + (g >> 1)) & 7) * 16u)
#define Z2OFF(w) (O_Z + (unsigned)(4 * (w) + (l15 >> 2)) * (4u * TS) + ZCH((unsigned)chA) + (unsigned)((l15 & 3) * 64 + g * 16))
    const unsigned a00 = (unsigned)((l15 & 7) * CV_STR + (2032 - 8 * (l15 >> 3) + 8 * g) * 2) - 128u * mg;
    const unsigned b00 = (unsigned)(CV_U + 480 + 32 * l15 + 16 * g) - 128u * mg;
#define CV_RA(AV, SL, rd) _Pragma("unroll") for (int i = 0; i < 8; ++i) AV[8 * (rd) + i] = *(const LAS u32x4*)(lds + (SL) + a00 - 64 * (rd) - 512 * i)
#define CV_RB(BV, SL, rd) _Pragma("unroll") for (int i = 7; i >= 0; --i) BV[8 * (rd) + i] = *(const LAS u32x4*)(lds + (SL) + b00 + 512 * i - 64 * (rd))
    const int zja = mg ? (half ? 5 : 4) : (half ? 1 : 0), zjb = mg ? (half ? 6 : 7) : (half ? 2 : 3);
    const int t8 = tid & 255;
#define W_DMA(blk, j) dma<1, 0>(gw[j], (blk) < 20 ? dW1 : dW2, (unsigned)((blk) < 20 ? (blk) : (blk) - 4) * (32 * D_ * 2), (unsigned)((blk) * 5120 + (j) * 1024))
    if (__builtin_expect(kh != 0, 1)) {
      {
        const u32x4 dS = mk_rs(ssm_k, SD2);
        dma<1, 0>(l16, dS, (unsigned)(chC * (S_ * 2) + mg * 1024), (unsigned)(SL_C + CV_U + 512 + mg * 1024));
        dma<1, 0>(l16, dS, (unsigned)(chA * (S_ * 2) + mg * 1024), (unsigned)(SL_A + CV_U + 512 + mg * 1024));
        dma<1, 0>(l16, dS, (unsigned)((chA + 1) * (S_ * 2) + mg * 1024), (unsigned)(SL_B + CV_U + 512 + mg * 1024));
      }
#define PF_W() do { const unsigned o_ = lane < 50 ? (unsigned)(bx * 4 + mg) * 800u + l16 : OOB;        \
        dma<1, 0>(o_, dW1, 0u, (unsigned)L_ST4); dma<1, 0>(o_, dW2, 0u, (unsigned)L_ST4); FENCE(); } while (0)
      if (PF == 2) PF_W();
      unsigned cpf = __builtin_amdgcn_raw_buffer_load_b32(rW, OOB, 0, 0);
      FENCE();
#define FILL_Z() do {                                                                                   \
      st128(u32x4{CODED, CODED, CODED, CODED}, rW, Z2OFF(mg), 0, 16);                                   \
      st128(u32x4{CODED, CODED, CODED, CODED}, rW, Z2OFF(wv), 0, 16);                                   \
      if (mg < 2) st128(u32x4{CODED, CODED, CODED, CODED}, rW, ZOFF(zja, zjb, chC), O_Z, 16);            \
    } while (0)
      unsigned fpa[3], fpv;
#pragma unroll
      for (int f_ = 0; f_ < 3; ++f_) {
        const unsigned o = (unsigned)(f_ * 4096 + t8 * 16);
        fpa[f_] = o < 10240u ? O_H1 + (unsigned)(bx >> 1) * TS + (bx & 1) * 10240u + o : o < 11264u ? O_ST4 + (unsigned)bx * 1024u + (o - 10240u)
                : (o < 11392u && (bx & 1)) ? O_ST3 + (unsigned)(bx >> 1) * 128u + (o - 11264u) : OOB;
        if (f_ == 2) fpv = o < 10240u ? CODED : SENT;
      }
#define FILL_P(a, b) _Pragma("unroll") for (int f_ = (a); f_ < (b); ++f_) { const unsigned fv = f_ < 2 ? CODED : fpv; st128(u32x4{fv, fv, fv, fv}, rW, fpa[f_], 0, 16); }
      u32x4 WS[10];
#define LD_WS(a, b) _Pragma("unroll") for (int x = (a); x < (b); ++x)                                   \
      WS[x] = __builtin_amdgcn_raw_buffer_load_b128(rW1, gw[x % 5], (9 + mg + 4 * (x / 5)) * (32 * D_ * 2), 0)
#define LD_WD(a, b) _Pragma("unroll") for (int x = (a); x < (b); ++x) {                                 \
      if (x < 10) W_DMA(mg + 4 * (x / 5), x % 5);                                                       \
      else if (x == 10) dma<1, 0>(mg == 0 ? gw[0] : mg == 1 ? gw[1] : mg == 2 ? gw[2] : gw[3], dW1, 8u * (32 * D_ * 2), (unsigned)(8 * 5120 + mg * 1024)); \
      else if (mg == 0) W_DMA(8, 4);                                                                    \
    }
      FILL_Z();
      FENCE();
      u32x4 wm[5];
      unsigned short cv;
      const __amdgpu_buffer_rsrc_t rM = __builtin_amdgcn_make_buffer_rsrc((void*)w_mod, 0, D_ * 6 * D_ * 2, RS3);
      unsigned wmo = bx < 240 ? (unsigned)(t8 >> 3) * (6 * D_ * 2) + (unsigned)(t8 & 7) * 16u : OOB;
#define LD_WM(a, b) do { _Pragma("unroll") for (int i = (a); i < (b); ++i) wm[i] = __builtin_amdgcn_raw_buffer_load_b128(rM, wmo, ((bx & 3) * 160 + i * 32) * (6 * D_ * 2) + (bx >> 2) * 128, 0); FENCE(); } while (0)
      {
        const __amdgpu_buffer_rsrc_t rC = __builtin_amdgcn_make_buffer_rsrc((void*)cin, 0, D_ * 2, RS3);
        cv = __builtin_amdgcn_raw_buffer_load_b16(rC, (unsigned)((t8 >> 3) + 32 * min(lane & 7, 4)) * 2u, (bx & 3) * 320, 0);
        LD_WM(0, WMD ? WME : 5);
      }
      FENCE();
      if (WMD) PIN(wmo);
      if (QE == 3) { REQ_Q(); REQ_D(); FENCE(); }
      W12_G(gw);
#pragma unroll
      for (int j = 0; j < 5; ++j) PIN(gw[j]);
      const int job = bx * 4 + mg;
      const bool j3 = job < 540, jok = job < 972;
      const int jks = j3 ? job / 27 : (job - 540) >> 3, jcg = j3 ? job % 27 : (job - 540) & 7;
      const unsigned rtl = (unsigned)(L_RT + mg * 5120);
      u32x4 rt[5];
      unsigned rta[5];
      {
        const unsigned v3 = ((unsigned)lane >> 3) * (F_ * 2) + (((unsigned)lane & 7u) * 16u ^ (RSW ? ((unsigned)lane & 24u) * 4u : 0u)) + (unsigned)jks * (32 * F_ * 2) + (unsigned)jcg * 128u;
        const unsigned s4 = (unsigned)jks * (32 * D_ * 2) + (unsigned)(jcg - nt) * 160u;
#pragma unroll
        for (int i = 0; i < 5; ++i) { rta[i] = j3 ? (i < 4 ? (v3 ^ (RSW ? (i & 1) * 32u : 0u)) + i * (8 * F_ * 2) : OOB) : gw[i] + s4;
          PIN(rta[i]); }
      }
#define RT_LD() do {                                                                                    \
      const u32x4 dS = mk_rs(j3 ? w3 : w4, jok ? DF2 : 0);                                              \
      _Pragma("unroll") for (int i = 0; i < 4; ++i) dma<1, 0>(rta[i], dS, 0u, rtl + i * 1024);          \
      if (!j3) dma<1, 0>(rta[4], dS, 0u, rtl + 4096);                                                   \
      FENCE();                                                                                          \
    } while (0)
      FENCE();
      STAMP(12);
      asm volatile("" : "+v"(cpf));
      asm volatile("" :: "v"(cp0));
      SIG(P_K);
      if constexpr (TSHI == 23) TX_(31);
      FENCE();
      TAGS();
      auto mod_p = [&]() __attribute__((always_inline)) {
        float a[8];
#pragma unroll
        for (int e = 0; e < 8; ++e) a[e] = 0.f;
        const float c_ = lo2f(cv);
        const int sci = __float_as_int(rbf(c_ * __builtin_amdgcn_rcpf(1.f + __builtin_amdgcn_exp2f(c_ * -1.4426950408889634f))));
#pragma unroll
        for (int i = 0; i < 5; ++i) {
          const float sc = __int_as_float(__builtin_amdgcn_ds_bpermute(((lane & 56) + i) * 4, sci));
#pragma unroll
          for (int e = 0; e < 4; ++e) { a[2 * e] += sc * lo2f(wm[i][e]); a[2 * e + 1] += sc * hi2f(wm[i][e]); }
        }
#pragma unroll
        for (int e = 0; e < 4; ++e) {
          const auto r = __builtin_amdgcn_permlane32_swap(__float_as_uint(a[e]), __float_as_uint(a[e + 4]), false, false);
          a[e] = __uint_as_float((unsigned)r[0]) + __uint_as_float((unsigned)r[1]);
        }
#pragma unroll
        for (int e = 0; e < 2; ++e) {
          const auto r = __builtin_amdgcn_permlane16_swap(__float_as_uint(a[e]), __float_as_uint(a[e + 2]), false, false);
          a[e] = dpp_add(__uint_as_float((unsigned)r[0]) + __uint_as_float((unsigned)r[1]), 0);
        }
        if ((lane & 8) == 0) *(LAS f32x2*)(lds + L_MP + mg * 256 + (lane & 7) * 32 + g * 8) = f32x2{a[0], a[1]};
        SIG(WMD ? P_M2 : P_M);
        TS_(52);
        if constexpr (TSHI == 23) TX_(32);
        FENCE();
        if (QE == 2) { REQ_Q(); REQ_D(); FENCE(); }
        if (PF == 1) PF_W();
        if (wv == 7) {
          WAIT_HALF(1, WMD ? P_M2 : P_M);
          f32x2 s = *(const LAS f32x2*)(lds + L_MP + (lane & 31) * 8);
#pragma unroll
          for (int w = 1; w < 4; ++w) s += *(const LAS f32x2*)(lds + L_MP + w * 256 + (lane & 31) * 8);
          st128(u32x4{__float_as_uint(s[0]), tA, __float_as_uint(s[1]), tB}, rW, lane < 32 ? l16 : OOB, O_MP + (unsigned)bx * 512u, 16);
          __builtin_amdgcn_raw_buffer_store_b64(u32x2{tA, tB}, rW, lane == 0 ? (unsigned)bx * 8u : OOB, O_FL1, 16);
        }
        FENCE();
      };
      if (!WMD) mod_p();
      if (WMD == 1) WAIT_HALF(0, P_KV);
      if (WMD == 1 || WMD == 2) LD_WM(WME, 5);
      STAMP(2);
      {
      WAIT_HALF(1, P_K);
      BUILD(SL_A, t8);
      if (QE == 4) { FENCE(); REQ_Q(); REQ_D(); FENCE(); }
      if (WMD == 4) { FENCE(); LD_WM(WME, 5); }
      BUILD(SL_B, t8);
      SIG(P_B2);
      if constexpr (TSHI == 23) TX_(33);
      FENCE();
      STAMP(1);
      }
      if (WMD == 3) LD_WM(WME, 5);
      if (QE == 1) { REQ_Q(); REQ_D(); FENCE(); }
      if (PF == 5) { PIN(qw[4]); PIN(dv2); FENCE(); }
      if (PF == 3 || PF == 5) PF_W();
      if (WMD) mod_p();
      if (!QE) { WAIT_HALF(1, P_B2); REQ_Q(); REQ_D(); }
      FENCE();
      FENCE();
      unsigned tra[10], rto[5];
      {
        const unsigned b3 = rtl + (unsigned)(8 * g + (l15 >> 2)) * 128u + (unsigned)((l15 >> 2) ^ (g & 1)) * 32u + ((unsigned)lane & 3u) * 8u, b4 = rtl + ((unsigned)lane >> 2) * 160u + ((unsigned)lane & 3u) * 8u;
        const unsigned dst = j3 ? O_WT3 + (unsigned)(jcg * 80 + jks) * 1024u : O_WT4 + (unsigned)(jcg * 54 + jks) * 5120u, dstr = j3 ? 20480u : 1024u;
#pragma unroll
        for (int jn = 0; jn < 5; ++jn) {
          tra[jn] = j3 ? b3 ^ (unsigned)(jn & 3) * 32u : b4 + jn * 32u;
          tra[5 + jn] = tra[jn] + (j3 ? 512u : 2560u);
          rto[jn] = (jok && !(j3 && jn == 4)) ? l16 + dst + jn * dstr : OOB;
          PIN(tra[jn]); PIN(tra[5 + jn]); PIN(rto[jn]);
        }
      }
#define RT_TR() do {                                                                                    \
        _Pragma("unroll") for (int jn = 0; jn < 5; ++jn) {                                              \
          const s16x4 lo = __builtin_amdgcn_ds_read_tr16_b64_v4i16((LAS s16x4*)tra[jn]);        \
          const s16x4 hi = __builtin_amdgcn_ds_read_tr16_b64_v4i16((LAS s16x4*)tra[5 + jn]);    \
          rt[jn] = __builtin_bit_cast(u32x4, bf16x8{lo[0], lo[1], lo[2], lo[3], hi[0], hi[1], hi[2], hi[3]}); \
          SB();                                                                                         \
        }                                                                                               \
      } while (0)
#define RT_OUT() do {                                                                                   \
        _Pragma("unroll") for (int jn = 0; jn < 4; ++jn) st128(rt[jn], rW, rto[jn], 0, 16);             \
        if (!j3) st128(rt[4], rW, rto[4], 0, 16);                                                       \
        FENCE();                                                                                        \
      } while (0)
      SIG(P_U);
      const bool mfin = wv == 7 && bx < 240 && lane < 32;
      const unsigned po = mfin ? ((unsigned)lane >> 3) * 512u + ((unsigned)lane & 7u) * 16u : OOB;
      u32x4 mp4;
#define LD_P() mp4 = __builtin_amdgcn_raw_buffer_load_b128(rW, po, O_MP + (unsigned)(bx & ~3) * 512u + (unsigned)(bx & 3) * 128u, 16)
#define MOD_BAD() (__builtin_amdgcn_ballot_w64(mfin && (mp4[1] != tA || mp4[3] != tB)) != 0ull)
#define MOD_FIN() do {                                                                                  \
        const float one = ((bx >= 40 && bx < 80) || (bx >= 160 && bx < 200)) ? 1.f : 0.f;               \
        const float s0_ = swap_add16(dpp_add(__uint_as_float(mp4[0]), 0)), s1_ = swap_add16(dpp_add(__uint_as_float(mp4[2]), 0)); \
        const unsigned d = pack2(one + rbf(s0_), one + rbf(s1_));                                       \
        const unsigned dn = (unsigned)__builtin_amdgcn_update_dpp(0, (int)d, 0xB1, 0xf, 0xf, true);     \
        st128(u32x4{d, tA, dn, tB}, rW, (lane < 8 && (lane & 1) == 0) ? (unsigned)lane * 8u : OOB, O_MOD + (unsigned)bx * 64u, 16); \
      } while (0)
#define MOD_L2(c) do {                                                                                  \
      if (c) {                                                                                          \
        unsigned spins = 0;                                                                             \
        while (MOD_BAD()) {                                                                             \
          SPIN_STEP();                                                                                  \
          if (spins > RETRY_CAP) break;                                                                 \
          __builtin_amdgcn_s_sleep(USLEEP);                                                             \
          LD_P();                                                                                       \
        }                                                                                               \
        MOD_FIN();                                                                                      \
      }                                                                                                 \
      FENCE();                                                                                          \
    } while (0)
      if (!ML && wv == 7) LD_P();
      FENCE();
      STAMP(3);
      FENCE();
      asm volatile("" : "+v"(fpa[0]), "+v"(fpa[1]), "+v"(fpa[2]), "+v"(fpv));
#define RT_GO() RT_LD()
      if (MB) MOD_L2(wv == 7);
      LN1H();
      GET_D();
      if (PF == 4) { FENCE(); PF_W(); }
      if constexpr (TSHI == 23) TX_(34);
      if (!ML && !MB) MOD_L2(wv == 7);
      if constexpr (TSHI == 23) TX_(35);
      TS_(27);
      if constexpr (TSHI == 23) TX_(36);
      STAMP(22);
      if (HP & 4) PRIO(3);
      WAIT_ALL(P_U);
      if (ML && wv == 7) LD_P();
      FENCE();
      RT_LD();
      TS_(4);
      STAMP(28);
      FILL_P(0, 3);
      FENCE();
      if constexpr (TSHI == 23) { TX_(49); VMW(3); TX_(50); }
      if (HP & 3) PRIO(3);
      if (F1L || RD) VMW(3);
      RT_TR();
      SIG(P_RT);
      TS_(48);
      if constexpr (TSHI == 23) TX_(48);
      RT_OUT();
      if (HP & 1) PRIO(0);
      if constexpr (TSHI == 23) TX_(51);
      bool mlate = false;
      if (ML && wv == 7) { mlate = MOD_BAD(); if (!mlate) MOD_FIN(); }
      FENCE();
      LD_WD(0, 5);
      if constexpr (TSHI == 23) TX_(52);
      WAIT_HALF(1, P_RT);
      LD_WD(5, NGA);
      FENCE();
      if constexpr (TSHI == 23) TX_(53);
      VMW(NGA + F1L + (GZ && GZP == 3 ? 5 : 0));
      SIG(P_GA);
      if (HP & 2) PRIO(0);
      if constexpr (TSHI != 23) TS_(5);
      if (ML) MOD_L2(__builtin_expect(mlate, 0));
      if constexpr (TSHI == 23) TX_(47);
      FENCE();
      LD_WD(NGA, ZPN);
      if (ZP) {
        const unsigned fl = *(volatile LAS unsigned*)(lds + L_CTL + (lane & 3) * 4);
        if (__builtin_amdgcn_ballot_w64(fl >= (unsigned)(ZP == 1 ? P_X : P_A2)) != 0ull) WAIT_HALF(0, P_X2);
        FENCE();
      }
      LD_WD(ZPN, 12);
      FENCE();
      if (mg < 2) {
        u32x2 uc[2];
        f32x4 yc[8];
        float v[8];
        uc[0] = *(const LAS u32x2*)(lds + SL_C + CV_U + 512 + (256 * zja + 16 * l15 + 4 * g) * 2);
        uc[1] = *(const LAS u32x2*)(lds + SL_C + CV_U + 512 + (256 * zjb + 16 * l15 + 4 * g) * 2);
        unsigned zq = O_Z + ZOFF(zja, zjb, chC);
        PIN(zq);
        WAIT_HALF(0, P_C2);
#pragma unroll
        for (int c = 0; c < 8; ++c) yc[c] = *(const LAS f32x4*)(lds + SL_C + mg * 2048 + l16 + (c >> 1) * 4096 + (c & 1) * 1024);
        STG(8, v[i] = (i & 1) ? hi2f(uc[i >> 2][(i >> 1) & 1]) : lo2f(uc[i >> 2][(i >> 1) & 1]); PIN(v[i]));
        STG(8, v[i] = __builtin_fmaf(v[i], __uint_as_float(dC), yc[i >> 2][i & 3]); PIN(v[i]));
#pragma unroll
        for (int w = 1; w < 4; ++w) STG(8, v[i] += yc[2 * w + (i >> 2)][i & 3]; PIN(v[i]));
        st128(widen(pack2(v[0], plainv(v[1])), pack2(v[2], v[3]), pack2(v[4], plainv(v[5])), pack2(v[6], v[7])), rW, zq, 0, 16);
      }
      FENCE();
      SIG(P_ZC);
      FENCE();
      LD_WS(0, GZ ? NS1 : NS2);
      FENCE();
      if (HPZ) { WAIT_HALF(0, P_X2); __builtin_amdgcn_s_sleep(HPZ); }
      STAMP(15);
      LD_WS(NS2, 10);
      if (mg > 0) {
        WAIT_HALF(1, P_ZC);
#pragma unroll
        for (int j = 0; j < 5; ++j) W_DMA(16 + mg, j);
      }
      LD_WS2U(0, NZ2);
      FENCE();
      STAMP(5);
      TS_(6);
      WAIT_HALF(0, P_X2);
#pragma unroll
      for (int x = 0; x < 10; ++x) *(LAS u32x4*)(lds + (9 + mg + 4 * (x / 5)) * 5120 + (x % 5) * 1024 + l16) = WS[x];
      TS_(1);
      WAIT_HALF(0, P_T);
      R_PRE();
      VMW(NZ2);
      SIG(P_X);
      TS_(2);
      TS_(3);
      WAIT_HALF(0, P_Z);
      LD_WS2U(NZ2, 10);
      FENCE();
      WAIT_2(P_RP, P_X);
#pragma unroll
      for (int j = 0; j < 5; ++j) W_DMA(20 + mg, j);
      FENCE();
      TS_(7);
      STAMP(7);
    } else {
      PRIO(3);
      const unsigned rt0 = (unsigned)__builtin_amdgcn_s_memrealtime();
      unsigned kpa = (unsigned)L_KV + ((unsigned)lane & 7u) * 1312u + (unsigned)mg * 64u + ((unsigned)lane >> 3) * 8u;
      unsigned kpb = (unsigned)L_KV + ((unsigned)l15 >> 1) * 1312u + 256u + (unsigned)mg * 16u + (unsigned)g * 4u + ((unsigned)lane & 1u) * 640u, kpc = kpb + ((unsigned)lane & 1u) * 32u;
      const unsigned cha_ = (unsigned)ch0 + 32u * mg + ((unsigned)lane >> 3) * 4u, chb_ = (unsigned)ch0 + 128u + 8u * mg + 2u * g;
      unsigned koa = KVROW(cha_) + (unsigned)tok0 * 4u + ((unsigned)lane & 7u) * 16u, koa2 = KVROW(cha_ + 2u) + (unsigned)tok0 * 4u + ((unsigned)lane & 7u) * 16u;
      unsigned kob = KVROW(chb_) + (unsigned)tok0 * 4u + (unsigned)l15 * 8u;
      asm volatile("" : "+v"(kpa), "+v"(kpb), "+v"(kpc), "+v"(koa), "+v"(koa2), "+v"(kob));
      u32x4 ug[6];
      unsigned uo = (unsigned)mg * 2048u + l16;
      unsigned udone;
      asm volatile("s_mov_b32 %0, 0" : "=s"(udone));
      unsigned ubA = KVROW((unsigned)chA), ubC = KVROW((unsigned)chC), ubB = KVROW((unsigned)(chA + 1));
      asm volatile("" : "+s"(ubA), "+s"(ubB), "+s"(ubC));
      PIN(uo);
#pragma unroll
      for (int r = 0; r < 6; ++r) asm volatile("" : "=v"(ug[r]));
#define TRY_U(EL, T, CODE)                                                                               \
        asm volatile("s_cmp_lg_u32 %6, 0\n\ts_cbranch_scc1 Lu%=\n\ts_cmp_lt_u32 %[el], %[t]\n\ts_cbranch_scc1 Lu%=\n\t" \
                     "buffer_load_dwordx4 %2, %[vo], %[rs], %[c] offen sc1\n\tbuffer_load_dwordx4 %3, %[vo], %[rs], %[c] offen offset:1024 sc1\n\t" \
                     "buffer_load_dwordx4 %0, %[vo], %[rs], %[a] offen sc1\n\tbuffer_load_dwordx4 %1, %[vo], %[rs], %[a] offen offset:1024 sc1\n\t" \
                     "buffer_load_dwordx4 %4, %[vo], %[rs], %[b] offen sc1\n\tbuffer_load_dwordx4 %5, %[vo], %[rs], %[b] offen offset:1024 sc1\n\t" \
                     "s_mov_b32 %6, %[cd]\n"                                                            \
                     "Lu%=:" : "+v"(ug[0]), "+v"(ug[1]), "+v"(ug[2]), "+v"(ug[3]), "+v"(ug[4]), "+v"(ug[5]), "+s"(udone) \
                     : [el] "s"(EL), [t] "i"(T), [cd] "i"(CODE), [vo] "v"(uo), [rs] "s"(dW), [a] "s"(ubA), [b] "s"(ubB), [c] "s"(ubC) : "scc", "memory")
      VMW(9);
      TAGS();
      unsigned tAB = (lane & 1) ? tB : tA;
      PIN(tAB);
      asm volatile("s_waitcnt vmcnt(0)" : "+v"(sk[0]), "+v"(sk[1]), "+v"(sk[2]));
      const unsigned rt1 = (unsigned)__builtin_amdgcn_s_memrealtime();
      SIG(P_KV);
      FENCE();
      f32x2 wa[3][4];
#pragma unroll
      for (int c = 0; c < 3; ++c)
#pragma unroll
        for (int t = 0; t < 4; ++t) {
          const f32x2 w = (t & 1) ? f32x2{hi2f(sk[c][t >> 1]), hi2f(sk[c][2 + (t >> 1)])} : f32x2{lo2f(sk[c][t >> 1]), lo2f(sk[c][2 + (t >> 1)])};
          wa[c][t] = w;
          PIN(wa[c][t]);
        }
      asm volatile("" : "+v"(tAB));
      FENCE();
      TS_(1);
      STAMP(1);
      WAIT_HALF(0, P_KV);
      if (TU1) TRY_U(rt1 - rt0, TU1, 1);
      if constexpr (TSHI == 23) TX_(31);
      STAMP(12);
      {
        u32x2 kw[7], vw[4];
        unsigned kx[5], vx[2];
#pragma unroll
        for (int i = 0; i < 7; ++i) kw[i] = *(const LAS u32x2*)(lds + kpa + (i < 3 ? 320 + 320 * i : 1312 + 320 * (i - 3)));
#pragma unroll
        for (int i = 0; i < 5; ++i) kx[i] = *(const LAS unsigned*)(lds + ((i == 1 || i == 2) ? kpc : kpb) + (i < 3 ? 320 + 320 * i : 1312 + 320 * (i - 3)));
#pragma unroll
        for (int i = 0; i < 4; ++i) vw[i] = *(const LAS u32x2*)(lds + kpa + 12288 + 320 * i);
#pragma unroll
        for (int i = 0; i < 2; ++i) vx[i] = *(const LAS unsigned*)(lds + kpb + 12288 + 320 * i);
        __builtin_amdgcn_sched_barrier(0);
        f32x2 kf[19], a[10], p[10], t_[10];
#define KX(o, t) kf[((o) >> 2) * 7 + ((o) & 3) + (t)]
#define STR(lo, hi, stmt) do { _Pragma("unroll") for (int i = (lo); i < (hi); ++i) { stmt; } __builtin_amdgcn_sched_barrier(0); } while (0)
#define KV_GROUP(lo, hi) do {                                                                           \
        STR(((lo) >> 2) * 7, (hi) == 10 ? 19 : ((hi) >> 2) * 7, kf[i] = up2(i < 14 ? kw[i % 7][i / 7] : kx[i < 14 ? 0 : i - 14]); PIN(kf[i])); \
        {                                                                                               \
          STR(lo, hi, a[i] = KX(i, 0) * wa[i >> 2][3]; PIN(a[i]));                                      \
          _Pragma("unroll") for (int t = 1; t < 4; ++t) STR(lo, hi, a[i] = FMA2(KX(i, t), wa[i >> 2][3 - t], a[i]); PIN(a[i])); \
          STR(lo, hi, t_[i] = FMA2(a[i], cp, a[i]); PIN(t_[i]));                                        \
          STR(lo, hi, a[i] = FMA2(a[i], cn, t_[i]); PIN(a[i]));                                         \
        }                                                                                               \
        STR(lo, hi, p[i] = up2(i < 8 ? vw[i & 3][i >> 2] : vx[i & 1]); PIN(p[i]));                      \
        STR(lo, hi, a[i] *= p[i]; PIN(a[i]));                                                           \
        if constexpr (TSHI == 23) TX_(32 + (lo) / 2);                                                   \
        _Pragma("unroll") for (int c = (lo) >> 1; c < ((hi) < 8 ? (hi) : 8) >> 1; ++c) {                \
          const int o = (c >> 1) * 4, e = c & 1;                                                        \
          st128(u32x4{pack2(a[o][e], plainv(a[o + 1][e])), tA, pack2(a[o + 2][e], plainv(a[o + 3][e])), tB}, rW, c < 2 ? koa : koa2, e * 8192u, 16); \
        }                                                                                               \
        if ((hi) == 10) { _Pragma("unroll") for (int e = 0; e < 2; ++e) __builtin_amdgcn_raw_buffer_store_b64(u32x2{pack2(a[8][e], plainv(a[9][e])), tAB}, rW, kob, e * 8192, 16); } \
        __builtin_amdgcn_sched_barrier(0);                                                              \
      } while (0)
        KV_GROUP(0, 4);
        if constexpr (TSHI == 23) TX_(33);
        STAMP(5);
        KV_GROUP(4, 10);
        if constexpr (TSHI == 23) TX_(35);
      }
      FENCE();
      const unsigned rt2 = (unsigned)__builtin_amdgcn_s_memrealtime();
      REQ_Q();
      REQ_D();
      FENCE();
      if (TU2) TRY_U(rt2 - rt0, TU2, 2);
      STAMP(2);
      TS_(2);
      WAIT_HALF(1, P_K);
      BUILD(SL_C, t8);
      SIG(P_B);
      if constexpr (TSHI == 23) TX_(36);
      FENCE();
#define LD_U(r) ug[r] = __builtin_amdgcn_raw_buffer_load_b128(rW, uo + ((r) & 1) * 1024, (r) < 2 ? ubA : (r) < 4 ? ubC : ubB, 16)
      u32x4 A2[16], B2[16];
      WAIT_HALF(0, P_B);
      CV_RA(A2, SL_C, 0);
      CV_RA(A2, SL_C, 1);
      W12_G(gw);
#pragma unroll
      for (int j = 0; j < 5; ++j) PIN(gw[j]);
      unsigned zq = Z2OFF(wv);
      unsigned pa = a00 + SL_A - 3648, pb = b00 + SL_A - 64;
      unsigned xw = SL_A + mg * 8192 + l16, xc = SL_C + mg * 4096 + l16;
      asm volatile("" : "+v"(zq), "+v"(pa), "+v"(pb), "+v"(xw), "+v"(xc));
      FENCE();
      if (udone == 0u) while ((unsigned)__builtin_amdgcn_s_memrealtime() - rt0 < (unsigned)TU) __builtin_amdgcn_s_sleep(1);
      STAMP(3);
      TS_(3);
      TRY_U(rt0, 0, 3);
      FENCE();
      if constexpr (TSHI == 23) TX_(37);
      unsigned uw = (unsigned)(SL_A + CV_U + 512 + mg * 1024 + lane * 8);
      PIN(uw);
      WAIT_HALF(1, P_B2);
      asm volatile("s_cmp_eq_u32 %[d], 1\n\ts_cbranch_scc1 Lw%=\n\ts_waitcnt vmcnt(0)\nLw%=:\n\ts_waitcnt vmcnt(9)"
                   : "+v"(ug[0]), "+v"(ug[1]), "+v"(ug[2]), "+v"(ug[3]), "+v"(ug[4]), "+v"(ug[5]) : [d] "s"(udone) : "scc", "memory");
      if constexpr (TSHI == 23) { TX_(38); PIN(ug[5]); __builtin_amdgcn_s_sleep(1); TX_(39); }
      {
#define U_BAD(r) (ug[r][1] != tA || ug[r][3] != tB)
        if (__builtin_expect(__builtin_amdgcn_ballot_w64(U_BAD(0) || U_BAD(1) || U_BAD(2) || U_BAD(3) || U_BAD(4) || U_BAD(5)) != 0ull, 1)) {
          unsigned spins = 0;
          do {
            __builtin_amdgcn_s_sleep(USLEEP);
#pragma unroll
            for (int r = 0; r < 6; ++r) if (U_BAD(r)) LD_U(r);
            SPIN_STEP();
          } while (__builtin_amdgcn_ballot_w64(U_BAD(0) || U_BAD(1) || U_BAD(2) || U_BAD(3) || U_BAD(4) || U_BAD(5)) != 0ull && spins < RETRY_CAP);
          VM0();
        }
#pragma unroll
        for (int r = 2; r < 8; ++r) *(LAS u32x2*)(lds + uw + ((r % 6) >> 1) * CV_SLOT + (r & 1) * 512) = u32x2{ug[r % 6][0], ug[r % 6][2]};
      }
      SIG(P_U);
      TS_(56);
      if constexpr (TSHI == 23) TX_(40);
      STAMP(22);
      GET_D();
#if !(LINW & 12)
      if (WI0 < 0) { LD_WS2(0, 10); } else { LD_WS2(0, WIE); }
#endif
#define WI_AT(x) do { if constexpr (WI0 >= 0 && (x) >= WI0 && ((x) - WI0) % WIS == 0 && ((x) - WI0) / WIS + WIE < 10) { constexpr int y0_ = ((x) - WI0) / WIS + WIE; LD_WS2(y0_, y0_ + 1); } } while (0)
      FENCE();
      WAIT_HALF(0, P_U);
      CV_RB(B2, SL_C, 0);
      FENCE();
      STAMP(28);
      TS_(4);
      f32x4 acc[8], ac[4];
      u32x4 A[16], B[16];
#define RA(P, rd, i_) *(const LAS u32x4*)(lds + (P) + 3648 - 64 * (rd) - 512 * (i_))
#define RB(P, rd, i_) *(const LAS u32x4*)(lds + (P) + 64 - 64 * (rd) + 512 * (i_))
#define CONV_C(J, lo, hi)                                                                               \
      sfor<lo, hi>([&](auto C_) __attribute__((always_inline)) {                                         \
        constexpr int c = decltype(C_)::v, rd = c / 18, i = cc_i(J, c % 18), t = cc_t(J, c % 18);       \
        ac[t] = mfma16(A2[8 * rd + i], B2[8 * rd + (((J) >> (4 * t)) & 15) - i], (rd == 0 && i == 0) ? f32x4{0.f, 0.f, 0.f, 0.f} : ac[t]); \
        SB();                                                                                           \
        WI_AT(c);                                                                                       \
        if constexpr (c < 8) B2[15 - c] = RB(pb + CV_SLOT, 1, 7 - c);                                   \
        if constexpr (c >= 18 && c < 34) { constexpr int x = (c - 18) >> 1; if constexpr ((c & 1) == 0) B[7 - x] = RB(pb, 0, 7 - x); else A[x] = RA(pa, 0, x); } \
        SB();                                                                                           \
      });
      if (half == 0) { CONV_C(0x7430, 0, 18) } else { CONV_C(0x6521, 0, 18) }
      SB();
      int half2 = half;
      asm volatile("" : "+s"(half2));
      if (half2 == 0) { CONV_C(0x7430, 18, 36) } else { CONV_C(0x6521, 18, 36) }
      u32x4 A3[16], B3[16];
      f32x4 ab_[8];
      unsigned pk_;
      float ud[16];
      u32x2 u2[4];
      f32x4 ya[8];
      unsigned ga_;
      unsigned pu = (unsigned)(SL_A + CV_U + 512 + (256 * wv + 16 * l15 + 4 * g) * 2), py = (unsigned)(SL_A + wv * 1024) + l16;
      const unsigned f0 = (unsigned)L_CTL + ((unsigned)lane & 3u) * 4u;
      sfor<0, 144>([&](auto M_) __attribute__((always_inline)) {
        constexpr int m = decltype(M_)::v, ch = m / 72, rd = (m / 36) & 1, r = m % 36, i = m < 108 ? cv_i(r) : cl_i(r), dd = m < 108 ? cv_d(r) : cl_t(r) - cl_i(r);
        if constexpr (ch == 0) acc[i + dd] = mfma16(A[8 * rd + i], B[8 * rd + dd], (rd == 0 && i == 0) ? f32x4{0.f, 0.f, 0.f, 0.f} : acc[i + dd]);
        else ab_[i + dd] = mfma16(A3[8 * rd + i], B3[8 * rd + dd], (rd == 0 && i == 0) ? f32x4{0.f, 0.f, 0.f, 0.f} : ab_[i + dd]);
        SB();
        WI_AT(36 + m);
        if constexpr (m == 30) { pa += SL_B - SL_A; pb += SL_B - SL_A; asm volatile("" : "+v"(pa), "+v"(pb)); }
        if constexpr (m == 94) { xw += SL_B - SL_A; PIN(xw); }
        if constexpr (r >= RS0 && r < RS0 + 16 && m < 108) {
          constexpr int x = (r - RS0) >> 1;
          if constexpr (m < 36) { if constexpr ((r & 1) == 0) B[15 - x] = RB(pb, 1, 7 - x); else A[8 + x] = RA(pa, 1, x); }
          else if constexpr (m < 72) { if constexpr ((r & 1) == 0) B3[7 - x] = RB(pb, 0, 7 - x); else A3[x] = RA(pa, 0, x); }
          else { if constexpr ((r & 1) == 0) B3[15 - x] = RB(pb, 1, 7 - x); else A3[8 + x] = RA(pa, 1, x); }
        }
        if constexpr (m == 1 || m == 4 || m == 23 || m == 26) { constexpr int c = m == 1 ? 0 : m == 4 ? 1 : m == 23 ? 2 : 3; *(LAS f32x4*)(lds + xc + c * 1024) = ac[c]; }
        if constexpr (m == 28) SIG(P_C2);
        if constexpr (m == 38 || m == 104) pk_ = *(volatile LAS unsigned*)(lds + f0);
        if constexpr (m == 45) NEED_AT(pk_, f0, P_C2);
        if constexpr (m == 111) NEED_AT(pk_, f0, P_A2);
        constexpr int wa = m == 47 ? 0 : m == 54 ? 1 : m == 60 ? 2 : m == 65 ? 3 : m == 69 ? 4 : m == 72 ? 5 : m == 75 ? 6 : m == 77 ? 7 : -1;
        constexpr int wb = m == 113 ? 0 : m == 116 ? 1 : m == 119 ? 2 : m == 122 ? 3 : m == 126 ? 4 : m == 132 ? 5 : m == 139 ? 6 : -1;
        if constexpr (wa >= 0) *(LAS f32x4*)(lds + xw + wa * 1024) = acc[wa];
        if constexpr (wb >= 0) *(LAS f32x4*)(lds + xw + wb * 1024) = ab_[wb];
        if constexpr (m == 95) SIG(P_A2);
        if constexpr (m == 58) PIN(pu);
        if constexpr (m == 59 || m == 61) { constexpr int c = m - 59; u2[c] = *(const LAS u32x2*)(lds + pu + c * 1024); u2[c + 1] = *(const LAS u32x2*)(lds + pu + (SL_B - SL_A) + c * 1024); }
        constexpr int us = cv_slot(m, 0);
        if constexpr (us >= 0 && us < 16) {
#pragma unroll
          for (int o = 2 * us; o < 2 * us + 2; ++o) {
            const int v = (o >> 3) * 4 + (o & 3);
            if ((o & 4) == 0) ud[v] = (v & 1) ? hi2f(u2[v >> 2][(v >> 1) & 1]) : lo2f(u2[v >> 2][(v >> 1) & 1]);
            else ud[v] *= ((v >> 2) & 1) ? hi2f(dAB) : lo2f(dAB);
            PIN(ud[v]);
          }
        }
        if constexpr (m == 110) PIN(py);
        if constexpr (m == 112 || m == 114 || m == 115 || m == 117) { constexpr int c = m == 112 ? 0 : m == 114 ? 2 : m == 115 ? 4 : 6;
          ya[c] = *(const volatile LAS f32x4*)(lds + py + (c >> 2) * 4096 + (c & 3) * 8192); ya[c + 1] = *(const volatile LAS f32x4*)(lds + py + (c >> 2) * 4096 + (c & 3) * 8192 + 8192); }
        constexpr int zs = cv_slot(m, 1);
        if constexpr (zs >= 0 && zs < 16) {
#pragma unroll
          for (int o = 2 * zs; o < 2 * zs + 2; ++o) { const int tl = (o >> 2) & 1, e = o & 3; ud[tl * 8 + e] += ya[4 * tl + (o >> 3)][e]; PIN(ud[tl * 8 + e]); }
        }
        if constexpr (TSHI == 10 && (m % 18 == 0 || m == 143)) TX_(44 + (m + 1) / 18);
        if constexpr (m == 131) ga_ = *(volatile LAS unsigned*)(lds + f0 + 16);
        if constexpr (m == 140) { py += SL_B - SL_A; PIN(py); }
        SB();
      });
      *(LAS f32x4*)(lds + xw + 7 * 1024) = ab_[7];
      SIG(P_X);
      TS_(5);
      STAMP(4);
      u32x4 mg4[4];
#define LD_MG(i) mg4[i] = __builtin_amdgcn_raw_buffer_load_b128(rW, (unsigned)t8 * 16u, O_MOD + (i) * 4096, 16)
      u32x2 f1v;
#define LD_F1() f1v = __builtin_amdgcn_raw_buffer_load_b64(rW, (unsigned)t8 * 8u, O_FL1, 16)
      NEED_AT(ga_, f0 + 16, P_GA);
      {
        unsigned spins = 0;
        for (;;) {
          const unsigned fl = *(volatile LAS unsigned*)(lds + f0);
#pragma unroll
          for (int c = 0; c < 8; ++c) ya[c] = *(const volatile LAS f32x4*)(lds + py + (c >> 2) * 4096 + (c & 3) * 8192);
          if (__builtin_amdgcn_ballot_w64(fl < (unsigned)P_X) == 0ull) break;
          SPIN_STEP();
          if (spins > SPIN_CAP) break;
        }
#pragma unroll
        for (int tl = 0; tl < 2; ++tl) {
          const f32x4 yb = (ya[4 * tl] + ya[4 * tl + 1]) + (ya[4 * tl + 2] + ya[4 * tl + 3]);
          const f32x2 v2 = f32x2{yb[0], yb[1]} + f32x2{ud[8 * tl + 4], ud[8 * tl + 5]}, v3 = f32x2{yb[2], yb[3]} + f32x2{ud[8 * tl + 6], ud[8 * tl + 7]};
          st128(u32x4{pack2(ud[8 * tl], plainv(v2[0])), pack2(ud[8 * tl + 1], v2[1]), pack2(ud[8 * tl + 2], plainv(v3[0])), pack2(ud[8 * tl + 3], v3[1])}, rW, zq + tl * 64u * TS, 0, 16);
        }
      }
      FENCE();
      SIG(P_X2);
      TS_(6);
      LD_F1(); LD_MG(0); LD_MG(1); LD_MG(2); LD_MG(3);
      FENCE();
      LN1();
      STAMP(6);
      u32x4 za, zb, zc = {0u, 0u, 0u, 0u}, zd = {0u, 0u, 0u, 0u};
      const unsigned zo = (unsigned)mt * (4u * TS) + ZCH((unsigned)t8 & ~1u) + (unsigned)nt * 32u + ((unsigned)t8 & 1u) * 16u;
      const unsigned zo1 = (unsigned)mt * (4u * TS) + ZCH(256u + ((unsigned)t8 & ~1u)) + (unsigned)nt * 32u + ((unsigned)t8 & 1u) * 16u;
      const unsigned zo2 = (unsigned)mt * (4u * TS) + ZCH(2u * t8 + 128u) + (unsigned)nt * 16u;
      FENCE();
      {
#define MG_BAD(i) (mg4[i][1] != tA || mg4[i][3] != tB)
#define T_BAD() (__builtin_amdgcn_ballot_w64(f1v[0] != tA || f1v[1] != tB || MG_BAD(0) || MG_BAD(1) || MG_BAD(2) || MG_BAD(3)) != 0ull)
        if (__builtin_expect(T_BAD(), 0)) {
          unsigned spins = 0;
          do {
            __builtin_amdgcn_s_sleep(USLEEP);
            if (f1v[0] != tA || f1v[1] != tB) LD_F1();
#pragma unroll
            for (int i = 0; i < 4; ++i) if (MG_BAD(i)) LD_MG(i);
            SPIN_STEP();
          } while (T_BAD() && spins < RETRY_CAP);
          VM0();
        }
#pragma unroll
        for (int i = 0; i < 4; ++i) {
          const int n = (t8 + 256 * i) * 4;
          if (n < 6 * D_) *(LAS u32x2*)(lds + L_MOD + n * 2) = u32x2{mg4[i][0], mg4[i][2]};
          if (i == 3 && n >= 5 * D_ + n0 && n < 5 * D_ + n0 + 80)
            *(LAS f32x4*)(lds + L_GB + (n - 5 * D_ - n0) * 4) = f32x4{lo2f(mg4[i][0]), hi2f(mg4[i][0]), lo2f(mg4[i][2]), hi2f(mg4[i][2])};
        }
        SIG(P_T);
        if constexpr (TSHI == 23) TX_(41);
      }
      WAIT_HALF(0, P_T);
      if (DLY0) __builtin_amdgcn_s_sleep(DLY0);
      PRIO(3);
      STAMP(7);
      TS_(7);
      za = __builtin_amdgcn_raw_buffer_load_b128(rW, zo, O_Z, 16); zb = __builtin_amdgcn_raw_buffer_load_b128(rW, zo1, O_Z, 16);
      if (wv == 3) { zc = __builtin_amdgcn_raw_buffer_load_b128(rW, zo2, O_Z, 16); zd = __builtin_amdgcn_raw_buffer_load_b128(rW, zo2 + 16u * ZG, O_Z, 16); }
      FENCE();
      SIG(P_Z);
      R_PRE();
      SIG(P_RP);
      unsigned zw0 = (unsigned)(L_XCH + 4 * (t8 & 1) * YROW + 4 * (t8 >> 1)), zw1 = (unsigned)(L_XCH + 256 + 4 * t8);
      asm volatile("" : "+v"(zw0), "+v"(zw1), "+v"(ry), "+v"(rz));
      asm volatile("" : "+s"(w3s[0]), "+s"(w3s[1]));
      unsigned spins = 0;
      for (;;) {
        const bool ba = amax2(za) >= PTHR, bb_ = amax2(zb) >= PTHR, bc_ = amax2(zc) >= PTHR, bd = amax2(zd) >= PTHR;
        if (__builtin_amdgcn_ballot_w64(ba || bb_ || bc_ || bd) == 0ull) break;
        SPIN_STEP();
        if (spins > RETRY_CAP) break;
        __builtin_amdgcn_s_sleep(ZSLEEP);
        if (ba) za = __builtin_amdgcn_raw_buffer_load_b128(rW, zo, O_Z, 16);
        if (bb_) zb = __builtin_amdgcn_raw_buffer_load_b128(rW, zo1, O_Z, 16);
        if (bc_) zc = __builtin_amdgcn_raw_buffer_load_b128(rW, zo2, O_Z, 16);
        if (bd) zd = __builtin_amdgcn_raw_buffer_load_b128(rW, zo2 + 16u * ZG, O_Z, 16);
      }
#pragma unroll
      for (int i = 0; i < 4; ++i) {
        *(LAS unsigned*)(lds + zw0 + i * YROW) = za[i];
        *(LAS unsigned*)(lds + zw0 + i * YROW + 512) = zb[i];
      }
      if (wv == 3) {
#pragma unroll
        for (int i = 0; i < 4; ++i) {
          *(LAS unsigned*)(lds + zw1 + (2 * i) * YROW) = __builtin_amdgcn_perm(zd[i], zc[i], 0x05040100u);
          *(LAS unsigned*)(lds + zw1 + (2 * i + 1) * YROW) = __builtin_amdgcn_perm(zd[i], zc[i], 0x07060302u);
        }
      }
      VM0();
      SIG(P_Z2);
    }
  }
  {
    f32x2 hh[5], t_[5];
    STAMP(8);
    asm volatile("" : "+v"(ry), "+v"(rz));
    asm volatile("" : "+s"(w3s[0]), "+s"(w3s[1]));
    if (kh) PRIO(PR1); else PRIO(PHI);
    WAIT_HALF(0, P_Z2);
    STAMP(9);
    TS_(8);
    unsigned zw[5], xp[5], hp_[5];
    {
      const u32x4 a = *(LAS u32x4*)(lds + ry);
      zw[0] = a[0]; zw[1] = a[1]; zw[2] = a[2]; zw[3] = a[3];
      zw[4] = *(LAS unsigned*)(lds + rz);
    }
    FENCE();
#define RQ3(k) do { LD_W3R((k) == 0 ? 0 : (k) == 1 ? NR0 : NR1, (k) == 0 ? NR0 : (k) == 1 ? NR1 : NWH); FENCE(); } while (0)
    RQ3(0);
    FENCE();
    if constexpr (TSHI == 22) TX_(30);
    STG(5, hh[i] = up2(zw[i]); PIN(hh[i]));
    STG(5, xq[i] = FMA2(hh[i], ha[i], xq[i]); PIN(xq[i]));
    STG(5, t_[i] = FMA2(xq[i], cp, xq[i]); PIN(t_[i]));
    STG(5, xq[i] = FMA2(xq[i], cn, t_[i]); PIN(xq[i]));
    if constexpr (TSHI == 22) TX_(31);
    RQ3(1);
    if constexpr (TSHI == 22) TX_(32);
    float m3;
    {
      const f32x2 s2 = ((xq[0] + xq[1]) + (xq[2] + xq[3])) + xq[4];
      const f32x2 q2 = FMA2(xq[0], xq[0], xq[1] * xq[1]) + FMA2(xq[2], xq[2], FMA2(xq[3], xq[3], xq[4] * xq[4]));
      const f32x2 p2 = FMA2(xq[0], ca[0], xq[1] * ca[1]) + FMA2(xq[2], ca[2], FMA2(xq[3], ca[3], xq[4] * ca[4]));
      float sm3[3] = {s2[0] + s2[1], q2[0] + q2[1], p2[0] + p2[1]};
      wave_sums<3>(sm3);
      const float mean = sm3[0] * (1.f / (float)D_), qs = sm3[1], ps = sm3[2];
      const float m = rbf(mean);
      f32x2 d[5];
#pragma unroll
      for (int j = 0; j < 5; ++j) d[j] = (xq[j] - m) * ca[j];
      const float var = rbf(__builtin_fmaxf(__builtin_fmaf(-mean, mean, qs * (1.f / (float)D_)), 0.f));
      const float den = rbf(__builtin_amdgcn_sqrtf(rbf(var + 1e-5f)));
      const float rinv = __builtin_amdgcn_rcpf(den);
      m3 = rbf(__builtin_fmaf(__builtin_fmaf(-m, mc, ps * (1.f / (float)D_)), rinv, mb));
      __builtin_amdgcn_sched_barrier(0);
      if constexpr (TSHI == 22) TX_(33);
      RQ3(2);
      if constexpr (TSHI == 22) TX_(34);
      STG(5, hh[i] = FMA2(d[i], (f32x2{rinv, rinv}), sb[i]); PIN(hh[i]));
      STG(5, hh[i] -= m3; PIN(hh[i]));
    }
#pragma unroll
    for (int j = 0; j < 5; ++j) hp_[j] = pack2(hh[j][0], (j & 1) ? hh[j][1] : (j < 4 || (lane & 1) == 0) ? codedv(hh[j][1]) : hh[j][1]);
    *(LAS u32x4*)(lds + ry) = u32x4{hp_[0], hp_[1], hp_[2], hp_[3]};
    *(LAS unsigned*)(lds + rz) = hp_[4];
    BARRIER();
    {
      const unsigned tb = (unsigned)(s0 >> 4) * 20480u + (unsigned)(s0 & 15) * 16u + (unsigned)(tid >> 3) * 256u + (unsigned)(tid & 7) * 16u;
      const LAS char* pr = lds + L_XCH + (tid & 7) * YROW + (tid >> 3) * 16;
      const u32x4 v0 = *(const LAS u32x4*)(pr);
      const u32x4 v1 = *(const LAS u32x4*)(pr + (tid < 128 ? 1024 : 0));
      st128(v0, rW, tb, O_HP, 16);
      if (wv < 2) st128(v1, rW, tb + 64u * 256u, O_HP, 16);
    }
    FENCE();
    TS_(9);
    PRIO(1);
#pragma unroll
    for (int j = 0; j < 5; ++j) xp[j] = pack2(xq[j][0], xq[j][1]);
    *(LAS u32x4*)(lds + ry + 8 * YROW) = u32x4{xp[0], xp[1], xp[2], xp[3]};
    *(LAS unsigned*)(lds + rz + 8 * YROW) = xp[4];
    FENCE();
    STAMP(10);
    {
      const f32x2 s2 = ((hh[0] + hh[1]) + (hh[2] + hh[3])) + hh[4];
      const f32x2 q2 = FMA2(hh[0], hh[0], hh[1] * hh[1]) + FMA2(hh[2], hh[2], FMA2(hh[3], hh[3], hh[4] * hh[4]));
      const float mean = wave_sum2(s2[0] + s2[1]) * (1.f / (float)D_), qs = wave_sum2(q2[0] + q2[1]);
      const float var = rbf(__builtin_fmaxf(__builtin_fmaf(-mean, mean, qs * (1.f / (float)D_)), 0.f));
      const float den = rbf(__builtin_amdgcn_sqrtf(rbf(var + 1e-5f)));
      const u32x2 st = {canon(__float_as_uint(m3)), __float_as_uint(den)};
      __builtin_amdgcn_raw_buffer_store_b64(st, rW, lane == 0 ? (unsigned)srow * 8u : OOB, O_ST3, 16);
    }
    {
      BARRIER();
      const unsigned tb = (unsigned)(s0 >> 4) * 20480u + (unsigned)(s0 & 15) * 16u + (unsigned)(tid >> 3) * 256u + (unsigned)(tid & 7) * 16u;
      const LAS char* pr = lds + L_XCH + (8 + (tid & 7)) * YROW + (tid >> 3) * 16;
      st128(*(const LAS u32x4*)(pr), rW, tb, O_XO, 16);
      if (wv >= 6) st128(*(const LAS u32x4*)(pr + 256), rW, tb + 16u * 256u, O_XO, 16);
      SIG(P_L1);
      if (bx == 0 && wv == 5) __builtin_amdgcn_raw_buffer_store_b64(u32x2{(unsigned)epn, (unsigned)(epn >> 32)}, rW, lane == 0 ? 0u : OOB, O_EP, 16);
    }
    FENCE();
    PRIO(0);
    FENCE();
  }

  float sthr = PTHR, scmx = CMAX;
  unsigned scsh = CSH;
  asm volatile("" : "+s"(sthr), "+s"(scsh), "+s"(scmx));
  unsigned xcw = (unsigned)(L_XCH + gm * 5120 + gk * 2048) + l16, xcr = (unsigned)(L_XCH + gm * 5120 + 2048 - gk * 2048) + l16, xcf = (unsigned)(L_CTL + gp * 4);
  asm volatile("" : "+v"(xcw), "+v"(xcr), "+v"(xcf));
  unsigned long long gkm;
  asm volatile("s_cmp_lg_u32 %1, 0\n\ts_cselect_b64 %0, -1, 0" : "=s"(gkm) : "s"(gk) : "scc");
  static_assert(KL >= 3 && KG <= 54 - 2 * KL, "the weights are read in front of the signal");
#define EXCHANGE(ph)                                                                        \
  do {                                                                                      \
    f32x4 e0, e1, e2;                                                                       \
    unsigned spins = 0;                                                                     \
    for (;;) {                                                                              \
      const unsigned fl = *(volatile LAS unsigned*)(lds + xcf);                                                           \
      e0 = *(volatile LAS f32x4*)(lds + xcr);                                               \
      e1 = *(volatile LAS f32x4*)(lds + xcr + 1024);                                        \
      e2 = *(volatile LAS f32x4*)(lds + xcr + 2048);                                        \
      if (__builtin_amdgcn_readfirstlane(fl) >= (ph)) break;                                \
      SPIN_STEP();                                                                          \
      if (spins > SPIN_CAP) break;                                                          \
    }                                                                                       \
    asm volatile("s_nop 3" : "+v"(acc[3]), "+v"(acc[4]));        \
    STG(12, e[i] = (i < 4 ? acc[3][i & 3] + e0[i & 3] : i < 8 ? acc[4][i & 3] + e1[i & 3] : acc[2][i & 3] + e2[i & 3]); PIN(e[i])); \
  } while (0)
#define KLOOP(CD, PB, BLK, VA, AOFF, XG, XS, ph)                                            \
  asm volatile("s_nop 1" : "+v"(A[0]));                                                     \
  sfor<0, 50>([&](auto M_) __attribute__((always_inline)) {                                 \
    constexpr int m = decltype(M_)::v, i = kl_i(m), j = kl_j(m), f = m / 4 + 1, c = m % 4;  \
    mf<i == 0 && (CD || !HFI || j < 2)>(acc[j], __builtin_bit_cast(u32x4, __builtin_shufflevector(Bh[2 * m], Bh[2 * m + 1], 0, 1, 2, 3, 4, 5, 6, 7)), A[i]); \
    SB();                                                                                   \
    constexpr int n0_ = m < KG ? (m * (100 - 2 * (PB))) / KG : 100, n1_ = m < KG ? ((m + 1) * (100 - 2 * (PB))) / KG : 100; \
    if constexpr (n1_ > n0_) RD_BH(2 * (PB) + n0_, BLK);                                    \
    if constexpr (n1_ > n0_ + 1) RD_BH(2 * (PB) + n0_ + 1, BLK);                            \
    static_assert(n1_ <= n0_ + 2 && (f > 9 || kl_i(m + 1) < f), "order");                   \
    if constexpr (f < 10) {                                                                 \
      if constexpr (c == 0) { m_ = CD ? amin2(A[f]) : __builtin_amdgcn_fmed3f(__builtin_fabsf(__uint_as_float(A[f][0])), __builtin_fabsf(__uint_as_float(A[f][2])), scmx); PIN(m_); } \
      if constexpr (c == 1) CMP_BAD(CD, bm, m_);                                            \
      if constexpr (c == 2 && CD) A[f][0] -= scsh;                                          \
      if constexpr (c == 3 && CD) { A[f][2] -= scsh; PIN(A[f]); }                           \
      if constexpr (c == 3) RETRY(CD, A[f], bm, VA, sa[f / 5], AOFF + (f % 5) * 2048, PHI); \
    }                                                                                       \
    if constexpr (m % 5 == 2 || m % 5 == 4) { XS(2 * (m / 5) + (m % 5 == 4)); }             \
    XG(m);                                                                                  \
                                \
    if constexpr (m == 51 - 2 * KL) { asm volatile("s_nop 3" : "+v"(acc[0])); *(volatile LAS f32x4*)(lds + xcw) = acc[0]; } \
    if constexpr (m == 52 - 2 * KL) *(volatile LAS f32x4*)(lds + xcw + 1024) = acc[1];      \
    if constexpr (m == 53 - 2 * KL) asm volatile("s_mov_b64 exec, %2\n\tds_write_b128 %0, %1 offset:2048\n\ts_mov_b64 exec, -1" :: "v"(xcw), "v"(acc[2]), "s"(gkm) : "memory"); \
    if constexpr (m == 54 - 2 * KL) SIG(ph);                                                \
    if constexpr (TSHI == 20 && m % 5 == 0) TX_((CD ? 30 : 44) + m / 5);                    \
    SB();                                                                                   \
  })
  {
    f32x4 acc[5];
    s16x4 Bh[100];
    u32x4 A[10];
    float m_, e[12];
    unsigned long long bm;
    unsigned sa[2] = {ROT ? (unsigned)(nt & 1) * 10240u : 0u, ROT ? 10240u - (unsigned)(nt & 1) * 10240u : 10240u};
    asm volatile("" : "+s"(sa[0]), "+s"(sa[1]));
    const unsigned va = (unsigned)(row >> 4) * 20480u + gk * 1024 + l16;
#define LD_A1(x) A[x] = __builtin_amdgcn_raw_buffer_load_b128(rW, va, sa[(x) / 5] + (O_HP + ((x) % 5) * 2048), 16)
    unsigned bb[3][2];
#pragma unroll
    for (int c = 0; c < 6; ++c) {
      bb[c >> 1][c & 1] = (unsigned)gk * 5120u + ((unsigned)lane >> 2) * 160u + ((unsigned)lane & 3u) * 8u + sa[c & 1] * 5u + (c < 2 ? 96u - 96u * gk : c < 4 ? 64u : 96u * gk);
      PIN(bb[c >> 1][c & 1]);
    }
#define RD_BH(h, BLK) Bh[h] = __builtin_amdgcn_ds_read_tr16_b64_v4i16((LAS s16x4*)(lds + bb[kl_j((h) / 2) < 2 ? 0 : kl_j((h) / 2) == 2 ? 1 : 2][kl_i((h) / 2) / 5] + (BLK) + (kl_i((h) / 2) % 5) * 10240 + (kl_j((h) / 2) % 3 == 1 ? 32 : 0) + ((h) & 1) * 2560))
#define BLK1 0
    FENCE();
#pragma unroll
    for (int n = 0; n < 2 * PBE; ++n) RD_BH(n, BLK1);
    FENCE();
    if (DLY1) __builtin_amdgcn_s_sleep(DLY1);
    PRIO(3);
    TS_(10);
#pragma unroll
    for (int i = 0; i < NA1; ++i) { LD_A1(i); SB(); }
    PRIO(1);
#pragma unroll
    for (int n = 2 * PBE; n < 2 * PB1; ++n) RD_BH(n, BLK1);
    FENCE();
#pragma unroll
    for (int i = NA1; i < NAF; ++i) { LD_A1(i); SB(); }
    u32x2 s3v;
    FENCE();
    if (kh) PRIO(PH1); else PRIO(PHI);
    STAMP(11);
    const int sh = (nt >> 1) == 0 ? 1 : (nt >> 1) == 1 ? -1 : (nt >> 1) == 2 ? 2 : -2;
    const int rr = row + sh, rw = rr & (S_ - 1);
    unsigned keep = rr == rw ? 0xffffffffu : 0u;
    unsigned oh1 = ofr - (unsigned)(row >> 4) * 20480u - (unsigned)l15 * 16u + (unsigned)(rw >> 4) * TS + (unsigned)(rw & 15) * 16u + O_H1;
    asm volatile("" : "+v"(keep), "+v"(oh1));
    asm volatile("" : "+s"(w3s[2]), "+s"(w3s[3]));
    if constexpr (TSHI == 21) { TX_(29); sfor<0, 10>([&](auto I_) __attribute__((always_inline)) { PIN(A[decltype(I_)::v]); TX_(30 + decltype(I_)::v); __builtin_amdgcn_s_sleep(1); }); }
    WAIT_ALL(P_L1);
    SB();
    CHECK_A(true, A[0], 0, NAF, LD_A1);
    SB();
#define XG1(m) do { if constexpr ((m) % 2 == 0 && NAF + (m) / 2 < 10) LD_A1(NAF + (m) / 2); if constexpr ((m) == 13) s3v = __builtin_amdgcn_raw_buffer_load_b64(rW, (unsigned)row * 8u, O_ST3, 16); if constexpr ((m) == 4) TS_(11); } while (0)
#define XSL(u, s0, n, f0) do { if constexpr ((u) >= (s0) && (((u) - (s0) + 1) * (n)) / (20 - (s0)) > (((u) - (s0)) * (n)) / (20 - (s0))) LD_W3U((f0) + (((u) - (s0)) * (n)) / (20 - (s0))); } while (0)
#define XS1(u) XSL(u, SL1, NL1, NWH)
    KLOOP(true, PB1, BLK1, va, O_HP, XG1, XS1, P_XF1);
    TS_(12);
    if (__builtin_expect(bad1(s3v[0]), 0)) {
      unsigned spins = 0;
      do {
        __builtin_amdgcn_s_sleep(RSLEEP);
        s3v = __builtin_amdgcn_raw_buffer_load_b64(rW, (unsigned)row * 8u, O_ST3, 16);
        SPIN_STEP();
      } while (bad1(s3v[0]) && spins < RETRY_CAP);
    }
    const float ld = __uint_as_float(s3v[1]), lr = __builtin_amdgcn_rcpf(ld);
    float gca = lr * GC1, gcb = lr * lr * lr * GC2;
    asm volatile("" : "+v"(gca), "+v"(gcb));
    EXCHANGE((unsigned)P_XF1);
    TS_(13);
    {
      unsigned pk[6];
      if (gk == 0) {
        gelu_s<12>(e, gca, gcb, ld, pk);
        __builtin_amdgcn_raw_buffer_store_b64(u32x2{pk[4] & keep, pk[5] & keep}, rW, oh1 + 1024, 0, 16);
      } else {
        gelu_s<8>(e, gca, gcb, ld, pk);
      }
      st128(widen(pk[0] & keep, pk[1] & keep, pk[2] & keep, pk[3] & keep), rW, oh1 + (unsigned)(g & 1) * 504u, 0, 16);
    }
    FENCE();
    TS_(14);
    PRIO(0);
    LD_W3R(NWH + NL1, NWH + NL1 + NP0);
    FENCE();
    WAIT_ALL(P_XF1);
#pragma unroll
    for (int i = 0; i < 10; ++i) *(LAS u32x4*)(lds + 20480 + wv * 5120 + (i / 5) * 40960 + (i % 5) * 1024 + l16) = WS2[i];
    SIG(P_G1);
    STAMP(13);
    FENCE();
    LD_W3R(NWH + NL1 + NP0, NWH + NL1 + NP1);
    FENCE();
    const unsigned va2 = (unsigned)(row >> 4) * TS + gk * 1024 + l16;
    u32x2 hpv[3];
#define LD_A2(x) A[x] = __builtin_amdgcn_raw_buffer_load_b128(rW, va2, sa[(x) / 5] + (O_H1 + ((x) % 5) * 2048), 16)
    u32x4 hp4;
    const unsigned ofw = ofr + (unsigned)(g & 1) * 504u;
    hp4 = __builtin_amdgcn_raw_buffer_load_b128(rW, ofw, O_HP, 16);
    hpv[2] = __builtin_amdgcn_raw_buffer_load_b64(rW, gk ? OOB : ofr + 1024, O_HP, 16);
    FENCE();
    WAIT_ALL(P_G1);
    if (DLY2) __builtin_amdgcn_s_sleep(DLY2);
    PRIO(3);
    TS_(15);
#pragma unroll
    for (int i = 0; i < NA1; ++i) { LD_A2(i); SB(); }
    PRIO(1);
#define BLK2 20480
#pragma unroll
    for (int n = 0; n < 2 * PB2; ++n) RD_BH(n, BLK2);
    FENCE();
#pragma unroll
    for (int i = NA1; i < NAF; ++i) { LD_A2(i); SB(); }
    FENCE();
    if (kh) PRIO(PH2); else PRIO(PHI);
    float hf[12];
    { const u32x4 n_ = widen(hp4[0], hp4[1], hp4[2], hp4[3]); hpv[0] = u32x2{n_[0], n_[1]}; hpv[1] = u32x2{n_[2], n_[3]}; }
#pragma unroll
    for (int j = 0; j < 3; ++j) { hpv[j][0] -= CSH; PIN(hpv[j]); }
    STG(12, hf[i] = ((i & 1) ? hi2f(hpv[i >> 2][(i >> 1) & 1]) : lo2f(hpv[i >> 2][(i >> 1) & 1])) + __uint_as_float(s3v[0]); PIN(hf[i]));
    acc[3] = f32x4{hf[0], hf[1], hf[2], hf[3]}; acc[4] = f32x4{hf[4], hf[5], hf[6], hf[7]};
    acc[2] = gk ? f32x4{0.f, 0.f, 0.f, 0.f} : f32x4{hf[8], hf[9], hf[10], hf[11]};
    PIN(acc[2]); PIN(acc[3]); PIN(acc[4]);
    STAMP(14);
    if constexpr (TSHI == 21) { TX_(43); sfor<0, 10>([&](auto I_) __attribute__((always_inline)) { PIN(A[decltype(I_)::v]); TX_(44 + decltype(I_)::v); __builtin_amdgcn_s_sleep(1); }); }
    SB();
    CHECK_A(false, A[0], 0, NAF, LD_A2);
    SB();
#define XG2(m) do { if constexpr ((m) % 2 == 0 && NAF + (m) / 2 < 10) LD_A2(NAF + (m) / 2); if constexpr ((m) == 4) TS_(16); } while (0)
#define XS2(u) XSL(u, SL2, NL2, NWH + NL1 + NP1)
    KLOOP(false, PB2, BLK2, va2, O_H1, XG2, XS2, P_XF2);
    TS_(17);
    EXCHANGE((unsigned)P_XF2);
    TS_(18);
    unsigned xk[6];
    {
      if (!HFI) STG(12, e[i] += hf[i]; PIN(e[i]));
      STG(3, e[4 * i + 1] = plainv(e[4 * i + 1]); PIN(e[4 * i + 1]));
      STG(6, xk[i] = pack2(e[2 * i], e[2 * i + 1]); PIN(xk[i]));
      st128(widen(xk[0], xk[1], xk[2], xk[3]), rW, ofw, O_X1, 16);
      if (gk == 0) __builtin_amdgcn_raw_buffer_store_b64(u32x2{xk[4], xk[5]}, rW, ofr + 1024, O_X1, 16);
    }
    FENCE();
    TS_(19);
    PRIO(1);
    SIG(P_G2);
    float v[12];
    float s = 0.f;
#pragma unroll
    for (int j = 0; j < 3; ++j) {
      v[4 * j] = lo2f(xk[2 * j]); v[4 * j + 1] = hi2f(xk[2 * j]); v[4 * j + 2] = lo2f(xk[2 * j + 1]); v[4 * j + 3] = hi2f(xk[2 * j + 1]);
      if (j < 2 || gk == 0) s += (v[4 * j] + v[4 * j + 1]) + (v[4 * j + 2] + v[4 * j + 3]);
    }
    s = swap_add32(swap_add16(s));
    const float mean = s * (gk ? (1.f / 32.f) : (1.f / 48.f));
    float m2 = 0.f;
#pragma unroll
    for (int i = 0; i < 12; ++i) {
      const float dd = v[i] - mean;
      if (i < 8 || gk == 0) m2 += dd * dd;
    }
    m2 = swap_add32(swap_add16(m2));
    {
      u32x2 st = {canon(__float_as_uint(mean)), __float_as_uint(m2)};
      __builtin_amdgcn_raw_buffer_store_b64(st, rW, g == 0 ? (unsigned)(((row >> 4) * 16 + nt * 2 + gk) * 16 + l15) * 8u : OOB, O_ST4, 16);
    }
    FENCE();
    PRIO(0);
    FENCE();
    STAMP(16);
  }
  {
    constexpr int NR3 = 40 - NWH - NL1 - NP1 - NL2;
#define NOW4D(...) __VA_ARGS__
#define LD_W4(f) W4[f] = __builtin_amdgcn_raw_buffer_load_b128(rW, l16, w4a + (f) * 1024, 16)
    const unsigned va4 = O_H + (unsigned)(m0 >> 4) * TS + (unsigned)k4 * 1024u;
#define A4OFF(x) (((x) / NS) * TS + ((x) % NS) * 1024)
#define LD_A4(x) A4[(x) % AW4] = __builtin_amdgcn_raw_buffer_load_b128(rW, l16, va4 + A4OFF(x), 16)
    const int lane0 = lane;
#define LANES() int lane = lane0; PIN(lane); const int l15 = lane & 15, g = lane >> 4; const unsigned l16 = (unsigned)lane * 16u; \
    const unsigned fa = (unsigned)L_CTL + ((unsigned)lane & 7u) * 4u
#define FLG(a) *(volatile LAS unsigned*)(lds + (a))
    auto g3 = [&](auto KH_, u32x4 (&W4)[35], u32x4 (&A4)[AW4], u32x2 (&x1v)[3]) __attribute__((always_inline)) {
      constexpr int KH = decltype(KH_)::v, NS = 7, NW0 = KH ? 6 : AW4;
      LANES();
      const unsigned hw0 = (unsigned)(m0 >> 4) * TS + (unsigned)l15 * 16u + (unsigned)(g >> 1) * 256u + O_H;
      const unsigned csp = (unsigned)(L_CS3 + wv * 128 + g * 16), f1 = (unsigned)L_CTL + 16u + ((unsigned)lane & 3u) * 4u;
      u32x2 stv[4];
#define LD_ST() _Pragma("unroll") for (int a = 0; a < 4; ++a) stv[a] = __builtin_amdgcn_raw_buffer_load_b64(rW, (unsigned)((((m0 >> 4) + mg) * 16 + 4 * g) * 16 + l15) * 8u, O_ST4 + a * 128, 16)
#define ST_BAD() bad1(max(max(stv[0][0], stv[1][0]), max(stv[2][0], stv[3][0])))
      const u32x4 ones = {0x3f803f80u, 0x3f803f80u, 0x3f803f80u, 0x3f803f80u};
      f32x4 c3a = {0.f, 0.f, 0.f, 0.f}, c3b = {0.f, 0.f, 0.f, 0.f};
#define CS3R(a, b) do { _Pragma("unroll") for (int f_ = (a); f_ < (b); f_ += 2) { c3a = mfma16(W3[f_], ones, c3a); if (!KH || f_ < 20) c3b = mfma16(W3[f_ + 1], ones, c3b); } } while (0)
      TS_(20);
      PRIO(PHI);
      LD_W3R(40 - NR3, 40);
      FENCE();
      const unsigned so = O_X1 + (unsigned)mt * 81920u + (unsigned)wv * 10240u, d0 = (unsigned)(KH ? L_XB : L_XA) + (unsigned)mg * 10240u;
      CS3R(0, KH ? KC3B : KC3);
      asm volatile("" : "+v"(c3a), "+v"(c3b));
      FENCE();
      TS_(21);
      WAIT_2(KH ? P_XF2 : P_G2, KH ? P_G2 : P_XF2);
      if (KH ? DLY3B : DLY3) __builtin_amdgcn_s_sleep(KH ? DLY3B : DLY3);
      PIN(W3[38]); PIN(W3[39]);
      if (KH) {
#pragma unroll
        for (int f = 0; f < 10; ++f) W4[f] = W3[21 + 2 * f];
      }
      FENCE();
      PRIO(3);
      TS_(22);
#define STG_DMA() do { dma<4, 1>(l16, dW, so, d0); dma<4, 1>(l16, dW, so + 4096, d0 + 4096); dma<1, 1>(l16, dW, so + 8192, d0 + 8192); dma<1, 1>(l16, dW, so + 9216, d0 + 9216); } while (0)
      if (KH) {
        asm volatile("buffer_load_dwordx2 %0, %4, %5, %6 offen sc1\n\tbuffer_load_dwordx2 %1, %4, %5, %6 offen offset:128 sc1\n\t"
                     "buffer_load_dwordx2 %2, %4, %5, %6 offen offset:256 sc1\n\tbuffer_load_dwordx2 %3, %4, %5, %6 offen offset:384 sc1"
                     : "=&v"(stv[0]), "=&v"(stv[1]), "=&v"(stv[2]), "=&v"(stv[3]) : "v"((unsigned)((((m0 >> 4) + mg) * 16 + 4 * g) * 16 + l15) * 8u), "s"(dW), "s"(O_ST4) : "memory");
      }
      STG_DMA();
      FENCE();
      TS_(23);
      PRIO(PHI);
      CS3R(KH ? KC3B : KC3, 40);
      if (l15 == 0) { *(LAS f32x4*)(lds + csp) = c3a; *(LAS f32x4*)(lds + csp + 64) = c3b; }
      FENCE();
      if (KH) {
        asm volatile("s_waitcnt vmcnt(10)" : "+v"(stv[0]), "+v"(stv[1]), "+v"(stv[2]), "+v"(stv[3]));
        if (__builtin_expect(ST_BAD(), 0)) {
          unsigned spins = 0;
          do {
            __builtin_amdgcn_s_sleep(RSLEEP);
            LD_ST();
            SPIN_STEP();
          } while (ST_BAD() && spins < RETRY_CAP);
        }
        const float m0_ = __uint_as_float(stv[0][0]), q0 = __uint_as_float(stv[0][1]), m1 = __uint_as_float(stv[1][0]), q1 = __uint_as_float(stv[1][1]);
        const float m2_ = __uint_as_float(stv[2][0]), q2 = __uint_as_float(stv[2][1]), m3 = __uint_as_float(stv[3][0]), q3 = __uint_as_float(stv[3][1]);
        float sm = 48.f * (m0_ + m2_) + 32.f * (m1 + m3);
        sm = swap_add32(swap_add16(sm));
        const float mean = sm * (1.f / (float)D_);
        const float d0_ = m0_ - mean, d1 = m1 - mean, d2 = m2_ - mean, d3 = m3 - mean;
        float qs = (q0 + q1) + (q2 + q3) + 48.f * (d0_ * d0_ + d2 * d2) + 32.f * (d1 * d1 + d3 * d3);
        qs = swap_add32(swap_add16(qs));
        const float var = rbf(qs * (1.f / (float)D_));
        const float den = rbf(__builtin_amdgcn_sqrtf(rbf(var + 1e-5f))), r = __builtin_amdgcn_rcpf(den);
        float rc1 = r * GC1;
        PIN(rc1);
        if (g == 0) *(volatile LAS f32x4*)(lds + L_ST4 + (mg * 16 + l15) * 16) = f32x4{rbf(mean), rc1, r * r * r * GC2, den};
        SIG(P_SF);
      }
      FENCE();
      TS_(24);
      VMW(0);
      TS_(25);
      {
        unsigned spins = 0;
        for (;;) {
          u32x4 a[10];
          float mx = 0.f;
#pragma unroll
          for (int i = 0; i < 10; ++i) a[i] = *(volatile LAS u32x4*)(lds + d0 + i * 1024 + l16);
#pragma unroll
          for (int i = 0; i < 10; ++i) mx = amax3(mx, a[i]);
          if (__builtin_expect(__builtin_amdgcn_ballot_w64(mx >= PTHR) == 0ull, 1)) break;
          SPIN_STEP();
          if (spins > RETRY_CAP) break;
          PRIO(0);
          __builtin_amdgcn_s_sleep(RSLEEP);
          STG_DMA();
          PRIO(PHI);
          asm volatile("s_waitcnt vmcnt(0)" ::: "memory");
        }
        SIG(P_SA);
      }
      TS_(26);
      STAMP(17);
      FENCE();
      constexpr int NP = KH ? NPH : NPK, F0 = KH ? 10 : 0, NEQ = NP + 6, EQ0 = KH ? EC0 : EA0, EQ1 = KH ? EC1 : EA1, NL0 = F0 + NEQ, NLT = (KH ? 30 : 35) - NL0;
      const unsigned pks = (unsigned)(KH ? L_PB + NPK * 1024 + mg * (NPH * 1024) : wv < 3 ? wv * (NPK * 1024) : L_PB);
      constexpr auto ecnt = [](int q) { return q <= EQ0 ? 0 : q >= EQ1 ? NEQ : ((q - EQ0) * NEQ + EQ1 - EQ0 - 1) / (EQ1 - EQ0); };
#define ESLOT(q) sfor<ecnt(q), ecnt((q) + 1)>([&](auto U_) __attribute__((always_inline)) { constexpr int u = decltype(U_)::v; \
        if constexpr (u < NP) { NOW4D(dma<1, 1>(l16, dW, w4a + (F0 + 6 + u) * 1024, pks + u * 1024)); } else LD_W4(F0 + u - NP); })
      static_assert(3 * NPK <= 32 && NPK + 4 * NPH <= 36 && MV0 + NP <= 20 && NLT <= 19 && NLT >= MV0, "w4");
      sfor<0, 2>([&](auto P_) __attribute__((always_inline)) {
        constexpr int p = decltype(P_)::v, NV = KH ? 12 : 16;
        TS_(28 + 8 * p + 0);
        if (p == 0) WAIT_2(P_SA, P_G2); else WAIT_HALF(1, P_SA);
        f32x4 acc[4];
        PRIO(KH ? (p ? PK1 : PK2) : PK0);
        TS_(28 + 8 * p + 1);
        u32x4 Ar[AD3][2];
        unsigned ab[2] = {l16 + (unsigned)((p ? L_XB : L_XA) + (KH ? hb * 30720 : 0)), l16 + (unsigned)((p ? L_XB : L_XA) + 20480 - (KH ? hb * 10240 : 0))};
        const unsigned sp = (unsigned)(L_ST4 + p * 512 + l15 * 16), fp = (unsigned)(L_CTL + (wv ^ 2) * 4), xs = (unsigned)(32768 + p * 4096) + l16;
        PIN(ab[0]);
        if (KH) PIN(ab[1]);
        f32x4 cs[3], st[2], rx;
        unsigned sf_, sx_;
#define RD_A3(s, h) Ar[(s) % AD3][h] = *(const LAS u32x4*)(lds + (KH ? ab[h] : ab[0] + (h) * 20480) + (s) * 1024)
#pragma unroll
        for (int s = 0; s < AD3 - 1; ++s) { RD_A3(s, 0); RD_A3(s, 1); }
        SB();
        sfor<0, 20>([&](auto S_) __attribute__((always_inline)) {
          constexpr int s = decltype(S_)::v;
          constexpr bool rd = s + AD3 - 1 < 20, T2 = !KH || s < 10;
          mf<s == 0>(acc[0], W3[2 * s], Ar[s % AD3][0]);
          SB();
          if constexpr (rd) RD_A3(s + AD3 - 1, 0);
          else if constexpr (s == 18) cs[0] = *(const LAS f32x4*)(lds + csp);
          else st[0] = *(const volatile LAS f32x4*)(lds + sp + (KH ? hb * 256 : 0));
          SB();
          mf<s == 0>(acc[1], W3[2 * s], Ar[s % AD3][1]);
          SB();
          if constexpr (rd) RD_A3(s + AD3 - 1, 1);
          else if constexpr (s == 18) cs[1] = *(const LAS f32x4*)(lds + csp + 64);
          else st[1] = *(const volatile LAS f32x4*)(lds + sp + 256 - (KH ? hb * 256 : 0));
          SB();
          if constexpr (T2) { mf<s == 0>(acc[2], W3[2 * s + 1], Ar[s % AD3][0]); SB(); }
          if constexpr (p == 0 && s == 15) sf_ = FLG(f1);
          if constexpr (p == 0 && s == 18) NEED_AT(sf_, f1, P_SF);
          if constexpr (p == 0) ESLOT(s);
          if constexpr (p == 1 && s >= 1 && s <= NLT) LD_W4(NL0 + s - 1);
          if constexpr (p == 1 && s == MV0 - 1) VMW(MV0 + 1);
          SB();
          if constexpr (T2) { mf<s == 0>(acc[3], W3[2 * s + 1], Ar[s % AD3][1]); SB(); }
          if constexpr (KH && s + AD3 == 10) { ab[0] -= hb * 20480; ab[1] -= hb * 20480; PIN(ab[0]); PIN(ab[1]); }
          if constexpr (KH && s == 12) { *(volatile LAS f32x4*)(lds + xs + mg * 1024) = acc[3]; SIG(p ? P_S1 : P_S0); }
          if constexpr (KH && s == 14) sx_ = FLG(fp);
          if constexpr (KH && s == 17) { NEED_AT(sx_, fp, p ? P_S1 : P_S0); rx = *(const volatile LAS f32x4*)(lds + xs + (mg ^ 2) * 1024); cs[2] = *(const volatile LAS f32x4*)(lds + csp + (hb ? -192 : 320)); }
          if constexpr (p == 1 && s >= MV0 && s < MV0 + NP) W4[F0 + 6 + s - MV0] = *(const LAS u32x4*)(lds + pks + l16 + (s - MV0) * 1024);
          if constexpr (p == 1 && s >= 1 && s < 4 - KH) {
            static_assert(H0S >= 4 && H0S < 12, "x1");
            const int tu = KH == 0 ? (s == 3 ? 3 : s - 1) : wv < 6 ? 2 : s == 1 ? wv & 1 : wv - 4, ju = KH == 0 ? wv : wv < 6 ? 2 * wv - 6 - s : 4;
            x1v[s - 1] = *(const LAS u32x2*)(lds + (tu < 2 ? L_XA : L_XB - 40960) + tu * 20480 + (nt * 10 + 2 * ju) * 256 + (g >> 1) * 256 + (g & 1) * 8 + l15 * 16);
          }
          if constexpr (p == 1 && s == H0S) SIG(P_H0);
          if constexpr (s == 9) TS_(28 + 8 * p + 2);
          SB();
        });
        TS_(28 + 8 * p + 3);
        PRIO(KH && p ? PE1 : PE0);
        SIG(p ? P_KB : P_KA);
        if constexpr (KH) asm volatile("s_nop 3" : "+v"(acc[0]), "+v"(acc[1]), "+v"(acc[2])); else asm volatile("s_nop 3" : "+v"(acc[0]), "+v"(acc[1]), "+v"(acc[2]), "+v"(acc[3]));
        STAMP(18 + 2 * p);
        {
          float e[NV], x[NV];
          unsigned pk[NV / 2];
#define SQ(i) st[((i) >> 2) & 1]
#define EPS(q) do { if constexpr (p == 0) { ESLOT(20 + (q)); } if constexpr (p == 1 && (q) == HG - 1) sf_ = FLG(fa); \
                    if constexpr (p == 1 && (q) == HG) { NEED_AT(sf_, fa, P_H0); _Pragma("unroll") for (int x = 0; x < NW0; ++x) { LD_A4(x); } } SB(); } while (0)
          if constexpr (KH) STG(4, x[i] = __builtin_fmaf(-st[0][0], cs[2][i], rx[i]); PIN(x[i]));
          STG(NV, e[i] = __builtin_fmaf(-SQ(i)[0], cs[i >> 3][i & 3], acc[i >> 2][i & 3]); PIN(e[i]));
          if constexpr (KH) STG(4, e[8 + i] += x[i]; PIN(e[8 + i]));
          TS_(28 + 8 * p + 4);
          EPS(0);
          STG(NV, x[i] = e[i] * e[i]; PIN(x[i]));
          EPS(1);
          STG(NV, x[i] = __builtin_fmaf(x[i], SQ(i)[2], SQ(i)[1]); PIN(x[i]));
          EPS(2);
          STG(NV, x[i] *= e[i]; PIN(x[i]));
          EPS(3);
          STG(NV, x[i] = __builtin_amdgcn_exp2f(x[i]); PIN(x[i]));
          TS_(28 + 8 * p + 5);
          EPS(4);
          STG(NV, x[i] = __builtin_fmaf(x[i], SQ(i)[3], SQ(i)[3]); PIN(x[i]));
          EPS(5);
          STG(NV, x[i] = __builtin_amdgcn_rcpf(x[i]); PIN(x[i]));
          EPS(6);
          STG(NV, e[i] *= x[i]; PIN(e[i]));
          TS_(28 + 8 * p + 6);
          EPS(7);
          STG(NV / 4, e[4 * i + 1] = codedv(e[4 * i + 1]); PIN(e[4 * i + 1]));
          STG(NV / 2, pk[i] = pack2(e[2 * i], e[2 * i + 1]); PIN(pk[i]));
          const unsigned hw1 = hw0 + (unsigned)((g & 1) ^ (KH ? hb : 0)) * TS;
          st128(widen(pk[0], pk[1], pk[2], pk[3]), rW, hw1, (unsigned)(sb3 + jl) * 512u + 2 * p * TS, 16);
          if constexpr (KH) __builtin_amdgcn_raw_buffer_store_b64(u32x2{pk[4], pk[5]}, rW, hw0 + (unsigned)(g & 1) * 8u, two ? (unsigned)(sb3 + jm) * 512u + (unsigned)(2 * p + hb) * TS : OOB, 16);
          else st128(widen(pk[4], pk[5], pk[6], pk[7]), rW, hw1, (unsigned)(sb3 + jm) * 512u + 2 * p * TS, 16);
          FENCE();
          EPS(8);
        }
        TS_(28 + 8 * p + 7);
        STAMP(19 + 2 * p);
      });
    };
#define PB(t) (81920 - (t) * 40960)
    const __amdgpu_buffer_rsrc_t rO = __builtin_amdgcn_make_buffer_rsrc((void*)out, 0, SD2, RS3);
    auto g4 = [&](auto KD_, u32x4 (&W4)[35], u32x4 (&A4)[AW4], u32x2 (&x1v)[3]) __attribute__((always_inline)) {
      constexpr int KD = decltype(KD_)::v, NS = KD == 1 ? 6 : 7, PR = KD ? P41 : P40;
      constexpr int GS = NS + (KD == 0 ? GSA : KD == 1 ? GSB : GSC);
      constexpr auto nreq = [](int x) { int n = AW4; for (int y = 0; y <= x; ++y) { int m = y + AW4 < 4 * NS ? y + AW4 : 4 * NS; if (y < GS && m > 2 * NS) m = 2 * NS; n = m < n + 3 ? (m > n ? m : n) : n + 3; } return n; };
      LANES();
      TS_(44);
      if constexpr (KD) {
#pragma unroll
        for (int x = 6; x < AW4; ++x) { LD_A4(x); }
      }
      if constexpr (KD == 2) {
#pragma unroll
        for (int f = 30; f < 35; ++f) { LD_W4(f); }
      }
      FENCE();
      PRIO(PR);
      u32x2 xv[2];
      unsigned fl_, fh_, fu_, pu_;
      f32x4 acc[2][5], rs[4], gq;
      float u_[4], y_[4], m4_;
      unsigned long long bm4;
      unsigned pw = (unsigned)(wv * 5120) + l16;
      PIN(pw);
      const unsigned fo0 = (unsigned)((m0 >> 4) * 20480 + ((n0 + 4 * g) >> 3) * 256 + (g & 1) * 8 + l15 * 16), oo0 = (unsigned)((m0 + l15) * D_ + n0 + 4 * g) * 2u;
#define U_LD(r, t, j) xv[r] = __builtin_amdgcn_raw_buffer_load_b64(rW, fo0, O_XO + (unsigned)((t) * 20480 + (j) * 512), 16)
#define U_PC(q, t, j, y)                                                                               \
  do {                                                                                                  \
    if constexpr ((q) == 0) { fu_ = FLG(fa); pu_ = (unsigned)(PB(t) + (j) * 1024) + l16; PIN(pu_); }    \
    if constexpr ((q) == 1) { gq = *(const LAS f32x4*)(lds + L_GB + ((j) * 16 + 4 * g) * 4); }          \
    if constexpr ((q) >= 1 && (q) < 5) { constexpr int i = (q) - 1; u_[i] = (i & 1) ? hi2f(x1v[y][i >> 1]) : lo2f(x1v[y][i >> 1]); PIN(u_[i]); } \
    if constexpr ((q) == 5) NEED_AT(fu_, fa, P_P0 + 1 + (t));                                           \
    if constexpr ((q) >= 6 && (q) < 10) rs[(q) - 6] = *(const volatile LAS f32x4*)(lds + pu_ + ((q) - 6) * 5120); \
    if constexpr ((q) >= 11 && (q) < 27) { constexpr int w = ((q) - 11) >> 1, h = (((q) - 11) & 1) * 2;  \
      u_[h] += rs[w & 3][h]; PIN(u_[h]); u_[h + 1] += rs[w & 3][h + 1]; PIN(u_[h + 1]);                 \
      if constexpr (h == 2 && w < 4) rs[w] = *(const volatile LAS f32x4*)(lds + pu_ + (4 + w) * 5120); } \
    if constexpr ((q) >= 6 && (q) < 10) { constexpr int i = (q) - 6; y_[i] = (i & 1) ? hi2f(xv[0][i >> 1]) : lo2f(xv[0][i >> 1]); PIN(y_[i]); } \
    if constexpr ((q) == 27 || (q) == 28) { constexpr int h = ((q) - 27) * 2; u_[h] = __builtin_fmaf(u_[h], gq[h], y_[h]); PIN(u_[h]); u_[h + 1] = __builtin_fmaf(u_[h + 1], gq[h + 1], y_[h + 1]); PIN(u_[h + 1]); } \
    if constexpr ((q) == 29) __builtin_amdgcn_raw_buffer_store_b64(u32x2{pack2(u_[0], u_[1]), pack2(u_[2], u_[3])}, rO, oo0, (unsigned)((t) * (16 * D_ * 2) + (j) * 32), 0); \
  } while (0)
      {
        unsigned long long b0;
        const float m0_ = amin2(A4[0]);
        CMP_BAD(1, b0, m0_);
        A4[0][0] -= scsh; A4[0][2] -= scsh;
        RETRY_C(A4[0], b0, l16, va4, 0, PR);
        asm volatile("s_nop 0" : "+v"(A4[0]));
      }
      SB();
      const int tc = KD == 0 ? 3 : KD == 1 ? 2 : wv - 4, jc = KD == 0 ? wv : KD == 1 ? 2 * wv - 8 : 4;
      sfor<0, 20 * NS>([&](auto M_) __attribute__((always_inline)) {
        constexpr int m = decltype(M_)::v, x = m / 5, j = m % 5, t = x / NS, s = x % NS, r0 = nreq(x - 2), r1 = nreq(x - 1);
        constexpr bool nx = x + 1 < 4 * NS;
        u32x4& an = A4[(x + 1) % AW4];
        mf<s == 0>(acc[t & 1][j], W4[5 * s + j], A4[x % AW4]);
        SB();
        if constexpr (j == 0) {
          if constexpr (x == GS + 1) NEED_AT(fh_, fa, P_H1);
          if constexpr (r0 < r1) { LD_A4(r0); }
        }
        if constexpr (j == 1 && nx) { m4_ = amin2(an); PIN(m4_); }
        if constexpr (j == 2 && nx) { CMP_BAD(1, bm4, m4_); an[0] -= scsh; }
        if constexpr (j == 3 && nx) { an[2] -= scsh; PIN(an); }
        if constexpr (j == 3 && r0 + 1 < r1) { LD_A4(r0 + 1); }
        if constexpr (j == 4) {
          if constexpr (nx) RETRY_C(an, bm4, l16, va4, A4OFF(x + 1), PR);
          if constexpr (r0 + 2 < r1) { LD_A4(r0 + 2); }
          if constexpr (x == GS) fh_ = FLG(fa);
          if constexpr (x == H1S) SIG(P_H1);
          if constexpr (s == 0) TS_(45 + 4 * t);
          if constexpr (s == 3) TS_(45 + 4 * t + 1);
          if constexpr (s == NS - 1) TS_(45 + 4 * t + 2);
          static_assert(GS + 1 < 2 * NS, "order");
          if constexpr (KD == 0 && t == 1 && s == 0) U_LD(0, 0, wv);
          if constexpr (KD == 0 && t == 2 && s == 3) U_LD(0, 1, wv);
          if constexpr (KD == 2 && t == 2 && s == 3) U_LD(0, wv & 1, 4);
          if constexpr (KD == 1 && t == 3 && s == 3) U_LD(0, 2, jc + 1);
          if constexpr (t == 2 && s == 5) U_LD(1, tc, jc);
          if constexpr (t == 3 && s == NS - 2) fl_ = FLG(L_CTL);
        }
        constexpr bool un = (KD == 0 && t >= 2) || (KD == 2 && t == 3);
        constexpr int wq = 5 * s + j, w0 = un ? W0U : 4;
        if constexpr (t > 0 && wq >= w0 && wq < w0 + 5 * WSP && (wq - w0) % WSP == 0) *(volatile LAS f32x4*)(lds + PB(t - 1) + pw + ((wq - w0) / WSP) * 1024) = acc[(t - 1) & 1][(wq - w0) / WSP];
        if constexpr (t > 0 && wq == w0 + 1 + 4 * WSP) SIG(P_P0 + t);
        static_assert(W0U + 1 + 4 * WSP < 35 && U0 + 30 <= 35, "unit");
        if constexpr (un && KD == 0 && wq >= U0) U_PC(wq - U0, t - 2, wv, t - 2);
        if constexpr (un && KD == 2 && wq >= U0) U_PC(wq - U0, wv & 1, 4, 0);
        SB();
      });
      asm volatile("s_nop 3" : "+v"(acc[1][0]), "+v"(acc[1][1]), "+v"(acc[1][2]), "+v"(acc[1][3]), "+v"(acc[1][4]));
#pragma unroll
      for (int j = 0; j < 4; ++j) *(volatile LAS f32x4*)(lds + L_PB + wv * 4096 + l16 + j * 1024) = acc[1][j];
      NEED_AT(fl_, (unsigned)L_CTL, P_P0 + 3);
      *(volatile LAS f32x4*)(lds + PB(0) + pw) = acc[1][4];
      SIG(P_P0 + 4);
      TS_(60);
#define UNIT(r, y, t, j, base, str)                                                                        \
  do {                                                                                                  \
    unsigned pq = (unsigned)(base) + l16, oq = oo0 + (unsigned)((t) * (16 * D_ * 2) + (j) * 32);        \
    f32x4 gq_ = *(const LAS f32x4*)(lds + L_GB + ((j) * 16 + 4 * g) * 4);                               \
    f32x2 xa = up2(xv[r][0]), xb = up2(xv[r][1]), ya = up2(x1v[y][0]), yb = up2(x1v[y][1]);             \
    asm volatile("" : "+v"(xa), "+v"(xb), "+v"(ya), "+v"(yb), "+v"(gq_), "+v"(pq), "+v"(oq));           \
    f32x4 r8[8];                                                                                        \
    unsigned spins = 0;                                                                                 \
    for (;;) {                                                         \
      const unsigned fl = FLG(fa);                                                                      \
      _Pragma("unroll") for (int w = 0; w < 8; ++w) r8[w] = *(const volatile LAS f32x4*)(lds + pq + w * (str)); \
      if (__builtin_amdgcn_ballot_w64(fl < (unsigned)(P_P0 + 1 + (t))) == 0ull) break;                  \
      SPIN_STEP();                                                                                      \
      if (spins > SPIN_CAP) break;                                                                      \
    }                                                                                                   \
    const f32x4 sm = ((r8[0] + r8[1]) + (r8[2] + r8[3])) + ((r8[4] + r8[5]) + (r8[6] + r8[7]));         \
    const f32x2 a_ = FMA2(f32x2{sm[0], sm[1]} + ya, (f32x2{gq_[0], gq_[1]}), xa), b_ = FMA2(f32x2{sm[2], sm[3]} + yb, (f32x2{gq_[2], gq_[3]}), xb); \
    __builtin_amdgcn_raw_buffer_store_b64(u32x2{pack2(a_[0], a_[1]), pack2(b_[0], b_[1])}, rO, oq, 0, 0); \
    FENCE();                                                                                            \
  } while (0)
      if constexpr (KD == 0) UNIT(1, 2, 3, wv, L_PB + wv * 1024, 4096);
      else UNIT(1, 1, tc, jc, tc == 3 ? PB(0) : PB(2) + jc * 1024, 5120);
      if constexpr (KD == 1) UNIT(0, 0, 2, jc + 1, PB(2) + jc * 1024 + 1024, 5120);
    };
#define KILL(V, N) _Pragma("unroll") for (int f_ = 0; f_ < (N); ++f_) asm volatile("" : "=v"(V[f_]))
    int arm = wv >> 1;
    if (arm < 2) {
      u32x4 W4[35], A4[AW4];
      u32x2 x1v[3];
      g3(IC<0>{}, W4, A4, x1v);
      g4(IC<0>{}, W4, A4, x1v);
      KILL(W3, 40 - NR3);
    }
    asm volatile("" : "+s"(arm));
    if (arm >= 2) {
      u32x4 W4[35], A4[AW4];
      u32x2 x1v[3];
      g3(IC<1>{}, W4, A4, x1v);
      if (arm == 3) {
        g4(IC<2>{}, W4, A4, x1v);
        KILL(W4, 30);
        KILL(A4, 6);
      }
      asm volatile("" : "+s"(arm));
      if (arm == 2) g4(IC<1>{}, W4, A4, x1v);
    }
    STAMP(27);
    CLK(31);
  }
}

extern "C" void kernel_launch(void* const* d_in, const int* in_sizes, int n_in, void* d_out, int out_size,
                              void* d_ws, size_t ws_size, hipStream_t stream) {
  const bf16* A_q = (const bf16*)d_in[0];
  const bf16* A_k = (const bf16*)d_in[1];
  const bf16* A_v = (const bf16*)d_in[2];
  const bf16* A_shift_k = (const bf16*)d_in[3];
  const bf16* A_ssm_k = (const bf16*)d_in[4];
  const bf16* A_ssm_D = (const bf16*)d_in[5];
  const bf16* w1 = (const bf16*)d_in[7];
  const bf16* w2 = (const bf16*)d_in[8];
  const bf16* w3 = (const bf16*)d_in[9];
  const bf16* w4 = (const bf16*)d_in[10];
  const bf16* c = (const bf16*)d_in[11];
  const bf16* w_mod = (const bf16*)d_in[12];
  char* ws = (char*)d_ws;
  (void)hipFuncSetAttribute((const void*)k_main, hipFuncAttributeMaxDynamicSharedMemorySize, L_TOT);
  k_main<<<256, 512, L_TOT, stream>>>(A_k, A_v, ws, A_shift_k, A_ssm_k, w_mod, c, w3, w4, A_q, w1, w2, A_ssm_D, (bf16*)d_out);
}
```

```cpp
#define OAX 2
#include <hip/hip_runtime.h>
#include <hip/hip_bf16.h>
#include <stdint.h>

typedef __hip_bfloat16 bf16;
typedef __attribute__((ext_vector_type(8))) short bf16x8;
typedef __attribute__((ext_vector_type(4))) short s16x4;
typedef __attribute__((ext_vector_type(4))) float f32x4;
typedef __attribute__((ext_vector_type(2))) float f32x2;
typedef __attribute__((ext_vector_type(4))) unsigned u32x4;
typedef __attribute__((ext_vector_type(2))) unsigned u32x2;
#define LAS __attribute__((address_space(3)))

static constexpr int S_ = 2048;
static constexpr int D_ = 640;
static constexpr int F_ = 1728;
static constexpr unsigned SENT = 0xffffffffu;
static constexpr int RS3 = 0x00020000;
static constexpr unsigned OOB = 0x40000000u;

static constexpr unsigned SD2 = S_ * D_ * 2;
static constexpr unsigned DF2 = D_ * F_ * 2;
static constexpr unsigned O_WT3 = 0, O_WT4 = DF2;
static constexpr unsigned O_EP = 2 * DF2;
static constexpr unsigned O_MOD = O_EP + 256;
static constexpr unsigned O_MP = O_MOD + 16384;
static constexpr unsigned O_FL1 = O_MP + 131072;
static constexpr unsigned O_KVT = O_FL1 + 2048;
static constexpr unsigned O_HP = O_KVT, O_X1 = O_KVT;
static constexpr unsigned TS = 57344;
static constexpr unsigned O_H = O_KVT + 384 * 8192;
static constexpr unsigned O_Z = O_H, O_H1 = O_H + 20480;
static constexpr unsigned O_ST3 = O_H + 128 * TS;
static constexpr unsigned O_ST4 = O_ST3 + S_ * 8;
static constexpr unsigned O_XO = O_ST4 + S_ * 16 * 8;
static constexpr unsigned WS_USED = O_XO + SD2;
#define KVROW(c) ((c) < 384u ? O_KVT + (c) * 8192u : O_H + 40960u + (((c) - 384u) >> 1) * TS + ((c) & 1u) * 8192u)
static constexpr unsigned CODED = 0x7f7f7f7fu;
static constexpr unsigned CSH = 98u << 23;
static constexpr float PTHR = 0x1p48f, PMAX = 0x1p47f, CMUL = 0x1p98f, CMAX = 0x1p127f;
__device__ __forceinline__ float plainv(float x) { return __builtin_amdgcn_fmed3f(x, -PMAX, PMAX); }
__device__ __forceinline__ float codedv(float x) { return __builtin_copysignf(__builtin_amdgcn_fmed3f(__builtin_fabsf(x * CMUL), PTHR, CMAX), x); }

__device__ __forceinline__ float rbf(float x) { return __bfloat162float(__float2bfloat16(x)); }
__device__ __forceinline__ float lo2f(unsigned w) { return __uint_as_float(w << 16); }
__device__ __forceinline__ float hi2f(unsigned w) { return __uint_as_float(w & 0xffff0000u); }
typedef __bf16 bf16v2 __attribute__((ext_vector_type(2)));
__device__ __forceinline__ unsigned pack2(float a, float b) {
  bf16v2 r = __builtin_convertvector(f32x2{a, b}, bf16v2);
  return __builtin_bit_cast(unsigned, r);
}

static constexpr float GC1 = -2.f * 0.7978845608028654f * 1.4426950408889634f;
static constexpr float GC2 = GC1 * 0.044715f;
__device__ __forceinline__ float row16_sum(float v) {
  v += __builtin_bit_cast(float, __builtin_amdgcn_update_dpp(0, __builtin_bit_cast(int, v), 0xB1, 0xf, 0xf, true));
  v += __builtin_bit_cast(float, __builtin_amdgcn_update_dpp(0, __builtin_bit_cast(int, v), 0x4E, 0xf, 0xf, true));
  v += __builtin_bit_cast(float, __builtin_amdgcn_update_dpp(0, __builtin_bit_cast(int, v), 0x141, 0xf, 0xf, true));
  v += __builtin_bit_cast(float, __builtin_amdgcn_update_dpp(0, __builtin_bit_cast(int, v), 0x140, 0xf, 0xf, true));
  return v;
}
typedef volatile LAS unsigned* ldsw_t;
static constexpr int CV_STR = 4160;
static constexpr int CV_U = 8 * CV_STR;
static constexpr int CV_SLOT = CV_U + 4608;
static constexpr int SL_A = 46592, SL_C = SL_A + CV_SLOT, SL_B = SL_C + CV_SLOT;
static constexpr int L_KV = 0;
static constexpr int L_RT = L_KV + 24576;
static constexpr int L_MP = L_RT + 20480;
static constexpr int L_XCH = 139264;
static constexpr int L_XA = 81920, L_XB = 40960;
static constexpr int L_PB = 122880;
static constexpr int L_CTL = 160256;
static constexpr int L_MOD = 103424;
static constexpr int L_GB = L_CTL + 64;
static constexpr int L_CS3 = L_CTL + 384;
static constexpr int L_ST4 = L_CTL + 1408;
static constexpr int L_STAMP = L_CTL + 2432;
static constexpr int L_TOT = 163840;
static_assert(L_MP + 1024 <= SL_A && SL_B + CV_SLOT <= L_CTL && L_MOD >= SL_C + 16384 && L_MOD >= 102400 && L_MOD + 7680 <= SL_C + CV_U, "lds");
static constexpr int YROW = 1296;
enum { P_KV = 1, P_K, P_B, P_M, P_B2, P_U, P_C2, P_A2, P_RT, P_GA, P_ZC, P_X, P_X2, P_T, P_Z, P_RP, P_Z2, P_L1, P_XF1, P_G1, P_XF2, P_G2, P_SF, P_SA, P_S0, P_KA, P_H0, P_S1, P_KB, P_H1, P_P0 };
static constexpr unsigned SPIN_CAP = 1u << 21;
static constexpr unsigned RETRY_CAP = 1u << 15;

__device__ __forceinline__ float swap_add16(float v) {
  const unsigned u = __float_as_uint(v);
  auto r = __builtin_amdgcn_permlane16_swap(u, u, false, false);
  return __uint_as_float((unsigned)r[0]) + __uint_as_float((unsigned)r[1]);
}
__device__ __forceinline__ float swap_add32(float v) {
  const unsigned u = __float_as_uint(v);
  auto r = __builtin_amdgcn_permlane32_swap(u, u, false, false);
  return __uint_as_float((unsigned)r[0]) + __uint_as_float((unsigned)r[1]);
}
__device__ __forceinline__ float wave_sum2(float v) { return swap_add32(swap_add16(row16_sum(v))); }
__device__ __forceinline__ f32x2 up2(unsigned u) { return f32x2{lo2f(u), hi2f(u)}; }
__device__ __forceinline__ f32x2 rbf2(f32x2 v) { return up2(pack2(v[0], v[1])); }
#define FMA2 __builtin_elementwise_fma
__device__ __forceinline__ f32x2 rf2(f32x2 v, f32x2 cp, f32x2 cn) { return FMA2(v, cn, FMA2(v, cp, v)); }
__device__ __forceinline__ void ln5(const f32x2* in, unsigned* out) {
  const f32x2 s2 = ((in[0] + in[1]) + (in[2] + in[3])) + in[4];
  const float mean = wave_sum2(s2[0] + s2[1]) * (1.f / (float)D_);
  f32x2 d[5];
#pragma unroll
  for (int j = 0; j < 5; ++j) d[j] = in[j] - mean;
  const f32x2 q2 = (d[0] * d[0] + d[1] * d[1]) + (d[2] * d[2] + d[3] * d[3]) + d[4] * d[4];
  const float m = rbf(mean);
#pragma unroll
  for (int j = 0; j < 5; ++j) d[j] = rbf2(in[j] - m);
  const float q = wave_sum2(q2[0] + q2[1]);
  const float var = rbf(q * (1.f / (float)D_));
  const float den = rbf(__builtin_amdgcn_sqrtf(rbf(var + 1e-5f)));
  const float rinv = __builtin_amdgcn_rcpf(den);
#pragma unroll
  for (int j = 0; j < 5; ++j) { const f32x2 o = d[j] * rinv; out[j] = pack2(o[0], o[1]); }
}
#define PIN(x) asm volatile("" : "+v"(x))
#define STG(N, stmt) do { _Pragma("unroll") for (int i = 0; i < N; ++i) { stmt; } __builtin_amdgcn_sched_barrier(0); } while (0)
__device__ __forceinline__ void ln5s(const unsigned* in, unsigned* out) {
  float x[10], d[10], t[4];
  STG(10, x[i] = (i & 1) ? hi2f(in[i >> 1]) : lo2f(in[i >> 1]); PIN(x[i]));
  STG(4, t[i] = x[(i & 1) + (i >> 1) * 4] + x[(i & 1) + (i >> 1) * 4 + 2]; PIN(t[i]));
  STG(2, t[i] += t[2 + i]; PIN(t[i]));
  STG(2, t[i] += x[8 + i]; PIN(t[i]));
  const float mean = wave_sum2(t[0] + t[1]) * (1.f / (float)D_);
  STG(10, d[i] = x[i] - mean; PIN(d[i]));
  STG(4, t[i] = d[(i & 1) + (i >> 1) * 4] * d[(i & 1) + (i >> 1) * 4]; PIN(t[i]));
  STG(4, t[i] = __builtin_fmaf(d[(i & 1) + (i >> 1) * 4 + 2], d[(i & 1) + (i >> 1) * 4 + 2], t[i]); PIN(t[i]));
  STG(2, t[i] += t[2 + i]; PIN(t[i]));
  STG(2, t[i] = __builtin_fmaf(d[8 + i], d[8 + i], t[i]); PIN(t[i]));
  const float m = rbf(mean);
  STG(10, d[i] = x[i] - m; PIN(d[i]));
  unsigned p[5];
  STG(5, p[i] = pack2(d[2 * i], d[2 * i + 1]); PIN(p[i]));
  const float q = wave_sum2(t[0] + t[1]);
  const float var = rbf(q * (1.f / (float)D_));
  const float den = rbf(__builtin_amdgcn_sqrtf(rbf(var + 1e-5f)));
  const float rinv = __builtin_amdgcn_rcpf(den);
  STG(10, d[i] = ((i & 1) ? hi2f(p[i >> 1]) : lo2f(p[i >> 1])) * rinv; PIN(d[i]));
  STG(5, out[i] = pack2(d[2 * i], d[2 * i + 1]); PIN(out[i]));
}
template <int N>
__device__ __forceinline__ void wave_sums(float* v) {
  STG(N, v[i] += __builtin_bit_cast(float, __builtin_amdgcn_update_dpp(0, __builtin_bit_cast(int, v[i]), 0xB1, 0xf, 0xf, true)); PIN(v[i]));
  STG(N, v[i] += __builtin_bit_cast(float, __builtin_amdgcn_update_dpp(0, __builtin_bit_cast(int, v[i]), 0x4E, 0xf, 0xf, true)); PIN(v[i]));
  STG(N, v[i] += __builtin_bit_cast(float, __builtin_amdgcn_update_dpp(0, __builtin_bit_cast(int, v[i]), 0x141, 0xf, 0xf, true)); PIN(v[i]));
  STG(N, v[i] += __builtin_bit_cast(float, __builtin_amdgcn_update_dpp(0, __builtin_bit_cast(int, v[i]), 0x140, 0xf, 0xf, true)); PIN(v[i]));
  unsigned a[N], b[N];
  STG(N, a[i] = __float_as_uint(v[i]); b[i] = a[i]; PIN(b[i]));
  STG(N, { auto r = __builtin_amdgcn_permlane16_swap(a[i], b[i], false, false); a[i] = r[0]; b[i] = r[1]; PIN(a[i]); PIN(b[i]); });
  STG(N, v[i] = __uint_as_float(a[i]) + __uint_as_float(b[i]); PIN(v[i]));
  STG(N, a[i] = __float_as_uint(v[i]); b[i] = a[i]; PIN(b[i]));
  STG(N, { auto r = __builtin_amdgcn_permlane32_swap(a[i], b[i], false, false); a[i] = r[0]; b[i] = r[1]; PIN(a[i]); PIN(b[i]); });
  STG(N, v[i] = __uint_as_float(a[i]) + __uint_as_float(b[i]); PIN(v[i]));
}
template <int N>
__device__ __forceinline__ void gelu_s(float* e, float ga_, float gb2, float gd, unsigned* pk) {
  float x[N];
  STG(N, x[i] = e[i] * e[i]; PIN(x[i]));
  STG(N, x[i] = __builtin_fmaf(x[i], gb2, ga_); PIN(x[i]));
  STG(N, x[i] *= e[i]; PIN(x[i]));
  STG(N, x[i] = __builtin_amdgcn_exp2f(x[i]); PIN(x[i]));
  STG(N, x[i] = __builtin_fmaf(x[i], gd, gd); PIN(x[i]));
  STG(N, x[i] = __builtin_amdgcn_rcpf(x[i]); PIN(x[i]));
  STG(N, e[i] *= x[i]; PIN(e[i]));
  STG(N / 4, e[4 * i + 1] = plainv(e[4 * i + 1]); PIN(e[4 * i + 1]));
  STG(N / 2, pk[i] = pack2(e[2 * i], e[2 * i + 1]); PIN(pk[i]));
}
__device__ __forceinline__ f32x4 mfma16(u32x4 a, u32x4 b, f32x4 c) {
  return __builtin_amdgcn_mfma_f32_16x16x32_bf16(__builtin_bit_cast(bf16x8, a), __builtin_bit_cast(bf16x8, b), c, 0, 0, 0);
}
template <bool FIRST> __device__ __forceinline__ void mf(f32x4& c, const u32x4& a, const u32x4& b) {
  if (FIRST) asm volatile("v_mfma_f32_16x16x32_bf16 %0, %1, %2, 0" : "=v"(c) : "v"(a), "v"(b));
  else asm volatile("v_mfma_f32_16x16x32_bf16 %0, %1, %2, %0" : "+v"(c) : "v"(a), "v"(b));
}
__device__ __forceinline__ unsigned canon(unsigned x) { return x < 0xfffffffeu ? x : 0xfffffffeu; }
__device__ __forceinline__ bool bad1(unsigned a) { return __builtin_amdgcn_ballot_w64(a == SENT) != 0ull; }
__device__ __forceinline__ float amax2(const u32x4& a) { return __builtin_amdgcn_fmed3f(__builtin_fabsf(__uint_as_float(a[0])), __builtin_fabsf(__uint_as_float(a[2])), CMAX); }
__device__ __forceinline__ float amin2(const u32x4& a) { return __builtin_amdgcn_fmed3f(__builtin_fabsf(__uint_as_float(a[0])), __builtin_fabsf(__uint_as_float(a[2])), 0.f); }
__device__ __forceinline__ float amax3(float m, const u32x4& a) { float r; asm("v_max3_f32 %0, %1, |%2|, |%3|" : "=v"(r) : "v"(m), "v"(a[0]), "v"(a[2])); return r; }
template <bool CD> __device__ __forceinline__ bool any_bad4(u32x4& a, float thr, unsigned csh, float cmx) {
  const float m = __builtin_amdgcn_fmed3f(__builtin_fabsf(__uint_as_float(a[0])), __builtin_fabsf(__uint_as_float(a[2])), CD ? 0.f : cmx);
  if (CD) { a[0] -= csh; a[2] -= csh; }
  return __builtin_amdgcn_ballot_w64(CD ? m < thr : m >= thr) != 0ull;
}
__device__ __forceinline__ u32x4 widen(unsigned x0, unsigned x1, unsigned y0, unsigned y1) {
  const auto p0 = __builtin_amdgcn_permlane16_swap(x0, y0, false, false);
  const auto p1 = __builtin_amdgcn_permlane16_swap(x1, y1, false, false);
  return u32x4{(unsigned)p0[0], (unsigned)p1[0], (unsigned)p0[1], (unsigned)p1[1]};
}
#define DMA1(M) asm volatile("s_mov_b32 m0, %3\n\ts_nop 0\n\t" "buffer_load_dwordx4 %0, %1, %2 offen " M "lds" :: "v"(voff), "s"(rs), "s"(soff), "s"(dst) : "memory")
#define DMA4(M) asm volatile("s_mov_b32 m0, %3\n\ts_nop 0\n\t" \
                   "buffer_load_dwordx4 %0, %1, %2 offen " M "lds\n\tbuffer_load_dwordx4 %0, %1, %2 offen offset:1024 " M "lds\n\t" \
                   "buffer_load_dwordx4 %0, %1, %2 offen offset:2048 " M "lds\n\tbuffer_load_dwordx4 %0, %1, %2 offen offset:3072 " M "lds" \
                   :: "v"(voff), "s"(rs), "s"(soff), "s"(dst) : "memory")
template <int N, int AUX>
__device__ __forceinline__ void dma(unsigned voff, u32x4 rs, unsigned soff, unsigned dst) {
  if (N == 4) { if (AUX == 1) DMA4("sc1 "); else if (AUX == 2) DMA4("nt "); else DMA4(""); }
  else { if (AUX == 1) DMA1("sc1 "); else if (AUX == 2) DMA1("nt "); else DMA1(""); }
}
__device__ __forceinline__ void st128(u32x4 v, __amdgpu_buffer_rsrc_t r, unsigned voff, unsigned base, int) {
  __builtin_amdgcn_raw_buffer_store_b128(v, r, voff + base, 0, 16);
}
__device__ __forceinline__ u32x4 mk_rs(const void* p, unsigned bytes) {
  const unsigned long long a = (unsigned long long)p;
  return u32x4{(unsigned)a, (unsigned)(a >> 32) & 0xffffu, bytes, (unsigned)RS3};
}

#define USLEEP 2
#define DLY0 8
#define DLY1 3
#define DLY2 0
#define DLY3 2
#define NWH 8
#define ZG 8
#define ZSLEEP 2
#define AW4 8
#define AD3 3
#define RSLEEP 1
#define PHI 2
#define CPW 4
#define CPG 128u
#define NZ2 5
#define NS2 5
#define RS0 6
#define DLY3B 0
#define MV0 7
#define H0S 6
#define H1S 3
#define PK0 2
#define PE0 1
#define PK1 2
#define PE1 1
#define P40 1
#define P41 2
#define GSA 2
#define GSB 1
#define GSC 2
#define EA0 4
#define EA1 28
#define EC0 4
#define EC1 22
#define PB1 26
#define PB2 20
#define NL1 14
#define NP1 2
#define NL2 12
#define ROT 1
#define NA1 3
#define NP0 0
#define SL1 6
#define SL2 6
#define PH1 2
#define PH2 2
#define KG 42
#define KC3 12
#define PK2 2
#define KC3B 40
#define NPK 10
#define HG 4
#define WSP 3
#define PR1 3
#define HPZ 8
#define WS3 1
#define RB1 0
#define HFI 1
#define MGE 0
#define TZ 0
#define KE 0
#define TU1 200
#define TU2 260
#define TU 290
#define KL 5
#define PBE 18
#define NAF 6
#define W0U 4
#define U0 5
#define NPH 6
#define NGA 5
#define NR0 2
#define NR1 5
#define KVR 0
static_assert((NWH + NL1 + NP1) % 2 == 0 && NL2 % 2 == 0 && NWH + NL1 + NP1 + NL2 <= 40, "w3 schedule");
#define SPIN_STEP() do { ++spins; asm volatile("" : "+s"(spins)); } while (0)
#define SB() __builtin_amdgcn_sched_barrier(0)
#define FENCE() do { asm volatile("" ::: "memory"); __builtin_amdgcn_sched_barrier(0); } while (0)
#define LGKM0() do { __builtin_amdgcn_sched_barrier(0); asm volatile("s_waitcnt lgkmcnt(0)" ::: "memory"); } while (0)
#define BARRIER() do { LGKM0(); __builtin_amdgcn_s_barrier(); asm volatile("" ::: "memory"); } while (0)
#define VM0() __builtin_amdgcn_s_waitcnt(0x0F70)
#define PRIO(n) __builtin_amdgcn_s_setprio(n)
#define CHECK_A(CD, AV, i, hi, LD)                                                          \
  do {                                                                                      \
    if (__builtin_expect(any_bad4<CD>(AV, sthr, scsh, scmx), 0)) {                          \
      unsigned spins = 0;                                                                   \
      PRIO(0);                                                                              \
      do {                                                                                  \
        __builtin_amdgcn_s_sleep(RSLEEP);                                                   \
        _Pragma("unroll") for (int y_ = (i); y_ < (hi); ++y_) { LD(y_); }                   \
        SPIN_STEP();                                                                        \
      } while (any_bad4<CD>(AV, sthr, scsh, scmx) && spins < RETRY_CAP);                    \
      PRIO(PHI);                                                                            \
      VM0();                                                                                \
    }                                                                                       \
  } while (0)

#define ASM_MACROS \
  ".macro regno op\n.set r_, 0\n.set st_, 0\n.irpc c, \"\\op\"\n.ifc \\c, :\n.set st_, 2\n.endif\n.if st_ == 1\n.set r_, r_ * 10 + \\c\n.endif\n.ifc \\c, [\n.set st_, 1\n.endif\n.endr\n.endm\n" \
  ".macro retry n, cd, a, so, ct, t, vo, rs, sb, im, th, k, pr\n"                                   \
  "s_cbranch_vccnz Lc\\n\nLb\\n:\n.subsection 1\nLc\\n:\nregno \\a\n"              \
  "s_setprio 0\ns_add_u32 \\so, \\sb, \\im\ns_mov_b32 \\ct, 0\n"                                        \
  "Ll\\n:\ns_sleep 1\nbuffer_load_dwordx4 \\a, \\vo, \\rs, \\so offen sc1\ns_add_u32 \\ct, \\ct, 1\ns_waitcnt vmcnt(0)\n" \
  ".if \\cd\nv_med3_f32 \\t, |v[r_]|, |v[r_+2]|, 0\nv_cmp_gt_f32 vcc, \\th, \\t\n.else\nv_med3_f32 \\t, |v[r_]|, |v[r_+2]|, \\k\nv_cmp_le_f32 vcc, \\th, \\t\n.endif\n" \
  "s_cbranch_vccz Ld\\n\ns_cmp_lt_u32 \\ct, 0x8000\ns_cbranch_scc1 Ll\\n\n"                             \
  "Ld\\n:\n.if \\cd\nv_subrev_u32 v[r_], \\k, v[r_]\nv_subrev_u32 v[r_+2], \\k, v[r_+2]\n.endif\ns_setprio \\pr\ns_nop 1\ns_branch Lb\\n\n.subsection 0\n.endm\n" \
  ".macro need n, v, ct, ad, k\n"                                                                       \
  "v_cmp_gt_u32 vcc, \\k, \\v\ns_cbranch_vccnz Lc\\n\nLb\\n:\n.subsection 1\nLc\\n:\ns_mov_b32 \\ct, 0\n" \
  "Ll\\n:\nds_read_b32 \\v, \\ad\ns_waitcnt lgkmcnt(0)\nv_cmp_gt_u32 vcc, \\k, \\v\ns_cbranch_vccz Lb\\n\n" \
  "s_sleep 1\ns_add_u32 \\ct, \\ct, 1\ns_cmp_lt_u32 \\ct, 0x200000\ns_cbranch_scc1 Ll\\n\ns_branch Lb\\n\n.subsection 0\n.endm\n"
#define RETRY(CD, AV, BM, VOFF, SBASE, IMM, PR)                                                         \
  do {                                                                                                  \
    unsigned so_, ct_; float t_; register unsigned long long vc_ asm("vcc") = (BM);                                                                     \
    asm volatile("retry %=, %5, \"%0\", %1, %2, %3, %6, \"%7\", %8, %9, %10, %11, %12"                   \
                 : "+v"(AV), "=&s"(so_), "=&s"(ct_), "=&v"(t_), "+s"(vc_)                            \
                 : "i"((int)(CD)), "v"(VOFF), "s"(dW), "s"(SBASE), "i"(IMM), "s"(sthr), "s"((CD) ? scsh : __float_as_uint(scmx)), "i"(PR) : "scc", "memory"); \
  } while (0)
#define CMP_BAD(CD, BM, M) do { register unsigned long long vc_ asm("vcc");                             \
    if (CD) asm volatile("v_cmp_gt_f32 vcc, %1, %2" : "=s"(vc_) : "s"(sthr), "v"(M)); else asm volatile("v_cmp_le_f32 vcc, %1, %2" : "=s"(vc_) : "s"(sthr), "v"(M)); \
    BM = vc_; } while (0)
#define RETRY_C(AV, BM, VOFF, SBASE, IMM, PR) RETRY(1, AV, BM, VOFF, SBASE, IMM, PR)
#define NEED_AT(V, ADDR, K) do { unsigned ct_; asm volatile("need %=, %0, %1, %2, %3" : "+v"(V), "=&s"(ct_) : "v"(ADDR), "s"(K) : "vcc", "scc", "memory"); } while (0)

#define STAMP(i)
#define CLK(i)
#define KATTR
#define TS_(i)
#define TX_(i)
#define TSHI 62
__device__ __forceinline__ u32x4 rd_tr(const LAS char* p) {
  const s16x4 lo = __builtin_amdgcn_ds_read_tr16_b64_v4i16((LAS s16x4*)p);
  const s16x4 hi = __builtin_amdgcn_ds_read_tr16_b64_v4i16((LAS s16x4*)(p + 2560));
  const bf16x8 o = {lo[0], lo[1], lo[2], lo[3], hi[0], hi[1], hi[2], hi[3]};
  return __builtin_bit_cast(u32x4, o);
}
__device__ __forceinline__ float dpp_add(float v, int ctrl) {
  return v + __builtin_bit_cast(float, ctrl == 0 ? __builtin_amdgcn_update_dpp(0, __builtin_bit_cast(int, v), 0x128, 0xf, 0xf, true)
                                    : ctrl == 1 ? __builtin_amdgcn_update_dpp(0, __builtin_bit_cast(int, v), 0x124, 0xf, 0xf, true)
                                                : __builtin_amdgcn_update_dpp(0, __builtin_bit_cast(int, v), 0x4E, 0xf, 0xf, true));
}

template <int V> struct IC { static constexpr int v = V; };
template <int I, int N, class F> __device__ __forceinline__ void sfor(F&& f) { if constexpr (I < N) { f(IC<I>{}); sfor<I + 1, N>(f); } }
constexpr int cv_i(int m) { int i = 0; while (m >= 8 - i) { m -= 8 - i; ++i; } return i; }
constexpr int cv_d(int m) { int i = 0; while (m >= 8 - i) { m -= 8 - i; ++i; } return 7 - i - m; }
constexpr int kl_i(int m) { return m < 50 - 5 * KL ? m / 5 : m < 50 - 2 * KL ? 10 - KL + (m - 50 + 5 * KL) / 3 : 10 - KL + (m - 50 + 2 * KL) / 2; }
constexpr int kl_j(int m) { return m < 50 - 5 * KL ? m % 5 : m < 50 - 2 * KL ? (m - 50 + 5 * KL) % 3 : 3 + (m - 50 + 2 * KL) % 2; }
constexpr int cl_t(int m) { int t = 0; while (m > t) { m -= t + 1; ++t; } return t; }
constexpr int cl_i(int m) { int t = 0; while (m > t) { m -= t + 1; ++t; } return m; }
constexpr bool cv_free(int m, int k) {
  return k == 0 ? (m >= 64 && m < 111 && m != 65 && m != 69 && m != 72 && m != 75 && m != 77 && m != 94 && m != 95 && m != 104 && m != 110 && (m < 72 + RS0 || m >= 88 + RS0))
                : (m >= 123 && m != 126 && m != 131 && m != 132 && m != 139 && m != 140);
}
constexpr int cv_slot(int m, int k) { if (!cv_free(m, k)) return -1; int n = 0; for (int y = 0; y < m; ++y) n += cv_free(y, k); return n; }
static_assert(cv_slot(109, 0) >= 15 && cv_slot(143, 1) >= 15, "slots");
constexpr int cc_i(int J, int c) { for (int i = 0; i < 8; ++i) for (int t = 3; t >= 0; --t) if (i <= ((J >> (4 * t)) & 15) && c-- == 0) return i; return 0; }
constexpr int cc_t(int J, int c) { for (int i = 0; i < 8; ++i) for (int t = 3; t >= 0; --t) if (i <= ((J >> (4 * t)) & 15) && c-- == 0) return t; return 0; }
#define KWAIT
__global__ __launch_bounds__(512, 2) KATTR void k_main(const bf16* kin_, const bf16* vin_, char* ws_, const bf16* shift_k_, const bf16* ssm_k_,
                                                 const bf16* w_mod_, const bf16* cin_, const bf16* w3_,
                                                 const bf16* w4_, const bf16* q_, const bf16* w1_, const bf16* w2_,
                                                 const bf16* ssm_D_, bf16* out_) {
  typedef __attribute__((ext_vector_type(8))) unsigned long long u64x8;
  typedef __attribute__((ext_vector_type(4))) unsigned long long u64x4;
  typedef __attribute__((ext_vector_type(2))) unsigned long long u64x2;
  u64x8 ka;
  u64x4 kb;
  u64x2 kc;
  int tid_ = threadIdx.x, bx_ = blockIdx.x;
  unsigned long long pc0;
  unsigned code_bytes;
  asm volatile("s_load_dwordx16 %0, %6, 0x0\n\ts_load_dwordx8 %1, %6, 0x40\n\ts_load_dwordx4 %2, %6, 0x60\n\ts_getpc_b64 %3\n"
               "Lpc0:\n\ts_mov_b32 %4, Lcold0 - Lpc0\n" KWAIT ".subsection 1\nLcold0:\n.subsection 0"
               : "=&s"(ka), "=&s"(kb), "=&s"(kc), "=&s"(pc0), "=&s"(code_bytes), "+s"(bx_) : "s"(__builtin_amdgcn_kernarg_segment_ptr()));
  asm volatile(ASM_MACROS ".subsection 2\n.fill 256, 4, 0xbf800000\n.subsection 0" : "+v"(tid_));
  extern __shared__ __attribute__((aligned(16))) char lds_[];
  LAS char* lds = (LAS char*)lds_;
  ldsw_t LW = (ldsw_t)(lds + L_CTL);
  const int tid = tid_ & 511, lane = tid & 63;
  const int wv = __builtin_amdgcn_readfirstlane(tid >> 6) & 7;
  const int bx = bx_ & 255;
  const int mt = (bx & 7) * 4 + (bx >> 6), nt = (bx >> 3) & 7;
  const int n0 = nt * 80, m0 = mt * 64;
  const int l15 = lane & 15, g = lane >> 4;
  const int mg = wv & 3, kh = wv >> 2;
  const int gm = (wv >> 2) * 2 + (wv & 1), gk = ((wv >> 1) ^ (wv >> 2)) & 1, gp = wv ^ 2;
  const unsigned l16 = (unsigned)lane * 16u;
  TS_(0);
  STAMP(0);
  CLK(30);
  unsigned cp0 = 0;
  if (wv == CPW) {
    asm volatile("buffer_load_dword %0, %1, %2, 0 offen" : "=v"(cp0) : "v"((unsigned)((bx >> 3) + 32 * lane) * CPG), "s"(mk_rs((const void*)pc0, code_bytes)) : "memory");
  }
  FENCE();
  const int tok0 = 32 * ((bx & 7) * 8 + (bx >> 5)), ch0 = 160 * ((bx >> 3) & 3);
  unsigned ko[3], vo[3];
#pragma unroll
  for (int i = 0; i < 3; ++i) {
    const unsigned s = (unsigned)((mg + 4 * i) * 64 + lane);
    const unsigned gr = __umul24(s, 800u) >> 16, w = s - __umul24(82u, gr), rr = __umul24(w, 205u) >> 12;
    unsigned o = (unsigned)(tok0 * (D_ * 2) + ch0 * 2) + __umul24(gr, 4u * D_ * 2) + __umul24(rr, D_ * 2 - 320) + w * 16u;
    asm volatile("" : "+v"(o));
    ko[i] = (w < 80u && gr < 9u) ? o - 4u * D_ * 2 : OOB;
    vo[i] = (w < 80u && gr < 8u) ? o : OOB;
    asm volatile("" : "+v"(ko[i]), "+v"(vo[i]));
  }
  FENCE();
  asm volatile("s_waitcnt lgkmcnt(0)" : "+s"(ka), "+s"(kb), "+s"(kc));
  const bf16* kin = (const bf16*)ka[0];
  const bf16* vin = (const bf16*)ka[1];
  char* ws = (char*)ka[2];
  const bf16* shift_k = (const bf16*)ka[3];
  const bf16* ssm_k = (const bf16*)ka[4];
  const bf16* w_mod = (const bf16*)ka[5];
  const bf16* cin = (const bf16*)ka[6];
  const bf16* w3 = (const bf16*)ka[7];
  const bf16* w4 = (const bf16*)kb[0];
  const bf16* q = (const bf16*)kb[1];
  const bf16* w1 = (const bf16*)kb[2];
  const bf16* w2 = (const bf16*)kb[3];
  const bf16* ssm_D = (const bf16*)kc[0];
  bf16* out = (bf16*)kc[1];
  const __amdgpu_buffer_rsrc_t rW = __builtin_amdgcn_make_buffer_rsrc((void*)ws, 0, WS_USED, RS3);
  const u32x4 dW = mk_rs(ws, WS_USED);
  u32x2 ep;
  asm volatile("buffer_load_dwordx2 %0, off, %1, %2 sc1" : "=v"(ep) : "s"(dW), "s"(O_EP) : "memory");
  FENCE();
  if (kh == 0) {
    const u32x4 dK = mk_rs(kin, SD2), dV = mk_rs(vin, SD2);
#pragma unroll
    for (int i = 0; i < 3; ++i) dma<1, 0>(ko[i], dK, 0u, (unsigned)(L_KV + (mg + 4 * i) * 1024));
#pragma unroll
    for (int i = 0; i < 3; ++i) dma<1, 0>(vo[i], dV, 0u, (unsigned)(L_KV + 12288 + (mg + 4 * i) * 1024));
  }
  u32x4 sk[3];
  if (kh == 0) {
    asm volatile("buffer_load_dwordx4 %0, %3, %5, %6 offen\n\tbuffer_load_dwordx4 %1, %3, %5, %6 offen offset:16\n\tbuffer_load_dwordx4 %2, %4, %5, %7 offen"
                 : "=&v"(sk[0]), "=&v"(sk[1]), "=&v"(sk[2])
                 : "v"(((unsigned)lane >> 3) * 32u), "v"((unsigned)g * 16u), "s"(mk_rs(shift_k, D_ * 8)), "s"(ch0 * 8 + mg * 256), "s"(ch0 * 8 + 1024 + mg * 64) : "memory");
  }
  FENCE();
  if (tid >= 504) LW[tid - 504] = 0u;
  if (tid >= 256 && tid < 352) *(LAS u32x4*)(lds + SL_A + ((tid - 256) >> 5) * CV_SLOT + CV_U + (tid & 31) * 16) = u32x4{0u, 0u, 0u, 0u};
  if (tid >= 384 && tid < 408) *(LAS u32x4*)(lds + SL_A + ((tid - 384) >> 3) * CV_SLOT + (tid & 7) * CV_STR + 257 * 16) = u32x4{0u, 0u, 0u, 0u};
  STAMP(29);
  if constexpr (TSHI == 23) TX_(30);
  BARRIER();

  const unsigned psig = (unsigned)L_CTL + (unsigned)wv * 4u;
#define SIG(k) do { unsigned long long sv_; asm volatile("s_mov_b64 %0, exec\n\ts_mov_b64 exec, 1\n\tds_write_b32 %1, %2\n\ts_mov_b64 exec, %0" : "=&s"(sv_) : "v"(psig), "v"((unsigned)(k)) : "memory"); } while (0)
#define WAIT_AT(addr, k)                                                                                \
  do {                                                                                                  \
    unsigned tv_, tc_;                                                                                  \
    asm volatile("s_mov_b32 %1, 0\n"                                                                    \
                 "L%=_a:\n\tds_read_b32 %0, %2\n\ts_waitcnt lgkmcnt(0)\n\tv_cmp_gt_u32 vcc, %3, %0\n\ts_cbranch_vccz L%=_b\n\t"    \
                 "s_sleep 1\n\ts_add_u32 %1, %1, 1\n\ts_cmp_lt_u32 %1, 0x200000\n\ts_cbranch_scc1 L%=_a\n"            \
                 "L%=_b:" : "=&v"(tv_), "=&s"(tc_) : "v"(addr), "i"(k) : "vcc", "scc", "memory");       \
  } while (0)
#define WAIT_2(k0, k1)                                                                                 \
  do {                                                                                                  \
    unsigned tv_, tc_;                                                                                  \
    asm volatile("s_mov_b32 %1, 0\n"                                                                    \
                 "L%=_a:\n\tds_read_b32 %0, %2\n\ts_waitcnt lgkmcnt(0)\n\tv_cmp_gt_u32 vcc, %3, %0\n\ts_cbranch_vccz L%=_b\n\t"    \
                 "s_sleep 1\n\ts_add_u32 %1, %1, 1\n\ts_cmp_lt_u32 %1, 0x200000\n\ts_cbranch_scc1 L%=_a\n"            \
                 "L%=_b:" : "=&v"(tv_), "=&s"(tc_) : "v"((unsigned)L_CTL + ((unsigned)lane & 7u) * 4u), "v"((lane & 4) ? (unsigned)(k1) : (unsigned)(k0)) : "vcc", "scc", "memory"); \
  } while (0)
#define WAIT_ALL(k) WAIT_AT((unsigned)L_CTL + ((unsigned)lane & 7u) * 4u, k)
#define WAIT_HALF(h, k) WAIT_AT((unsigned)L_CTL + ((unsigned)lane & 3u) * 4u + (unsigned)(h) * 16u, k)

  const int sb3 = nt < 6 ? 14 * nt : 84 + 12 * (nt - 6);
  const bool two = kh == 0 || nt < 6;
  const int jl = kh ? 4 + wv : 2 * wv, jm = kh ? 12 + (wv & 1) : 2 * wv + 1, hb = wv > 5 ? 1 : 0;
  const int k4 = 7 * wv - (wv > 5 ? 2 : wv > 4 ? 1 : 0);
  const unsigned w4a = O_WT4 + (unsigned)(nt * 54 + k4) * 5120u;
  unsigned w3s[4] = {O_WT3 + (unsigned)(sb3 + jl) * 20480u + (unsigned)hb * 10240u, two ? O_WT3 + (unsigned)(sb3 + jm) * 20480u + (unsigned)hb * 10240u : OOB,
                     O_WT3 + (unsigned)(sb3 + jl) * 20480u - (unsigned)hb * 10240u, kh ? w4a - 10240u : O_WT3 + (unsigned)(sb3 + jm) * 20480u};
  f32x2 cp = {65536.f, 65536.f}, cn = {-65536.f, -65536.f};
  asm volatile("" : "+s"(cp), "+s"(cn));
  u32x4 W3[40];
#define LD_W3U(f) W3[f] = __builtin_amdgcn_raw_buffer_load_b128(rW, l16 + ((f) < 8 ? ((f) >> 1) * 1024 : 0), w3s[((f) & 1) + ((f) >= 20 ? 2 : 0)] + ((f) < 8 ? 0 : ((f) >> 1) * 1024), 16)
#define LD_W3R(a, b) do { _Pragma("unroll") for (int f_ = (a); f_ < (b); ++f_) LD_W3U(f_); } while (0)
#define VMW(n) asm volatile("s_waitcnt vmcnt(%0)" :: "i"(n) : "memory")
  const __amdgpu_buffer_rsrc_t rW1 = __builtin_amdgcn_make_buffer_rsrc((void*)w1, 0, D_ * D_ * 2, RS3);
  const __amdgpu_buffer_rsrc_t rW2 = __builtin_amdgcn_make_buffer_rsrc((void*)w2, 0, D_ * D_ * 2, RS3);
  const u32x4 dW1 = mk_rs(w1, D_ * D_ * 2), dW2 = mk_rs(w2, D_ * D_ * 2);
#define W12_G(gw) unsigned gw[5]; { unsigned ln_ = (unsigned)lane; asm volatile("" : "+v"(ln_)); \
    _Pragma("unroll") for (int j_ = 0; j_ < 5; ++j_) { const unsigned p_ = ln_ + 64u * j_, r_ = __umul24(p_, 6554u) >> 16; \
      gw[j_] = __umul24((r_ & 12u) * 2u + (r_ >> 4) * 4u + (r_ & 3u), D_ * 2) + (p_ - __umul24(r_, 10u)) * 16u + (unsigned)n0 * 2u; } }

  const int s0 = m0 + nt * 8;
  const int srow = s0 + wv;
  unsigned qw[5], hw[5];
  unsigned long long epn;
  unsigned tA, tB;
  u32x4 WS2[10];
#define LD_WS2U(a, b) _Pragma("unroll") for (int y_ = (a); y_ < (b); ++y_) asm volatile("buffer_load_dwordx4 %0, %1, %2, %3 offen ; ws2" : "=v"(WS2[y_]) : "v"(gw[y_ % 5]), "s"(dW2), "s"((wv + 8 * (y_ / 5)) * (32 * D_ * 2)) : "memory")
#define LD_WS2(a, b) _Pragma("unroll") for (int y_ = (a); y_ < (b); ++y_) WS2[y_] = __builtin_amdgcn_raw_buffer_load_b128(rW2, gw[y_ % 5], (wv + 8 * (y_ / 5)) * (32 * D_ * 2), 0)
  unsigned ry = (unsigned)(L_XCH + wv * YROW) + l16, rz = (unsigned)(L_XCH + wv * YROW + 1024 + 4 * lane);
  f32x2 xq[5], ha[5], ca[5], sb[5];
  float mc, mb;
  const int row = m0 + 16 * gm + l15;
  const int jb = 3 * gk;
  const unsigned ofr = (unsigned)(row >> 4) * 20480u + (unsigned)((n0 + 16 * jb + 4 * g) >> 3) * 256u + (unsigned)(g & 1) * 8u + (unsigned)l15 * 16u;
  {
    const int pi = (bx & 7) + 8 * (bx >> 4), half = (bx >> 3) & 1;
    const int chA = 2 * bx, chC = 512 + pi;
#define RD_MOD(v) do { const u32x4 b_ = *(const LAS u32x4*)(lds + L_MOD + (v) * (D_ * 2) + l16); mw[v][0] = b_[0]; mw[v][1] = b_[1]; mw[v][2] = b_[2]; mw[v][3] = b_[3]; \
                       mw[v][4] = *(const LAS unsigned*)(lds + L_MOD + (v) * (D_ * 2) + 1024 + lane * 4); } while (0)
#define LN1()                                                                                           \
  do {                                                                                                  \
    f32x2 xq_[5];                                                                                       \
    _Pragma("unroll") for (int j = 0; j < 5; ++j) xq_[j] = up2(qw[j]);                                  \
    ln5(xq_, hw);                                                                                       \
    _Pragma("unroll") for (int j = 0; j < 5; ++j) asm volatile("" : "+v"(hw[j]));                       \
  } while (0)
#define R_PRE() do {                                                                                    \
      unsigned mw[5][5];                                                                                \
      _Pragma("unroll") for (int v = 0; v < 5; ++v) RD_MOD(v);                                          \
      _Pragma("unroll") for (int j = 0; j < 5; ++j) {                                                   \
        xq[j] = up2(qw[j]);                                                                             \
        ha[j] = FMA2(up2(hw[j]), up2(mw[1][j]), up2(mw[0][j])) * up2(mw[2][j]); ca[j] = up2(mw[4][j]); sb[j] = up2(mw[3][j]); \
        asm volatile("" : "+v"(xq[j]), "+v"(ha[j]), "+v"(ca[j]), "+v"(sb[j]));                          \
      }                                                                                                 \
      const f32x2 c2_ = ((ca[0] + ca[1]) + (ca[2] + ca[3])) + ca[4], b2_ = ((sb[0] + sb[1]) + (sb[2] + sb[3])) + sb[4]; \
      mc = wave_sum2(c2_[0] + c2_[1]) * (1.f / (float)D_); mb = wave_sum2(b2_[0] + b2_[1]) * (1.f / (float)D_); \
      asm volatile("" : "+v"(mc), "+v"(mb));                                                            \
    } while (0)
    unsigned dv2;
#define GET_D() const unsigned dAB = __builtin_amdgcn_readlane(dv2, 0), dC_ = __builtin_amdgcn_readlane(dv2, 1), dC = (chC & 1) ? dC_ & 0xffff0000u : dC_ << 16
#define REQ_Q() do {                                                                                    \
      const __amdgpu_buffer_rsrc_t rQ = __builtin_amdgcn_make_buffer_rsrc((void*)q, 0, SD2, RS3);       \
      const u32x4 a = __builtin_amdgcn_raw_buffer_load_b128(rQ, l16, srow * (D_ * 2), 0);               \
      qw[0] = a[0]; qw[1] = a[1]; qw[2] = a[2]; qw[3] = a[3];                                           \
      qw[4] = __builtin_amdgcn_raw_buffer_load_b32(rQ, (unsigned)lane * 4u, srow * (D_ * 2) + 1024, 0); \
    } while (0)
#define REQ_D() dv2 = __builtin_amdgcn_raw_buffer_load_b32(__builtin_amdgcn_make_buffer_rsrc((void*)ssm_D, 0, D_ * 2, RS3), (unsigned)(lane ? (chC & ~1) : chA) * 2u, 0, 0)
#define TAGS()                                                                                          \
    {                                                                                                   \
      unsigned e0_, e1_;                                                                                \
      asm volatile("v_readfirstlane_b32 %0, %2\n\tv_readfirstlane_b32 %1, %3" : "=s"(e0_), "=s"(e1_) : "v"(ep[0]), "v"(ep[1])); \
      unsigned long long h = (unsigned long long)e1_ << 32 | e0_;                                       \
      epn = h + 1ull;                                                                                   \
      h += 0x9E3779B97F4A7C15ull;                                                                       \
      h = (h ^ (h >> 30)) * 0xBF58476D1CE4E5B9ull;                                                      \
      h = (h ^ (h >> 27)) * 0x94D049BB133111EBull;                                                      \
      h ^= h >> 31;                                                                                     \
      tA = (unsigned)h; tB = (unsigned)(h >> 32);                                                       \
    }
#define BUILD(SL, CC)                                                                                   \
  do {                                                                                                  \
    const u32x4 klo = *(const LAS u32x4*)(lds + (SL) + CV_U + 512 + 4064 - 16 * (CC)), khi = *(const LAS u32x4*)(lds + (SL) + CV_U + 512 + 4080 - 16 * (CC)); \
    const unsigned w[8] = {klo[0], klo[1], klo[2], klo[3], khi[0], khi[1], khi[2], khi[3]};             \
    _Pragma("unroll") for (int m = 0; m < 8; ++m) {                                                     \
      u32x4 o;                                                                                          \
      const int a = m >> 1;                                                                             \
      _Pragma("unroll") for (int t = 0; t < 4; ++t) {                                                   \
        if ((m & 1) == 0) o[t] = (w[a - t + 4] & 0xffffu) | (w[a - t + 3] & 0xffff0000u);               \
        else o[t] = (w[a - t + 4] >> 16) | (w[a - t + 4] << 16);                                        \
      }                                                                                                 \
      *(LAS u32x4*)(lds + (SL) + m * CV_STR + ((CC) + 1) * 16) = o;                                     \
    }                                                                                                   \
  } while (0)
#define ZCH(c) ((c) * 128u + ((c) / 160u) * (TS - 20480u))
#define ZOFF(JA, JB, CH) ((unsigned)((32 * ((g & 1) ? (JB) : (JA)) + 2 * l15 + (g >> 1)) >> 3) * (4u * TS) + ZCH((unsigned)(CH)) + (unsigned)((2 * l15 + (g >> 1)) & 7) * 16u)
#define Z2OFF(w) (O_Z + (unsigned)(4 * (w) + (l15 >> 2)) * (4u * TS) + ZCH((unsigned)chA) + (unsigned)((l15 & 3) * 64 + g * 16))
    const unsigned a00 = (unsigned)((l15 & 7) * CV_STR + (2032 - 8 * (l15 >> 3) + 8 * g) * 2) - 128u * mg;
    const unsigned b00 = (unsigned)(CV_U + 480 + 32 * l15 + 16 * g) - 128u * mg;
#define CV_RA(AV, SL, rd) _Pragma("unroll") for (int i = 0; i < 8; ++i) AV[8 * (rd) + i] = *(const LAS u32x4*)(lds + (SL) + a00 - 64 * (rd) - 512 * i)
#define CV_RB(BV, SL, rd) _Pragma("unroll") for (int i = 7; i >= 0; --i) BV[8 * (rd) + i] = *(const LAS u32x4*)(lds + (SL) + b00 + 512 * i - 64 * (rd))
    const int zja = mg ? (half ? 5 : 4) : (half ? 1 : 0), zjb = mg ? (half ? 6 : 7) : (half ? 2 : 3);
    const int t8 = tid & 255;
#define W_DMA(blk, j) dma<1, 0>(gw[j], (blk) < 20 ? dW1 : dW2, (unsigned)((blk) < 20 ? (blk) : (blk) - 4) * (32 * D_ * 2), (unsigned)((blk) * 5120 + (j) * 1024))
    if (__builtin_expect(kh != 0, 1)) {
      {
        const u32x4 dS = mk_rs(ssm_k, SD2);
        dma<1, 0>(l16, dS, (unsigned)(chC * (S_ * 2) + mg * 1024), (unsigned)(SL_C + CV_U + 512 + mg * 1024));
        dma<1, 0>(l16, dS, (unsigned)(chA * (S_ * 2) + mg * 1024), (unsigned)(SL_A + CV_U + 512 + mg * 1024));
        dma<1, 0>(l16, dS, (unsigned)((chA + 1) * (S_ * 2) + mg * 1024), (unsigned)(SL_B + CV_U + 512 + mg * 1024));
      }
      unsigned cpf = __builtin_amdgcn_raw_buffer_load_b32(rW, OOB, 0, 0);
      FENCE();
#define FILL_Z() do {                                                                                   \
      st128(u32x4{CODED, CODED, CODED, CODED}, rW, Z2OFF(mg), 0, 16);                                   \
      st128(u32x4{CODED, CODED, CODED, CODED}, rW, Z2OFF(wv), 0, 16);                                   \
      if (mg < 2) st128(u32x4{CODED, CODED, CODED, CODED}, rW, ZOFF(zja, zjb, chC), O_Z, 16);            \
    } while (0)
      unsigned fpa[3], fpv;
#pragma unroll
      for (int f_ = 0; f_ < 3; ++f_) {
        const unsigned o = (unsigned)(f_ * 4096 + t8 * 16);
        fpa[f_] = o < 10240u ? O_H1 + (unsigned)(bx >> 1) * TS + (bx & 1) * 10240u + o : o < 11264u ? O_ST4 + (unsigned)bx * 1024u + (o - 10240u)
                : (o < 11392u && (bx & 1)) ? O_ST3 + (unsigned)(bx >> 1) * 128u + (o - 11264u) : OOB;
        if (f_ == 2) fpv = o < 10240u ? CODED : SENT;
      }
#define FILL_P(a, b) _Pragma("unroll") for (int f_ = (a); f_ < (b); ++f_) { const unsigned fv = f_ < 2 ? CODED : fpv; st128(u32x4{fv, fv, fv, fv}, rW, fpa[f_], 0, 16); }
      u32x4 WS[10];
#define LD_WS(a, b) _Pragma("unroll") for (int x = (a); x < (b); ++x)                                   \
      WS[x] = __builtin_amdgcn_raw_buffer_load_b128(rW1, gw[x % 5], (9 + mg + 4 * (x / 5)) * (32 * D_ * 2), 0)
#define LD_WD(a, b) _Pragma("unroll") for (int x = (a); x < (b); ++x) {                                 \
      if (x < 10) W_DMA(mg + 4 * (x / 5), x % 5);                                                       \
      else if (x == 10) dma<1, 0>(mg == 0 ? gw[0] : mg == 1 ? gw[1] : mg == 2 ? gw[2] : gw[3], dW1, 8u * (32 * D_ * 2), (unsigned)(8 * 5120 + mg * 1024)); \
      else if (mg == 0) W_DMA(8, 4);                                                                    \
    }
      FILL_Z();
      FENCE();
      u32x4 wm[5];
      unsigned short cv;
      {
        const __amdgpu_buffer_rsrc_t rM = __builtin_amdgcn_make_buffer_rsrc((void*)w_mod, 0, D_ * 6 * D_ * 2, RS3);
        const __amdgpu_buffer_rsrc_t rC = __builtin_amdgcn_make_buffer_rsrc((void*)cin, 0, D_ * 2, RS3);
        const unsigned o = bx < 240 ? (unsigned)(t8 >> 3) * (6 * D_ * 2) + (unsigned)(t8 & 7) * 16u : OOB;
        cv = __builtin_amdgcn_raw_buffer_load_b16(rC, (unsigned)((t8 >> 3) + 32 * min(lane & 7, 4)) * 2u, (bx & 3) * 320, 0);
#pragma unroll
        for (int i = 0; i < 5; ++i) wm[i] = __builtin_amdgcn_raw_buffer_load_b128(rM, o, ((bx & 3) * 160 + i * 32) * (6 * D_ * 2) + (bx >> 2) * 128, 0);
      }
      FENCE();
      W12_G(gw);
#pragma unroll
      for (int j = 0; j < 5; ++j) PIN(gw[j]);
      const int job = bx * 4 + mg;
      const bool j3 = job < 540, jok = job < 972;
      const int jks = j3 ? job / 27 : (job - 540) >> 3, jcg = j3 ? job % 27 : (job - 540) & 7;
      const unsigned rtl = (unsigned)(L_RT + mg * 5120);
      u32x4 rt[5];
      unsigned rta[5];
      {
        const unsigned v3 = ((unsigned)lane >> 3) * (F_ * 2) + (((unsigned)lane & 7u) * 16u ^ ((unsigned)lane & 24u) * 4u) + (unsigned)jks * (32 * F_ * 2) + (unsigned)jcg * 128u;
        const unsigned s4 = (unsigned)jks * (32 * D_ * 2) + (unsigned)(jcg - nt) * 160u;
#pragma unroll
        for (int i = 0; i < 5; ++i) { rta[i] = j3 ? (i < 4 ? (v3 ^ (i & 1) * 32u) + i * (8 * F_ * 2) : OOB) : gw[i] + s4; PIN(rta[i]); }
      }
#define RT_LD() do {                                                                                    \
      const u32x4 dS = mk_rs(j3 ? w3 : w4, jok ? DF2 : 0);                                              \
      _Pragma("unroll") for (int i = 0; i < 4; ++i) dma<1, 0>(rta[i], dS, 0u, rtl + i * 1024);          \
      if (!j3) dma<1, 0>(rta[4], dS, 0u, rtl + 4096);                                                   \
      FENCE();                                                                                          \
    } while (0)
      FENCE();
      STAMP(12);
      asm volatile("" : "+v"(cpf));
      asm volatile("" :: "v"(cp0));
      SIG(P_K);
      if constexpr (TSHI == 23) TX_(31);
      FENCE();
      TAGS();
      {
        float a[8];
#pragma unroll
        for (int e = 0; e < 8; ++e) a[e] = 0.f;
        const float c_ = lo2f(cv);
        const int sci = __float_as_int(rbf(c_ * __builtin_amdgcn_rcpf(1.f + __builtin_amdgcn_exp2f(c_ * -1.4426950408889634f))));
#pragma unroll
        for (int i = 0; i < 5; ++i) {
          const float sc = __int_as_float(__builtin_amdgcn_ds_bpermute(((lane & 56) + i) * 4, sci));
#pragma unroll
          for (int e = 0; e < 4; ++e) { a[2 * e] += sc * lo2f(wm[i][e]); a[2 * e + 1] += sc * hi2f(wm[i][e]); }
        }
#pragma unroll
        for (int e = 0; e < 4; ++e) {
          const auto r = __builtin_amdgcn_permlane32_swap(__float_as_uint(a[e]), __float_as_uint(a[e + 4]), false, false);
          a[e] = __uint_as_float((unsigned)r[0]) + __uint_as_float((unsigned)r[1]);
        }
#pragma unroll
        for (int e = 0; e < 2; ++e) {
          const auto r = __builtin_amdgcn_permlane16_swap(__float_as_uint(a[e]), __float_as_uint(a[e + 2]), false, false);
          a[e] = dpp_add(__uint_as_float((unsigned)r[0]) + __uint_as_float((unsigned)r[1]), 0);
        }
        if ((lane & 8) == 0) *(LAS f32x2*)(lds + L_MP + mg * 256 + (lane & 7) * 32 + g * 8) = f32x2{a[0], a[1]};
        SIG(P_M);
        TS_(52);
        if constexpr (TSHI == 23) TX_(32);
        FENCE();
        REQ_Q();
        REQ_D();
        FENCE();
        if (wv == 7) {
          WAIT_HALF(1, P_M);
          f32x2 s = *(const LAS f32x2*)(lds + L_MP + (lane & 31) * 8);
#pragma unroll
          for (int w = 1; w < 4; ++w) s += *(const LAS f32x2*)(lds + L_MP + w * 256 + (lane & 31) * 8);
          st128(u32x4{__float_as_uint(s[0]), tA, __float_as_uint(s[1]), tB}, rW, lane < 32 ? l16 : OOB, O_MP + (unsigned)bx * 512u, 16);
          __builtin_amdgcn_raw_buffer_store_b64(u32x2{tA, tB}, rW, lane == 0 ? (unsigned)bx * 8u : OOB, O_FL1, 16);
        }
        FENCE();
      }
      STAMP(2);
      {
      WAIT_HALF(1, P_K);
      BUILD(SL_A, t8);
      BUILD(SL_B, t8);
      SIG(P_B2);
      if constexpr (TSHI == 23) TX_(33);
      FENCE();
      STAMP(1);
      }
      {
        const unsigned o_ = lane < 50 ? (unsigned)(bx * 4 + mg) * 800u + l16 : OOB;
        dma<1, 0>(o_, dW1, 0u, (unsigned)L_ST4);
        dma<1, 0>(o_, dW2, 0u, (unsigned)L_ST4);
        FENCE();
      }
      FENCE();
      FENCE();
      unsigned tra[10], rto[5];
      {
        const unsigned b3 = rtl + (unsigned)(8 * g + (l15 >> 2)) * 128u + (unsigned)((l15 >> 2) ^ (g & 1)) * 32u + ((unsigned)lane & 3u) * 8u, b4 = rtl + ((unsigned)lane >> 2) * 160u + ((unsigned)lane & 3u) * 8u;
        const unsigned dst = j3 ? O_WT3 + (unsigned)(jcg * 80 + jks) * 1024u : O_WT4 + (unsigned)(jcg * 54 + jks) * 5120u, dstr = j3 ? 20480u : 1024u;
#pragma unroll
        for (int jn = 0; jn < 5; ++jn) {
          tra[jn] = j3 ? b3 ^ (unsigned)(jn & 3) * 32u : b4 + jn * 32u;
          tra[5 + jn] = tra[jn] + (j3 ? 512u : 2560u);
          rto[jn] = (jok && !(j3 && jn == 4)) ? l16 + dst + jn * dstr : OOB;
          PIN(tra[jn]); PIN(tra[5 + jn]); PIN(rto[jn]);
        }
      }
#define RT_TR() do {                                                                                    \
        _Pragma("unroll") for (int jn = 0; jn < 5; ++jn) {                                              \
          const s16x4 lo = __builtin_amdgcn_ds_read_tr16_b64_v4i16((LAS s16x4*)(size_t)tra[jn]);        \
          const s16x4 hi = __builtin_amdgcn_ds_read_tr16_b64_v4i16((LAS s16x4*)(size_t)tra[5 + jn]);    \
          rt[jn] = __builtin_bit_cast(u32x4, bf16x8{lo[0], lo[1], lo[2], lo[3], hi[0], hi[1], hi[2], hi[3]}); \
          SB();                                                                                         \
        }                                                                                               \
      } while (0)
#define RT_OUT() do {                                                                                   \
        _Pragma("unroll") for (int jn = 0; jn < 4; ++jn) st128(rt[jn], rW, rto[jn], 0, 16);             \
        if (!j3) st128(rt[4], rW, rto[4], 0, 16);                                                       \
        FENCE();                                                                                        \
      } while (0)
      SIG(P_U);
      const bool mfin = wv == 7 && bx < 240 && lane < 32;
      const unsigned po = mfin ? ((unsigned)lane >> 3) * 512u + ((unsigned)lane & 7u) * 16u : OOB;
      u32x4 mp4;
#define LD_P() mp4 = __builtin_amdgcn_raw_buffer_load_b128(rW, po, O_MP + (unsigned)(bx & ~3) * 512u + (unsigned)(bx & 3) * 128u, 16)
#define MOD_L2() do {                                                                                   \
      if (wv == 7) {                                                                                    \
        unsigned spins = 0;                                                                             \
        while (__builtin_amdgcn_ballot_w64(mfin && (mp4[1] != tA || mp4[3] != tB)) != 0ull) {           \
          SPIN_STEP();                                                                                  \
          if (spins > RETRY_CAP) break;                                                                 \
          __builtin_amdgcn_s_sleep(USLEEP);                                                             \
          LD_P();                                                                                       \
        }                                                                                               \
        const float one = ((bx >= 40 && bx < 80) || (bx >= 160 && bx < 200)) ? 1.f : 0.f;               \
        const float s0_ = swap_add16(dpp_add(__uint_as_float(mp4[0]), 0)), s1_ = swap_add16(dpp_add(__uint_as_float(mp4[2]), 0)); \
        const unsigned d = pack2(one + rbf(s0_), one + rbf(s1_));                                       \
        const unsigned dn = (unsigned)__builtin_amdgcn_update_dpp(0, (int)d, 0xB1, 0xf, 0xf, true);     \
        st128(u32x4{d, tA, dn, tB}, rW, (lane < 8 && (lane & 1) == 0) ? (unsigned)lane * 8u : OOB, O_MOD + (unsigned)bx * 64u, 16); \
      }                                                                                                 \
      FENCE();                                                                                          \
    } while (0)
      if (wv == 7) LD_P();
      FENCE();
      STAMP(3);
      FENCE();
      asm volatile("" : "+v"(fpa[0]), "+v"(fpa[1]), "+v"(fpa[2]), "+v"(fpv));
      ln5s(qw, hw);
      GET_D();
      if constexpr (TSHI == 23) TX_(34);
      MOD_L2();
      if constexpr (TSHI == 23) TX_(35);
      TS_(27);
      if constexpr (TSHI == 23) TX_(36);
      STAMP(22);
      WAIT_ALL(P_U);
      FENCE();
      RT_LD();
      TS_(4);
      STAMP(28);
      FILL_P(0, 3);
      FENCE();
      VMW(3);
      RT_TR();
      SIG(P_RT);
      TS_(48);
      RT_OUT();
      LD_WD(0, 5);
      WAIT_HALF(1, P_RT);
      LD_WD(5, NGA);
      FENCE();
      VMW(NGA);
      SIG(P_GA);
      TS_(5);
      LD_WD(NGA, 12);
      FENCE();
      if (mg < 2) {
        u32x2 uc[2];
        f32x4 yc[8];
        float v[8];
        uc[0] = *(const LAS u32x2*)(lds + SL_C + CV_U + 512 + (256 * zja + 16 * l15 + 4 * g) * 2);
        uc[1] = *(const LAS u32x2*)(lds + SL_C + CV_U + 512 + (256 * zjb + 16 * l15 + 4 * g) * 2);
        unsigned zq = O_Z + ZOFF(zja, zjb, chC);
        PIN(zq);
        WAIT_HALF(0, P_C2);
#pragma unroll
        for (int c = 0; c < 8; ++c) yc[c] = *(const LAS f32x4*)(lds + SL_C + mg * 2048 + l16 + (c >> 1) * 4096 + (c & 1) * 1024);
        STG(8, v[i] = (i & 1) ? hi2f(uc[i >> 2][(i >> 1) & 1]) : lo2f(uc[i >> 2][(i >> 1) & 1]); PIN(v[i]));
        STG(8, v[i] = __builtin_fmaf(v[i], __uint_as_float(dC), yc[i >> 2][i & 3]); PIN(v[i]));
#pragma unroll
        for (int w = 1; w < 4; ++w) STG(8, v[i] += yc[2 * w + (i >> 2)][i & 3]; PIN(v[i]));
        st128(widen(pack2(v[0], plainv(v[1])), pack2(v[2], v[3]), pack2(v[4], plainv(v[5])), pack2(v[6], v[7])), rW, zq, 0, 16);
      }
      FENCE();
      SIG(P_ZC);
      FENCE();
      LD_WS(0, NS2);
      FENCE();
      if (HPZ) { WAIT_HALF(0, P_X2); __builtin_amdgcn_s_sleep(HPZ); }
      STAMP(15);
      LD_WS(NS2, 10);
      if (mg > 0) {
        WAIT_HALF(1, P_ZC);
#pragma unroll
        for (int j = 0; j < 5; ++j) W_DMA(16 + mg, j);
      }
      LD_WS2U(0, NZ2);
      FENCE();
      STAMP(5);
      TS_(6);
      WAIT_HALF(0, P_X2);
#pragma unroll
      for (int x = 0; x < 10; ++x) *(LAS u32x4*)(lds + (9 + mg + 4 * (x / 5)) * 5120 + (x % 5) * 1024 + l16) = WS[x];
      TS_(1);
      WAIT_HALF(0, P_T);
      R_PRE();
      VMW(NZ2);
      SIG(P_X);
      TS_(2);
      TS_(3);
      WAIT_HALF(0, P_Z);
      LD_WS2U(NZ2, 10);
      FENCE();
      WAIT_2(P_RP, P_X);
#pragma unroll
      for (int j = 0; j < 5; ++j) W_DMA(20 + mg, j);
      FENCE();
      TS_(7);
      STAMP(7);
    } else {
      PRIO(3);
      const unsigned rt0 = (unsigned)__builtin_amdgcn_s_memrealtime();
      unsigned kpa = (unsigned)L_KV + ((unsigned)lane & 7u) * 1312u + (unsigned)mg * 64u + ((unsigned)lane >> 3) * 8u;
      unsigned kpb = (unsigned)L_KV + ((unsigned)l15 >> 1) * 1312u + 256u + (unsigned)mg * 16u + (unsigned)g * 4u + ((unsigned)lane & 1u) * 640u, kpc = kpb + ((unsigned)lane & 1u) * 32u;
      const unsigned cha_ = (unsigned)ch0 + 32u * mg + ((unsigned)lane >> 3) * 4u, chb_ = (unsigned)ch0 + 128u + 8u * mg + 2u * g;
      unsigned koa = KVROW(cha_) + (unsigned)tok0 * 4u + ((unsigned)lane & 7u) * 16u, koa2 = KVROW(cha_ + 2u) + (unsigned)tok0 * 4u + ((unsigned)lane & 7u) * 16u;
      unsigned kob = KVROW(chb_) + (unsigned)tok0 * 4u + (unsigned)l15 * 8u;
      asm volatile("" : "+v"(kpa), "+v"(kpb), "+v"(kpc), "+v"(koa), "+v"(koa2), "+v"(kob));
      u32x4 ug[6];
      unsigned uo = (unsigned)mg * 2048u + l16;
      unsigned udone;
      asm volatile("s_mov_b32 %0, 0" : "=s"(udone));
      unsigned ubA = KVROW((unsigned)chA), ubC = KVROW((unsigned)chC), ubB = KVROW((unsigned)(chA + 1));
      asm volatile("" : "+s"(ubA), "+s"(ubB), "+s"(ubC));
      PIN(uo);
#pragma unroll
      for (int r = 0; r < 6; ++r) asm volatile("" : "=v"(ug[r]));
#define TRY_U(EL, T, CODE)                                                                               \
        asm volatile("s_cmp_lg_u32 %6, 0\n\ts_cbranch_scc1 Lu%=\n\ts_cmp_lt_u32 %[el], %[t]\n\ts_cbranch_scc1 Lu%=\n\t" \
                     "buffer_load_dwordx4 %2, %[vo], %[rs], %[c] offen sc1\n\tbuffer_load_dwordx4 %3, %[vo], %[rs], %[c] offen offset:1024 sc1\n\t" \
                     "buffer_load_dwordx4 %0, %[vo], %[rs], %[a] offen sc1\n\tbuffer_load_dwordx4 %1, %[vo], %[rs], %[a] offen offset:1024 sc1\n\t" \
                     "buffer_load_dwordx4 %4, %[vo], %[rs], %[b] offen sc1\n\tbuffer_load_dwordx4 %5, %[vo], %[rs], %[b] offen offset:1024 sc1\n\t" \
                     "s_mov_b32 %6, %[cd]\n"                                                            \
                     "Lu%=:" : "+v"(ug[0]), "+v"(ug[1]), "+v"(ug[2]), "+v"(ug[3]), "+v"(ug[4]), "+v"(ug[5]), "+s"(udone) \
                     : [el] "s"(EL), [t] "i"(T), [cd] "i"(CODE), [vo] "v"(uo), [rs] "s"(dW), [a] "s"(ubA), [b] "s"(ubB), [c] "s"(ubC) : "scc", "memory")
      VMW(9);
      TAGS();
      unsigned tAB = (lane & 1) ? tB : tA;
      PIN(tAB);
      asm volatile("s_waitcnt vmcnt(0)" : "+v"(sk[0]), "+v"(sk[1]), "+v"(sk[2]));
      const unsigned rt1 = (unsigned)__builtin_amdgcn_s_memrealtime();
      SIG(P_KV);
      FENCE();
      f32x2 wa[3][4];
#pragma unroll
      for (int c = 0; c < 3; ++c)
#pragma unroll
        for (int t = 0; t < 4; ++t) {
          const f32x2 w = (t & 1) ? f32x2{hi2f(sk[c][t >> 1]), hi2f(sk[c][2 + (t >> 1)])} : f32x2{lo2f(sk[c][t >> 1]), lo2f(sk[c][2 + (t >> 1)])};
          wa[c][t] = w;
          PIN(wa[c][t]);
        }
      asm volatile("" : "+v"(tAB));
      FENCE();
      TS_(1);
      STAMP(1);
      WAIT_HALF(0, P_KV);
      if (TU1) TRY_U(rt1 - rt0, TU1, 1);
      if constexpr (TSHI == 23) TX_(31);
      STAMP(12);
      {
        u32x2 kw[7], vw[4];
        unsigned kx[5], vx[2];
#pragma unroll
        for (int i = 0; i < 7; ++i) kw[i] = *(const LAS u32x2*)(lds + kpa + (i < 3 ? 320 + 320 * i : 1312 + 320 * (i - 3)));
#pragma unroll
        for (int i = 0; i < 5; ++i) kx[i] = *(const LAS unsigned*)(lds + ((i == 1 || i == 2) ? kpc : kpb) + (i < 3 ? 320 + 320 * i : 1312 + 320 * (i - 3)));
#pragma unroll
        for (int i = 0; i < 4; ++i) vw[i] = *(const LAS u32x2*)(lds + kpa + 12288 + 320 * i);
#pragma unroll
        for (int i = 0; i < 2; ++i) vx[i] = *(const LAS unsigned*)(lds + kpb + 12288 + 320 * i);
        __builtin_amdgcn_sched_barrier(0);
        f32x2 kf[19], a[10], p[10], t_[10];
#define KX(o, t) kf[((o) >> 2) * 7 + ((o) & 3) + (t)]
#define STR(lo, hi, stmt) do { _Pragma("unroll") for (int i = (lo); i < (hi); ++i) { stmt; } __builtin_amdgcn_sched_barrier(0); } while (0)
#define KV_GROUP(lo, hi) do {                                                                           \
        STR(((lo) >> 2) * 7, (hi) == 10 ? 19 : ((hi) >> 2) * 7, kf[i] = up2(i < 14 ? kw[i % 7][i / 7] : kx[i < 14 ? 0 : i - 14]); PIN(kf[i])); \
        {                                                                                               \
          STR(lo, hi, a[i] = KX(i, 0) * wa[i >> 2][3]; PIN(a[i]));                                      \
          _Pragma("unroll") for (int t = 1; t < 4; ++t) STR(lo, hi, a[i] = FMA2(KX(i, t), wa[i >> 2][3 - t], a[i]); PIN(a[i])); \
          STR(lo, hi, t_[i] = FMA2(a[i], cp, a[i]); PIN(t_[i]));                                        \
          STR(lo, hi, a[i] = FMA2(a[i], cn, t_[i]); PIN(a[i]));                                         \
        }                                                                                               \
        STR(lo, hi, p[i] = up2(i < 8 ? vw[i & 3][i >> 2] : vx[i & 1]); PIN(p[i]));                      \
        STR(lo, hi, a[i] *= p[i]; PIN(a[i]));                                                           \
        if constexpr (TSHI == 23) TX_(32 + (lo) / 2);                                                   \
        _Pragma("unroll") for (int c = (lo) >> 1; c < ((hi) < 8 ? (hi) : 8) >> 1; ++c) {                \
          const int o = (c >> 1) * 4, e = c & 1;                                                        \
          st128(u32x4{pack2(a[o][e], plainv(a[o + 1][e])), tA, pack2(a[o + 2][e], plainv(a[o + 3][e])), tB}, rW, c < 2 ? koa : koa2, e * 8192u, 16); \
        }                                                                                               \
        if ((hi) == 10) { _Pragma("unroll") for (int e = 0; e < 2; ++e) __builtin_amdgcn_raw_buffer_store_b64(u32x2{pack2(a[8][e], plainv(a[9][e])), tAB}, rW, kob, e * 8192, 16); } \
        __builtin_amdgcn_sched_barrier(0);                                                              \
      } while (0)
        KV_GROUP(0, 4);
        if constexpr (TSHI == 23) TX_(33);
        STAMP(5);
        KV_GROUP(4, 10);
        if constexpr (TSHI == 23) TX_(35);
      }
      FENCE();
      const unsigned rt2 = (unsigned)__builtin_amdgcn_s_memrealtime();
      REQ_Q();
      REQ_D();
      FENCE();
      if (TU2) TRY_U(rt2 - rt0, TU2, 2);
      STAMP(2);
      TS_(2);
      WAIT_HALF(1, P_K);
      BUILD(SL_C, t8);
      SIG(P_B);
      if constexpr (TSHI == 23) TX_(36);
      FENCE();
#define LD_U(r) ug[r] = __builtin_amdgcn_raw_buffer_load_b128(rW, uo + ((r) & 1) * 1024, (r) < 2 ? ubA : (r) < 4 ? ubC : ubB, 16)
      u32x4 A2[16], B2[16];
      WAIT_HALF(0, P_B);
      CV_RA(A2, SL_C, 0);
      CV_RA(A2, SL_C, 1);
      W12_G(gw);
#pragma unroll
      for (int j = 0; j < 5; ++j) PIN(gw[j]);
      unsigned zq = Z2OFF(wv);
      unsigned pa = a00 + SL_A - 3648, pb = b00 + SL_A - 64;
      unsigned xw = SL_A + mg * 8192 + l16, xc = SL_C + mg * 4096 + l16;
      asm volatile("" : "+v"(zq), "+v"(pa), "+v"(pb), "+v"(xw), "+v"(xc));
      FENCE();
      if (udone == 0u) while ((unsigned)__builtin_amdgcn_s_memrealtime() - rt0 < (unsigned)TU) __builtin_amdgcn_s_sleep(1);
      STAMP(3);
      TS_(3);
      TRY_U(rt0, 0, 3);
      FENCE();
      if constexpr (TSHI == 23) TX_(37);
      unsigned uw = (unsigned)(SL_A + CV_U + 512 + mg * 1024 + lane * 8);
      PIN(uw);
      WAIT_HALF(1, P_B2);
      asm volatile("s_cmp_eq_u32 %[d], 1\n\ts_cbranch_scc1 Lw%=\n\ts_waitcnt vmcnt(0)\nLw%=:\n\ts_waitcnt vmcnt(9)"
                   : "+v"(ug[0]), "+v"(ug[1]), "+v"(ug[2]), "+v"(ug[3]), "+v"(ug[4]), "+v"(ug[5]) : [d] "s"(udone) : "scc", "memory");
      if constexpr (TSHI == 23) { TX_(38); PIN(ug[5]); __builtin_amdgcn_s_sleep(1); TX_(39); }
      {
#define U_BAD(r) (ug[r][1] != tA || ug[r][3] != tB)
        if (__builtin_expect(__builtin_amdgcn_ballot_w64(U_BAD(0) || U_BAD(1) || U_BAD(2) || U_BAD(3) || U_BAD(4) || U_BAD(5)) != 0ull, 1)) {
          unsigned spins = 0;
          do {
            __builtin_amdgcn_s_sleep(USLEEP);
#pragma unroll
            for (int r = 0; r < 6; ++r) if (U_BAD(r)) LD_U(r);
            SPIN_STEP();
          } while (__builtin_amdgcn_ballot_w64(U_BAD(0) || U_BAD(1) || U_BAD(2) || U_BAD(3) || U_BAD(4) || U_BAD(5)) != 0ull && spins < RETRY_CAP);
          VM0();
        }
#pragma unroll
        for (int r = 2; r < 8; ++r) *(LAS u32x2*)(lds + uw + ((r % 6) >> 1) * CV_SLOT + (r & 1) * 512) = u32x2{ug[r % 6][0], ug[r % 6][2]};
      }
      SIG(P_U);
      TS_(56);
      if constexpr (TSHI == 23) TX_(40);
      STAMP(22);
      GET_D();
      LD_WS2(0, 10);
      FENCE();
      WAIT_HALF(0, P_U);
      CV_RB(B2, SL_C, 0);
      FENCE();
      STAMP(28);
      TS_(4);
      f32x4 acc[8], ac[4];
      u32x4 A[16], B[16];
#define RA(P, rd, i_) *(const LAS u32x4*)(lds + (P) + 3648 - 64 * (rd) - 512 * (i_))
#define RB(P, rd, i_) *(const LAS u32x4*)(lds + (P) + 64 - 64 * (rd) + 512 * (i_))
#define CONV_C(J, lo, hi)                                                                               \
      sfor<lo, hi>([&](auto C_) __attribute__((always_inline)) {                                         \
        constexpr int c = decltype(C_)::v, rd = c / 18, i = cc_i(J, c % 18), t = cc_t(J, c % 18);       \
        ac[t] = mfma16(A2[8 * rd + i], B2[8 * rd + (((J) >> (4 * t)) & 15) - i], (rd == 0 && i == 0) ? f32x4{0.f, 0.f, 0.f, 0.f} : ac[t]); \
        SB();                                                                                           \
        if constexpr (c < 8) B2[15 - c] = RB(pb + CV_SLOT, 1, 7 - c);                                   \
        if constexpr (c >= 18 && c < 34) { constexpr int x = (c - 18) >> 1; if constexpr ((c & 1) == 0) B[7 - x] = RB(pb, 0, 7 - x); else A[x] = RA(pa, 0, x); } \
        SB();                                                                                           \
      });
      if (half == 0) { CONV_C(0x7430, 0, 18) } else { CONV_C(0x6521, 0, 18) }
      SB();
      int half2 = half;
      asm volatile("" : "+s"(half2));
      if (half2 == 0) { CONV_C(0x7430, 18, 36) } else { CONV_C(0x6521, 18, 36) }
      u32x4 A3[16], B3[16];
      f32x4 ab_[8];
      unsigned pk_;
      float ud[16];
      u32x2 u2[4];
      f32x4 ya[8];
      unsigned ga_;
      unsigned pu = (unsigned)(SL_A + CV_U + 512 + (256 * wv + 16 * l15 + 4 * g) * 2), py = (unsigned)(SL_A + wv * 1024) + l16;
      const unsigned f0 = (unsigned)L_CTL + ((unsigned)lane & 3u) * 4u;
      sfor<0, 144>([&](auto M_) __attribute__((always_inline)) {
        constexpr int m = decltype(M_)::v, ch = m / 72, rd = (m / 36) & 1, r = m % 36, i = m < 108 ? cv_i(r) : cl_i(r), dd = m < 108 ? cv_d(r) : cl_t(r) - cl_i(r);
        if constexpr (ch == 0) acc[i + dd] = mfma16(A[8 * rd + i], B[8 * rd + dd], (rd == 0 && i == 0) ? f32x4{0.f, 0.f, 0.f, 0.f} : acc[i + dd]);
        else ab_[i + dd] = mfma16(A3[8 * rd + i], B3[8 * rd + dd], (rd == 0 && i == 0) ? f32x4{0.f, 0.f, 0.f, 0.f} : ab_[i + dd]);
        SB();
        if constexpr (m == 30) { pa += SL_B - SL_A; pb += SL_B - SL_A; asm volatile("" : "+v"(pa), "+v"(pb)); }
        if constexpr (m == 94) { xw += SL_B - SL_A; PIN(xw); }
        if constexpr (r >= RS0 && r < RS0 + 16 && m < 108) {
          constexpr int x = (r - RS0) >> 1;
          if constexpr (m < 36) { if constexpr ((r & 1) == 0) B[15 - x] = RB(pb, 1, 7 - x); else A[8 + x] = RA(pa, 1, x); }
          else if constexpr (m < 72) { if constexpr ((r & 1) == 0) B3[7 - x] = RB(pb, 0, 7 - x); else A3[x] = RA(pa, 0, x); }
          else { if constexpr ((r & 1) == 0) B3[15 - x] = RB(pb, 1, 7 - x); else A3[8 + x] = RA(pa, 1, x); }
        }
        if constexpr (m == 1 || m == 4 || m == 23 || m == 26) { constexpr int c = m == 1 ? 0 : m == 4 ? 1 : m == 23 ? 2 : 3; *(LAS f32x4*)(lds + xc + c * 1024) = ac[c]; }
        if constexpr (m == 28) SIG(P_C2);
        if constexpr (m == 38 || m == 104) pk_ = *(volatile LAS unsigned*)(lds + f0);
        if constexpr (m == 45) NEED_AT(pk_, f0, P_C2);
        if constexpr (m == 111) NEED_AT(pk_, f0, P_A2);
        constexpr int wa = m == 47 ? 0 : m == 54 ? 1 : m == 60 ? 2 : m == 65 ? 3 : m == 69 ? 4 : m == 72 ? 5 : m == 75 ? 6 : m == 77 ? 7 : -1;
        constexpr int wb = m == 113 ? 0 : m == 116 ? 1 : m == 119 ? 2 : m == 122 ? 3 : m == 126 ? 4 : m == 132 ? 5 : m == 139 ? 6 : -1;
        if constexpr (wa >= 0) *(LAS f32x4*)(lds + xw + wa * 1024) = acc[wa];
        if constexpr (wb >= 0) *(LAS f32x4*)(lds + xw + wb * 1024) = ab_[wb];
        if constexpr (m == 95) SIG(P_A2);
        if constexpr (m == 58) PIN(pu);
        if constexpr (m == 59 || m == 61) { constexpr int c = m - 59; u2[c] = *(const LAS u32x2*)(lds + pu + c * 1024); u2[c + 1] = *(const LAS u32x2*)(lds + pu + (SL_B - SL_A) + c * 1024); }
        constexpr int us = cv_slot(m, 0);
        if constexpr (us >= 0 && us < 16) {
#pragma unroll
          for (int o = 2 * us; o < 2 * us + 2; ++o) {
            const int v = (o >> 3) * 4 + (o & 3);
            if ((o & 4) == 0) ud[v] = (v & 1) ? hi2f(u2[v >> 2][(v >> 1) & 1]) : lo2f(u2[v >> 2][(v >> 1) & 1]);
            else ud[v] *= ((v >> 2) & 1) ? hi2f(dAB) : lo2f(dAB);
            PIN(ud[v]);
          }
        }
        if constexpr (m == 110) PIN(py);
        if constexpr (m == 112 || m == 114 || m == 115 || m == 117) { constexpr int c = m == 112 ? 0 : m == 114 ? 2 : m == 115 ? 4 : 6;
          ya[c] = *(const volatile LAS f32x4*)(lds + py + (c >> 2) * 4096 + (c & 3) * 8192); ya[c + 1] = *(const volatile LAS f32x4*)(lds + py + (c >> 2) * 4096 + (c & 3) * 8192 + 8192); }
        constexpr int zs = cv_slot(m, 1);
        if constexpr (zs >= 0 && zs < 16) {
#pragma unroll
          for (int o = 2 * zs; o < 2 * zs + 2; ++o) { const int tl = (o >> 2) & 1, e = o & 3; ud[tl * 8 + e] += ya[4 * tl + (o >> 3)][e]; PIN(ud[tl * 8 + e]); }
        }
        if constexpr (TSHI == 10 && (m % 18 == 0 || m == 143)) TX_(44 + (m + 1) / 18);
        if constexpr (m == 131) ga_ = *(volatile LAS unsigned*)(lds + f0 + 16);
        if constexpr (m == 140) { py += SL_B - SL_A; PIN(py); }
        SB();
      });
      *(LAS f32x4*)(lds + xw + 7 * 1024) = ab_[7];
      SIG(P_X);
      TS_(5);
      STAMP(4);
      u32x4 mg4[4];
#define LD_MG(i) mg4[i] = __builtin_amdgcn_raw_buffer_load_b128(rW, (unsigned)t8 * 16u, O_MOD + (i) * 4096, 16)
      u32x2 f1v;
#define LD_F1() f1v = __builtin_amdgcn_raw_buffer_load_b64(rW, (unsigned)t8 * 8u, O_FL1, 16)
      NEED_AT(ga_, f0 + 16, P_GA);
      {
        unsigned spins = 0;
        for (;;) {
          const unsigned fl = *(volatile LAS unsigned*)(lds + f0);
#pragma unroll
          for (int c = 0; c < 8; ++c) ya[c] = *(const volatile LAS f32x4*)(lds + py + (c >> 2) * 4096 + (c & 3) * 8192);
          if (__builtin_amdgcn_ballot_w64(fl < (unsigned)P_X) == 0ull) break;
          SPIN_STEP();
          if (spins > SPIN_CAP) break;
        }
#pragma unroll
        for (int tl = 0; tl < 2; ++tl) {
          const f32x4 yb = (ya[4 * tl] + ya[4 * tl + 1]) + (ya[4 * tl + 2] + ya[4 * tl + 3]);
          const f32x2 v2 = f32x2{yb[0], yb[1]} + f32x2{ud[8 * tl + 4], ud[8 * tl + 5]}, v3 = f32x2{yb[2], yb[3]} + f32x2{ud[8 * tl + 6], ud[8 * tl + 7]};
          st128(u32x4{pack2(ud[8 * tl], plainv(v2[0])), pack2(ud[8 * tl + 1], v2[1]), pack2(ud[8 * tl + 2], plainv(v3[0])), pack2(ud[8 * tl + 3], v3[1])}, rW, zq + tl * 64u * TS, 0, 16);
        }
      }
      FENCE();
      SIG(P_X2);
      TS_(6);
      LD_F1(); LD_MG(0); LD_MG(1); LD_MG(2); LD_MG(3);
      FENCE();
      LN1();
      STAMP(6);
      u32x4 za, zb, zc = {0u, 0u, 0u, 0u}, zd = {0u, 0u, 0u, 0u};
      const unsigned zo = (unsigned)mt * (4u * TS) + ZCH((unsigned)t8 & ~1u) + (unsigned)nt * 32u + ((unsigned)t8 & 1u) * 16u;
      const unsigned zo1 = (unsigned)mt * (4u * TS) + ZCH(256u + ((unsigned)t8 & ~1u)) + (unsigned)nt * 32u + ((unsigned)t8 & 1u) * 16u;
      const unsigned zo2 = (unsigned)mt * (4u * TS) + ZCH(2u * t8 + 128u) + (unsigned)nt * 16u;
      FENCE();
      {
#define MG_BAD(i) (mg4[i][1] != tA || mg4[i][3] != tB)
#define T_BAD() (__builtin_amdgcn_ballot_w64(f1v[0] != tA || f1v[1] != tB || MG_BAD(0) || MG_BAD(1) || MG_BAD(2) || MG_BAD(3)) != 0ull)
        if (__builtin_expect(T_BAD(), 0)) {
          unsigned spins = 0;
          do {
            __builtin_amdgcn_s_sleep(USLEEP);
            if (f1v[0] != tA || f1v[1] != tB) LD_F1();
#pragma unroll
            for (int i = 0; i < 4; ++i) if (MG_BAD(i)) LD_MG(i);
            SPIN_STEP();
          } while (T_BAD() && spins < RETRY_CAP);
          VM0();
        }
#pragma unroll
        for (int i = 0; i < 4; ++i) {
          const int n = (t8 + 256 * i) * 4;
          if (n < 6 * D_) *(LAS u32x2*)(lds + L_MOD + n * 2) = u32x2{mg4[i][0], mg4[i][2]};
          if (i == 3 && n >= 5 * D_ + n0 && n < 5 * D_ + n0 + 80)
            *(LAS f32x4*)(lds + L_GB + (n - 5 * D_ - n0) * 4) = f32x4{lo2f(mg4[i][0]), hi2f(mg4[i][0]), lo2f(mg4[i][2]), hi2f(mg4[i][2])};
        }
        SIG(P_T);
        if constexpr (TSHI == 23) TX_(41);
      }
      WAIT_HALF(0, P_T);
      if (DLY0) __builtin_amdgcn_s_sleep(DLY0);
      PRIO(3);
      STAMP(7);
      TS_(7);
      za = __builtin_amdgcn_raw_buffer_load_b128(rW, zo, O_Z, 16); zb = __builtin_amdgcn_raw_buffer_load_b128(rW, zo1, O_Z, 16);
      if (wv == 3) { zc = __builtin_amdgcn_raw_buffer_load_b128(rW, zo2, O_Z, 16); zd = __builtin_amdgcn_raw_buffer_load_b128(rW, zo2 + 16u * ZG, O_Z, 16); }
      FENCE();
      SIG(P_Z);
      R_PRE();
      SIG(P_RP);
      unsigned zw0 = (unsigned)(L_XCH + 4 * (t8 & 1) * YROW + 4 * (t8 >> 1)), zw1 = (unsigned)(L_XCH + 256 + 4 * t8);
      asm volatile("" : "+v"(zw0), "+v"(zw1), "+v"(ry), "+v"(rz));
      asm volatile("" : "+s"(w3s[0]), "+s"(w3s[1]));
      unsigned spins = 0;
      for (;;) {
        const bool ba = amax2(za) >= PTHR, bb_ = amax2(zb) >= PTHR, bc_ = amax2(zc) >= PTHR, bd = amax2(zd) >= PTHR;
        if (__builtin_amdgcn_ballot_w64(ba || bb_ || bc_ || bd) == 0ull) break;
        SPIN_STEP();
        if (spins > RETRY_CAP) break;
        __builtin_amdgcn_s_sleep(ZSLEEP);
        if (ba) za = __builtin_amdgcn_raw_buffer_load_b128(rW, zo, O_Z, 16);
        if (bb_) zb = __builtin_amdgcn_raw_buffer_load_b128(rW, zo1, O_Z, 16);
        if (bc_) zc = __builtin_amdgcn_raw_buffer_load_b128(rW, zo2, O_Z, 16);
        if (bd) zd = __builtin_amdgcn_raw_buffer_load_b128(rW, zo2 + 16u * ZG, O_Z, 16);
      }
#pragma unroll
      for (int i = 0; i < 4; ++i) {
        *(LAS unsigned*)(lds + zw0 + i * YROW) = za[i];
        *(LAS unsigned*)(lds + zw0 + i * YROW + 512) = zb[i];
      }
      if (wv == 3) {
#pragma unroll
        for (int i = 0; i < 4; ++i) {
          *(LAS unsigned*)(lds + zw1 + (2 * i) * YROW) = __builtin_amdgcn_perm(zd[i], zc[i], 0x05040100u);
          *(LAS unsigned*)(lds + zw1 + (2 * i + 1) * YROW) = __builtin_amdgcn_perm(zd[i], zc[i], 0x07060302u);
        }
      }
      VM0();
      SIG(P_Z2);
    }
  }
  {
    f32x2 hh[5], t_[5];
    STAMP(8);
    asm volatile("" : "+v"(ry), "+v"(rz));
    asm volatile("" : "+s"(w3s[0]), "+s"(w3s[1]));
    if (kh) PRIO(PR1); else PRIO(PHI);
    WAIT_HALF(0, P_Z2);
    STAMP(9);
    TS_(8);
    unsigned zw[5], xp[5], hp_[5];
    {
      const u32x4 a = *(LAS u32x4*)(lds + ry);
      zw[0] = a[0]; zw[1] = a[1]; zw[2] = a[2]; zw[3] = a[3];
      zw[4] = *(LAS unsigned*)(lds + rz);
    }
    FENCE();
#define RQ3(k) do { LD_W3R((k) == 0 ? 0 : (k) == 1 ? NR0 : NR1, (k) == 0 ? NR0 : (k) == 1 ? NR1 : NWH); FENCE(); } while (0)
    RQ3(0);
    FENCE();
    if constexpr (TSHI == 22) TX_(30);
    STG(5, hh[i] = up2(zw[i]); PIN(hh[i]));
    STG(5, xq[i] = FMA2(hh[i], ha[i], xq[i]); PIN(xq[i]));
    STG(5, t_[i] = FMA2(xq[i], cp, xq[i]); PIN(t_[i]));
    STG(5, xq[i] = FMA2(xq[i], cn, t_[i]); PIN(xq[i]));
    if constexpr (TSHI == 22) TX_(31);
    RQ3(1);
    if constexpr (TSHI == 22) TX_(32);
    float m3;
    {
      const f32x2 s2 = ((xq[0] + xq[1]) + (xq[2] + xq[3])) + xq[4];
      const f32x2 q2 = FMA2(xq[0], xq[0], xq[1] * xq[1]) + FMA2(xq[2], xq[2], FMA2(xq[3], xq[3], xq[4] * xq[4]));
      const f32x2 p2 = FMA2(xq[0], ca[0], xq[1] * ca[1]) + FMA2(xq[2], ca[2], FMA2(xq[3], ca[3], xq[4] * ca[4]));
      float sm3[3] = {s2[0] + s2[1], q2[0] + q2[1], p2[0] + p2[1]};
      wave_sums<3>(sm3);
      const float mean = sm3[0] * (1.f / (float)D_), qs = sm3[1], ps = sm3[2];
      const float m = rbf(mean);
      f32x2 d[5];
#pragma unroll
      for (int j = 0; j < 5; ++j) d[j] = (xq[j] - m) * ca[j];
      const float var = rbf(__builtin_fmaxf(__builtin_fmaf(-mean, mean, qs * (1.f / (float)D_)), 0.f));
      const float den = rbf(__builtin_amdgcn_sqrtf(rbf(var + 1e-5f)));
      const float rinv = __builtin_amdgcn_rcpf(den);
      m3 = rbf(__builtin_fmaf(__builtin_fmaf(-m, mc, ps * (1.f / (float)D_)), rinv, mb));
      __builtin_amdgcn_sched_barrier(0);
      if constexpr (TSHI == 22) TX_(33);
      RQ3(2);
      if constexpr (TSHI == 22) TX_(34);
      STG(5, hh[i] = FMA2(d[i], (f32x2{rinv, rinv}), sb[i]); PIN(hh[i]));
      STG(5, hh[i] -= m3; PIN(hh[i]));
    }
#pragma unroll
    for (int j = 0; j < 5; ++j) hp_[j] = pack2(hh[j][0], (j & 1) ? hh[j][1] : (j < 4 || (lane & 1) == 0) ? codedv(hh[j][1]) : hh[j][1]);
    *(LAS u32x4*)(lds + ry) = u32x4{hp_[0], hp_[1], hp_[2], hp_[3]};
    *(LAS unsigned*)(lds + rz) = hp_[4];
    BARRIER();
    {
      const unsigned tb = (unsigned)(s0 >> 4) * 20480u + (unsigned)(s0 & 15) * 16u + (unsigned)(tid >> 3) * 256u + (unsigned)(tid & 7) * 16u;
      const LAS char* pr = lds + L_XCH + (tid & 7) * YROW + (tid >> 3) * 16;
      const u32x4 v0 = *(const LAS u32x4*)(pr);
      const u32x4 v1 = *(const LAS u32x4*)(pr + (tid < 128 ? 1024 : 0));
      st128(v0, rW, tb, O_HP, 16);
      if (wv < 2) st128(v1, rW, tb + 64u * 256u, O_HP, 16);
    }
    FENCE();
    TS_(9);
    PRIO(1);
#pragma unroll
    for (int j = 0; j < 5; ++j) xp[j] = pack2(xq[j][0], xq[j][1]);
    *(LAS u32x4*)(lds + ry + 8 * YROW) = u32x4{xp[0], xp[1], xp[2], xp[3]};
    *(LAS unsigned*)(lds + rz + 8 * YROW) = xp[4];
    FENCE();
    STAMP(10);
    {
      const f32x2 s2 = ((hh[0] + hh[1]) + (hh[2] + hh[3])) + hh[4];
      const f32x2 q2 = FMA2(hh[0], hh[0], hh[1] * hh[1]) + FMA2(hh[2], hh[2], FMA2(hh[3], hh[3], hh[4] * hh[4]));
      const float mean = wave_sum2(s2[0] + s2[1]) * (1.f / (float)D_), qs = wave_sum2(q2[0] + q2[1]);
      const float var = rbf(__builtin_fmaxf(__builtin_fmaf(-mean, mean, qs * (1.f / (float)D_)), 0.f));
      const float den = rbf(__builtin_amdgcn_sqrtf(rbf(var + 1e-5f)));
      const u32x2 st = {canon(__float_as_uint(m3)), __float_as_uint(den)};
      __builtin_amdgcn_raw_buffer_store_b64(st, rW, lane == 0 ? (unsigned)srow * 8u : OOB, O_ST3, 16);
    }
    {
      BARRIER();
      const unsigned tb = (unsigned)(s0 >> 4) * 20480u + (unsigned)(s0 & 15) * 16u + (unsigned)(tid >> 3) * 256u + (unsigned)(tid & 7) * 16u;
      const LAS char* pr = lds + L_XCH + (8 + (tid & 7)) * YROW + (tid >> 3) * 16;
      st128(*(const LAS u32x4*)(pr), rW, tb, O_XO, 16);
      if (wv >= 6) st128(*(const LAS u32x4*)(pr + 256), rW, tb + 16u * 256u, O_XO, 16);
      SIG(P_L1);
      if (bx == 0 && wv == 5) __builtin_amdgcn_raw_buffer_store_b64(u32x2{(unsigned)epn, (unsigned)(epn >> 32)}, rW, lane == 0 ? 0u : OOB, O_EP, 16);
    }
    FENCE();
    PRIO(0);
    FENCE();
  }

  float sthr = PTHR, scmx = CMAX;
  unsigned scsh = CSH;
  asm volatile("" : "+s"(sthr), "+s"(scsh), "+s"(scmx));
  unsigned xcw = (unsigned)(L_XCH + gm * 5120 + gk * 2048) + l16, xcr = (unsigned)(L_XCH + gm * 5120 + 2048 - gk * 2048) + l16, xcf = (unsigned)(L_CTL + gp * 4);
  asm volatile("" : "+v"(xcw), "+v"(xcr), "+v"(xcf));
  unsigned long long gkm;
  asm volatile("s_cmp_lg_u32 %1, 0\n\ts_cselect_b64 %0, -1, 0" : "=s"(gkm) : "s"(gk) : "scc");
  static_assert(KL >= 3 && KG <= 54 - 2 * KL, "the weights are read in front of the signal");
#define EXCHANGE(ph)                                                                        \
  do {                                                                                      \
    f32x4 e0, e1, e2;                                                                       \
    unsigned spins = 0;                                                                     \
    for (;;) {                                                                              \
      const unsigned fl = *(volatile LAS unsigned*)(lds + xcf);                                                           \
      e0 = *(volatile LAS f32x4*)(lds + xcr);                                               \
      e1 = *(volatile LAS f32x4*)(lds + xcr + 1024);                                        \
      e2 = *(volatile LAS f32x4*)(lds + xcr + 2048);                                        \
      if (__builtin_amdgcn_readfirstlane(fl) >= (ph)) break;                                \
      SPIN_STEP();                                                                          \
      if (spins > SPIN_CAP) break;                                                          \
    }                                                                                       \
    asm volatile("s_nop 3" : "+v"(acc[3]), "+v"(acc[4]));        \
    STG(12, e[i] = (i < 4 ? acc[3][i & 3] + e0[i & 3] : i < 8 ? acc[4][i & 3] + e1[i & 3] : acc[2][i & 3] + e2[i & 3]); PIN(e[i])); \
  } while (0)
#define KLOOP(CD, PB, BLK, VA, AOFF, XG, XS, ph)                                            \
  asm volatile("s_nop 1" : "+v"(A[0]));                                                     \
  sfor<0, 50>([&](auto M_) __attribute__((always_inline)) {                                 \
    constexpr int m = decltype(M_)::v, i = kl_i(m), j = kl_j(m), f = m / 4 + 1, c = m % 4;  \
    mf<i == 0 && (CD || !HFI || j < 2)>(acc[j], __builtin_bit_cast(u32x4, __builtin_shufflevector(Bh[2 * m], Bh[2 * m + 1], 0, 1, 2, 3, 4, 5, 6, 7)), A[i]); \
    SB();                                                                                   \
    constexpr int n0_ = m < KG ? (m * (100 - 2 * (PB))) / KG : 100, n1_ = m < KG ? ((m + 1) * (100 - 2 * (PB))) / KG : 100; \
    if constexpr (n1_ > n0_) RD_BH(2 * (PB) + n0_, BLK);                                    \
    if constexpr (n1_ > n0_ + 1) RD_BH(2 * (PB) + n0_ + 1, BLK);                            \
    static_assert(n1_ <= n0_ + 2 && (f > 9 || kl_i(m + 1) < f), "order");                   \
    if constexpr (f < 10) {                                                                 \
      if constexpr (c == 0) { m_ = CD ? amin2(A[f]) : __builtin_amdgcn_fmed3f(__builtin_fabsf(__uint_as_float(A[f][0])), __builtin_fabsf(__uint_as_float(A[f][2])), scmx); PIN(m_); } \
      if constexpr (c == 1) CMP_BAD(CD, bm, m_);                                            \
      if constexpr (c == 2 && CD) A[f][0] -= scsh;                                          \
      if constexpr (c == 3 && CD) { A[f][2] -= scsh; PIN(A[f]); }                           \
      if constexpr (c == 3) RETRY(CD, A[f], bm, VA, sa[f / 5], AOFF + (f % 5) * 2048, PHI); \
    }                                                                                       \
    if constexpr (m % 5 == 2 || m % 5 == 4) { XS(2 * (m / 5) + (m % 5 == 4)); }             \
    XG(m);                                                                                  \
                                \
    if constexpr (m == 51 - 2 * KL) { asm volatile("s_nop 3" : "+v"(acc[0])); *(volatile LAS f32x4*)(lds + xcw) = acc[0]; } \
    if constexpr (m == 52 - 2 * KL) *(volatile LAS f32x4*)(lds + xcw + 1024) = acc[1];      \
    if constexpr (m == 53 - 2 * KL) asm volatile("s_mov_b64 exec, %2\n\tds_write_b128 %0, %1 offset:2048\n\ts_mov_b64 exec, -1" :: "v"(xcw), "v"(acc[2]), "s"(gkm) : "memory"); \
    if constexpr (m == 54 - 2 * KL) SIG(ph);                                                \
    if constexpr (TSHI == 20 && m % 5 == 0) TX_((CD ? 30 : 44) + m / 5);                    \
    SB();                                                                                   \
  })
  {
    f32x4 acc[5];
    s16x4 Bh[100];
    u32x4 A[10];
    float m_, e[12];
    unsigned long long bm;
    unsigned sa[2] = {ROT ? (unsigned)(nt & 1) * 10240u : 0u, ROT ? 10240u - (unsigned)(nt & 1) * 10240u : 10240u};
    asm volatile("" : "+s"(sa[0]), "+s"(sa[1]));
    const unsigned va = (unsigned)(row >> 4) * 20480u + gk * 1024 + l16;
#define LD_A1(x) A[x] = __builtin_amdgcn_raw_buffer_load_b128(rW, va, sa[(x) / 5] + (O_HP + ((x) % 5) * 2048), 16)
    unsigned bb[3][2];
#pragma unroll
    for (int c = 0; c < 6; ++c) {
      bb[c >> 1][c & 1] = (unsigned)gk * 5120u + ((unsigned)lane >> 2) * 160u + ((unsigned)lane & 3u) * 8u + sa[c & 1] * 5u + (c < 2 ? 96u - 96u * gk : c < 4 ? 64u : 96u * gk);
      PIN(bb[c >> 1][c & 1]);
    }
#define RD_BH(h, BLK) Bh[h] = __builtin_amdgcn_ds_read_tr16_b64_v4i16((LAS s16x4*)(lds + bb[kl_j((h) / 2) < 2 ? 0 : kl_j((h) / 2) == 2 ? 1 : 2][kl_i((h) / 2) / 5] + (BLK) + (kl_i((h) / 2) % 5) * 10240 + (kl_j((h) / 2) % 3 == 1 ? 32 : 0) + ((h) & 1) * 2560))
#define BLK1 0
    FENCE();
#pragma unroll
    for (int n = 0; n < 2 * PBE; ++n) RD_BH(n, BLK1);
    FENCE();
    if (DLY1) __builtin_amdgcn_s_sleep(DLY1);
    PRIO(3);
    TS_(10);
#pragma unroll
    for (int i = 0; i < NA1; ++i) { LD_A1(i); SB(); }
    PRIO(1);
#pragma unroll
    for (int n = 2 * PBE; n < 2 * PB1; ++n) RD_BH(n, BLK1);
    FENCE();
#pragma unroll
    for (int i = NA1; i < NAF; ++i) { LD_A1(i); SB(); }
    u32x2 s3v;
    FENCE();
    if (kh) PRIO(PH1); else PRIO(PHI);
    STAMP(11);
    const int sh = (nt >> 1) == 0 ? 1 : (nt >> 1) == 1 ? -1 : (nt >> 1) == 2 ? 2 : -2;
    const int rr = row + sh, rw = rr & (S_ - 1);
    unsigned keep = rr == rw ? 0xffffffffu : 0u;
    unsigned oh1 = ofr - (unsigned)(row >> 4) * 20480u - (unsigned)l15 * 16u + (unsigned)(rw >> 4) * TS + (unsigned)(rw & 15) * 16u + O_H1;
    asm volatile("" : "+v"(keep), "+v"(oh1));
    asm volatile("" : "+s"(w3s[2]), "+s"(w3s[3]));
    if constexpr (TSHI == 21) { TX_(29); sfor<0, 10>([&](auto I_) __attribute__((always_inline)) { PIN(A[decltype(I_)::v]); TX_(30 + decltype(I_)::v); __builtin_amdgcn_s_sleep(1); }); }
    WAIT_ALL(P_L1);
    SB();
    CHECK_A(true, A[0], 0, NAF, LD_A1);
    SB();
#define XG1(m) do { if constexpr ((m) % 2 == 0 && NAF + (m) / 2 < 10) LD_A1(NAF + (m) / 2); if constexpr ((m) == 13) s3v = __builtin_amdgcn_raw_buffer_load_b64(rW, (unsigned)row * 8u, O_ST3, 16); if constexpr ((m) == 4) TS_(11); } while (0)
#define XSL(u, s0, n, f0) do { if constexpr ((u) >= (s0) && (((u) - (s0) + 1) * (n)) / (20 - (s0)) > (((u) - (s0)) * (n)) / (20 - (s0))) LD_W3U((f0) + (((u) - (s0)) * (n)) / (20 - (s0))); } while (0)
#define XS1(u) XSL(u, SL1, NL1, NWH)
    KLOOP(true, PB1, BLK1, va, O_HP, XG1, XS1, P_XF1);
    TS_(12);
    if (__builtin_expect(bad1(s3v[0]), 0)) {
      unsigned spins = 0;
      do {
        __builtin_amdgcn_s_sleep(RSLEEP);
        s3v = __builtin_amdgcn_raw_buffer_load_b64(rW, (unsigned)row * 8u, O_ST3, 16);
        SPIN_STEP();
      } while (bad1(s3v[0]) && spins < RETRY_CAP);
    }
    const float ld = __uint_as_float(s3v[1]), lr = __builtin_amdgcn_rcpf(ld);
    float gca = lr * GC1, gcb = lr * lr * lr * GC2;
    asm volatile("" : "+v"(gca), "+v"(gcb));
    EXCHANGE((unsigned)P_XF1);
    TS_(13);
    {
      unsigned pk[6];
      if (gk == 0) {
        gelu_s<12>(e, gca, gcb, ld, pk);
        __builtin_amdgcn_raw_buffer_store_b64(u32x2{pk[4] & keep, pk[5] & keep}, rW, oh1 + 1024, 0, 16);
      } else {
        gelu_s<8>(e, gca, gcb, ld, pk);
      }
      st128(widen(pk[0] & keep, pk[1] & keep, pk[2] & keep, pk[3] & keep), rW, oh1 + (unsigned)(g & 1) * 504u, 0, 16);
    }
    FENCE();
    TS_(14);
    PRIO(0);
    LD_W3R(NWH + NL1, NWH + NL1 + NP0);
    FENCE();
    WAIT_ALL(P_XF1);
#pragma unroll
    for (int i = 0; i < 10; ++i) *(LAS u32x4*)(lds + 20480 + wv * 5120 + (i / 5) * 40960 + (i % 5) * 1024 + l16) = WS2[i];
    SIG(P_G1);
    STAMP(13);
    FENCE();
    LD_W3R(NWH + NL1 + NP0, NWH + NL1 + NP1);
    FENCE();
    const unsigned va2 = (unsigned)(row >> 4) * TS + gk * 1024 + l16;
    u32x2 hpv[3];
#define LD_A2(x) A[x] = __builtin_amdgcn_raw_buffer_load_b128(rW, va2, sa[(x) / 5] + (O_H1 + ((x) % 5) * 2048), 16)
    u32x4 hp4;
    const unsigned ofw = ofr + (unsigned)(g & 1) * 504u;
    hp4 = __builtin_amdgcn_raw_buffer_load_b128(rW, ofw, O_HP, 16);
    hpv[2] = __builtin_amdgcn_raw_buffer_load_b64(rW, gk ? OOB : ofr + 1024, O_HP, 16);
    FENCE();
    WAIT_ALL(P_G1);
    if (DLY2) __builtin_amdgcn_s_sleep(DLY2);
    PRIO(3);
    TS_(15);
#pragma unroll
    for (int i = 0; i < NA1; ++i) { LD_A2(i); SB(); }
    PRIO(1);
#define BLK2 20480
#pragma unroll
    for (int n = 0; n < 2 * PB2; ++n) RD_BH(n, BLK2);
    FENCE();
#pragma unroll
    for (int i = NA1; i < NAF; ++i) { LD_A2(i); SB(); }
    FENCE();
    if (kh) PRIO(PH2); else PRIO(PHI);
    float hf[12];
    { const u32x4 n_ = widen(hp4[0], hp4[1], hp4[2], hp4[3]); hpv[0] = u32x2{n_[0], n_[1]}; hpv[1] = u32x2{n_[2], n_[3]}; }
#pragma unroll
    for (int j = 0; j < 3; ++j) { hpv[j][0] -= CSH; PIN(hpv[j]); }
    STG(12, hf[i] = ((i & 1) ? hi2f(hpv[i >> 2][(i >> 1) & 1]) : lo2f(hpv[i >> 2][(i >> 1) & 1])) + __uint_as_float(s3v[0]); PIN(hf[i]));
    acc[3] = f32x4{hf[0], hf[1], hf[2], hf[3]}; acc[4] = f32x4{hf[4], hf[5], hf[6], hf[7]};
    acc[2] = gk ? f32x4{0.f, 0.f, 0.f, 0.f} : f32x4{hf[8], hf[9], hf[10], hf[11]};
    PIN(acc[2]); PIN(acc[3]); PIN(acc[4]);
    STAMP(14);
    if constexpr (TSHI == 21) { TX_(43); sfor<0, 10>([&](auto I_) __attribute__((always_inline)) { PIN(A[decltype(I_)::v]); TX_(44 + decltype(I_)::v); __builtin_amdgcn_s_sleep(1); }); }
    SB();
    CHECK_A(false, A[0], 0, NAF, LD_A2);
    SB();
#define XG2(m) do { if constexpr ((m) % 2 == 0 && NAF + (m) / 2 < 10) LD_A2(NAF + (m) / 2); if constexpr ((m) == 4) TS_(16); } while (0)
#define XS2(u) XSL(u, SL2, NL2, NWH + NL1 + NP1)
    KLOOP(false, PB2, BLK2, va2, O_H1, XG2, XS2, P_XF2);
    TS_(17);
    EXCHANGE((unsigned)P_XF2);
    TS_(18);
    unsigned xk[6];
    {
      if (!HFI) STG(12, e[i] += hf[i]; PIN(e[i]));
      STG(3, e[4 * i + 1] = plainv(e[4 * i + 1]); PIN(e[4 * i + 1]));
      STG(6, xk[i] = pack2(e[2 * i], e[2 * i + 1]); PIN(xk[i]));
      st128(widen(xk[0], xk[1], xk[2], xk[3]), rW, ofw, O_X1, 16);
      if (gk == 0) __builtin_amdgcn_raw_buffer_store_b64(u32x2{xk[4], xk[5]}, rW, ofr + 1024, O_X1, 16);
    }
    FENCE();
    TS_(19);
    PRIO(1);
    SIG(P_G2);
    float v[12];
    float s = 0.f;
#pragma unroll
    for (int j = 0; j < 3; ++j) {
      v[4 * j] = lo2f(xk[2 * j]); v[4 * j + 1] = hi2f(xk[2 * j]); v[4 * j + 2] = lo2f(xk[2 * j + 1]); v[4 * j + 3] = hi2f(xk[2 * j + 1]);
      if (j < 2 || gk == 0) s += (v[4 * j] + v[4 * j + 1]) + (v[4 * j + 2] + v[4 * j + 3]);
    }
    s = swap_add32(swap_add16(s));
    const float mean = s * (gk ? (1.f / 32.f) : (1.f / 48.f));
    float m2 = 0.f;
#pragma unroll
    for (int i = 0; i < 12; ++i) {
      const float dd = v[i] - mean;
      if (i < 8 || gk == 0) m2 += dd * dd;
    }
    m2 = swap_add32(swap_add16(m2));
    {
      u32x2 st = {canon(__float_as_uint(mean)), __float_as_uint(m2)};
      __builtin_amdgcn_raw_buffer_store_b64(st, rW, g == 0 ? (unsigned)(((row >> 4) * 16 + nt * 2 + gk) * 16 + l15) * 8u : OOB, O_ST4, 16);
    }
    FENCE();
    PRIO(0);
    FENCE();
    STAMP(16);
  }
  {
    constexpr int NR3 = 40 - NWH - NL1 - NP1 - NL2;
#define NOW4D(...) __VA_ARGS__
#define LD_W4(f) W4[f] = __builtin_amdgcn_raw_buffer_load_b128(rW, l16, w4a + (f) * 1024, 16)
    const unsigned va4 = O_H + (unsigned)(m0 >> 4) * TS + (unsigned)k4 * 1024u;
#define A4OFF(x) (((x) / NS) * TS + ((x) % NS) * 1024)
#define LD_A4(x) A4[(x) % AW4] = __builtin_amdgcn_raw_buffer_load_b128(rW, l16, va4 + A4OFF(x), 16)
    const int lane0 = lane;
#define LANES() int lane = lane0; PIN(lane); const int l15 = lane & 15, g = lane >> 4; const unsigned l16 = (unsigned)lane * 16u; \
    const unsigned fa = (unsigned)L_CTL + ((unsigned)lane & 7u) * 4u
#define FLG(a) *(volatile LAS unsigned*)(lds + (a))
    auto g3 = [&](auto KH_, u32x4 (&W4)[35], u32x4 (&A4)[AW4], u32x2 (&x1v)[3]) __attribute__((always_inline)) {
      constexpr int KH = decltype(KH_)::v, NS = 7, NW0 = KH ? 6 : AW4;
      LANES();
      const unsigned hw0 = (unsigned)(m0 >> 4) * TS + (unsigned)l15 * 16u + (unsigned)(g >> 1) * 256u + O_H;
      const unsigned csp = (unsigned)(L_CS3 + wv * 128 + g * 16), f1 = (unsigned)L_CTL + 16u + ((unsigned)lane & 3u) * 4u;
      u32x2 stv[4];
#define LD_ST() _Pragma("unroll") for (int a = 0; a < 4; ++a) stv[a] = __builtin_amdgcn_raw_buffer_load_b64(rW, (unsigned)((((m0 >> 4) + mg) * 16 + 4 * g) * 16 + l15) * 8u, O_ST4 + a * 128, 16)
#define ST_BAD() bad1(max(max(stv[0][0], stv[1][0]), max(stv[2][0], stv[3][0])))
      const u32x4 ones = {0x3f803f80u, 0x3f803f80u, 0x3f803f80u, 0x3f803f80u};
      f32x4 c3a = {0.f, 0.f, 0.f, 0.f}, c3b = {0.f, 0.f, 0.f, 0.f};
#define CS3R(a, b) do { _Pragma("unroll") for (int f_ = (a); f_ < (b); f_ += 2) { c3a = mfma16(W3[f_], ones, c3a); if (!KH || f_ < 20) c3b = mfma16(W3[f_ + 1], ones, c3b); } } while (0)
      TS_(20);
      PRIO(PHI);
      LD_W3R(40 - NR3, 40);
      FENCE();
      const unsigned so = O_X1 + (unsigned)mt * 81920u + (unsigned)wv * 10240u, d0 = (unsigned)(KH ? L_XB : L_XA) + (unsigned)mg * 10240u;
      CS3R(0, KH ? KC3B : KC3);
      asm volatile("" : "+v"(c3a), "+v"(c3b));
      FENCE();
      TS_(21);
      WAIT_2(KH ? P_XF2 : P_G2, KH ? P_G2 : P_XF2);
      if (KH ? DLY3B : DLY3) __builtin_amdgcn_s_sleep(KH ? DLY3B : DLY3);
      PIN(W3[38]); PIN(W3[39]);
      if (KH) {
#pragma unroll
        for (int f = 0; f < 10; ++f) W4[f] = W3[21 + 2 * f];
      }
      FENCE();
      PRIO(3);
      TS_(22);
#define STG_DMA() do { dma<4, 1>(l16, dW, so, d0); dma<4, 1>(l16, dW, so + 4096, d0 + 4096); dma<1, 1>(l16, dW, so + 8192, d0 + 8192); dma<1, 1>(l16, dW, so + 9216, d0 + 9216); } while (0)
      if (KH) {
        asm volatile("buffer_load_dwordx2 %0, %4, %5, %6 offen sc1\n\tbuffer_load_dwordx2 %1, %4, %5, %6 offen offset:128 sc1\n\t"
                     "buffer_load_dwordx2 %2, %4, %5, %6 offen offset:256 sc1\n\tbuffer_load_dwordx2 %3, %4, %5, %6 offen offset:384 sc1"
                     : "=&v"(stv[0]), "=&v"(stv[1]), "=&v"(stv[2]), "=&v"(stv[3]) : "v"((unsigned)((((m0 >> 4) + mg) * 16 + 4 * g) * 16 + l15) * 8u), "s"(dW), "s"(O_ST4) : "memory");
      }
      STG_DMA();
      FENCE();
      TS_(23);
      PRIO(PHI);
      CS3R(KH ? KC3B : KC3, 40);
      if (l15 == 0) { *(LAS f32x4*)(lds + csp) = c3a; *(LAS f32x4*)(lds + csp + 64) = c3b; }
      FENCE();
      if (KH) {
        asm volatile("s_waitcnt vmcnt(10)" : "+v"(stv[0]), "+v"(stv[1]), "+v"(stv[2]), "+v"(stv[3]));
        if (__builtin_expect(ST_BAD(), 0)) {
          unsigned spins = 0;
          do {
            __builtin_amdgcn_s_sleep(RSLEEP);
            LD_ST();
            SPIN_STEP();
          } while (ST_BAD() && spins < RETRY_CAP);
        }
        const float m0_ = __uint_as_float(stv[0][0]), q0 = __uint_as_float(stv[0][1]), m1 = __uint_as_float(stv[1][0]), q1 = __uint_as_float(stv[1][1]);
        const float m2_ = __uint_as_float(stv[2][0]), q2 = __uint_as_float(stv[2][1]), m3 = __uint_as_float(stv[3][0]), q3 = __uint_as_float(stv[3][1]);
        float sm = 48.f * (m0_ + m2_) + 32.f * (m1 + m3);
        sm = swap_add32(swap_add16(sm));
        const float mean = sm * (1.f / (float)D_);
        const float d0_ = m0_ - mean, d1 = m1 - mean, d2 = m2_ - mean, d3 = m3 - mean;
        float qs = (q0 + q1) + (q2 + q3) + 48.f * (d0_ * d0_ + d2 * d2) + 32.f * (d1 * d1 + d3 * d3);
        qs = swap_add32(swap_add16(qs));
        const float var = rbf(qs * (1.f / (float)D_));
        const float den = rbf(__builtin_amdgcn_sqrtf(rbf(var + 1e-5f))), r = __builtin_amdgcn_rcpf(den);
        float rc1 = r * GC1;
        PIN(rc1);
        if (g == 0) *(volatile LAS f32x4*)(lds + L_ST4 + (mg * 16 + l15) * 16) = f32x4{rbf(mean), rc1, r * r * r * GC2, den};
        SIG(P_SF);
      }
      FENCE();
      TS_(24);
      VMW(0);
      TS_(25);
      {
        unsigned spins = 0;
        for (;;) {
          u32x4 a[10];
          float mx = 0.f;
#pragma unroll
          for (int i = 0; i < 10; ++i) a[i] = *(volatile LAS u32x4*)(lds + d0 + i * 1024 + l16);
#pragma unroll
          for (int i = 0; i < 10; ++i) mx = amax3(mx, a[i]);
          if (__builtin_expect(__builtin_amdgcn_ballot_w64(mx >= PTHR) == 0ull, 1)) break;
          SPIN_STEP();
          if (spins > RETRY_CAP) break;
          PRIO(0);
          __builtin_amdgcn_s_sleep(RSLEEP);
          STG_DMA();
          PRIO(PHI);
          asm volatile("s_waitcnt vmcnt(0)" ::: "memory");
        }
        SIG(P_SA);
      }
      TS_(26);
      STAMP(17);
      FENCE();
      constexpr int NP = KH ? NPH : NPK, F0 = KH ? 10 : 0, NEQ = NP + 6, EQ0 = KH ? EC0 : EA0, EQ1 = KH ? EC1 : EA1, NL0 = F0 + NEQ, NLT = (KH ? 30 : 35) - NL0;
      const unsigned pks = (unsigned)(KH ? L_PB + NPK * 1024 + mg * (NPH * 1024) : wv < 3 ? wv * (NPK * 1024) : L_PB);
      constexpr auto ecnt = [](int q) { return q <= EQ0 ? 0 : q >= EQ1 ? NEQ : ((q - EQ0) * NEQ + EQ1 - EQ0 - 1) / (EQ1 - EQ0); };
#define ESLOT(q) sfor<ecnt(q), ecnt((q) + 1)>([&](auto U_) __attribute__((always_inline)) { constexpr int u = decltype(U_)::v; \
        if constexpr (u < NP) { NOW4D(dma<1, 1>(l16, dW, w4a + (F0 + 6 + u) * 1024, pks + u * 1024)); } else LD_W4(F0 + u - NP); })
      static_assert(3 * NPK <= 32 && NPK + 4 * NPH <= 36 && MV0 + NP <= 20 && NLT <= 19 && NLT >= MV0, "w4");
      sfor<0, 2>([&](auto P_) __attribute__((always_inline)) {
        constexpr int p = decltype(P_)::v, NV = KH ? 12 : 16;
        TS_(28 + 8 * p + 0);
        if (p == 0) WAIT_2(P_SA, P_G2); else WAIT_HALF(1, P_SA);
        f32x4 acc[4];
        PRIO(KH ? (p ? PK1 : PK2) : PK0);
        TS_(28 + 8 * p + 1);
        u32x4 Ar[AD3][2];
        unsigned ab[2] = {l16 + (unsigned)((p ? L_XB : L_XA) + (KH ? hb * 30720 : 0)), l16 + (unsigned)((p ? L_XB : L_XA) + 20480 - (KH ? hb * 10240 : 0))};
        const unsigned sp = (unsigned)(L_ST4 + p * 512 + l15 * 16), fp = (unsigned)(L_CTL + (wv ^ 2) * 4), xs = (unsigned)(32768 + p * 4096) + l16;
        PIN(ab[0]);
        if (KH) PIN(ab[1]);
        f32x4 cs[3], st[2], rx;
        unsigned sf_, sx_;
#define RD_A3(s, h) Ar[(s) % AD3][h] = *(const LAS u32x4*)(lds + (KH ? ab[h] : ab[0] + (h) * 20480) + (s) * 1024)
#pragma unroll
        for (int s = 0; s < AD3 - 1; ++s) { RD_A3(s, 0); RD_A3(s, 1); }
        SB();
        sfor<0, 20>([&](auto S_) __attribute__((always_inline)) {
          constexpr int s = decltype(S_)::v;
          constexpr bool rd = s + AD3 - 1 < 20, T2 = !KH || s < 10;
          mf<s == 0>(acc[0], W3[2 * s], Ar[s % AD3][0]);
          SB();
          if constexpr (rd) RD_A3(s + AD3 - 1, 0);
          else if constexpr (s == 18) cs[0] = *(const LAS f32x4*)(lds + csp);
          else st[0] = *(const volatile LAS f32x4*)(lds + sp + (KH ? hb * 256 : 0));
          SB();
          mf<s == 0>(acc[1], W3[2 * s], Ar[s % AD3][1]);
          SB();
          if constexpr (rd) RD_A3(s + AD3 - 1, 1);
          else if constexpr (s == 18) cs[1] = *(const LAS f32x4*)(lds + csp + 64);
          else st[1] = *(const volatile LAS f32x4*)(lds + sp + 256 - (KH ? hb * 256 : 0));
          SB();
          if constexpr (T2) { mf<s == 0>(acc[2], W3[2 * s + 1], Ar[s % AD3][0]); SB(); }
          if constexpr (p == 0 && s == 15) sf_ = FLG(f1);
          if constexpr (p == 0 && s == 18) NEED_AT(sf_, f1, P_SF);
          if constexpr (p == 0) ESLOT(s);
          if constexpr (p == 1 && s >= 1 && s <= NLT) LD_W4(NL0 + s - 1);
          if constexpr (p == 1 && s == MV0 - 1) VMW(MV0 + 1);
          SB();
          if constexpr (T2) { mf<s == 0>(acc[3], W3[2 * s + 1], Ar[s % AD3][1]); SB(); }
          if constexpr (KH && s + AD3 == 10) { ab[0] -= hb * 20480; ab[1] -= hb * 20480; PIN(ab[0]); PIN(ab[1]); }
          if constexpr (KH && s == 12) { *(volatile LAS f32x4*)(lds + xs + mg * 1024) = acc[3]; SIG(p ? P_S1 : P_S0); }
          if constexpr (KH && s == 14) sx_ = FLG(fp);
          if constexpr (KH && s == 17) { NEED_AT(sx_, fp, p ? P_S1 : P_S0); rx = *(const volatile LAS f32x4*)(lds + xs + (mg ^ 2) * 1024); cs[2] = *(const volatile LAS f32x4*)(lds + csp + (hb ? -192 : 320)); }
          if constexpr (p == 1 && s >= MV0 && s < MV0 + NP) W4[F0 + 6 + s - MV0] = *(const LAS u32x4*)(lds + pks + l16 + (s - MV0) * 1024);
          if constexpr (p == 1 && s >= 1 && s < 4 - KH) {
            static_assert(H0S >= 4 && H0S < 12, "x1");
            const int tu = KH == 0 ? (s == 3 ? 3 : s - 1) : wv < 6 ? 2 : s == 1 ? wv & 1 : wv - 4, ju = KH == 0 ? wv : wv < 6 ? 2 * wv - 6 - s : 4;
            x1v[s - 1] = *(const LAS u32x2*)(lds + (tu < 2 ? L_XA : L_XB - 40960) + tu * 20480 + (nt * 10 + 2 * ju) * 256 + (g >> 1) * 256 + (g & 1) * 8 + l15 * 16);
          }
          if constexpr (p == 1 && s == H0S) SIG(P_H0);
          if constexpr (s == 9) TS_(28 + 8 * p + 2);
          SB();
        });
        TS_(28 + 8 * p + 3);
        PRIO(KH && p ? PE1 : PE0);
        SIG(p ? P_KB : P_KA);
        if constexpr (KH) asm volatile("s_nop 3" : "+v"(acc[0]), "+v"(acc[1]), "+v"(acc[2])); else asm volatile("s_nop 3" : "+v"(acc[0]), "+v"(acc[1]), "+v"(acc[2]), "+v"(acc[3]));
        STAMP(18 + 2 * p);
        {
          float e[NV], x[NV];
          unsigned pk[NV / 2];
#define SQ(i) st[((i) >> 2) & 1]
#define EPS(q) do { if constexpr (p == 0) { ESLOT(20 + (q)); } if constexpr (p == 1 && (q) == HG - 1) sf_ = FLG(fa); \
                    if constexpr (p == 1 && (q) == HG) { NEED_AT(sf_, fa, P_H0); _Pragma("unroll") for (int x = 0; x < NW0; ++x) { LD_A4(x); } } SB(); } while (0)
          if constexpr (KH) STG(4, x[i] = __builtin_fmaf(-st[0][0], cs[2][i], rx[i]); PIN(x[i]));
          STG(NV, e[i] = __builtin_fmaf(-SQ(i)[0], cs[i >> 3][i & 3], acc[i >> 2][i & 3]); PIN(e[i]));
          if constexpr (KH) STG(4, e[8 + i] += x[i]; PIN(e[8 + i]));
          TS_(28 + 8 * p + 4);
          EPS(0);
          STG(NV, x[i] = e[i] * e[i]; PIN(x[i]));
          EPS(1);
          STG(NV, x[i] = __builtin_fmaf(x[i], SQ(i)[2], SQ(i)[1]); PIN(x[i]));
          EPS(2);
          STG(NV, x[i] *= e[i]; PIN(x[i]));
          EPS(3);
          STG(NV, x[i] = __builtin_amdgcn_exp2f(x[i]); PIN(x[i]));
          TS_(28 + 8 * p + 5);
          EPS(4);
          STG(NV, x[i] = __builtin_fmaf(x[i], SQ(i)[3], SQ(i)[3]); PIN(x[i]));
          EPS(5);
          STG(NV, x[i] = __builtin_amdgcn_rcpf(x[i]); PIN(x[i]));
          EPS(6);
          STG(NV, e[i] *= x[i]; PIN(e[i]));
          TS_(28 + 8 * p + 6);
          EPS(7);
          STG(NV / 4, e[4 * i + 1] = codedv(e[4 * i + 1]); PIN(e[4 * i + 1]));
          STG(NV / 2, pk[i] = pack2(e[2 * i], e[2 * i + 1]); PIN(pk[i]));
          const unsigned hw1 = hw0 + (unsigned)((g & 1) ^ (KH ? hb : 0)) * TS;
          st128(widen(pk[0], pk[1], pk[2], pk[3]), rW, hw1, (unsigned)(sb3 + jl) * 512u + 2 * p * TS, 16);
          if constexpr (KH) __builtin_amdgcn_raw_buffer_store_b64(u32x2{pk[4], pk[5]}, rW, hw0 + (unsigned)(g & 1) * 8u, two ? (unsigned)(sb3 + jm) * 512u + (unsigned)(2 * p + hb) * TS : OOB, 16);
          else st128(widen(pk[4], pk[5], pk[6], pk[7]), rW, hw1, (unsigned)(sb3 + jm) * 512u + 2 * p * TS, 16);
          FENCE();
          EPS(8);
        }
        TS_(28 + 8 * p + 7);
        STAMP(19 + 2 * p);
      });
    };
#define PB(t) (81920 - (t) * 40960)
    const __amdgpu_buffer_rsrc_t rO = __builtin_amdgcn_make_buffer_rsrc((void*)out, 0, SD2, RS3);
    auto g4 = [&](auto KD_, u32x4 (&W4)[35], u32x4 (&A4)[AW4], u32x2 (&x1v)[3]) __attribute__((always_inline)) {
      constexpr int KD = decltype(KD_)::v, NS = KD == 1 ? 6 : 7, PR = KD ? P41 : P40;
      constexpr int GS = NS + (KD == 0 ? GSA : KD == 1 ? GSB : GSC);
      constexpr auto nreq = [](int x) { int n = AW4; for (int y = 0; y <= x; ++y) { int m = y + AW4 < 4 * NS ? y + AW4 : 4 * NS; if (y < GS && m > 2 * NS) m = 2 * NS; n = m < n + 3 ? (m > n ? m : n) : n + 3; } return n; };
      LANES();
      TS_(44);
      if constexpr (KD) {
#pragma unroll
        for (int x = 6; x < AW4; ++x) { LD_A4(x); }
      }
      if constexpr (KD == 2) {
#pragma unroll
        for (int f = 30; f < 35; ++f) { LD_W4(f); }
      }
      FENCE();
      PRIO(PR);
      u32x2 xv[2];
      unsigned fl_, fh_, fu_, pu_;
      f32x4 acc[2][5], rs[4], gq;
      float u_[4], y_[4], m4_;
      unsigned long long bm4;
      unsigned pw = (unsigned)(wv * 5120) + l16;
      PIN(pw);
      const unsigned fo0 = (unsigned)((m0 >> 4) * 20480 + ((n0 + 4 * g) >> 3) * 256 + (g & 1) * 8 + l15 * 16), oo0 = (unsigned)((m0 + l15) * D_ + n0 + 4 * g) * 2u;
#define U_LD(r, t, j) xv[r] = __builtin_amdgcn_raw_buffer_load_b64(rW, fo0, O_XO + (unsigned)((t) * 20480 + (j) * 512), 16)
#define U_PC(q, t, j, y)                                                                               \
  do {                                                                                                  \
    if constexpr ((q) == 0) { fu_ = FLG(fa); pu_ = (unsigned)(PB(t) + (j) * 1024) + l16; PIN(pu_); }    \
    if constexpr ((q) == 1) { gq = *(const LAS f32x4*)(lds + L_GB + ((j) * 16 + 4 * g) * 4); }          \
    if constexpr ((q) >= 1 && (q) < 5) { constexpr int i = (q) - 1; u_[i] = (i & 1) ? hi2f(x1v[y][i >> 1]) : lo2f(x1v[y][i >> 1]); PIN(u_[i]); } \
    if constexpr ((q) == 5) NEED_AT(fu_, fa, P_P0 + 1 + (t));                                           \
    if constexpr ((q) >= 6 && (q) < 10) rs[(q) - 6] = *(const volatile LAS f32x4*)(lds + pu_ + ((q) - 6) * 5120); \
    if constexpr ((q) >= 11 && (q) < 27) { constexpr int w = ((q) - 11) >> 1, h = (((q) - 11) & 1) * 2;  \
      u_[h] += rs[w & 3][h]; PIN(u_[h]); u_[h + 1] += rs[w & 3][h + 1]; PIN(u_[h + 1]);                 \
      if constexpr (h == 2 && w < 4) rs[w] = *(const volatile LAS f32x4*)(lds + pu_ + (4 + w) * 5120); } \
    if constexpr ((q) >= 6 && (q) < 10) { constexpr int i = (q) - 6; y_[i] = (i & 1) ? hi2f(xv[0][i >> 1]) : lo2f(xv[0][i >> 1]); PIN(y_[i]); } \
    if constexpr ((q) == 27 || (q) == 28) { constexpr int h = ((q) - 27) * 2; u_[h] = __builtin_fmaf(u_[h], gq[h], y_[h]); PIN(u_[h]); u_[h + 1] = __builtin_fmaf(u_[h + 1], gq[h + 1], y_[h + 1]); PIN(u_[h + 1]); } \
    if constexpr ((q) == 29) __builtin_amdgcn_raw_buffer_store_b64(u32x2{pack2(u_[0], u_[1]), pack2(u_[2], u_[3])}, rO, oo0, (unsigned)((t) * (16 * D_ * 2) + (j) * 32), OAX); \
  } while (0)
      {
        unsigned long long b0;
        const float m0_ = amin2(A4[0]);
        CMP_BAD(1, b0, m0_);
        A4[0][0] -= scsh; A4[0][2] -= scsh;
        RETRY_C(A4[0], b0, l16, va4, 0, PR);
        asm volatile("s_nop 0" : "+v"(A4[0]));
      }
      SB();
      const int tc = KD == 0 ? 3 : KD == 1 ? 2 : wv - 4, jc = KD == 0 ? wv : KD == 1 ? 2 * wv - 8 : 4;
      sfor<0, 20 * NS>([&](auto M_) __attribute__((always_inline)) {
        constexpr int m = decltype(M_)::v, x = m / 5, j = m % 5, t = x / NS, s = x % NS, r0 = nreq(x - 2), r1 = nreq(x - 1);
        constexpr bool nx = x + 1 < 4 * NS;
        u32x4& an = A4[(x + 1) % AW4];
        mf<s == 0>(acc[t & 1][j], W4[5 * s + j], A4[x % AW4]);
        SB();
        if constexpr (j == 0) {
          if constexpr (x == GS + 1) NEED_AT(fh_, fa, P_H1);
          if constexpr (r0 < r1) { LD_A4(r0); }
        }
        if constexpr (j == 1 && nx) { m4_ = amin2(an); PIN(m4_); }
        if constexpr (j == 2 && nx) { CMP_BAD(1, bm4, m4_); an[0] -= scsh; }
        if constexpr (j == 3 && nx) { an[2] -= scsh; PIN(an); }
        if constexpr (j == 3 && r0 + 1 < r1) { LD_A4(r0 + 1); }
        if constexpr (j == 4) {
          if constexpr (nx) RETRY_C(an, bm4, l16, va4, A4OFF(x + 1), PR);
          if constexpr (r0 + 2 < r1) { LD_A4(r0 + 2); }
          if constexpr (x == GS) fh_ = FLG(fa);
          if constexpr (x == H1S) SIG(P_H1);
          if constexpr (s == 0) TS_(45 + 4 * t);
          if constexpr (s == 3) TS_(45 + 4 * t + 1);
          if constexpr (s == NS - 1) TS_(45 + 4 * t + 2);
          static_assert(GS + 1 < 2 * NS, "order");
          if constexpr (KD == 0 && t == 1 && s == 0) U_LD(0, 0, wv);
          if constexpr (KD == 0 && t == 2 && s == 3) U_LD(0, 1, wv);
          if constexpr (KD == 2 && t == 2 && s == 3) U_LD(0, wv & 1, 4);
          if constexpr (KD == 1 && t == 3 && s == 3) U_LD(0, 2, jc + 1);
          if constexpr (t == 2 && s == 5) U_LD(1, tc, jc);
          if constexpr (t == 3 && s == NS - 2) fl_ = FLG(L_CTL);
        }
        constexpr bool un = (KD == 0 && t >= 2) || (KD == 2 && t == 3);
        constexpr int wq = 5 * s + j, w0 = un ? W0U : 4;
        if constexpr (t > 0 && wq >= w0 && wq < w0 + 5 * WSP && (wq - w0) % WSP == 0) *(volatile LAS f32x4*)(lds + PB(t - 1) + pw + ((wq - w0) / WSP) * 1024) = acc[(t - 1) & 1][(wq - w0) / WSP];
        if constexpr (t > 0 && wq == w0 + 1 + 4 * WSP) SIG(P_P0 + t);
        static_assert(W0U + 1 + 4 * WSP < 35 && U0 + 30 <= 35, "unit");
        if constexpr (un && KD == 0 && wq >= U0) U_PC(wq - U0, t - 2, wv, t - 2);
        if constexpr (un && KD == 2 && wq >= U0) U_PC(wq - U0, wv & 1, 4, 0);
        SB();
      });
      asm volatile("s_nop 3" : "+v"(acc[1][0]), "+v"(acc[1][1]), "+v"(acc[1][2]), "+v"(acc[1][3]), "+v"(acc[1][4]));
#pragma unroll
      for (int j = 0; j < 4; ++j) *(volatile LAS f32x4*)(lds + L_PB + wv * 4096 + l16 + j * 1024) = acc[1][j];
      NEED_AT(fl_, (unsigned)L_CTL, P_P0 + 3);
      *(volatile LAS f32x4*)(lds + PB(0) + pw) = acc[1][4];
      SIG(P_P0 + 4);
      TS_(60);
#define UNIT(r, y, t, j, base, str)                                                                        \
  do {                                                                                                  \
    unsigned pq = (unsigned)(base) + l16, oq = oo0 + (unsigned)((t) * (16 * D_ * 2) + (j) * 32);        \
    f32x4 gq_ = *(const LAS f32x4*)(lds + L_GB + ((j) * 16 + 4 * g) * 4);                               \
    f32x2 xa = up2(xv[r][0]), xb = up2(xv[r][1]), ya = up2(x1v[y][0]), yb = up2(x1v[y][1]);             \
    asm volatile("" : "+v"(xa), "+v"(xb), "+v"(ya), "+v"(yb), "+v"(gq_), "+v"(pq), "+v"(oq));           \
    f32x4 r8[8];                                                                                        \
    unsigned spins = 0;                                                                                 \
    for (;;) {                                                         \
      const unsigned fl = FLG(fa);                                                                      \
      _Pragma("unroll") for (int w = 0; w < 8; ++w) r8[w] = *(const volatile LAS f32x4*)(lds + pq + w * (str)); \
      if (__builtin_amdgcn_ballot_w64(fl < (unsigned)(P_P0 + 1 + (t))) == 0ull) break;                  \
      SPIN_STEP();                                                                                      \
      if (spins > SPIN_CAP) break;                                                                      \
    }                                                                                                   \
    const f32x4 sm = ((r8[0] + r8[1]) + (r8[2] + r8[3])) + ((r8[4] + r8[5]) + (r8[6] + r8[7]));         \
    const f32x2 a_ = FMA2(f32x2{sm[0], sm[1]} + ya, (f32x2{gq_[0], gq_[1]}), xa), b_ = FMA2(f32x2{sm[2], sm[3]} + yb, (f32x2{gq_[2], gq_[3]}), xb); \
    __builtin_amdgcn_raw_buffer_store_b64(u32x2{pack2(a_[0], a_[1]), pack2(b_[0], b_[1])}, rO, oq, 0, OAX); \
    FENCE();                                                                                            \
  } while (0)
      if constexpr (KD == 0) UNIT(1, 2, 3, wv, L_PB + wv * 1024, 4096);
      else UNIT(1, 1, tc, jc, tc == 3 ? PB(0) : PB(2) + jc * 1024, 5120);
      if constexpr (KD == 1) UNIT(0, 0, 2, jc + 1, PB(2) + jc * 1024 + 1024, 5120);
    };
#define KILL(V, N) _Pragma("unroll") for (int f_ = 0; f_ < (N); ++f_) asm volatile("" : "=v"(V[f_]))
    int arm = wv >> 1;
    if (arm < 2) {
      u32x4 W4[35], A4[AW4];
      u32x2 x1v[3];
      g3(IC<0>{}, W4, A4, x1v);
      g4(IC<0>{}, W4, A4, x1v);
      KILL(W3, 40 - NR3);
    }
    asm volatile("" : "+s"(arm));
    if (arm >= 2) {
      u32x4 W4[35], A4[AW4];
      u32x2 x1v[3];
      g3(IC<1>{}, W4, A4, x1v);
      if (arm == 3) {
        g4(IC<2>{}, W4, A4, x1v);
        KILL(W4, 30);
        KILL(A4, 6);
      }
      asm volatile("" : "+s"(arm));
      if (arm == 2) g4(IC<1>{}, W4, A4, x1v);
    }
    STAMP(27);
    CLK(31);
  }
}

extern "C" void kernel_launch(void* const* d_in, const int* in_sizes, int n_in, void* d_out, int out_size,
                              void* d_ws, size_t ws_size, hipStream_t stream) {
  const bf16* A_q = (const bf16*)d_in[0];
  const bf16* A_k = (const bf16*)d_in[1];
  const bf16* A_v = (const bf16*)d_in[2];
  const bf16* A_shift_k = (const bf16*)d_in[3];
  const bf16* A_ssm_k = (const bf16*)d_in[4];
  const bf16* A_ssm_D = (const bf16*)d_in[5];
  const bf16* w1 = (const bf16*)d_in[7];
  const bf16* w2 = (const bf16*)d_in[8];
  const bf16* w3 = (const bf16*)d_in[9];
  const bf16* w4 = (const bf16*)d_in[10];
  const bf16* c = (const bf16*)d_in[11];
  const bf16* w_mod = (const bf16*)d_in[12];
  char* ws = (char*)d_ws;
  (void)hipFuncSetAttribute((const void*)k_main, hipFuncAttributeMaxDynamicSharedMemorySize, L_TOT);
  k_main<<<256, 512, L_TOT, stream>>>(A_k, A_v, ws, A_shift_k, A_ssm_k, w_mod, c, w3, w4, A_q, w1, w2, A_ssm_D, (bf16*)d_out);
}
```

```cpp
#define GK 0
#define GW(w, b) ((b) < 8)
#include <hip/hip_runtime.h>
#include <hip/hip_bf16.h>
#include <stdint.h>

typedef __hip_bfloat16 bf16;
typedef __attribute__((ext_vector_type(8))) short bf16x8;
typedef __attribute__((ext_vector_type(4))) short s16x4;
typedef __attribute__((ext_vector_type(4))) float f32x4;
typedef __attribute__((ext_vector_type(2))) float f32x2;
typedef __attribute__((ext_vector_type(4))) unsigned u32x4;
typedef __attribute__((ext_vector_type(2))) unsigned u32x2;
#define LAS __attribute__((address_space(3)))

static constexpr int S_ = 2048;
static constexpr int D_ = 640;
static constexpr int F_ = 1728;
static constexpr unsigned SENT = 0xffffffffu;
static constexpr int RS3 = 0x00020000;
static constexpr unsigned OOB = 0x40000000u;

static constexpr unsigned SD2 = S_ * D_ * 2;
static constexpr unsigned DF2 = D_ * F_ * 2;
static constexpr unsigned O_WT3 = 0, O_WT4 = DF2;
static constexpr unsigned O_EP = 2 * DF2;
static constexpr unsigned O_MOD = O_EP + 256;
static constexpr unsigned O_MP = O_MOD + 16384;
static constexpr unsigned O_FL1 = O_MP + 131072;
static constexpr unsigned O_KVT = O_FL1 + 2048;
static constexpr unsigned O_HP = O_KVT, O_X1 = O_KVT;
static constexpr unsigned TS = 57344;
static constexpr unsigned O_H = O_KVT + 384 * 8192;
static constexpr unsigned O_Z = O_H, O_H1 = O_H + 20480;
static constexpr unsigned O_ST3 = O_H + 128 * TS;
static constexpr unsigned O_ST4 = O_ST3 + S_ * 8;
static constexpr unsigned O_XO = O_ST4 + S_ * 16 * 8;
static constexpr unsigned WS_USED = O_XO + SD2;
#define KVROW(c) ((c) < 384u ? O_KVT + (c) * 8192u : O_H + 40960u + (((c) - 384u) >> 1) * TS + ((c) & 1u) * 8192u)
static constexpr unsigned CODED = 0x7f7f7f7fu;
static constexpr unsigned CSH = 98u << 23;
static constexpr float PTHR = 0x1p48f, PMAX = 0x1p47f, CMUL = 0x1p98f, CMAX = 0x1p127f;
__device__ __forceinline__ float plainv(float x) { return __builtin_amdgcn_fmed3f(x, -PMAX, PMAX); }
__device__ __forceinline__ float codedv(float x) { return __builtin_copysignf(__builtin_amdgcn_fmed3f(__builtin_fabsf(x * CMUL), PTHR, CMAX), x); }

__device__ __forceinline__ float rbf(float x) { return __bfloat162float(__float2bfloat16(x)); }
__device__ __forceinline__ float lo2f(unsigned w) { return __uint_as_float(w << 16); }
__device__ __forceinline__ float hi2f(unsigned w) { return __uint_as_float(w & 0xffff0000u); }
typedef __bf16 bf16v2 __attribute__((ext_vector_type(2)));
__device__ __forceinline__ unsigned pack2(float a, float b) {
  bf16v2 r = __builtin_convertvector(f32x2{a, b}, bf16v2);
  return __builtin_bit_cast(unsigned, r);
}

static constexpr float GC1 = -2.f * 0.7978845608028654f * 1.4426950408889634f;
static constexpr float GC2 = GC1 * 0.044715f;
__device__ __forceinline__ float row16_sum(float v) {
  v += __builtin_bit_cast(float, __builtin_amdgcn_update_dpp(0, __builtin_bit_cast(int, v), 0xB1, 0xf, 0xf, true));
  v += __builtin_bit_cast(float, __builtin_amdgcn_update_dpp(0, __builtin_bit_cast(int, v), 0x4E, 0xf, 0xf, true));
  v += __builtin_bit_cast(float, __builtin_amdgcn_update_dpp(0, __builtin_bit_cast(int, v), 0x141, 0xf, 0xf, true));
  v += __builtin_bit_cast(float, __builtin_amdgcn_update_dpp(0, __builtin_bit_cast(int, v), 0x140, 0xf, 0xf, true));
  return v;
}
typedef volatile LAS unsigned* ldsw_t;
static constexpr int CV_STR = 4160;
static constexpr int CV_U = 8 * CV_STR;
static constexpr int CV_SLOT = CV_U + 4608;
static constexpr int SL_A = 46592, SL_C = SL_A + CV_SLOT, SL_B = SL_C + CV_SLOT;
static constexpr int L_KV = 0;
static constexpr int L_RT = L_KV + 24576;
static constexpr int L_MP = L_RT + 20480;
static constexpr int L_XCH = 139264;
static constexpr int L_XA = 81920, L_XB = 40960;
static constexpr int L_PB = 122880;
static constexpr int L_CTL = 160256;
static constexpr int L_MOD = 103424;
static constexpr int L_GB = L_CTL + 64;
static constexpr int L_CS3 = L_CTL + 384;
static constexpr int L_ST4 = L_CTL + 1408;
static constexpr int L_STAMP = L_CTL + 2432;
static constexpr int L_TOT = 163840;
static_assert(L_MP + 1024 <= SL_A && SL_B + CV_SLOT <= L_CTL && L_MOD >= SL_C + 16384 && L_MOD >= 102400 && L_MOD + 7680 <= SL_C + CV_U, "lds");
static constexpr int YROW = 1296;
enum { P_KV = 1, P_K, P_B, P_M, P_B2, P_U, P_C2, P_A2, P_RT, P_GA, P_ZC, P_X, P_X2, P_T, P_Z, P_RP, P_Z2, P_L1, P_XF1, P_G1, P_XF2, P_G2, P_SF, P_SA, P_S0, P_KA, P_H0, P_S1, P_KB, P_H1, P_P0 };
static constexpr unsigned SPIN_CAP = 1u << 21;
static constexpr unsigned RETRY_CAP = 1u << 15;

__device__ __forceinline__ float swap_add16(float v) {
  const unsigned u = __float_as_uint(v);
  auto r = __builtin_amdgcn_permlane16_swap(u, u, false, false);
  return __uint_as_float((unsigned)r[0]) + __uint_as_float((unsigned)r[1]);
}
__device__ __forceinline__ float swap_add32(float v) {
  const unsigned u = __float_as_uint(v);
  auto r = __builtin_amdgcn_permlane32_swap(u, u, false, false);
  return __uint_as_float((unsigned)r[0]) + __uint_as_float((unsigned)r[1]);
}
__device__ __forceinline__ float wave_sum2(float v) { return swap_add32(swap_add16(row16_sum(v))); }
__device__ __forceinline__ f32x2 up2(unsigned u) { return f32x2{lo2f(u), hi2f(u)}; }
__device__ __forceinline__ f32x2 rbf2(f32x2 v) { return up2(pack2(v[0], v[1])); }
#define FMA2 __builtin_elementwise_fma
__device__ __forceinline__ f32x2 rf2(f32x2 v, f32x2 cp, f32x2 cn) { return FMA2(v, cn, FMA2(v, cp, v)); }
__device__ __forceinline__ void ln5(const f32x2* in, unsigned* out) {
  const f32x2 s2 = ((in[0] + in[1]) + (in[2] + in[3])) + in[4];
  const float mean = wave_sum2(s2[0] + s2[1]) * (1.f / (float)D_);
  f32x2 d[5];
#pragma unroll
  for (int j = 0; j < 5; ++j) d[j] = in[j] - mean;
  const f32x2 q2 = (d[0] * d[0] + d[1] * d[1]) + (d[2] * d[2] + d[3] * d[3]) + d[4] * d[4];
  const float m = rbf(mean);
#pragma unroll
  for (int j = 0; j < 5; ++j) d[j] = rbf2(in[j] - m);
  const float q = wave_sum2(q2[0] + q2[1]);
  const float var = rbf(q * (1.f / (float)D_));
  const float den = rbf(__builtin_amdgcn_sqrtf(rbf(var + 1e-5f)));
  const float rinv = __builtin_amdgcn_rcpf(den);
#pragma unroll
  for (int j = 0; j < 5; ++j) { const f32x2 o = d[j] * rinv; out[j] = pack2(o[0], o[1]); }
}
#define PIN(x) asm volatile("" : "+v"(x))
#define STG(N, stmt) do { _Pragma("unroll") for (int i = 0; i < N; ++i) { stmt; } __builtin_amdgcn_sched_barrier(0); } while (0)
__device__ __forceinline__ void ln5s(const unsigned* in, unsigned* out) {
  float x[10], d[10], t[4];
  STG(10, x[i] = (i & 1) ? hi2f(in[i >> 1]) : lo2f(in[i >> 1]); PIN(x[i]));
  STG(4, t[i] = x[(i & 1) + (i >> 1) * 4] + x[(i & 1) + (i >> 1) * 4 + 2]; PIN(t[i]));
  STG(2, t[i] += t[2 + i]; PIN(t[i]));
  STG(2, t[i] += x[8 + i]; PIN(t[i]));
  const float mean = wave_sum2(t[0] + t[1]) * (1.f / (float)D_);
  STG(10, d[i] = x[i] - mean; PIN(d[i]));
  STG(4, t[i] = d[(i & 1) + (i >> 1) * 4] * d[(i & 1) + (i >> 1) * 4]; PIN(t[i]));
  STG(4, t[i] = __builtin_fmaf(d[(i & 1) + (i >> 1) * 4 + 2], d[(i & 1) + (i >> 1) * 4 + 2], t[i]); PIN(t[i]));
  STG(2, t[i] += t[2 + i]; PIN(t[i]));
  STG(2, t[i] = __builtin_fmaf(d[8 + i], d[8 + i], t[i]); PIN(t[i]));
  const float m = rbf(mean);
  STG(10, d[i] = x[i] - m; PIN(d[i]));
  unsigned p[5];
  STG(5, p[i] = pack2(d[2 * i], d[2 * i + 1]); PIN(p[i]));
  const float q = wave_sum2(t[0] + t[1]);
  const float var = rbf(q * (1.f / (float)D_));
  const float den = rbf(__builtin_amdgcn_sqrtf(rbf(var + 1e-5f)));
  const float rinv = __builtin_amdgcn_rcpf(den);
  STG(10, d[i] = ((i & 1) ? hi2f(p[i >> 1]) : lo2f(p[i >> 1])) * rinv; PIN(d[i]));
  STG(5, out[i] = pack2(d[2 * i], d[2 * i + 1]); PIN(out[i]));
}
template <int N>
__device__ __forceinline__ void wave_sums(float* v) {
  STG(N, v[i] += __builtin_bit_cast(float, __builtin_amdgcn_update_dpp(0, __builtin_bit_cast(int, v[i]), 0xB1, 0xf, 0xf, true)); PIN(v[i]));
  STG(N, v[i] += __builtin_bit_cast(float, __builtin_amdgcn_update_dpp(0, __builtin_bit_cast(int, v[i]), 0x4E, 0xf, 0xf, true)); PIN(v[i]));
  STG(N, v[i] += __builtin_bit_cast(float, __builtin_amdgcn_update_dpp(0, __builtin_bit_cast(int, v[i]), 0x141, 0xf, 0xf, true)); PIN(v[i]));
  STG(N, v[i] += __builtin_bit_cast(float, __builtin_amdgcn_update_dpp(0, __builtin_bit_cast(int, v[i]), 0x140, 0xf, 0xf, true)); PIN(v[i]));
  unsigned a[N], b[N];
  STG(N, a[i] = __float_as_uint(v[i]); b[i] = a[i]; PIN(b[i]));
  STG(N, { auto r = __builtin_amdgcn_permlane16_swap(a[i], b[i], false, false); a[i] = r[0]; b[i] = r[1]; PIN(a[i]); PIN(b[i]); });
  STG(N, v[i] = __uint_as_float(a[i]) + __uint_as_float(b[i]); PIN(v[i]));
  STG(N, a[i] = __float_as_uint(v[i]); b[i] = a[i]; PIN(b[i]));
  STG(N, { auto r = __builtin_amdgcn_permlane32_swap(a[i], b[i], false, false); a[i] = r[0]; b[i] = r[1]; PIN(a[i]); PIN(b[i]); });
  STG(N, v[i] = __uint_as_float(a[i]) + __uint_as_float(b[i]); PIN(v[i]));
}
template <int N>
__device__ __forceinline__ void gelu_s(float* e, float ga_, float gb2, float gd, unsigned* pk) {
  float x[N];
  STG(N, x[i] = e[i] * e[i]; PIN(x[i]));
  STG(N, x[i] = __builtin_fmaf(x[i], gb2, ga_); PIN(x[i]));
  STG(N, x[i] *= e[i]; PIN(x[i]));
  STG(N, x[i] = __builtin_amdgcn_exp2f(x[i]); PIN(x[i]));
  STG(N, x[i] = __builtin_fmaf(x[i], gd, gd); PIN(x[i]));
  STG(N, x[i] = __builtin_amdgcn_rcpf(x[i]); PIN(x[i]));
  STG(N, e[i] *= x[i]; PIN(e[i]));
  STG(N / 4, e[4 * i + 1] = plainv(e[4 * i + 1]); PIN(e[4 * i + 1]));
  STG(N / 2, pk[i] = pack2(e[2 * i], e[2 * i + 1]); PIN(pk[i]));
}
__device__ __forceinline__ f32x4 mfma16(u32x4 a, u32x4 b, f32x4 c) {
  return __builtin_amdgcn_mfma_f32_16x16x32_bf16(__builtin_bit_cast(bf16x8, a), __builtin_bit_cast(bf16x8, b), c, 0, 0, 0);
}
template <bool FIRST> __device__ __forceinline__ void mf(f32x4& c, const u32x4& a, const u32x4& b) {
  if (FIRST) asm volatile("v_mfma_f32_16x16x32_bf16 %0, %1, %2, 0" : "=v"(c) : "v"(a), "v"(b));
  else asm volatile("v_mfma_f32_16x16x32_bf16 %0, %1, %2, %0" : "+v"(c) : "v"(a), "v"(b));
}
__device__ __forceinline__ unsigned canon(unsigned x) { return x < 0xfffffffeu ? x : 0xfffffffeu; }
__device__ __forceinline__ bool bad1(unsigned a) { return __builtin_amdgcn_ballot_w64(a == SENT) != 0ull; }
__device__ __forceinline__ float amax2(const u32x4& a) { return __builtin_amdgcn_fmed3f(__builtin_fabsf(__uint_as_float(a[0])), __builtin_fabsf(__uint_as_float(a[2])), CMAX); }
__device__ __forceinline__ float amin2(const u32x4& a) { return __builtin_amdgcn_fmed3f(__builtin_fabsf(__uint_as_float(a[0])), __builtin_fabsf(__uint_as_float(a[2])), 0.f); }
__device__ __forceinline__ float amax3(float m, const u32x4& a) { float r; asm("v_max3_f32 %0, %1, |%2|, |%3|" : "=v"(r) : "v"(m), "v"(a[0]), "v"(a[2])); return r; }
template <bool CD> __device__ __forceinline__ bool any_bad4(u32x4& a, float thr, unsigned csh, float cmx) {
  const float m = __builtin_amdgcn_fmed3f(__builtin_fabsf(__uint_as_float(a[0])), __builtin_fabsf(__uint_as_float(a[2])), CD ? 0.f : cmx);
  if (CD) { a[0] -= csh; a[2] -= csh; }
  return __builtin_amdgcn_ballot_w64(CD ? m < thr : m >= thr) != 0ull;
}
__device__ __forceinline__ u32x4 widen(unsigned x0, unsigned x1, unsigned y0, unsigned y1) {
  const auto p0 = __builtin_amdgcn_permlane16_swap(x0, y0, false, false);
  const auto p1 = __builtin_amdgcn_permlane16_swap(x1, y1, false, false);
  return u32x4{(unsigned)p0[0], (unsigned)p1[0], (unsigned)p0[1], (unsigned)p1[1]};
}
#define DMA1(M) asm volatile("s_mov_b32 m0, %3\n\ts_nop 0\n\t" "buffer_load_dwordx4 %0, %1, %2 offen " M "lds" :: "v"(voff), "s"(rs), "s"(soff), "s"(dst) : "memory")
#define DMA4(M) asm volatile("s_mov_b32 m0, %3\n\ts_nop 0\n\t" \
                   "buffer_load_dwordx4 %0, %1, %2 offen " M "lds\n\tbuffer_load_dwordx4 %0, %1, %2 offen offset:1024 " M "lds\n\t" \
                   "buffer_load_dwordx4 %0, %1, %2 offen offset:2048 " M "lds\n\tbuffer_load_dwordx4 %0, %1, %2 offen offset:3072 " M "lds" \
                   :: "v"(voff), "s"(rs), "s"(soff), "s"(dst) : "memory")
template <int N, int AUX>
__device__ __forceinline__ void dma(unsigned voff, u32x4 rs, unsigned soff, unsigned dst) {
  if (N == 4) { if (AUX == 1) DMA4("sc1 "); else if (AUX == 2) DMA4("nt "); else DMA4(""); }
  else { if (AUX == 1) DMA1("sc1 "); else if (AUX == 2) DMA1("nt "); else DMA1(""); }
}
__device__ __forceinline__ void st128(u32x4 v, __amdgpu_buffer_rsrc_t r, unsigned voff, unsigned base, int) {
  __builtin_amdgcn_raw_buffer_store_b128(v, r, voff + base, 0, 16);
}
__device__ __forceinline__ u32x4 mk_rs(const void* p, unsigned bytes) {
  const unsigned long long a = (unsigned long long)p;
  return u32x4{(unsigned)a, (unsigned)(a >> 32) & 0xffffu, bytes, (unsigned)RS3};
}

#define USLEEP 2
#define DLY0 8
#define DLY1 3
#define DLY2 0
#define DLY3 2
#define NWH 8
#define ZG 8
#define ZSLEEP 2
#define AW4 8
#define AD3 3
#define RSLEEP 1
#define PHI 2
#define CPW 4
#define CPG 128u
#define NZ2 5
#define NS2 5
#define RS0 6
#define DLY3B 0
#define MV0 7
#define H0S 6
#define H1S 3
#define PK0 2
#define PE0 1
#define PK1 2
#define PE1 1
#define P40 1
#define P41 2
#define GSA 2
#define GSB 1
#define GSC 2
#define EA0 4
#define EA1 28
#define EC0 4
#define EC1 22
#define PB1 26
#define PB2 20
#define NL1 14
#define NP1 2
#define NL2 12
#define ROT 1
#define NA1 3
#define NP0 0
#define SL1 6
#define SL2 6
#define PH1 2
#define PH2 2
#define KG 42
#define KC3 12
#define PK2 2
#define KC3B 40
#define NPK 10
#define HG 4
#define WSP 3
#define PR1 3
#define HPZ 8
#define WS3 1
#define RB1 0
#define HFI 1
#define OAX 0
#define MGE 0
#define TZ 0
#define KE 0
#define TU1 200
#define TU2 260
#define TU 290
#define KL 5
#define PBE 18
#define NAF 6
#define W0U 4
#define U0 5
#define NPH 6
#define NGA 5
#define NR0 2
#define NR1 5
#define KVR 0
static_assert((NWH + NL1 + NP1) % 2 == 0 && NL2 % 2 == 0 && NWH + NL1 + NP1 + NL2 <= 40, "w3 schedule");
#define SPIN_STEP() do { ++spins; asm volatile("" : "+s"(spins)); } while (0)
#define SB() __builtin_amdgcn_sched_barrier(0)
#define FENCE() do { asm volatile("" ::: "memory"); __builtin_amdgcn_sched_barrier(0); } while (0)
#define LGKM0() do { __builtin_amdgcn_sched_barrier(0); asm volatile("s_waitcnt lgkmcnt(0)" ::: "memory"); } while (0)
#define BARRIER() do { LGKM0(); __builtin_amdgcn_s_barrier(); asm volatile("" ::: "memory"); } while (0)
#define VM0() __builtin_amdgcn_s_waitcnt(0x0F70)
#define PRIO(n) __builtin_amdgcn_s_setprio(n)
#define CHECK_A(CD, AV, i, hi, LD)                                                          \
  do {                                                                                      \
    if (__builtin_expect(any_bad4<CD>(AV, sthr, scsh, scmx), 0)) {                          \
      unsigned spins = 0;                                                                   \
      PRIO(0);                                                                              \
      do {                                                                                  \
        __builtin_amdgcn_s_sleep(RSLEEP);                                                   \
        _Pragma("unroll") for (int y_ = (i); y_ < (hi); ++y_) { LD(y_); }                   \
        SPIN_STEP();                                                                        \
      } while (any_bad4<CD>(AV, sthr, scsh, scmx) && spins < RETRY_CAP);                    \
      PRIO(PHI);                                                                            \
      VM0();                                                                                \
    }                                                                                       \
  } while (0)

#define ASM_MACROS \
  ".macro regno op\n.set r_, 0\n.set st_, 0\n.irpc c, \"\\op\"\n.ifc \\c, :\n.set st_, 2\n.endif\n.if st_ == 1\n.set r_, r_ * 10 + \\c\n.endif\n.ifc \\c, [\n.set st_, 1\n.endif\n.endr\n.endm\n" \
  ".macro retry n, cd, a, so, ct, t, vo, rs, sb, im, th, k, pr\n"                                   \
  "s_cbranch_vccnz Lc\\n\nLb\\n:\n.subsection 1\nLc\\n:\nregno \\a\n"              \
  "s_setprio 0\ns_add_u32 \\so, \\sb, \\im\ns_mov_b32 \\ct, 0\n"                                        \
  "Ll\\n:\ns_sleep 1\nbuffer_load_dwordx4 \\a, \\vo, \\rs, \\so offen sc1\ns_add_u32 \\ct, \\ct, 1\ns_waitcnt vmcnt(0)\n" \
  ".if \\cd\nv_med3_f32 \\t, |v[r_]|, |v[r_+2]|, 0\nv_cmp_gt_f32 vcc, \\th, \\t\n.else\nv_med3_f32 \\t, |v[r_]|, |v[r_+2]|, \\k\nv_cmp_le_f32 vcc, \\th, \\t\n.endif\n" \
  "s_cbranch_vccz Ld\\n\ns_cmp_lt_u32 \\ct, 0x8000\ns_cbranch_scc1 Ll\\n\n"                             \
  "Ld\\n:\n.if \\cd\nv_subrev_u32 v[r_], \\k, v[r_]\nv_subrev_u32 v[r_+2], \\k, v[r_+2]\n.endif\ns_setprio \\pr\ns_nop 1\ns_branch Lb\\n\n.subsection 0\n.endm\n" \
  ".macro need n, v, ct, ad, k\n"                                                                       \
  "v_cmp_gt_u32 vcc, \\k, \\v\ns_cbranch_vccnz Lc\\n\nLb\\n:\n.subsection 1\nLc\\n:\ns_mov_b32 \\ct, 0\n" \
  "Ll\\n:\nds_read_b32 \\v, \\ad\ns_waitcnt lgkmcnt(0)\nv_cmp_gt_u32 vcc, \\k, \\v\ns_cbranch_vccz Lb\\n\n" \
  "s_sleep 1\ns_add_u32 \\ct, \\ct, 1\ns_cmp_lt_u32 \\ct, 0x200000\ns_cbranch_scc1 Ll\\n\ns_branch Lb\\n\n.subsection 0\n.endm\n"
#define RETRY(CD, AV, BM, VOFF, SBASE, IMM, PR)                                                         \
  do {                                                                                                  \
    unsigned so_, ct_; float t_; register unsigned long long vc_ asm("vcc") = (BM);                                                                     \
    asm volatile("retry %=, %5, \"%0\", %1, %2, %3, %6, \"%7\", %8, %9, %10, %11, %12"                   \
                 : "+v"(AV), "=&s"(so_), "=&s"(ct_), "=&v"(t_), "+s"(vc_)                            \
                 : "i"((int)(CD)), "v"(VOFF), "s"(dW), "s"(SBASE), "i"(IMM), "s"(sthr), "s"((CD) ? scsh : __float_as_uint(scmx)), "i"(PR) : "scc", "memory"); \
  } while (0)
#define CMP_BAD(CD, BM, M) do { register unsigned long long vc_ asm("vcc");                             \
    if (CD) asm volatile("v_cmp_gt_f32 vcc, %1, %2" : "=s"(vc_) : "s"(sthr), "v"(M)); else asm volatile("v_cmp_le_f32 vcc, %1, %2" : "=s"(vc_) : "s"(sthr), "v"(M)); \
    BM = vc_; } while (0)
#define RETRY_C(AV, BM, VOFF, SBASE, IMM, PR) RETRY(1, AV, BM, VOFF, SBASE, IMM, PR)
#define NEED_AT(V, ADDR, K) do { unsigned ct_; asm volatile("need %=, %0, %1, %2, %3" : "+v"(V), "=&s"(ct_) : "v"(ADDR), "s"(K) : "vcc", "scc", "memory"); } while (0)

#define STAMP(i)
#define CLK(i)
#define TSHI 62
#define TX_(i)
#define TS_(i) do { if ((i) == 999) asm volatile("s_memrealtime s[98:99]" ::: "s98", "s99"); } while (0)
#define KATTR __attribute__((amdgpu_num_sgpr(96)))
__device__ __forceinline__ u32x4 rd_tr(const LAS char* p) {
  const s16x4 lo = __builtin_amdgcn_ds_read_tr16_b64_v4i16((LAS s16x4*)p);
  const s16x4 hi = __builtin_amdgcn_ds_read_tr16_b64_v4i16((LAS s16x4*)(p + 2560));
  const bf16x8 o = {lo[0], lo[1], lo[2], lo[3], hi[0], hi[1], hi[2], hi[3]};
  return __builtin_bit_cast(u32x4, o);
}
__device__ __forceinline__ float dpp_add(float v, int ctrl) {
  return v + __builtin_bit_cast(float, ctrl == 0 ? __builtin_amdgcn_update_dpp(0, __builtin_bit_cast(int, v), 0x128, 0xf, 0xf, true)
                                    : ctrl == 1 ? __builtin_amdgcn_update_dpp(0, __builtin_bit_cast(int, v), 0x124, 0xf, 0xf, true)
                                                : __builtin_amdgcn_update_dpp(0, __builtin_bit_cast(int, v), 0x4E, 0xf, 0xf, true));
}

template <int V> struct IC { static constexpr int v = V; };
template <int I, int N, class F> __device__ __forceinline__ void sfor(F&& f) { if constexpr (I < N) { f(IC<I>{}); sfor<I + 1, N>(f); } }
constexpr int cv_i(int m) { int i = 0; while (m >= 8 - i) { m -= 8 - i; ++i; } return i; }
constexpr int cv_d(int m) { int i = 0; while (m >= 8 - i) { m -= 8 - i; ++i; } return 7 - i - m; }
constexpr int kl_i(int m) { return m < 50 - 5 * KL ? m / 5 : m < 50 - 2 * KL ? 10 - KL + (m - 50 + 5 * KL) / 3 : 10 - KL + (m - 50 + 2 * KL) / 2; }
constexpr int kl_j(int m) { return m < 50 - 5 * KL ? m % 5 : m < 50 - 2 * KL ? (m - 50 + 5 * KL) % 3 : 3 + (m - 50 + 2 * KL) % 2; }
constexpr int cl_t(int m) { int t = 0; while (m > t) { m -= t + 1; ++t; } return t; }
constexpr int cl_i(int m) { int t = 0; while (m > t) { m -= t + 1; ++t; } return m; }
constexpr bool cv_free(int m, int k) {
  return k == 0 ? (m >= 64 && m < 111 && m != 65 && m != 69 && m != 72 && m != 75 && m != 77 && m != 94 && m != 95 && m != 104 && m != 110 && (m < 72 + RS0 || m >= 88 + RS0))
                : (m >= 123 && m != 126 && m != 131 && m != 132 && m != 139 && m != 140);
}
constexpr int cv_slot(int m, int k) { if (!cv_free(m, k)) return -1; int n = 0; for (int y = 0; y < m; ++y) n += cv_free(y, k); return n; }
static_assert(cv_slot(109, 0) >= 15 && cv_slot(143, 1) >= 15, "slots");
constexpr int cc_i(int J, int c) { for (int i = 0; i < 8; ++i) for (int t = 3; t >= 0; --t) if (i <= ((J >> (4 * t)) & 15) && c-- == 0) return i; return 0; }
constexpr int cc_t(int J, int c) { for (int i = 0; i < 8; ++i) for (int t = 3; t >= 0; --t) if (i <= ((J >> (4 * t)) & 15) && c-- == 0) return t; return 0; }
#define KWAIT "s_waitcnt lgkmcnt(0)\n"
__global__ __launch_bounds__(512, 2) KATTR void k_main(const bf16* kin_, const bf16* vin_, char* ws_, const bf16* shift_k_, const bf16* ssm_k_,
                                                 const bf16* w_mod_, const bf16* cin_, const bf16* w3_,
                                                 const bf16* w4_, const bf16* q_, const bf16* w1_, const bf16* w2_,
                                                 const bf16* ssm_D_, bf16* out_) {
  typedef __attribute__((ext_vector_type(8))) unsigned long long u64x8;
  typedef __attribute__((ext_vector_type(4))) unsigned long long u64x4;
  typedef __attribute__((ext_vector_type(2))) unsigned long long u64x2;
  u64x8 ka;
  u64x4 kb;
  u64x2 kc;
  int tid_ = threadIdx.x, bx_ = blockIdx.x;
  unsigned long long pc0;
  unsigned code_bytes;
  asm volatile("s_memrealtime s[98:99]\n\ts_load_dwordx16 %0, %6, 0x0\n\ts_load_dwordx8 %1, %6, 0x40\n\ts_load_dwordx4 %2, %6, 0x60\n\ts_getpc_b64 %3\n"
               "Lpc0:\n\ts_mov_b32 %4, Lcold0 - Lpc0\n" KWAIT ".subsection 1\nLcold0:\n.subsection 0"
               : "=&s"(ka), "=&s"(kb), "=&s"(kc), "=&s"(pc0), "=&s"(code_bytes), "+s"(bx_) : "s"(__builtin_amdgcn_kernarg_segment_ptr()));
  asm volatile(ASM_MACROS ".subsection 2\n.fill 256, 4, 0xbf800000\n.subsection 0" : "+v"(tid_));
  extern __shared__ __attribute__((aligned(16))) char lds_[];
  LAS char* lds = (LAS char*)lds_;
  ldsw_t LW = (ldsw_t)(lds + L_CTL);
  const int tid = tid_ & 511, lane = tid & 63;
  const int wv = __builtin_amdgcn_readfirstlane(tid >> 6) & 7;
  const int bx = bx_ & 255;
  const int mt = (bx & 7) * 4 + (bx >> 6), nt = (bx >> 3) & 7;
  const int n0 = nt * 80, m0 = mt * 64;
  const int l15 = lane & 15, g = lane >> 4;
  const int mg = wv & 3, kh = wv >> 2;
  const int gm = (wv >> 2) * 2 + (wv & 1), gk = ((wv >> 1) ^ (wv >> 2)) & 1, gp = wv ^ 2;
  const unsigned l16 = (unsigned)lane * 16u;
  TS_(0);
  STAMP(0);
  CLK(30);
  unsigned cp0 = 0;
  if (wv == CPW) {
    asm volatile("buffer_load_dword %0, %1, %2, 0 offen" : "=v"(cp0) : "v"((unsigned)((bx >> 3) + 32 * lane) * CPG), "s"(mk_rs((const void*)pc0, code_bytes)) : "memory");
  }
  FENCE();
  const int tok0 = 32 * ((bx & 7) * 8 + (bx >> 5)), ch0 = 160 * ((bx >> 3) & 3);
  unsigned ko[3], vo[3];
#pragma unroll
  for (int i = 0; i < 3; ++i) {
    const unsigned s = (unsigned)((mg + 4 * i) * 64 + lane);
    const unsigned gr = __umul24(s, 800u) >> 16, w = s - __umul24(82u, gr), rr = __umul24(w, 205u) >> 12;
    unsigned o = (unsigned)(tok0 * (D_ * 2) + ch0 * 2) + __umul24(gr, 4u * D_ * 2) + __umul24(rr, D_ * 2 - 320) + w * 16u;
    asm volatile("" : "+v"(o));
    ko[i] = (w < 80u && gr < 9u) ? o - 4u * D_ * 2 : OOB;
    vo[i] = (w < 80u && gr < 8u) ? o : OOB;
    asm volatile("" : "+v"(ko[i]), "+v"(vo[i]));
  }
  FENCE();
  asm volatile("s_waitcnt lgkmcnt(0)" : "+s"(ka), "+s"(kb), "+s"(kc));
  const bf16* kin = (const bf16*)ka[0];
  const bf16* vin = (const bf16*)ka[1];
  char* ws = (char*)ka[2];
  const bf16* shift_k = (const bf16*)ka[3];
  const bf16* ssm_k = (const bf16*)ka[4];
  const bf16* w_mod = (const bf16*)ka[5];
  const bf16* cin = (const bf16*)ka[6];
  const bf16* w3 = (const bf16*)ka[7];
  const bf16* w4 = (const bf16*)kb[0];
  const bf16* q = (const bf16*)kb[1];
  const bf16* w1 = (const bf16*)kb[2];
  const bf16* w2 = (const bf16*)kb[3];
  const bf16* ssm_D = (const bf16*)kc[0];
  bf16* out = (bf16*)kc[1];
  const __amdgpu_buffer_rsrc_t rW = __builtin_amdgcn_make_buffer_rsrc((void*)ws, 0, WS_USED, RS3);
  const u32x4 dW = mk_rs(ws, WS_USED);
  u32x2 ep;
  asm volatile("buffer_load_dwordx2 %0, off, %1, %2 sc1" : "=v"(ep) : "s"(dW), "s"(O_EP) : "memory");
  FENCE();
  if (kh == 0) {
    const u32x4 dK = mk_rs(kin, SD2), dV = mk_rs(vin, SD2);
#pragma unroll
    for (int i = 0; i < 3; ++i) dma<1, 0>(ko[i], dK, 0u, (unsigned)(L_KV + (mg + 4 * i) * 1024));
#pragma unroll
    for (int i = 0; i < 3; ++i) dma<1, 0>(vo[i], dV, 0u, (unsigned)(L_KV + 12288 + (mg + 4 * i) * 1024));
  }
  u32x4 sk[3];
  if (kh == 0) {
    asm volatile("buffer_load_dwordx4 %0, %3, %5, %6 offen\n\tbuffer_load_dwordx4 %1, %3, %5, %6 offen offset:16\n\tbuffer_load_dwordx4 %2, %4, %5, %7 offen"
                 : "=&v"(sk[0]), "=&v"(sk[1]), "=&v"(sk[2])
                 : "v"(((unsigned)lane >> 3) * 32u), "v"((unsigned)g * 16u), "s"(mk_rs(shift_k, D_ * 8)), "s"(ch0 * 8 + mg * 256), "s"(ch0 * 8 + 1024 + mg * 64) : "memory");
  }
  FENCE();
  if (tid >= 504) LW[tid - 504] = 0u;
  if (tid >= 256 && tid < 352) *(LAS u32x4*)(lds + SL_A + ((tid - 256) >> 5) * CV_SLOT + CV_U + (tid & 31) * 16) = u32x4{0u, 0u, 0u, 0u};
  if (tid >= 384 && tid < 408) *(LAS u32x4*)(lds + SL_A + ((tid - 384) >> 3) * CV_SLOT + (tid & 7) * CV_STR + 257 * 16) = u32x4{0u, 0u, 0u, 0u};
  STAMP(29);
  if constexpr (TSHI == 23) TX_(30);
  BARRIER();

  const unsigned psig = (unsigned)L_CTL + (unsigned)wv * 4u;
#define SIG(k) do { unsigned long long sv_; asm volatile("s_mov_b64 %0, exec\n\ts_mov_b64 exec, 1\n\tds_write_b32 %1, %2\n\ts_mov_b64 exec, %0" : "=&s"(sv_) : "v"(psig), "v"((unsigned)(k)) : "memory"); } while (0)
#define WAIT_AT(addr, k)                                                                                \
  do {                                                                                                  \
    unsigned tv_, tc_;                                                                                  \
    asm volatile("s_mov_b32 %1, 0\n"                                                                    \
                 "L%=_a:\n\tds_read_b32 %0, %2\n\ts_waitcnt lgkmcnt(0)\n\tv_cmp_gt_u32 vcc, %3, %0\n\ts_cbranch_vccz L%=_b\n\t"    \
                 "s_sleep 1\n\ts_add_u32 %1, %1, 1\n\ts_cmp_lt_u32 %1, 0x200000\n\ts_cbranch_scc1 L%=_a\n"            \
                 "L%=_b:" : "=&v"(tv_), "=&s"(tc_) : "v"(addr), "i"(k) : "vcc", "scc", "memory");       \
  } while (0)
#define WAIT_2(k0, k1)                                                                                 \
  do {                                                                                                  \
    unsigned tv_, tc_;                                                                                  \
    asm volatile("s_mov_b32 %1, 0\n"                                                                    \
                 "L%=_a:\n\tds_read_b32 %0, %2\n\ts_waitcnt lgkmcnt(0)\n\tv_cmp_gt_u32 vcc, %3, %0\n\ts_cbranch_vccz L%=_b\n\t"    \
                 "s_sleep 1\n\ts_add_u32 %1, %1, 1\n\ts_cmp_lt_u32 %1, 0x200000\n\ts_cbranch_scc1 L%=_a\n"            \
                 "L%=_b:" : "=&v"(tv_), "=&s"(tc_) : "v"((unsigned)L_CTL + ((unsigned)lane & 7u) * 4u), "v"((lane & 4) ? (unsigned)(k1) : (unsigned)(k0)) : "vcc", "scc", "memory"); \
  } while (0)
#define WAIT_ALL(k) WAIT_AT((unsigned)L_CTL + ((unsigned)lane & 7u) * 4u, k)
#define WAIT_HALF(h, k) WAIT_AT((unsigned)L_CTL + ((unsigned)lane & 3u) * 4u + (unsigned)(h) * 16u, k)

  const int sb3 = nt < 6 ? 14 * nt : 84 + 12 * (nt - 6);
  const bool two = kh == 0 || nt < 6;
  const int jl = kh ? 4 + wv : 2 * wv, jm = kh ? 12 + (wv & 1) : 2 * wv + 1, hb = wv > 5 ? 1 : 0;
  const int k4 = 7 * wv - (wv > 5 ? 2 : wv > 4 ? 1 : 0);
  const unsigned w4a = O_WT4 + (unsigned)(nt * 54 + k4) * 5120u;
  unsigned w3s[4] = {O_WT3 + (unsigned)(sb3 + jl) * 20480u + (unsigned)hb * 10240u, two ? O_WT3 + (unsigned)(sb3 + jm) * 20480u + (unsigned)hb * 10240u : OOB,
                     O_WT3 + (unsigned)(sb3 + jl) * 20480u - (unsigned)hb * 10240u, kh ? w4a - 10240u : O_WT3 + (unsigned)(sb3 + jm) * 20480u};
  f32x2 cp = {65536.f, 65536.f}, cn = {-65536.f, -65536.f};
  asm volatile("" : "+s"(cp), "+s"(cn));
  u32x4 W3[40];
#define LD_W3U(f) W3[f] = __builtin_amdgcn_raw_buffer_load_b128(rW, l16 + ((f) < 8 ? ((f) >> 1) * 1024 : 0), w3s[((f) & 1) + ((f) >= 20 ? 2 : 0)] + ((f) < 8 ? 0 : ((f) >> 1) * 1024), 16)
#define LD_W3R(a, b) do { _Pragma("unroll") for (int f_ = (a); f_ < (b); ++f_) LD_W3U(f_); } while (0)
#define VMW(n) asm volatile("s_waitcnt vmcnt(%0)" :: "i"(n) : "memory")
  const __amdgpu_buffer_rsrc_t rW1 = __builtin_amdgcn_make_buffer_rsrc((void*)w1, 0, D_ * D_ * 2, RS3);
  const __amdgpu_buffer_rsrc_t rW2 = __builtin_amdgcn_make_buffer_rsrc((void*)w2, 0, D_ * D_ * 2, RS3);
  const u32x4 dW1 = mk_rs(w1, D_ * D_ * 2), dW2 = mk_rs(w2, D_ * D_ * 2);
#define W12_G(gw) unsigned gw[5]; { unsigned ln_ = (unsigned)lane; asm volatile("" : "+v"(ln_)); \
    _Pragma("unroll") for (int j_ = 0; j_ < 5; ++j_) { const unsigned p_ = ln_ + 64u * j_, r_ = __umul24(p_, 6554u) >> 16; \
      gw[j_] = __umul24((r_ & 12u) * 2u + (r_ >> 4) * 4u + (r_ & 3u), D_ * 2) + (p_ - __umul24(r_, 10u)) * 16u + (unsigned)n0 * 2u; } }

  const int s0 = m0 + nt * 8;
  const int srow = s0 + wv;
  unsigned qw[5], hw[5];
  unsigned long long epn;
  unsigned tA, tB;
  u32x4 WS2[10];
#define LD_WS2U(a, b) _Pragma("unroll") for (int y_ = (a); y_ < (b); ++y_) asm volatile("buffer_load_dwordx4 %0, %1, %2, %3 offen ; ws2" : "=v"(WS2[y_]) : "v"(gw[y_ % 5]), "s"(dW2), "s"((wv + 8 * (y_ / 5)) * (32 * D_ * 2)) : "memory")
#define LD_WS2(a, b) _Pragma("unroll") for (int y_ = (a); y_ < (b); ++y_) WS2[y_] = __builtin_amdgcn_raw_buffer_load_b128(rW2, gw[y_ % 5], (wv + 8 * (y_ / 5)) * (32 * D_ * 2), 0)
  unsigned ry = (unsigned)(L_XCH + wv * YROW) + l16, rz = (unsigned)(L_XCH + wv * YROW + 1024 + 4 * lane);
  f32x2 xq[5], ha[5], ca[5], sb[5];
  float mc, mb;
  const int row = m0 + 16 * gm + l15;
  const int jb = 3 * gk;
  const unsigned ofr = (unsigned)(row >> 4) * 20480u + (unsigned)((n0 + 16 * jb + 4 * g) >> 3) * 256u + (unsigned)(g & 1) * 8u + (unsigned)l15 * 16u;
  {
    const int pi = (bx & 7) + 8 * (bx >> 4), half = (bx >> 3) & 1;
    const int chA = 2 * bx, chC = 512 + pi;
#define RD_MOD(v) do { const u32x4 b_ = *(const LAS u32x4*)(lds + L_MOD + (v) * (D_ * 2) + l16); mw[v][0] = b_[0]; mw[v][1] = b_[1]; mw[v][2] = b_[2]; mw[v][3] = b_[3]; \
                       mw[v][4] = *(const LAS unsigned*)(lds + L_MOD + (v) * (D_ * 2) + 1024 + lane * 4); } while (0)
#define LN1()                                                                                           \
  do {                                                                                                  \
    f32x2 xq_[5];                                                                                       \
    _Pragma("unroll") for (int j = 0; j < 5; ++j) xq_[j] = up2(qw[j]);                                  \
    ln5(xq_, hw);                                                                                       \
    _Pragma("unroll") for (int j = 0; j < 5; ++j) asm volatile("" : "+v"(hw[j]));                       \
  } while (0)
#define R_PRE() do {                                                                                    \
      unsigned mw[5][5];                                                                                \
      _Pragma("unroll") for (int v = 0; v < 5; ++v) RD_MOD(v);                                          \
      _Pragma("unroll") for (int j = 0; j < 5; ++j) {                                                   \
        xq[j] = up2(qw[j]);                                                                             \
        ha[j] = FMA2(up2(hw[j]), up2(mw[1][j]), up2(mw[0][j])) * up2(mw[2][j]); ca[j] = up2(mw[4][j]); sb[j] = up2(mw[3][j]); \
        asm volatile("" : "+v"(xq[j]), "+v"(ha[j]), "+v"(ca[j]), "+v"(sb[j]));                          \
      }                                                                                                 \
      const f32x2 c2_ = ((ca[0] + ca[1]) + (ca[2] + ca[3])) + ca[4], b2_ = ((sb[0] + sb[1]) + (sb[2] + sb[3])) + sb[4]; \
      mc = wave_sum2(c2_[0] + c2_[1]) * (1.f / (float)D_); mb = wave_sum2(b2_[0] + b2_[1]) * (1.f / (float)D_); \
      asm volatile("" : "+v"(mc), "+v"(mb));                                                            \
    } while (0)
    unsigned dv2;
#define GET_D() const unsigned dAB = __builtin_amdgcn_readlane(dv2, 0), dC_ = __builtin_amdgcn_readlane(dv2, 1), dC = (chC & 1) ? dC_ & 0xffff0000u : dC_ << 16
#define REQ_Q() do {                                                                                    \
      const __amdgpu_buffer_rsrc_t rQ = __builtin_amdgcn_make_buffer_rsrc((void*)q, 0, SD2, RS3);       \
      const u32x4 a = __builtin_amdgcn_raw_buffer_load_b128(rQ, l16, srow * (D_ * 2), 0);               \
      qw[0] = a[0]; qw[1] = a[1]; qw[2] = a[2]; qw[3] = a[3];                                           \
      qw[4] = __builtin_amdgcn_raw_buffer_load_b32(rQ, (unsigned)lane * 4u, srow * (D_ * 2) + 1024, 0); \
    } while (0)
#define REQ_D() dv2 = __builtin_amdgcn_raw_buffer_load_b32(__builtin_amdgcn_make_buffer_rsrc((void*)ssm_D, 0, D_ * 2, RS3), (unsigned)(lane ? (chC & ~1) : chA) * 2u, 0, 0)
#define TAGS()                                                                                          \
    {                                                                                                   \
      unsigned e0_, e1_;                                                                                \
      asm volatile("v_readfirstlane_b32 %0, %2\n\tv_readfirstlane_b32 %1, %3" : "=s"(e0_), "=s"(e1_) : "v"(ep[0]), "v"(ep[1])); \
      unsigned long long h = (unsigned long long)e1_ << 32 | e0_;                                       \
      epn = h + 1ull;                                                                                   \
      h += 0x9E3779B97F4A7C15ull;                                                                       \
      h = (h ^ (h >> 30)) * 0xBF58476D1CE4E5B9ull;                                                      \
      h = (h ^ (h >> 27)) * 0x94D049BB133111EBull;                                                      \
      h ^= h >> 31;                                                                                     \
      tA = (unsigned)h; tB = (unsigned)(h >> 32);                                                       \
    }
#define BUILD(SL, CC)                                                                                   \
  do {                                                                                                  \
    const u32x4 klo = *(const LAS u32x4*)(lds + (SL) + CV_U + 512 + 4064 - 16 * (CC)), khi = *(const LAS u32x4*)(lds + (SL) + CV_U + 512 + 4080 - 16 * (CC)); \
    const unsigned w[8] = {klo[0], klo[1], klo[2], klo[3], khi[0], khi[1], khi[2], khi[3]};             \
    _Pragma("unroll") for (int m = 0; m < 8; ++m) {                                                     \
      u32x4 o;                                                                                          \
      const int a = m >> 1;                                                                             \
      _Pragma("unroll") for (int t = 0; t < 4; ++t) {                                                   \
        if ((m & 1) == 0) o[t] = (w[a - t + 4] & 0xffffu) | (w[a - t + 3] & 0xffff0000u);               \
        else o[t] = (w[a - t + 4] >> 16) | (w[a - t + 4] << 16);                                        \
      }                                                                                                 \
      *(LAS u32x4*)(lds + (SL) + m * CV_STR + ((CC) + 1) * 16) = o;                                     \
    }                                                                                                   \
  } while (0)
#define ZCH(c) ((c) * 128u + ((c) / 160u) * (TS - 20480u))
#define ZOFF(JA, JB, CH) ((unsigned)((32 * ((g & 1) ? (JB) : (JA)) + 2 * l15 + (g >> 1)) >> 3) * (4u * TS) + ZCH((unsigned)(CH)) + (unsigned)((2 * l15 + (g >> 1)) & 7) * 16u)
#define Z2OFF(w) (O_Z + (unsigned)(4 * (w) + (l15 >> 2)) * (4u * TS) + ZCH((unsigned)chA) + (unsigned)((l15 & 3) * 64 + g * 16))
    const unsigned a00 = (unsigned)((l15 & 7) * CV_STR + (2032 - 8 * (l15 >> 3) + 8 * g) * 2) - 128u * mg;
    const unsigned b00 = (unsigned)(CV_U + 480 + 32 * l15 + 16 * g) - 128u * mg;
#define CV_RA(AV, SL, rd) _Pragma("unroll") for (int i = 0; i < 8; ++i) AV[8 * (rd) + i] = *(const LAS u32x4*)(lds + (SL) + a00 - 64 * (rd) - 512 * i)
#define CV_RB(BV, SL, rd) _Pragma("unroll") for (int i = 7; i >= 0; --i) BV[8 * (rd) + i] = *(const LAS u32x4*)(lds + (SL) + b00 + 512 * i - 64 * (rd))
    const int zja = mg ? (half ? 5 : 4) : (half ? 1 : 0), zjb = mg ? (half ? 6 : 7) : (half ? 2 : 3);
    const int t8 = tid & 255;
#define W_DMA(blk, j) dma<1, 0>(gw[j], (blk) < 20 ? dW1 : dW2, (unsigned)((blk) < 20 ? (blk) : (blk) - 4) * (32 * D_ * 2), (unsigned)((blk) * 5120 + (j) * 1024))
    if (__builtin_expect(kh != 0, 1)) {
      {
        const u32x4 dS = mk_rs(ssm_k, SD2);
        dma<1, 0>(l16, dS, (unsigned)(chC * (S_ * 2) + mg * 1024), (unsigned)(SL_C + CV_U + 512 + mg * 1024));
        dma<1, 0>(l16, dS, (unsigned)(chA * (S_ * 2) + mg * 1024), (unsigned)(SL_A + CV_U + 512 + mg * 1024));
        dma<1, 0>(l16, dS, (unsigned)((chA + 1) * (S_ * 2) + mg * 1024), (unsigned)(SL_B + CV_U + 512 + mg * 1024));
      }
      unsigned cpf = __builtin_amdgcn_raw_buffer_load_b32(rW, OOB, 0, 0);
      FENCE();
#define FILL_Z() do {                                                                                   \
      st128(u32x4{CODED, CODED, CODED, CODED}, rW, Z2OFF(mg), 0, 16);                                   \
      st128(u32x4{CODED, CODED, CODED, CODED}, rW, Z2OFF(wv), 0, 16);                                   \
      if (mg < 2) st128(u32x4{CODED, CODED, CODED, CODED}, rW, ZOFF(zja, zjb, chC), O_Z, 16);            \
    } while (0)
      unsigned fpa[3], fpv;
#pragma unroll
      for (int f_ = 0; f_ < 3; ++f_) {
        const unsigned o = (unsigned)(f_ * 4096 + t8 * 16);
        fpa[f_] = o < 10240u ? O_H1 + (unsigned)(bx >> 1) * TS + (bx & 1) * 10240u + o : o < 11264u ? O_ST4 + (unsigned)bx * 1024u + (o - 10240u)
                : (o < 11392u && (bx & 1)) ? O_ST3 + (unsigned)(bx >> 1) * 128u + (o - 11264u) : OOB;
        if (f_ == 2) fpv = o < 10240u ? CODED : SENT;
      }
#define FILL_P(a, b) _Pragma("unroll") for (int f_ = (a); f_ < (b); ++f_) { const unsigned fv = f_ < 2 ? CODED : fpv; st128(u32x4{fv, fv, fv, fv}, rW, fpa[f_], 0, 16); }
      u32x4 WS[10];
#define LD_WS(a, b) _Pragma("unroll") for (int x = (a); x < (b); ++x)                                   \
      WS[x] = __builtin_amdgcn_raw_buffer_load_b128(rW1, gw[x % 5], (9 + mg + 4 * (x / 5)) * (32 * D_ * 2), 0)
#define LD_WD(a, b) _Pragma("unroll") for (int x = (a); x < (b); ++x) {                                 \
      if (x < 10) W_DMA(mg + 4 * (x / 5), x % 5);                                                       \
      else if (x == 10) dma<1, 0>(mg == 0 ? gw[0] : mg == 1 ? gw[1] : mg == 2 ? gw[2] : gw[3], dW1, 8u * (32 * D_ * 2), (unsigned)(8 * 5120 + mg * 1024)); \
      else if (mg == 0) W_DMA(8, 4);                                                                    \
    }
      FILL_Z();
      FENCE();
      u32x4 wm[5];
      unsigned short cv;
      {
        const __amdgpu_buffer_rsrc_t rM = __builtin_amdgcn_make_buffer_rsrc((void*)w_mod, 0, D_ * 6 * D_ * 2, RS3);
        const __amdgpu_buffer_rsrc_t rC = __builtin_amdgcn_make_buffer_rsrc((void*)cin, 0, D_ * 2, RS3);
        const unsigned o = bx < 240 ? (unsigned)(t8 >> 3) * (6 * D_ * 2) + (unsigned)(t8 & 7) * 16u : OOB;
        cv = __builtin_amdgcn_raw_buffer_load_b16(rC, (unsigned)((t8 >> 3) + 32 * min(lane & 7, 4)) * 2u, (bx & 3) * 320, 0);
#pragma unroll
        for (int i = 0; i < 5; ++i) wm[i] = __builtin_amdgcn_raw_buffer_load_b128(rM, o, ((bx & 3) * 160 + i * 32) * (6 * D_ * 2) + (bx >> 2) * 128, 0);
      }
      FENCE();
      W12_G(gw);
#pragma unroll
      for (int j = 0; j < 5; ++j) PIN(gw[j]);
      const int job = bx * 4 + mg;
      const bool j3 = job < 540, jok = job < 972;
      const int jks = j3 ? job / 27 : (job - 540) >> 3, jcg = j3 ? job % 27 : (job - 540) & 7;
      const unsigned rtl = (unsigned)(L_RT + mg * 5120);
      u32x4 rt[5];
      unsigned rta[5];
      {
        const unsigned v3 = ((unsigned)lane >> 3) * (F_ * 2) + (((unsigned)lane & 7u) * 16u ^ ((unsigned)lane & 24u) * 4u) + (unsigned)jks * (32 * F_ * 2) + (unsigned)jcg * 128u;
        const unsigned s4 = (unsigned)jks * (32 * D_ * 2) + (unsigned)(jcg - nt) * 160u;
#pragma unroll
        for (int i = 0; i < 5; ++i) { rta[i] = j3 ? (i < 4 ? (v3 ^ (i & 1) * 32u) + i * (8 * F_ * 2) : OOB) : gw[i] + s4; PIN(rta[i]); }
      }
#define RT_LD() do {                                                                                    \
      const u32x4 dS = mk_rs(j3 ? w3 : w4, jok ? DF2 : 0);                                              \
      _Pragma("unroll") for (int i = 0; i < 4; ++i) dma<1, 0>(rta[i], dS, 0u, rtl + i * 1024);          \
      if (!j3) dma<1, 0>(rta[4], dS, 0u, rtl + 4096);                                                   \
      FENCE();                                                                                          \
    } while (0)
      FENCE();
      STAMP(12);
      asm volatile("" : "+v"(cpf));
      asm volatile("" :: "v"(cp0));
      SIG(P_K);
      if constexpr (TSHI == 23) TX_(31);
      FENCE();
      TAGS();
      {
        float a[8];
#pragma unroll
        for (int e = 0; e < 8; ++e) a[e] = 0.f;
        const float c_ = lo2f(cv);
        const int sci = __float_as_int(rbf(c_ * __builtin_amdgcn_rcpf(1.f + __builtin_amdgcn_exp2f(c_ * -1.4426950408889634f))));
#pragma unroll
        for (int i = 0; i < 5; ++i) {
          const float sc = __int_as_float(__builtin_amdgcn_ds_bpermute(((lane & 56) + i) * 4, sci));
#pragma unroll
          for (int e = 0; e < 4; ++e) { a[2 * e] += sc * lo2f(wm[i][e]); a[2 * e + 1] += sc * hi2f(wm[i][e]); }
        }
#pragma unroll
        for (int e = 0; e < 4; ++e) {
          const auto r = __builtin_amdgcn_permlane32_swap(__float_as_uint(a[e]), __float_as_uint(a[e + 4]), false, false);
          a[e] = __uint_as_float((unsigned)r[0]) + __uint_as_float((unsigned)r[1]);
        }
#pragma unroll
        for (int e = 0; e < 2; ++e) {
          const auto r = __builtin_amdgcn_permlane16_swap(__float_as_uint(a[e]), __float_as_uint(a[e + 2]), false, false);
          a[e] = dpp_add(__uint_as_float((unsigned)r[0]) + __uint_as_float((unsigned)r[1]), 0);
        }
        if ((lane & 8) == 0) *(LAS f32x2*)(lds + L_MP + mg * 256 + (lane & 7) * 32 + g * 8) = f32x2{a[0], a[1]};
        SIG(P_M);
        TS_(52);
        if constexpr (TSHI == 23) TX_(32);
        FENCE();
        REQ_Q();
        REQ_D();
        FENCE();
        if (wv == 7) {
          WAIT_HALF(1, P_M);
          f32x2 s = *(const LAS f32x2*)(lds + L_MP + (lane & 31) * 8);
#pragma unroll
          for (int w = 1; w < 4; ++w) s += *(const LAS f32x2*)(lds + L_MP + w * 256 + (lane & 31) * 8);
          st128(u32x4{__float_as_uint(s[0]), tA, __float_as_uint(s[1]), tB}, rW, lane < 32 ? l16 : OOB, O_MP + (unsigned)bx * 512u, 16);
          __builtin_amdgcn_raw_buffer_store_b64(u32x2{tA, tB}, rW, lane == 0 ? (unsigned)bx * 8u : OOB, O_FL1, 16);
        }
        FENCE();
      }
      STAMP(2);
      {
      WAIT_HALF(1, P_K);
      BUILD(SL_A, t8);
      BUILD(SL_B, t8);
      SIG(P_B2);
      if constexpr (TSHI == 23) TX_(33);
      FENCE();
      STAMP(1);
      }
      {
        const unsigned o_ = lane < 50 ? (unsigned)(bx * 4 + mg) * 800u + l16 : OOB;
        dma<1, 0>(o_, dW1, 0u, (unsigned)L_ST4);
        dma<1, 0>(o_, dW2, 0u, (unsigned)L_ST4);
        FENCE();
      }
      FENCE();
      FENCE();
      unsigned tra[10], rto[5];
      {
        const unsigned b3 = rtl + (unsigned)(8 * g + (l15 >> 2)) * 128u + (unsigned)((l15 >> 2) ^ (g & 1)) * 32u + ((unsigned)lane & 3u) * 8u, b4 = rtl + ((unsigned)lane >> 2) * 160u + ((unsigned)lane & 3u) * 8u;
        const unsigned dst = j3 ? O_WT3 + (unsigned)(jcg * 80 + jks) * 1024u : O_WT4 + (unsigned)(jcg * 54 + jks) * 5120u, dstr = j3 ? 20480u : 1024u;
#pragma unroll
        for (int jn = 0; jn < 5; ++jn) {
          tra[jn] = j3 ? b3 ^ (unsigned)(jn & 3) * 32u : b4 + jn * 32u;
          tra[5 + jn] = tra[jn] + (j3 ? 512u : 2560u);
          rto[jn] = (jok && !(j3 && jn == 4)) ? l16 + dst + jn * dstr : OOB;
          PIN(tra[jn]); PIN(tra[5 + jn]); PIN(rto[jn]);
        }
      }
#define RT_TR() do {                                                                                    \
        _Pragma("unroll") for (int jn = 0; jn < 5; ++jn) {                                              \
          const s16x4 lo = __builtin_amdgcn_ds_read_tr16_b64_v4i16((LAS s16x4*)(size_t)tra[jn]);        \
          const s16x4 hi = __builtin_amdgcn_ds_read_tr16_b64_v4i16((LAS s16x4*)(size_t)tra[5 + jn]);    \
          rt[jn] = __builtin_bit_cast(u32x4, bf16x8{lo[0], lo[1], lo[2], lo[3], hi[0], hi[1], hi[2], hi[3]}); \
          SB();                                                                                         \
        }                                                                                               \
      } while (0)
#define RT_OUT() do {                                                                                   \
        _Pragma("unroll") for (int jn = 0; jn < 4; ++jn) st128(rt[jn], rW, rto[jn], 0, 16);             \
        if (!j3) st128(rt[4], rW, rto[4], 0, 16);                                                       \
        FENCE();                                                                                        \
      } while (0)
      SIG(P_U);
      const bool mfin = wv == 7 && bx < 240 && lane < 32;
      const unsigned po = mfin ? ((unsigned)lane >> 3) * 512u + ((unsigned)lane & 7u) * 16u : OOB;
      u32x4 mp4;
#define LD_P() mp4 = __builtin_amdgcn_raw_buffer_load_b128(rW, po, O_MP + (unsigned)(bx & ~3) * 512u + (unsigned)(bx & 3) * 128u, 16)
#define MOD_L2() do {                                                                                   \
      if (wv == 7) {                                                                                    \
        unsigned spins = 0;                                                                             \
        while (__builtin_amdgcn_ballot_w64(mfin && (mp4[1] != tA || mp4[3] != tB)) != 0ull) {           \
          SPIN_STEP();                                                                                  \
          if (spins > RETRY_CAP) break;                                                                 \
          __builtin_amdgcn_s_sleep(USLEEP);                                                             \
          LD_P();                                                                                       \
        }                                                                                               \
        const float one = ((bx >= 40 && bx < 80) || (bx >= 160 && bx < 200)) ? 1.f : 0.f;               \
        const float s0_ = swap_add16(dpp_add(__uint_as_float(mp4[0]), 0)), s1_ = swap_add16(dpp_add(__uint_as_float(mp4[2]), 0)); \
        const unsigned d = pack2(one + rbf(s0_), one + rbf(s1_));                                       \
        const unsigned dn = (unsigned)__builtin_amdgcn_update_dpp(0, (int)d, 0xB1, 0xf, 0xf, true);     \
        st128(u32x4{d, tA, dn, tB}, rW, (lane < 8 && (lane & 1) == 0) ? (unsigned)lane * 8u : OOB, O_MOD + (unsigned)bx * 64u, 16); \
      }                                                                                                 \
      FENCE();                                                                                          \
    } while (0)
      if (wv == 7) LD_P();
      FENCE();
      STAMP(3);
      FENCE();
      asm volatile("" : "+v"(fpa[0]), "+v"(fpa[1]), "+v"(fpa[2]), "+v"(fpv));
      ln5s(qw, hw);
      GET_D();
      if constexpr (TSHI == 23) TX_(34);
      MOD_L2();
      if constexpr (TSHI == 23) TX_(35);
      TS_(27);
      if constexpr (TSHI == 23) TX_(36);
      STAMP(22);
      WAIT_ALL(P_U);
      FENCE();
      RT_LD();
      TS_(4);
      STAMP(28);
      FILL_P(0, 3);
      FENCE();
      VMW(3);
      RT_TR();
      SIG(P_RT);
      TS_(48);
      RT_OUT();
      LD_WD(0, 5);
      WAIT_HALF(1, P_RT);
      LD_WD(5, NGA);
      FENCE();
      VMW(NGA);
      SIG(P_GA);
      TS_(5);
      LD_WD(NGA, 12);
      FENCE();
      if (mg < 2) {
        u32x2 uc[2];
        f32x4 yc[8];
        float v[8];
        uc[0] = *(const LAS u32x2*)(lds + SL_C + CV_U + 512 + (256 * zja + 16 * l15 + 4 * g) * 2);
        uc[1] = *(const LAS u32x2*)(lds + SL_C + CV_U + 512 + (256 * zjb + 16 * l15 + 4 * g) * 2);
        unsigned zq = O_Z + ZOFF(zja, zjb, chC);
        PIN(zq);
        WAIT_HALF(0, P_C2);
#pragma unroll
        for (int c = 0; c < 8; ++c) yc[c] = *(const LAS f32x4*)(lds + SL_C + mg * 2048 + l16 + (c >> 1) * 4096 + (c & 1) * 1024);
        STG(8, v[i] = (i & 1) ? hi2f(uc[i >> 2][(i >> 1) & 1]) : lo2f(uc[i >> 2][(i >> 1) & 1]); PIN(v[i]));
        STG(8, v[i] = __builtin_fmaf(v[i], __uint_as_float(dC), yc[i >> 2][i & 3]); PIN(v[i]));
#pragma unroll
        for (int w = 1; w < 4; ++w) STG(8, v[i] += yc[2 * w + (i >> 2)][i & 3]; PIN(v[i]));
        st128(widen(pack2(v[0], plainv(v[1])), pack2(v[2], v[3]), pack2(v[4], plainv(v[5])), pack2(v[6], v[7])), rW, zq, 0, 16);
      }
      FENCE();
      SIG(P_ZC);
      FENCE();
      LD_WS(0, NS2);
      FENCE();
      if (HPZ) { WAIT_HALF(0, P_X2); __builtin_amdgcn_s_sleep(HPZ); }
      STAMP(15);
      LD_WS(NS2, 10);
      if (mg > 0) {
        WAIT_HALF(1, P_ZC);
#pragma unroll
        for (int j = 0; j < 5; ++j) W_DMA(16 + mg, j);
      }
      LD_WS2U(0, NZ2);
      FENCE();
      STAMP(5);
      TS_(6);
      WAIT_HALF(0, P_X2);
#pragma unroll
      for (int x = 0; x < 10; ++x) *(LAS u32x4*)(lds + (9 + mg + 4 * (x / 5)) * 5120 + (x % 5) * 1024 + l16) = WS[x];
      TS_(1);
      WAIT_HALF(0, P_T);
      R_PRE();
      VMW(NZ2);
      SIG(P_X);
      TS_(2);
      TS_(3);
      WAIT_HALF(0, P_Z);
      LD_WS2U(NZ2, 10);
      FENCE();
      WAIT_2(P_RP, P_X);
#pragma unroll
      for (int j = 0; j < 5; ++j) W_DMA(20 + mg, j);
      FENCE();
      TS_(7);
      STAMP(7);
    } else {
      PRIO(3);
      const unsigned rt0 = (unsigned)__builtin_amdgcn_s_memrealtime();
      unsigned kpa = (unsigned)L_KV + ((unsigned)lane & 7u) * 1312u + (unsigned)mg * 64u + ((unsigned)lane >> 3) * 8u;
      unsigned kpb = (unsigned)L_KV + ((unsigned)l15 >> 1) * 1312u + 256u + (unsigned)mg * 16u + (unsigned)g * 4u + ((unsigned)lane & 1u) * 640u, kpc = kpb + ((unsigned)lane & 1u) * 32u;
      const unsigned cha_ = (unsigned)ch0 + 32u * mg + ((unsigned)lane >> 3) * 4u, chb_ = (unsigned)ch0 + 128u + 8u * mg + 2u * g;
      unsigned koa = KVROW(cha_) + (unsigned)tok0 * 4u + ((unsigned)lane & 7u) * 16u, koa2 = KVROW(cha_ + 2u) + (unsigned)tok0 * 4u + ((unsigned)lane & 7u) * 16u;
      unsigned kob = KVROW(chb_) + (unsigned)tok0 * 4u + (unsigned)l15 * 8u;
      asm volatile("" : "+v"(kpa), "+v"(kpb), "+v"(kpc), "+v"(koa), "+v"(koa2), "+v"(kob));
      u32x4 ug[6];
      unsigned uo = (unsigned)mg * 2048u + l16;
      unsigned udone;
      asm volatile("s_mov_b32 %0, 0" : "=s"(udone));
      unsigned ubA = KVROW((unsigned)chA), ubC = KVROW((unsigned)chC), ubB = KVROW((unsigned)(chA + 1));
      asm volatile("" : "+s"(ubA), "+s"(ubB), "+s"(ubC));
      PIN(uo);
#pragma unroll
      for (int r = 0; r < 6; ++r) asm volatile("" : "=v"(ug[r]));
#define TRY_U(EL, T, CODE)                                                                               \
        asm volatile("s_cmp_lg_u32 %6, 0\n\ts_cbranch_scc1 Lu%=\n\ts_cmp_lt_u32 %[el], %[t]\n\ts_cbranch_scc1 Lu%=\n\t" \
                     "buffer_load_dwordx4 %2, %[vo], %[rs], %[c] offen sc1\n\tbuffer_load_dwordx4 %3, %[vo], %[rs], %[c] offen offset:1024 sc1\n\t" \
                     "buffer_load_dwordx4 %0, %[vo], %[rs], %[a] offen sc1\n\tbuffer_load_dwordx4 %1, %[vo], %[rs], %[a] offen offset:1024 sc1\n\t" \
                     "buffer_load_dwordx4 %4, %[vo], %[rs], %[b] offen sc1\n\tbuffer_load_dwordx4 %5, %[vo], %[rs], %[b] offen offset:1024 sc1\n\t" \
                     "s_mov_b32 %6, %[cd]\n"                                                            \
                     "Lu%=:" : "+v"(ug[0]), "+v"(ug[1]), "+v"(ug[2]), "+v"(ug[3]), "+v"(ug[4]), "+v"(ug[5]), "+s"(udone) \
                     : [el] "s"(EL), [t] "i"(T), [cd] "i"(CODE), [vo] "v"(uo), [rs] "s"(dW), [a] "s"(ubA), [b] "s"(ubB), [c] "s"(ubC) : "scc", "memory")
      VMW(9);
      TAGS();
      unsigned tAB = (lane & 1) ? tB : tA;
      PIN(tAB);
      asm volatile("s_waitcnt vmcnt(0)" : "+v"(sk[0]), "+v"(sk[1]), "+v"(sk[2]));
      const unsigned rt1 = (unsigned)__builtin_amdgcn_s_memrealtime();
      SIG(P_KV);
      FENCE();
      f32x2 wa[3][4];
#pragma unroll
      for (int c = 0; c < 3; ++c)
#pragma unroll
        for (int t = 0; t < 4; ++t) {
          const f32x2 w = (t & 1) ? f32x2{hi2f(sk[c][t >> 1]), hi2f(sk[c][2 + (t >> 1)])} : f32x2{lo2f(sk[c][t >> 1]), lo2f(sk[c][2 + (t >> 1)])};
          wa[c][t] = w;
          PIN(wa[c][t]);
        }
      asm volatile("" : "+v"(tAB));
      FENCE();
      TS_(1);
      STAMP(1);
      WAIT_HALF(0, P_KV);
      if (TU1) TRY_U(rt1 - rt0, TU1, 1);
      if constexpr (TSHI == 23) TX_(31);
      STAMP(12);
      {
        u32x2 kw[7], vw[4];
        unsigned kx[5], vx[2];
#pragma unroll
        for (int i = 0; i < 7; ++i) kw[i] = *(const LAS u32x2*)(lds + kpa + (i < 3 ? 320 + 320 * i : 1312 + 320 * (i - 3)));
#pragma unroll
        for (int i = 0; i < 5; ++i) kx[i] = *(const LAS unsigned*)(lds + ((i == 1 || i == 2) ? kpc : kpb) + (i < 3 ? 320 + 320 * i : 1312 + 320 * (i - 3)));
#pragma unroll
        for (int i = 0; i < 4; ++i) vw[i] = *(const LAS u32x2*)(lds + kpa + 12288 + 320 * i);
#pragma unroll
        for (int i = 0; i < 2; ++i) vx[i] = *(const LAS unsigned*)(lds + kpb + 12288 + 320 * i);
        __builtin_amdgcn_sched_barrier(0);
        f32x2 kf[19], a[10], p[10], t_[10];
#define KX(o, t) kf[((o) >> 2) * 7 + ((o) & 3) + (t)]
#define STR(lo, hi, stmt) do { _Pragma("unroll") for (int i = (lo); i < (hi); ++i) { stmt; } __builtin_amdgcn_sched_barrier(0); } while (0)
#define KV_GROUP(lo, hi) do {                                                                           \
        STR(((lo) >> 2) * 7, (hi) == 10 ? 19 : ((hi) >> 2) * 7, kf[i] = up2(i < 14 ? kw[i % 7][i / 7] : kx[i < 14 ? 0 : i - 14]); PIN(kf[i])); \
        {                                                                                               \
          STR(lo, hi, a[i] = KX(i, 0) * wa[i >> 2][3]; PIN(a[i]));                                      \
          _Pragma("unroll") for (int t = 1; t < 4; ++t) STR(lo, hi, a[i] = FMA2(KX(i, t), wa[i >> 2][3 - t], a[i]); PIN(a[i])); \
          STR(lo, hi, t_[i] = FMA2(a[i], cp, a[i]); PIN(t_[i]));                                        \
          STR(lo, hi, a[i] = FMA2(a[i], cn, t_[i]); PIN(a[i]));                                         \
        }                                                                                               \
        STR(lo, hi, p[i] = up2(i < 8 ? vw[i & 3][i >> 2] : vx[i & 1]); PIN(p[i]));                      \
        STR(lo, hi, a[i] *= p[i]; PIN(a[i]));                                                           \
        if constexpr (TSHI == 23) TX_(32 + (lo) / 2);                                                   \
        _Pragma("unroll") for (int c = (lo) >> 1; c < ((hi) < 8 ? (hi) : 8) >> 1; ++c) {                \
          const int o = (c >> 1) * 4, e = c & 1;                                                        \
          st128(u32x4{pack2(a[o][e], plainv(a[o + 1][e])), tA, pack2(a[o + 2][e], plainv(a[o + 3][e])), tB}, rW, c < 2 ? koa : koa2, e * 8192u, 16); \
        }                                                                                               \
        if ((hi) == 10) { _Pragma("unroll") for (int e = 0; e < 2; ++e) __builtin_amdgcn_raw_buffer_store_b64(u32x2{pack2(a[8][e], plainv(a[9][e])), tAB}, rW, kob, e * 8192, 16); } \
        __builtin_amdgcn_sched_barrier(0);                                                              \
      } while (0)
        KV_GROUP(0, 4);
        if constexpr (TSHI == 23) TX_(33);
        STAMP(5);
        KV_GROUP(4, 10);
        if constexpr (TSHI == 23) TX_(35);
      }
      FENCE();
      const unsigned rt2 = (unsigned)__builtin_amdgcn_s_memrealtime();
      REQ_Q();
      REQ_D();
      FENCE();
      if (TU2) TRY_U(rt2 - rt0, TU2, 2);
      STAMP(2);
      TS_(2);
      WAIT_HALF(1, P_K);
      BUILD(SL_C, t8);
      SIG(P_B);
      if constexpr (TSHI == 23) TX_(36);
      FENCE();
#define LD_U(r) ug[r] = __builtin_amdgcn_raw_buffer_load_b128(rW, uo + ((r) & 1) * 1024, (r) < 2 ? ubA : (r) < 4 ? ubC : ubB, 16)
      u32x4 A2[16], B2[16];
      WAIT_HALF(0, P_B);
      CV_RA(A2, SL_C, 0);
      CV_RA(A2, SL_C, 1);
      W12_G(gw);
#pragma unroll
      for (int j = 0; j < 5; ++j) PIN(gw[j]);
      unsigned zq = Z2OFF(wv);
      unsigned pa = a00 + SL_A - 3648, pb = b00 + SL_A - 64;
      unsigned xw = SL_A + mg * 8192 + l16, xc = SL_C + mg * 4096 + l16;
      asm volatile("" : "+v"(zq), "+v"(pa), "+v"(pb), "+v"(xw), "+v"(xc));
      FENCE();
      if (udone == 0u) while ((unsigned)__builtin_amdgcn_s_memrealtime() - rt0 < (unsigned)TU) __builtin_amdgcn_s_sleep(1);
      STAMP(3);
      TS_(3);
      TRY_U(rt0, 0, 3);
      FENCE();
      if constexpr (TSHI == 23) TX_(37);
      unsigned uw = (unsigned)(SL_A + CV_U + 512 + mg * 1024 + lane * 8);
      PIN(uw);
      WAIT_HALF(1, P_B2);
      asm volatile("s_cmp_eq_u32 %[d], 1\n\ts_cbranch_scc1 Lw%=\n\ts_waitcnt vmcnt(0)\nLw%=:\n\ts_waitcnt vmcnt(9)"
                   : "+v"(ug[0]), "+v"(ug[1]), "+v"(ug[2]), "+v"(ug[3]), "+v"(ug[4]), "+v"(ug[5]) : [d] "s"(udone) : "scc", "memory");
      if constexpr (TSHI == 23) { TX_(38); PIN(ug[5]); __builtin_amdgcn_s_sleep(1); TX_(39); }
      {
#define U_BAD(r) (ug[r][1] != tA || ug[r][3] != tB)
        if (__builtin_expect(__builtin_amdgcn_ballot_w64(U_BAD(0) || U_BAD(1) || U_BAD(2) || U_BAD(3) || U_BAD(4) || U_BAD(5)) != 0ull, 1)) {
          unsigned spins = 0;
          do {
            __builtin_amdgcn_s_sleep(USLEEP);
#pragma unroll
            for (int r = 0; r < 6; ++r) if (U_BAD(r)) LD_U(r);
            SPIN_STEP();
          } while (__builtin_amdgcn_ballot_w64(U_BAD(0) || U_BAD(1) || U_BAD(2) || U_BAD(3) || U_BAD(4) || U_BAD(5)) != 0ull && spins < RETRY_CAP);
          VM0();
        }
#pragma unroll
        for (int r = 2; r < 8; ++r) *(LAS u32x2*)(lds + uw + ((r % 6) >> 1) * CV_SLOT + (r & 1) * 512) = u32x2{ug[r % 6][0], ug[r % 6][2]};
      }
      SIG(P_U);
      TS_(56);
      if constexpr (TSHI == 23) TX_(40);
      STAMP(22);
      GET_D();
      LD_WS2(0, 10);
      FENCE();
      WAIT_HALF(0, P_U);
      CV_RB(B2, SL_C, 0);
      FENCE();
      STAMP(28);
      TS_(4);
      f32x4 acc[8], ac[4];
      u32x4 A[16], B[16];
#define RA(P, rd, i_) *(const LAS u32x4*)(lds + (P) + 3648 - 64 * (rd) - 512 * (i_))
#define RB(P, rd, i_) *(const LAS u32x4*)(lds + (P) + 64 - 64 * (rd) + 512 * (i_))
#define CONV_C(J, lo, hi)                                                                               \
      sfor<lo, hi>([&](auto C_) __attribute__((always_inline)) {                                         \
        constexpr int c = decltype(C_)::v, rd = c / 18, i = cc_i(J, c % 18), t = cc_t(J, c % 18);       \
        ac[t] = mfma16(A2[8 * rd + i], B2[8 * rd + (((J) >> (4 * t)) & 15) - i], (rd == 0 && i == 0) ? f32x4{0.f, 0.f, 0.f, 0.f} : ac[t]); \
        SB();                                                                                           \
        if constexpr (c < 8) B2[15 - c] = RB(pb + CV_SLOT, 1, 7 - c);                                   \
        if constexpr (c >= 18 && c < 34) { constexpr int x = (c - 18) >> 1; if constexpr ((c & 1) == 0) B[7 - x] = RB(pb, 0, 7 - x); else A[x] = RA(pa, 0, x); } \
        SB();                                                                                           \
      });
      if (half == 0) { CONV_C(0x7430, 0, 18) } else { CONV_C(0x6521, 0, 18) }
      SB();
      int half2 = half;
      asm volatile("" : "+s"(half2));
      if (half2 == 0) { CONV_C(0x7430, 18, 36) } else { CONV_C(0x6521, 18, 36) }
      u32x4 A3[16], B3[16];
      f32x4 ab_[8];
      unsigned pk_;
      float ud[16];
      u32x2 u2[4];
      f32x4 ya[8];
      unsigned ga_;
      unsigned pu = (unsigned)(SL_A + CV_U + 512 + (256 * wv + 16 * l15 + 4 * g) * 2), py = (unsigned)(SL_A + wv * 1024) + l16;
      const unsigned f0 = (unsigned)L_CTL + ((unsigned)lane & 3u) * 4u;
      sfor<0, 144>([&](auto M_) __attribute__((always_inline)) {
        constexpr int m = decltype(M_)::v, ch = m / 72, rd = (m / 36) & 1, r = m % 36, i = m < 108 ? cv_i(r) : cl_i(r), dd = m < 108 ? cv_d(r) : cl_t(r) - cl_i(r);
        if constexpr (ch == 0) acc[i + dd] = mfma16(A[8 * rd + i], B[8 * rd + dd], (rd == 0 && i == 0) ? f32x4{0.f, 0.f, 0.f, 0.f} : acc[i + dd]);
        else ab_[i + dd] = mfma16(A3[8 * rd + i], B3[8 * rd + dd], (rd == 0 && i == 0) ? f32x4{0.f, 0.f, 0.f, 0.f} : ab_[i + dd]);
        SB();
        if constexpr (m == 30) { pa += SL_B - SL_A; pb += SL_B - SL_A; asm volatile("" : "+v"(pa), "+v"(pb)); }
        if constexpr (m == 94) { xw += SL_B - SL_A; PIN(xw); }
        if constexpr (r >= RS0 && r < RS0 + 16 && m < 108) {
          constexpr int x = (r - RS0) >> 1;
          if constexpr (m < 36) { if constexpr ((r & 1) == 0) B[15 - x] = RB(pb, 1, 7 - x); else A[8 + x] = RA(pa, 1, x); }
          else if constexpr (m < 72) { if constexpr ((r & 1) == 0) B3[7 - x] = RB(pb, 0, 7 - x); else A3[x] = RA(pa, 0, x); }
          else { if constexpr ((r & 1) == 0) B3[15 - x] = RB(pb, 1, 7 - x); else A3[8 + x] = RA(pa, 1, x); }
        }
        if constexpr (m == 1 || m == 4 || m == 23 || m == 26) { constexpr int c = m == 1 ? 0 : m == 4 ? 1 : m == 23 ? 2 : 3; *(LAS f32x4*)(lds + xc + c * 1024) = ac[c]; }
        if constexpr (m == 28) SIG(P_C2);
        if constexpr (m == 38 || m == 104) pk_ = *(volatile LAS unsigned*)(lds + f0);
        if constexpr (m == 45) NEED_AT(pk_, f0, P_C2);
        if constexpr (m == 111) NEED_AT(pk_, f0, P_A2);
        constexpr int wa = m == 47 ? 0 : m == 54 ? 1 : m == 60 ? 2 : m == 65 ? 3 : m == 69 ? 4 : m == 72 ? 5 : m == 75 ? 6 : m == 77 ? 7 : -1;
        constexpr int wb = m == 113 ? 0 : m == 116 ? 1 : m == 119 ? 2 : m == 122 ? 3 : m == 126 ? 4 : m == 132 ? 5 : m == 139 ? 6 : -1;
        if constexpr (wa >= 0) *(LAS f32x4*)(lds + xw + wa * 1024) = acc[wa];
        if constexpr (wb >= 0) *(LAS f32x4*)(lds + xw + wb * 1024) = ab_[wb];
        if constexpr (m == 95) SIG(P_A2);
        if constexpr (m == 58) PIN(pu);
        if constexpr (m == 59 || m == 61) { constexpr int c = m - 59; u2[c] = *(const LAS u32x2*)(lds + pu + c * 1024); u2[c + 1] = *(const LAS u32x2*)(lds + pu + (SL_B - SL_A) + c * 1024); }
        constexpr int us = cv_slot(m, 0);
        if constexpr (us >= 0 && us < 16) {
#pragma unroll
          for (int o = 2 * us; o < 2 * us + 2; ++o) {
            const int v = (o >> 3) * 4 + (o & 3);
            if ((o & 4) == 0) ud[v] = (v & 1) ? hi2f(u2[v >> 2][(v >> 1) & 1]) : lo2f(u2[v >> 2][(v >> 1) & 1]);
            else ud[v] *= ((v >> 2) & 1) ? hi2f(dAB) : lo2f(dAB);
            PIN(ud[v]);
          }
        }
        if constexpr (m == 110) PIN(py);
        if constexpr (m == 112 || m == 114 || m == 115 || m == 117) { constexpr int c = m == 112 ? 0 : m == 114 ? 2 : m == 115 ? 4 : 6;
          ya[c] = *(const volatile LAS f32x4*)(lds + py + (c >> 2) * 4096 + (c & 3) * 8192); ya[c + 1] = *(const volatile LAS f32x4*)(lds + py + (c >> 2) * 4096 + (c & 3) * 8192 + 8192); }
        constexpr int zs = cv_slot(m, 1);
        if constexpr (zs >= 0 && zs < 16) {
#pragma unroll
          for (int o = 2 * zs; o < 2 * zs + 2; ++o) { const int tl = (o >> 2) & 1, e = o & 3; ud[tl * 8 + e] += ya[4 * tl + (o >> 3)][e]; PIN(ud[tl * 8 + e]); }
        }
        if constexpr (TSHI == 10 && (m % 18 == 0 || m == 143)) TX_(44 + (m + 1) / 18);
        if constexpr (m == 131) ga_ = *(volatile LAS unsigned*)(lds + f0 + 16);
        if constexpr (m == 140) { py += SL_B - SL_A; PIN(py); }
        SB();
      });
      *(LAS f32x4*)(lds + xw + 7 * 1024) = ab_[7];
      SIG(P_X);
      TS_(5);
      STAMP(4);
      u32x4 mg4[4];
#define LD_MG(i) mg4[i] = __builtin_amdgcn_raw_buffer_load_b128(rW, (unsigned)t8 * 16u, O_MOD + (i) * 4096, 16)
      u32x2 f1v;
#define LD_F1() f1v = __builtin_amdgcn_raw_buffer_load_b64(rW, (unsigned)t8 * 8u, O_FL1, 16)
      NEED_AT(ga_, f0 + 16, P_GA);
      {
        unsigned spins = 0;
        for (;;) {
          const unsigned fl = *(volatile LAS unsigned*)(lds + f0);
#pragma unroll
          for (int c = 0; c < 8; ++c) ya[c] = *(const volatile LAS f32x4*)(lds + py + (c >> 2) * 4096 + (c & 3) * 8192);
          if (__builtin_amdgcn_ballot_w64(fl < (unsigned)P_X) == 0ull) break;
          SPIN_STEP();
          if (spins > SPIN_CAP) break;
        }
#pragma unroll
        for (int tl = 0; tl < 2; ++tl) {
          const f32x4 yb = (ya[4 * tl] + ya[4 * tl + 1]) + (ya[4 * tl + 2] + ya[4 * tl + 3]);
          const f32x2 v2 = f32x2{yb[0], yb[1]} + f32x2{ud[8 * tl + 4], ud[8 * tl + 5]}, v3 = f32x2{yb[2], yb[3]} + f32x2{ud[8 * tl + 6], ud[8 * tl + 7]};
          st128(u32x4{pack2(ud[8 * tl], plainv(v2[0])), pack2(ud[8 * tl + 1], v2[1]), pack2(ud[8 * tl + 2], plainv(v3[0])), pack2(ud[8 * tl + 3], v3[1])}, rW, zq + tl * 64u * TS, 0, 16);
        }
      }
      FENCE();
      SIG(P_X2);
      TS_(6);
      LD_F1(); LD_MG(0); LD_MG(1); LD_MG(2); LD_MG(3);
      FENCE();
      LN1();
      STAMP(6);
      u32x4 za, zb, zc = {0u, 0u, 0u, 0u}, zd = {0u, 0u, 0u, 0u};
      const unsigned zo = (unsigned)mt * (4u * TS) + ZCH((unsigned)t8 & ~1u) + (unsigned)nt * 32u + ((unsigned)t8 & 1u) * 16u;
      const unsigned zo1 = (unsigned)mt * (4u * TS) + ZCH(256u + ((unsigned)t8 & ~1u)) + (unsigned)nt * 32u + ((unsigned)t8 & 1u) * 16u;
      const unsigned zo2 = (unsigned)mt * (4u * TS) + ZCH(2u * t8 + 128u) + (unsigned)nt * 16u;
      FENCE();
      {
#define MG_BAD(i) (mg4[i][1] != tA || mg4[i][3] != tB)
#define T_BAD() (__builtin_amdgcn_ballot_w64(f1v[0] != tA || f1v[1] != tB || MG_BAD(0) || MG_BAD(1) || MG_BAD(2) || MG_BAD(3)) != 0ull)
        if (__builtin_expect(T_BAD(), 0)) {
          unsigned spins = 0;
          do {
            __builtin_amdgcn_s_sleep(USLEEP);
            if (f1v[0] != tA || f1v[1] != tB) LD_F1();
#pragma unroll
            for (int i = 0; i < 4; ++i) if (MG_BAD(i)) LD_MG(i);
            SPIN_STEP();
          } while (T_BAD() && spins < RETRY_CAP);
          VM0();
        }
#pragma unroll
        for (int i = 0; i < 4; ++i) {
          const int n = (t8 + 256 * i) * 4;
          if (n < 6 * D_) *(LAS u32x2*)(lds + L_MOD + n * 2) = u32x2{mg4[i][0], mg4[i][2]};
          if (i == 3 && n >= 5 * D_ + n0 && n < 5 * D_ + n0 + 80)
            *(LAS f32x4*)(lds + L_GB + (n - 5 * D_ - n0) * 4) = f32x4{lo2f(mg4[i][0]), hi2f(mg4[i][0]), lo2f(mg4[i][2]), hi2f(mg4[i][2])};
        }
        SIG(P_T);
        if constexpr (TSHI == 23) TX_(41);
      }
      WAIT_HALF(0, P_T);
      if (DLY0) __builtin_amdgcn_s_sleep(DLY0);
      PRIO(3);
      STAMP(7);
      TS_(7);
      za = __builtin_amdgcn_raw_buffer_load_b128(rW, zo, O_Z, 16); zb = __builtin_amdgcn_raw_buffer_load_b128(rW, zo1, O_Z, 16);
      if (wv == 3) { zc = __builtin_amdgcn_raw_buffer_load_b128(rW, zo2, O_Z, 16); zd = __builtin_amdgcn_raw_buffer_load_b128(rW, zo2 + 16u * ZG, O_Z, 16); }
      FENCE();
      SIG(P_Z);
      R_PRE();
      SIG(P_RP);
      unsigned zw0 = (unsigned)(L_XCH + 4 * (t8 & 1) * YROW + 4 * (t8 >> 1)), zw1 = (unsigned)(L_XCH + 256 + 4 * t8);
      asm volatile("" : "+v"(zw0), "+v"(zw1), "+v"(ry), "+v"(rz));
      asm volatile("" : "+s"(w3s[0]), "+s"(w3s[1]));
      unsigned spins = 0;
      for (;;) {
        const bool ba = amax2(za) >= PTHR, bb_ = amax2(zb) >= PTHR, bc_ = amax2(zc) >= PTHR, bd = amax2(zd) >= PTHR;
        if (__builtin_amdgcn_ballot_w64(ba || bb_ || bc_ || bd) == 0ull) break;
        SPIN_STEP();
        if (spins > RETRY_CAP) break;
        __builtin_amdgcn_s_sleep(ZSLEEP);
        if (ba) za = __builtin_amdgcn_raw_buffer_load_b128(rW, zo, O_Z, 16);
        if (bb_) zb = __builtin_amdgcn_raw_buffer_load_b128(rW, zo1, O_Z, 16);
        if (bc_) zc = __builtin_amdgcn_raw_buffer_load_b128(rW, zo2, O_Z, 16);
        if (bd) zd = __builtin_amdgcn_raw_buffer_load_b128(rW, zo2 + 16u * ZG, O_Z, 16);
      }
#pragma unroll
      for (int i = 0; i < 4; ++i) {
        *(LAS unsigned*)(lds + zw0 + i * YROW) = za[i];
        *(LAS unsigned*)(lds + zw0 + i * YROW + 512) = zb[i];
      }
      if (wv == 3) {
#pragma unroll
        for (int i = 0; i < 4; ++i) {
          *(LAS unsigned*)(lds + zw1 + (2 * i) * YROW) = __builtin_amdgcn_perm(zd[i], zc[i], 0x05040100u);
          *(LAS unsigned*)(lds + zw1 + (2 * i + 1) * YROW) = __builtin_amdgcn_perm(zd[i], zc[i], 0x07060302u);
        }
      }
      VM0();
      SIG(P_Z2);
    }
  }
  {
    f32x2 hh[5], t_[5];
    STAMP(8);
    asm volatile("" : "+v"(ry), "+v"(rz));
    asm volatile("" : "+s"(w3s[0]), "+s"(w3s[1]));
    if (kh) PRIO(PR1); else PRIO(PHI);
    WAIT_HALF(0, P_Z2);
    STAMP(9);
    TS_(8);
    unsigned zw[5], xp[5], hp_[5];
    {
      const u32x4 a = *(LAS u32x4*)(lds + ry);
      zw[0] = a[0]; zw[1] = a[1]; zw[2] = a[2]; zw[3] = a[3];
      zw[4] = *(LAS unsigned*)(lds + rz);
    }
    FENCE();
#define RQ3(k) do { LD_W3R((k) == 0 ? 0 : (k) == 1 ? NR0 : NR1, (k) == 0 ? NR0 : (k) == 1 ? NR1 : NWH); FENCE(); } while (0)
    RQ3(0);
    FENCE();
    if constexpr (TSHI == 22) TX_(30);
    STG(5, hh[i] = up2(zw[i]); PIN(hh[i]));
    STG(5, xq[i] = FMA2(hh[i], ha[i], xq[i]); PIN(xq[i]));
    STG(5, t_[i] = FMA2(xq[i], cp, xq[i]); PIN(t_[i]));
    STG(5, xq[i] = FMA2(xq[i], cn, t_[i]); PIN(xq[i]));
    if constexpr (TSHI == 22) TX_(31);
    RQ3(1);
    if constexpr (TSHI == 22) TX_(32);
    float m3;
    {
      const f32x2 s2 = ((xq[0] + xq[1]) + (xq[2] + xq[3])) + xq[4];
      const f32x2 q2 = FMA2(xq[0], xq[0], xq[1] * xq[1]) + FMA2(xq[2], xq[2], FMA2(xq[3], xq[3], xq[4] * xq[4]));
      const f32x2 p2 = FMA2(xq[0], ca[0], xq[1] * ca[1]) + FMA2(xq[2], ca[2], FMA2(xq[3], ca[3], xq[4] * ca[4]));
      float sm3[3] = {s2[0] + s2[1], q2[0] + q2[1], p2[0] + p2[1]};
      wave_sums<3>(sm3);
      const float mean = sm3[0] * (1.f / (float)D_), qs = sm3[1], ps = sm3[2];
      const float m = rbf(mean);
      f32x2 d[5];
#pragma unroll
      for (int j = 0; j < 5; ++j) d[j] = (xq[j] - m) * ca[j];
      const float var = rbf(__builtin_fmaxf(__builtin_fmaf(-mean, mean, qs * (1.f / (float)D_)), 0.f));
      const float den = rbf(__builtin_amdgcn_sqrtf(rbf(var + 1e-5f)));
      const float rinv = __builtin_amdgcn_rcpf(den);
      m3 = rbf(__builtin_fmaf(__builtin_fmaf(-m, mc, ps * (1.f / (float)D_)), rinv, mb));
      __builtin_amdgcn_sched_barrier(0);
      if constexpr (TSHI == 22) TX_(33);
      RQ3(2);
      if constexpr (TSHI == 22) TX_(34);
      STG(5, hh[i] = FMA2(d[i], (f32x2{rinv, rinv}), sb[i]); PIN(hh[i]));
      STG(5, hh[i] -= m3; PIN(hh[i]));
    }
#pragma unroll
    for (int j = 0; j < 5; ++j) hp_[j] = pack2(hh[j][0], (j & 1) ? hh[j][1] : (j < 4 || (lane & 1) == 0) ? codedv(hh[j][1]) : hh[j][1]);
    *(LAS u32x4*)(lds + ry) = u32x4{hp_[0], hp_[1], hp_[2], hp_[3]};
    *(LAS unsigned*)(lds + rz) = hp_[4];
    BARRIER();
    {
      const unsigned tb = (unsigned)(s0 >> 4) * 20480u + (unsigned)(s0 & 15) * 16u + (unsigned)(tid >> 3) * 256u + (unsigned)(tid & 7) * 16u;
      const LAS char* pr = lds + L_XCH + (tid & 7) * YROW + (tid >> 3) * 16;
      const u32x4 v0 = *(const LAS u32x4*)(pr);
      const u32x4 v1 = *(const LAS u32x4*)(pr + (tid < 128 ? 1024 : 0));
      st128(v0, rW, tb, O_HP, 16);
      if (wv < 2) st128(v1, rW, tb + 64u * 256u, O_HP, 16);
    }
    FENCE();
    TS_(9);
    PRIO(1);
#pragma unroll
    for (int j = 0; j < 5; ++j) xp[j] = pack2(xq[j][0], xq[j][1]);
    *(LAS u32x4*)(lds + ry + 8 * YROW) = u32x4{xp[0], xp[1], xp[2], xp[3]};
    *(LAS unsigned*)(lds + rz + 8 * YROW) = xp[4];
    FENCE();
    STAMP(10);
    {
      const f32x2 s2 = ((hh[0] + hh[1]) + (hh[2] + hh[3])) + hh[4];
      const f32x2 q2 = FMA2(hh[0], hh[0], hh[1] * hh[1]) + FMA2(hh[2], hh[2], FMA2(hh[3], hh[3], hh[4] * hh[4]));
      const float mean = wave_sum2(s2[0] + s2[1]) * (1.f / (float)D_), qs = wave_sum2(q2[0] + q2[1]);
      const float var = rbf(__builtin_fmaxf(__builtin_fmaf(-mean, mean, qs * (1.f / (float)D_)), 0.f));
      const float den = rbf(__builtin_amdgcn_sqrtf(rbf(var + 1e-5f)));
      const u32x2 st = {canon(__float_as_uint(m3)), __float_as_uint(den)};
      __builtin_amdgcn_raw_buffer_store_b64(st, rW, lane == 0 ? (unsigned)srow * 8u : OOB, O_ST3, 16);
    }
    {
      BARRIER();
      const unsigned tb = (unsigned)(s0 >> 4) * 20480u + (unsigned)(s0 & 15) * 16u + (unsigned)(tid >> 3) * 256u + (unsigned)(tid & 7) * 16u;
      const LAS char* pr = lds + L_XCH + (8 + (tid & 7)) * YROW + (tid >> 3) * 16;
      st128(*(const LAS u32x4*)(pr), rW, tb, O_XO, 16);
      if (wv >= 6) st128(*(const LAS u32x4*)(pr + 256), rW, tb + 16u * 256u, O_XO, 16);
      SIG(P_L1);
      if (bx == 0 && wv == 5) __builtin_amdgcn_raw_buffer_store_b64(u32x2{(unsigned)epn, (unsigned)(epn >> 32)}, rW, lane == 0 ? 0u : OOB, O_EP, 16);
    }
    FENCE();
    PRIO(0);
    FENCE();
  }

  float sthr = PTHR, scmx = CMAX;
  unsigned scsh = CSH;
  asm volatile("" : "+s"(sthr), "+s"(scsh), "+s"(scmx));
  unsigned xcw = (unsigned)(L_XCH + gm * 5120 + gk * 2048) + l16, xcr = (unsigned)(L_XCH + gm * 5120 + 2048 - gk * 2048) + l16, xcf = (unsigned)(L_CTL + gp * 4);
  asm volatile("" : "+v"(xcw), "+v"(xcr), "+v"(xcf));
  unsigned long long gkm;
  asm volatile("s_cmp_lg_u32 %1, 0\n\ts_cselect_b64 %0, -1, 0" : "=s"(gkm) : "s"(gk) : "scc");
  static_assert(KL >= 3 && KG <= 54 - 2 * KL, "the weights are read in front of the signal");
#define EXCHANGE(ph)                                                                        \
  do {                                                                                      \
    f32x4 e0, e1, e2;                                                                       \
    unsigned spins = 0;                                                                     \
    for (;;) {                                                                              \
      const unsigned fl = *(volatile LAS unsigned*)(lds + xcf);                                                           \
      e0 = *(volatile LAS f32x4*)(lds + xcr);                                               \
      e1 = *(volatile LAS f32x4*)(lds + xcr + 1024);                                        \
      e2 = *(volatile LAS f32x4*)(lds + xcr + 2048);                                        \
      if (__builtin_amdgcn_readfirstlane(fl) >= (ph)) break;                                \
      SPIN_STEP();                                                                          \
      if (spins > SPIN_CAP) break;                                                          \
    }                                                                                       \
    asm volatile("s_nop 3" : "+v"(acc[3]), "+v"(acc[4]));        \
    STG(12, e[i] = (i < 4 ? acc[3][i & 3] + e0[i & 3] : i < 8 ? acc[4][i & 3] + e1[i & 3] : acc[2][i & 3] + e2[i & 3]); PIN(e[i])); \
  } while (0)
#define KLOOP(CD, PB, BLK, VA, AOFF, XG, XS, ph)                                            \
  asm volatile("s_nop 1" : "+v"(A[0]));                                                     \
  sfor<0, 50>([&](auto M_) __attribute__((always_inline)) {                                 \
    constexpr int m = decltype(M_)::v, i = kl_i(m), j = kl_j(m), f = m / 4 + 1, c = m % 4;  \
    mf<i == 0 && (CD || !HFI || j < 2)>(acc[j], __builtin_bit_cast(u32x4, __builtin_shufflevector(Bh[2 * m], Bh[2 * m + 1], 0, 1, 2, 3, 4, 5, 6, 7)), A[i]); \
    SB();                                                                                   \
    constexpr int n0_ = m < KG ? (m * (100 - 2 * (PB))) / KG : 100, n1_ = m < KG ? ((m + 1) * (100 - 2 * (PB))) / KG : 100; \
    if constexpr (n1_ > n0_) RD_BH(2 * (PB) + n0_, BLK);                                    \
    if constexpr (n1_ > n0_ + 1) RD_BH(2 * (PB) + n0_ + 1, BLK);                            \
    static_assert(n1_ <= n0_ + 2 && (f > 9 || kl_i(m + 1) < f), "order");                   \
    if constexpr (f < 10) {                                                                 \
      if constexpr (c == 0) { m_ = CD ? amin2(A[f]) : __builtin_amdgcn_fmed3f(__builtin_fabsf(__uint_as_float(A[f][0])), __builtin_fabsf(__uint_as_float(A[f][2])), scmx); PIN(m_); } \
      if constexpr (c == 1) CMP_BAD(CD, bm, m_);                                            \
      if constexpr (c == 2 && CD) A[f][0] -= scsh;                                          \
      if constexpr (c == 3 && CD) { A[f][2] -= scsh; PIN(A[f]); }                           \
      if constexpr (c == 3) RETRY(CD, A[f], bm, VA, sa[f / 5], AOFF + (f % 5) * 2048, PHI); \
    }                                                                                       \
    if constexpr (m % 5 == 2 || m % 5 == 4) { XS(2 * (m / 5) + (m % 5 == 4)); }             \
    XG(m);                                                                                  \
                                \
    if constexpr (m == 51 - 2 * KL) { asm volatile("s_nop 3" : "+v"(acc[0])); *(volatile LAS f32x4*)(lds + xcw) = acc[0]; } \
    if constexpr (m == 52 - 2 * KL) *(volatile LAS f32x4*)(lds + xcw + 1024) = acc[1];      \
    if constexpr (m == 53 - 2 * KL) asm volatile("s_mov_b64 exec, %2\n\tds_write_b128 %0, %1 offset:2048\n\ts_mov_b64 exec, -1" :: "v"(xcw), "v"(acc[2]), "s"(gkm) : "memory"); \
    if constexpr (m == 54 - 2 * KL) SIG(ph);                                                \
    if constexpr (TSHI == 20 && m % 5 == 0) TX_((CD ? 30 : 44) + m / 5);                    \
    SB();                                                                                   \
  })
  {
    f32x4 acc[5];
    s16x4 Bh[100];
    u32x4 A[10];
    float m_, e[12];
    unsigned long long bm;
    unsigned sa[2] = {ROT ? (unsigned)(nt & 1) * 10240u : 0u, ROT ? 10240u - (unsigned)(nt & 1) * 10240u : 10240u};
    asm volatile("" : "+s"(sa[0]), "+s"(sa[1]));
    const unsigned va = (unsigned)(row >> 4) * 20480u + gk * 1024 + l16;
#define LD_A1(x) A[x] = __builtin_amdgcn_raw_buffer_load_b128(rW, va, sa[(x) / 5] + (O_HP + ((x) % 5) * 2048), 16)
    unsigned bb[3][2];
#pragma unroll
    for (int c = 0; c < 6; ++c) {
      bb[c >> 1][c & 1] = (unsigned)gk * 5120u + ((unsigned)lane >> 2) * 160u + ((unsigned)lane & 3u) * 8u + sa[c & 1] * 5u + (c < 2 ? 96u - 96u * gk : c < 4 ? 64u : 96u * gk);
      PIN(bb[c >> 1][c & 1]);
    }
#define RD_BH(h, BLK) Bh[h] = __builtin_amdgcn_ds_read_tr16_b64_v4i16((LAS s16x4*)(lds + bb[kl_j((h) / 2) < 2 ? 0 : kl_j((h) / 2) == 2 ? 1 : 2][kl_i((h) / 2) / 5] + (BLK) + (kl_i((h) / 2) % 5) * 10240 + (kl_j((h) / 2) % 3 == 1 ? 32 : 0) + ((h) & 1) * 2560))
#define BLK1 0
    FENCE();
#pragma unroll
    for (int n = 0; n < 2 * PBE; ++n) RD_BH(n, BLK1);
    FENCE();
    if (DLY1) __builtin_amdgcn_s_sleep(DLY1);
    PRIO(3);
    TS_(10);
#pragma unroll
    for (int i = 0; i < NA1; ++i) { LD_A1(i); SB(); }
    PRIO(1);
#pragma unroll
    for (int n = 2 * PBE; n < 2 * PB1; ++n) RD_BH(n, BLK1);
    FENCE();
#pragma unroll
    for (int i = NA1; i < NAF; ++i) { LD_A1(i); SB(); }
    u32x2 s3v;
    FENCE();
    if (kh) PRIO(PH1); else PRIO(PHI);
    STAMP(11);
    const int sh = (nt >> 1) == 0 ? 1 : (nt >> 1) == 1 ? -1 : (nt >> 1) == 2 ? 2 : -2;
    const int rr = row + sh, rw = rr & (S_ - 1);
    unsigned keep = rr == rw ? 0xffffffffu : 0u;
    unsigned oh1 = ofr - (unsigned)(row >> 4) * 20480u - (unsigned)l15 * 16u + (unsigned)(rw >> 4) * TS + (unsigned)(rw & 15) * 16u + O_H1;
    asm volatile("" : "+v"(keep), "+v"(oh1));
    asm volatile("" : "+s"(w3s[2]), "+s"(w3s[3]));
    if constexpr (TSHI == 21) { TX_(29); sfor<0, 10>([&](auto I_) __attribute__((always_inline)) { PIN(A[decltype(I_)::v]); TX_(30 + decltype(I_)::v); __builtin_amdgcn_s_sleep(1); }); }
    WAIT_ALL(P_L1);
    SB();
    CHECK_A(true, A[0], 0, NAF, LD_A1);
    SB();
#define XG1(m) do { if constexpr ((m) % 2 == 0 && NAF + (m) / 2 < 10) LD_A1(NAF + (m) / 2); if constexpr ((m) == 13) s3v = __builtin_amdgcn_raw_buffer_load_b64(rW, (unsigned)row * 8u, O_ST3, 16); if constexpr ((m) == 4) TS_(11); } while (0)
#define XSL(u, s0, n, f0) do { if constexpr ((u) >= (s0) && (((u) - (s0) + 1) * (n)) / (20 - (s0)) > (((u) - (s0)) * (n)) / (20 - (s0))) LD_W3U((f0) + (((u) - (s0)) * (n)) / (20 - (s0))); } while (0)
#define XS1(u) XSL(u, SL1, NL1, NWH)
    KLOOP(true, PB1, BLK1, va, O_HP, XG1, XS1, P_XF1);
    TS_(12);
    if (__builtin_expect(bad1(s3v[0]), 0)) {
      unsigned spins = 0;
      do {
        __builtin_amdgcn_s_sleep(RSLEEP);
        s3v = __builtin_amdgcn_raw_buffer_load_b64(rW, (unsigned)row * 8u, O_ST3, 16);
        SPIN_STEP();
      } while (bad1(s3v[0]) && spins < RETRY_CAP);
    }
    const float ld = __uint_as_float(s3v[1]), lr = __builtin_amdgcn_rcpf(ld);
    float gca = lr * GC1, gcb = lr * lr * lr * GC2;
    asm volatile("" : "+v"(gca), "+v"(gcb));
    EXCHANGE((unsigned)P_XF1);
    TS_(13);
    {
      unsigned pk[6];
      if (gk == 0) {
        gelu_s<12>(e, gca, gcb, ld, pk);
        __builtin_amdgcn_raw_buffer_store_b64(u32x2{pk[4] & keep, pk[5] & keep}, rW, oh1 + 1024, 0, 16);
      } else {
        gelu_s<8>(e, gca, gcb, ld, pk);
      }
      st128(widen(pk[0] & keep, pk[1] & keep, pk[2] & keep, pk[3] & keep), rW, oh1 + (unsigned)(g & 1) * 504u, 0, 16);
    }
    FENCE();
    TS_(14);
    PRIO(0);
    LD_W3R(NWH + NL1, NWH + NL1 + NP0);
    FENCE();
    WAIT_ALL(P_XF1);
#pragma unroll
    for (int i = 0; i < 10; ++i) *(LAS u32x4*)(lds + 20480 + wv * 5120 + (i / 5) * 40960 + (i % 5) * 1024 + l16) = WS2[i];
    SIG(P_G1);
    STAMP(13);
    FENCE();
    LD_W3R(NWH + NL1 + NP0, NWH + NL1 + NP1);
    FENCE();
    const unsigned va2 = (unsigned)(row >> 4) * TS + gk * 1024 + l16;
    u32x2 hpv[3];
#define LD_A2(x) A[x] = __builtin_amdgcn_raw_buffer_load_b128(rW, va2, sa[(x) / 5] + (O_H1 + ((x) % 5) * 2048), 16)
    u32x4 hp4;
    const unsigned ofw = ofr + (unsigned)(g & 1) * 504u;
    hp4 = __builtin_amdgcn_raw_buffer_load_b128(rW, ofw, O_HP, 16);
    hpv[2] = __builtin_amdgcn_raw_buffer_load_b64(rW, gk ? OOB : ofr + 1024, O_HP, 16);
    FENCE();
    WAIT_ALL(P_G1);
    if (DLY2) __builtin_amdgcn_s_sleep(DLY2);
    PRIO(3);
    TS_(15);
#pragma unroll
    for (int i = 0; i < NA1; ++i) { LD_A2(i); SB(); }
    PRIO(1);
#define BLK2 20480
#pragma unroll
    for (int n = 0; n < 2 * PB2; ++n) RD_BH(n, BLK2);
    FENCE();
#pragma unroll
    for (int i = NA1; i < NAF; ++i) { LD_A2(i); SB(); }
    FENCE();
    if (kh) PRIO(PH2); else PRIO(PHI);
    float hf[12];
    { const u32x4 n_ = widen(hp4[0], hp4[1], hp4[2], hp4[3]); hpv[0] = u32x2{n_[0], n_[1]}; hpv[1] = u32x2{n_[2], n_[3]}; }
#pragma unroll
    for (int j = 0; j < 3; ++j) { hpv[j][0] -= CSH; PIN(hpv[j]); }
    STG(12, hf[i] = ((i & 1) ? hi2f(hpv[i >> 2][(i >> 1) & 1]) : lo2f(hpv[i >> 2][(i >> 1) & 1])) + __uint_as_float(s3v[0]); PIN(hf[i]));
    acc[3] = f32x4{hf[0], hf[1], hf[2], hf[3]}; acc[4] = f32x4{hf[4], hf[5], hf[6], hf[7]};
    acc[2] = gk ? f32x4{0.f, 0.f, 0.f, 0.f} : f32x4{hf[8], hf[9], hf[10], hf[11]};
    PIN(acc[2]); PIN(acc[3]); PIN(acc[4]);
    STAMP(14);
    if constexpr (TSHI == 21) { TX_(43); sfor<0, 10>([&](auto I_) __attribute__((always_inline)) { PIN(A[decltype(I_)::v]); TX_(44 + decltype(I_)::v); __builtin_amdgcn_s_sleep(1); }); }
    SB();
    CHECK_A(false, A[0], 0, NAF, LD_A2);
    SB();
#define XG2(m) do { if constexpr ((m) % 2 == 0 && NAF + (m) / 2 < 10) LD_A2(NAF + (m) / 2); if constexpr ((m) == 4) TS_(16); } while (0)
#define XS2(u) XSL(u, SL2, NL2, NWH + NL1 + NP1)
    KLOOP(false, PB2, BLK2, va2, O_H1, XG2, XS2, P_XF2);
    TS_(17);
    EXCHANGE((unsigned)P_XF2);
    TS_(18);
    unsigned xk[6];
    {
      if (!HFI) STG(12, e[i] += hf[i]; PIN(e[i]));
      STG(3, e[4 * i + 1] = plainv(e[4 * i + 1]); PIN(e[4 * i + 1]));
      STG(6, xk[i] = pack2(e[2 * i], e[2 * i + 1]); PIN(xk[i]));
      st128(widen(xk[0], xk[1], xk[2], xk[3]), rW, ofw, O_X1, 16);
      if (gk == 0) __builtin_amdgcn_raw_buffer_store_b64(u32x2{xk[4], xk[5]}, rW, ofr + 1024, O_X1, 16);
    }
    FENCE();
    TS_(19);
    PRIO(1);
    SIG(P_G2);
    float v[12];
    float s = 0.f;
#pragma unroll
    for (int j = 0; j < 3; ++j) {
      v[4 * j] = lo2f(xk[2 * j]); v[4 * j + 1] = hi2f(xk[2 * j]); v[4 * j + 2] = lo2f(xk[2 * j + 1]); v[4 * j + 3] = hi2f(xk[2 * j + 1]);
      if (j < 2 || gk == 0) s += (v[4 * j] + v[4 * j + 1]) + (v[4 * j + 2] + v[4 * j + 3]);
    }
    s = swap_add32(swap_add16(s));
    const float mean = s * (gk ? (1.f / 32.f) : (1.f / 48.f));
    float m2 = 0.f;
#pragma unroll
    for (int i = 0; i < 12; ++i) {
      const float dd = v[i] - mean;
      if (i < 8 || gk == 0) m2 += dd * dd;
    }
    m2 = swap_add32(swap_add16(m2));
    {
      u32x2 st = {canon(__float_as_uint(mean)), __float_as_uint(m2)};
      __builtin_amdgcn_raw_buffer_store_b64(st, rW, g == 0 ? (unsigned)(((row >> 4) * 16 + nt * 2 + gk) * 16 + l15) * 8u : OOB, O_ST4, 16);
    }
    FENCE();
    PRIO(0);
    FENCE();
    STAMP(16);
  }
  {
    constexpr int NR3 = 40 - NWH - NL1 - NP1 - NL2;
#define NOW4D(...) __VA_ARGS__
#define LD_W4(f) W4[f] = __builtin_amdgcn_raw_buffer_load_b128(rW, l16, w4a + (f) * 1024, 16)
    const unsigned va4 = O_H + (unsigned)(m0 >> 4) * TS + (unsigned)k4 * 1024u;
#define A4OFF(x) (((x) / NS) * TS + ((x) % NS) * 1024)
#define LD_A4(x) A4[(x) % AW4] = __builtin_amdgcn_raw_buffer_load_b128(rW, l16, va4 + A4OFF(x), 16)
    const int lane0 = lane;
#define LANES() int lane = lane0; PIN(lane); const int l15 = lane & 15, g = lane >> 4; const unsigned l16 = (unsigned)lane * 16u; \
    const unsigned fa = (unsigned)L_CTL + ((unsigned)lane & 7u) * 4u
#define FLG(a) *(volatile LAS unsigned*)(lds + (a))
    auto g3 = [&](auto KH_, u32x4 (&W4)[35], u32x4 (&A4)[AW4], u32x2 (&x1v)[3]) __attribute__((always_inline)) {
      constexpr int KH = decltype(KH_)::v, NS = 7, NW0 = KH ? 6 : AW4;
      LANES();
      const unsigned hw0 = (unsigned)(m0 >> 4) * TS + (unsigned)l15 * 16u + (unsigned)(g >> 1) * 256u + O_H;
      const unsigned csp = (unsigned)(L_CS3 + wv * 128 + g * 16), f1 = (unsigned)L_CTL + 16u + ((unsigned)lane & 3u) * 4u;
      u32x2 stv[4];
#define LD_ST() _Pragma("unroll") for (int a = 0; a < 4; ++a) stv[a] = __builtin_amdgcn_raw_buffer_load_b64(rW, (unsigned)((((m0 >> 4) + mg) * 16 + 4 * g) * 16 + l15) * 8u, O_ST4 + a * 128, 16)
#define ST_BAD() bad1(max(max(stv[0][0], stv[1][0]), max(stv[2][0], stv[3][0])))
      const u32x4 ones = {0x3f803f80u, 0x3f803f80u, 0x3f803f80u, 0x3f803f80u};
      f32x4 c3a = {0.f, 0.f, 0.f, 0.f}, c3b = {0.f, 0.f, 0.f, 0.f};
#define CS3R(a, b) do { _Pragma("unroll") for (int f_ = (a); f_ < (b); f_ += 2) { c3a = mfma16(W3[f_], ones, c3a); if (!KH || f_ < 20) c3b = mfma16(W3[f_ + 1], ones, c3b); } } while (0)
      TS_(20);
      PRIO(PHI);
      LD_W3R(40 - NR3, 40);
      FENCE();
      const unsigned so = O_X1 + (unsigned)mt * 81920u + (unsigned)wv * 10240u, d0 = (unsigned)(KH ? L_XB : L_XA) + (unsigned)mg * 10240u;
      CS3R(0, KH ? KC3B : KC3);
      asm volatile("" : "+v"(c3a), "+v"(c3b));
      FENCE();
      TS_(21);
      WAIT_2(KH ? P_XF2 : P_G2, KH ? P_G2 : P_XF2);
      if (KH ? DLY3B : DLY3) __builtin_amdgcn_s_sleep(KH ? DLY3B : DLY3);
      PIN(W3[38]); PIN(W3[39]);
      if (KH) {
#pragma unroll
        for (int f = 0; f < 10; ++f) W4[f] = W3[21 + 2 * f];
      }
      FENCE();
      PRIO(3);
      TS_(22);
#define STG_DMA() do { dma<4, 1>(l16, dW, so, d0); dma<4, 1>(l16, dW, so + 4096, d0 + 4096); dma<1, 1>(l16, dW, so + 8192, d0 + 8192); dma<1, 1>(l16, dW, so + 9216, d0 + 9216); } while (0)
      if (KH) {
        asm volatile("buffer_load_dwordx2 %0, %4, %5, %6 offen sc1\n\tbuffer_load_dwordx2 %1, %4, %5, %6 offen offset:128 sc1\n\t"
                     "buffer_load_dwordx2 %2, %4, %5, %6 offen offset:256 sc1\n\tbuffer_load_dwordx2 %3, %4, %5, %6 offen offset:384 sc1"
                     : "=&v"(stv[0]), "=&v"(stv[1]), "=&v"(stv[2]), "=&v"(stv[3]) : "v"((unsigned)((((m0 >> 4) + mg) * 16 + 4 * g) * 16 + l15) * 8u), "s"(dW), "s"(O_ST4) : "memory");
      }
      STG_DMA();
      FENCE();
      TS_(23);
      PRIO(PHI);
      CS3R(KH ? KC3B : KC3, 40);
      if (l15 == 0) { *(LAS f32x4*)(lds + csp) = c3a; *(LAS f32x4*)(lds + csp + 64) = c3b; }
      FENCE();
      if (KH) {
        asm volatile("s_waitcnt vmcnt(10)" : "+v"(stv[0]), "+v"(stv[1]), "+v"(stv[2]), "+v"(stv[3]));
        if (__builtin_expect(ST_BAD(), 0)) {
          unsigned spins = 0;
          do {
            __builtin_amdgcn_s_sleep(RSLEEP);
            LD_ST();
            SPIN_STEP();
          } while (ST_BAD() && spins < RETRY_CAP);
        }
        const float m0_ = __uint_as_float(stv[0][0]), q0 = __uint_as_float(stv[0][1]), m1 = __uint_as_float(stv[1][0]), q1 = __uint_as_float(stv[1][1]);
        const float m2_ = __uint_as_float(stv[2][0]), q2 = __uint_as_float(stv[2][1]), m3 = __uint_as_float(stv[3][0]), q3 = __uint_as_float(stv[3][1]);
        float sm = 48.f * (m0_ + m2_) + 32.f * (m1 + m3);
        sm = swap_add32(swap_add16(sm));
        const float mean = sm * (1.f / (float)D_);
        const float d0_ = m0_ - mean, d1 = m1 - mean, d2 = m2_ - mean, d3 = m3 - mean;
        float qs = (q0 + q1) + (q2 + q3) + 48.f * (d0_ * d0_ + d2 * d2) + 32.f * (d1 * d1 + d3 * d3);
        qs = swap_add32(swap_add16(qs));
        const float var = rbf(qs * (1.f / (float)D_));
        const float den = rbf(__builtin_amdgcn_sqrtf(rbf(var + 1e-5f))), r = __builtin_amdgcn_rcpf(den);
        float rc1 = r * GC1;
        PIN(rc1);
        if (g == 0) *(volatile LAS f32x4*)(lds + L_ST4 + (mg * 16 + l15) * 16) = f32x4{rbf(mean), rc1, r * r * r * GC2, den};
        SIG(P_SF);
      }
      FENCE();
      TS_(24);
      VMW(0);
      TS_(25);
      {
        unsigned spins = 0;
        for (;;) {
          u32x4 a[10];
          float mx = 0.f;
#pragma unroll
          for (int i = 0; i < 10; ++i) a[i] = *(volatile LAS u32x4*)(lds + d0 + i * 1024 + l16);
#pragma unroll
          for (int i = 0; i < 10; ++i) mx = amax3(mx, a[i]);
          if (__builtin_expect(__builtin_amdgcn_ballot_w64(mx >= PTHR) == 0ull, 1)) break;
          SPIN_STEP();
          if (spins > RETRY_CAP) break;
          PRIO(0);
          __builtin_amdgcn_s_sleep(RSLEEP);
          STG_DMA();
          PRIO(PHI);
          asm volatile("s_waitcnt vmcnt(0)" ::: "memory");
        }
        SIG(P_SA);
      }
      TS_(26);
      STAMP(17);
      FENCE();
      constexpr int NP = KH ? NPH : NPK, F0 = KH ? 10 : 0, NEQ = NP + 6, EQ0 = KH ? EC0 : EA0, EQ1 = KH ? EC1 : EA1, NL0 = F0 + NEQ, NLT = (KH ? 30 : 35) - NL0;
      const unsigned pks = (unsigned)(KH ? L_PB + NPK * 1024 + mg * (NPH * 1024) : wv < 3 ? wv * (NPK * 1024) : L_PB);
      constexpr auto ecnt = [](int q) { return q <= EQ0 ? 0 : q >= EQ1 ? NEQ : ((q - EQ0) * NEQ + EQ1 - EQ0 - 1) / (EQ1 - EQ0); };
#define ESLOT(q) sfor<ecnt(q), ecnt((q) + 1)>([&](auto U_) __attribute__((always_inline)) { constexpr int u = decltype(U_)::v; \
        if constexpr (u < NP) { NOW4D(dma<1, 1>(l16, dW, w4a + (F0 + 6 + u) * 1024, pks + u * 1024)); } else LD_W4(F0 + u - NP); })
      static_assert(3 * NPK <= 32 && NPK + 4 * NPH <= 36 && MV0 + NP <= 20 && NLT <= 19 && NLT >= MV0, "w4");
      sfor<0, 2>([&](auto P_) __attribute__((always_inline)) {
        constexpr int p = decltype(P_)::v, NV = KH ? 12 : 16;
        TS_(28 + 8 * p + 0);
        if (p == 0) WAIT_2(P_SA, P_G2); else WAIT_HALF(1, P_SA);
        f32x4 acc[4];
        PRIO(KH ? (p ? PK1 : PK2) : PK0);
        TS_(28 + 8 * p + 1);
        u32x4 Ar[AD3][2];
        unsigned ab[2] = {l16 + (unsigned)((p ? L_XB : L_XA) + (KH ? hb * 30720 : 0)), l16 + (unsigned)((p ? L_XB : L_XA) + 20480 - (KH ? hb * 10240 : 0))};
        const unsigned sp = (unsigned)(L_ST4 + p * 512 + l15 * 16), fp = (unsigned)(L_CTL + (wv ^ 2) * 4), xs = (unsigned)(32768 + p * 4096) + l16;
        PIN(ab[0]);
        if (KH) PIN(ab[1]);
        f32x4 cs[3], st[2], rx;
        unsigned sf_, sx_;
#define RD_A3(s, h) Ar[(s) % AD3][h] = *(const LAS u32x4*)(lds + (KH ? ab[h] : ab[0] + (h) * 20480) + (s) * 1024)
#pragma unroll
        for (int s = 0; s < AD3 - 1; ++s) { RD_A3(s, 0); RD_A3(s, 1); }
        SB();
        sfor<0, 20>([&](auto S_) __attribute__((always_inline)) {
          constexpr int s = decltype(S_)::v;
          constexpr bool rd = s + AD3 - 1 < 20, T2 = !KH || s < 10;
          mf<s == 0>(acc[0], W3[2 * s], Ar[s % AD3][0]);
          SB();
          if constexpr (rd) RD_A3(s + AD3 - 1, 0);
          else if constexpr (s == 18) cs[0] = *(const LAS f32x4*)(lds + csp);
          else st[0] = *(const volatile LAS f32x4*)(lds + sp + (KH ? hb * 256 : 0));
          SB();
          mf<s == 0>(acc[1], W3[2 * s], Ar[s % AD3][1]);
          SB();
          if constexpr (rd) RD_A3(s + AD3 - 1, 1);
          else if constexpr (s == 18) cs[1] = *(const LAS f32x4*)(lds + csp + 64);
          else st[1] = *(const volatile LAS f32x4*)(lds + sp + 256 - (KH ? hb * 256 : 0));
          SB();
          if constexpr (T2) { mf<s == 0>(acc[2], W3[2 * s + 1], Ar[s % AD3][0]); SB(); }
          if constexpr (p == 0 && s == 15) sf_ = FLG(f1);
          if constexpr (p == 0 && s == 18) NEED_AT(sf_, f1, P_SF);
          if constexpr (p == 0) ESLOT(s);
          if constexpr (p == 1 && s >= 1 && s <= NLT) LD_W4(NL0 + s - 1);
          if constexpr (p == 1 && s == MV0 - 1) VMW(MV0 + 1);
          SB();
          if constexpr (T2) { mf<s == 0>(acc[3], W3[2 * s + 1], Ar[s % AD3][1]); SB(); }
          if constexpr (KH && s + AD3 == 10) { ab[0] -= hb * 20480; ab[1] -= hb * 20480; PIN(ab[0]); PIN(ab[1]); }
          if constexpr (KH && s == 12) { *(volatile LAS f32x4*)(lds + xs + mg * 1024) = acc[3]; SIG(p ? P_S1 : P_S0); }
          if constexpr (KH && s == 14) sx_ = FLG(fp);
          if constexpr (KH && s == 17) { NEED_AT(sx_, fp, p ? P_S1 : P_S0); rx = *(const volatile LAS f32x4*)(lds + xs + (mg ^ 2) * 1024); cs[2] = *(const volatile LAS f32x4*)(lds + csp + (hb ? -192 : 320)); }
          if constexpr (p == 1 && s >= MV0 && s < MV0 + NP) W4[F0 + 6 + s - MV0] = *(const LAS u32x4*)(lds + pks + l16 + (s - MV0) * 1024);
          if constexpr (p == 1 && s >= 1 && s < 4 - KH) {
            static_assert(H0S >= 4 && H0S < 12, "x1");
            const int tu = KH == 0 ? (s == 3 ? 3 : s - 1) : wv < 6 ? 2 : s == 1 ? wv & 1 : wv - 4, ju = KH == 0 ? wv : wv < 6 ? 2 * wv - 6 - s : 4;
            x1v[s - 1] = *(const LAS u32x2*)(lds + (tu < 2 ? L_XA : L_XB - 40960) + tu * 20480 + (nt * 10 + 2 * ju) * 256 + (g >> 1) * 256 + (g & 1) * 8 + l15 * 16);
          }
          if constexpr (p == 1 && s == H0S) SIG(P_H0);
          if constexpr (s == 9) TS_(28 + 8 * p + 2);
          SB();
        });
        TS_(28 + 8 * p + 3);
        PRIO(KH && p ? PE1 : PE0);
        SIG(p ? P_KB : P_KA);
        if constexpr (KH) asm volatile("s_nop 3" : "+v"(acc[0]), "+v"(acc[1]), "+v"(acc[2])); else asm volatile("s_nop 3" : "+v"(acc[0]), "+v"(acc[1]), "+v"(acc[2]), "+v"(acc[3]));
        STAMP(18 + 2 * p);
        {
          float e[NV], x[NV];
          unsigned pk[NV / 2];
#define SQ(i) st[((i) >> 2) & 1]
#define EPS(q) do { if constexpr (p == 0) { ESLOT(20 + (q)); } if constexpr (p == 1 && (q) == HG - 1) sf_ = FLG(fa); \
                    if constexpr (p == 1 && (q) == HG) { NEED_AT(sf_, fa, P_H0); _Pragma("unroll") for (int x = 0; x < NW0; ++x) { LD_A4(x); } } SB(); } while (0)
          if constexpr (KH) STG(4, x[i] = __builtin_fmaf(-st[0][0], cs[2][i], rx[i]); PIN(x[i]));
          STG(NV, e[i] = __builtin_fmaf(-SQ(i)[0], cs[i >> 3][i & 3], acc[i >> 2][i & 3]); PIN(e[i]));
          if constexpr (KH) STG(4, e[8 + i] += x[i]; PIN(e[8 + i]));
          TS_(28 + 8 * p + 4);
          EPS(0);
          STG(NV, x[i] = e[i] * e[i]; PIN(x[i]));
          EPS(1);
          STG(NV, x[i] = __builtin_fmaf(x[i], SQ(i)[2], SQ(i)[1]); PIN(x[i]));
          EPS(2);
          STG(NV, x[i] *= e[i]; PIN(x[i]));
          EPS(3);
          STG(NV, x[i] = __builtin_amdgcn_exp2f(x[i]); PIN(x[i]));
          TS_(28 + 8 * p + 5);
          EPS(4);
          STG(NV, x[i] = __builtin_fmaf(x[i], SQ(i)[3], SQ(i)[3]); PIN(x[i]));
          EPS(5);
          STG(NV, x[i] = __builtin_amdgcn_rcpf(x[i]); PIN(x[i]));
          EPS(6);
          STG(NV, e[i] *= x[i]; PIN(e[i]));
          TS_(28 + 8 * p + 6);
          EPS(7);
          STG(NV / 4, e[4 * i + 1] = codedv(e[4 * i + 1]); PIN(e[4 * i + 1]));
          STG(NV / 2, pk[i] = pack2(e[2 * i], e[2 * i + 1]); PIN(pk[i]));
          const unsigned hw1 = hw0 + (unsigned)((g & 1) ^ (KH ? hb : 0)) * TS;
          st128(widen(pk[0], pk[1], pk[2], pk[3]), rW, hw1, (unsigned)(sb3 + jl) * 512u + 2 * p * TS, 16);
          if constexpr (KH) __builtin_amdgcn_raw_buffer_store_b64(u32x2{pk[4], pk[5]}, rW, hw0 + (unsigned)(g & 1) * 8u, two ? (unsigned)(sb3 + jm) * 512u + (unsigned)(2 * p + hb) * TS : OOB, 16);
          else st128(widen(pk[4], pk[5], pk[6], pk[7]), rW, hw1, (unsigned)(sb3 + jm) * 512u + 2 * p * TS, 16);
          FENCE();
          EPS(8);
        }
        TS_(28 + 8 * p + 7);
        STAMP(19 + 2 * p);
      });
    };
#define PB(t) (81920 - (t) * 40960)
    const __amdgpu_buffer_rsrc_t rO = __builtin_amdgcn_make_buffer_rsrc((void*)out, 0, SD2, RS3);
    auto g4 = [&](auto KD_, u32x4 (&W4)[35], u32x4 (&A4)[AW4], u32x2 (&x1v)[3]) __attribute__((always_inline)) {
      constexpr int KD = decltype(KD_)::v, NS = KD == 1 ? 6 : 7, PR = KD ? P41 : P40;
      constexpr int GS = NS + (KD == 0 ? GSA : KD == 1 ? GSB : GSC);
      constexpr auto nreq = [](int x) { int n = AW4; for (int y = 0; y <= x; ++y) { int m = y + AW4 < 4 * NS ? y + AW4 : 4 * NS; if (y < GS && m > 2 * NS) m = 2 * NS; n = m < n + 3 ? (m > n ? m : n) : n + 3; } return n; };
      LANES();
      TS_(44);
      if constexpr (KD) {
#pragma unroll
        for (int x = 6; x < AW4; ++x) { LD_A4(x); }
      }
      if constexpr (KD == 2) {
#pragma unroll
        for (int f = 30; f < 35; ++f) { LD_W4(f); }
      }
      FENCE();
      PRIO(PR);
      u32x2 xv[2];
      unsigned fl_, fh_, fu_, pu_;
      f32x4 acc[2][5], rs[4], gq;
      float u_[4], y_[4], m4_;
      unsigned long long bm4;
      unsigned pw = (unsigned)(wv * 5120) + l16;
      PIN(pw);
      const unsigned fo0 = (unsigned)((m0 >> 4) * 20480 + ((n0 + 4 * g) >> 3) * 256 + (g & 1) * 8 + l15 * 16), oo0 = (unsigned)((m0 + l15) * D_ + n0 + 4 * g) * 2u;
#define U_LD(r, t, j) xv[r] = __builtin_amdgcn_raw_buffer_load_b64(rW, fo0, O_XO + (unsigned)((t) * 20480 + (j) * 512), 16)
#define U_PC(q, t, j, y)                                                                               \
  do {                                                                                                  \
    if constexpr ((q) == 0) { fu_ = FLG(fa); pu_ = (unsigned)(PB(t) + (j) * 1024) + l16; PIN(pu_); }    \
    if constexpr ((q) == 1) { gq = *(const LAS f32x4*)(lds + L_GB + ((j) * 16 + 4 * g) * 4); }          \
    if constexpr ((q) >= 1 && (q) < 5) { constexpr int i = (q) - 1; u_[i] = (i & 1) ? hi2f(x1v[y][i >> 1]) : lo2f(x1v[y][i >> 1]); PIN(u_[i]); } \
    if constexpr ((q) == 5) NEED_AT(fu_, fa, P_P0 + 1 + (t));                                           \
    if constexpr ((q) >= 6 && (q) < 10) rs[(q) - 6] = *(const volatile LAS f32x4*)(lds + pu_ + ((q) - 6) * 5120); \
    if constexpr ((q) >= 11 && (q) < 27) { constexpr int w = ((q) - 11) >> 1, h = (((q) - 11) & 1) * 2;  \
      u_[h] += rs[w & 3][h]; PIN(u_[h]); u_[h + 1] += rs[w & 3][h + 1]; PIN(u_[h + 1]);                 \
      if constexpr (h == 2 && w < 4) rs[w] = *(const volatile LAS f32x4*)(lds + pu_ + (4 + w) * 5120); } \
    if constexpr ((q) >= 6 && (q) < 10) { constexpr int i = (q) - 6; y_[i] = (i & 1) ? hi2f(xv[0][i >> 1]) : lo2f(xv[0][i >> 1]); PIN(y_[i]); } \
    if constexpr ((q) == 27 || (q) == 28) { constexpr int h = ((q) - 27) * 2; u_[h] = __builtin_fmaf(u_[h], gq[h], y_[h]); PIN(u_[h]); u_[h + 1] = __builtin_fmaf(u_[h + 1], gq[h + 1], y_[h + 1]); PIN(u_[h + 1]); } \
    if constexpr ((q) == 29) __builtin_amdgcn_raw_buffer_store_b64(u32x2{pack2(u_[0], u_[1]), pack2(u_[2], u_[3])}, rO, oo0, (unsigned)((t) * (16 * D_ * 2) + (j) * 32), OAX); \
  } while (0)
      {
        unsigned long long b0;
        const float m0_ = amin2(A4[0]);
        CMP_BAD(1, b0, m0_);
        A4[0][0] -= scsh; A4[0][2] -= scsh;
        RETRY_C(A4[0], b0, l16, va4, 0, PR);
        asm volatile("s_nop 0" : "+v"(A4[0]));
      }
      SB();
      const int tc = KD == 0 ? 3 : KD == 1 ? 2 : wv - 4, jc = KD == 0 ? wv : KD == 1 ? 2 * wv - 8 : 4;
      sfor<0, 20 * NS>([&](auto M_) __attribute__((always_inline)) {
        constexpr int m = decltype(M_)::v, x = m / 5, j = m % 5, t = x / NS, s = x % NS, r0 = nreq(x - 2), r1 = nreq(x - 1);
        constexpr bool nx = x + 1 < 4 * NS;
        u32x4& an = A4[(x + 1) % AW4];
        mf<s == 0>(acc[t & 1][j], W4[5 * s + j], A4[x % AW4]);
        SB();
        if constexpr (j == 0) {
          if constexpr (x == GS + 1) NEED_AT(fh_, fa, P_H1);
          if constexpr (r0 < r1) { LD_A4(r0); }
        }
        if constexpr (j == 1 && nx) { m4_ = amin2(an); PIN(m4_); }
        if constexpr (j == 2 && nx) { CMP_BAD(1, bm4, m4_); an[0] -= scsh; }
        if constexpr (j == 3 && nx) { an[2] -= scsh; PIN(an); }
        if constexpr (j == 3 && r0 + 1 < r1) { LD_A4(r0 + 1); }
        if constexpr (j == 4) {
          if constexpr (nx) RETRY_C(an, bm4, l16, va4, A4OFF(x + 1), PR);
          if constexpr (r0 + 2 < r1) { LD_A4(r0 + 2); }
          if constexpr (x == GS) fh_ = FLG(fa);
          if constexpr (x == H1S) SIG(P_H1);
          if constexpr (s == 0) TS_(45 + 4 * t);
          if constexpr (s == 3) TS_(45 + 4 * t + 1);
          if constexpr (s == NS - 1) TS_(45 + 4 * t + 2);
          static_assert(GS + 1 < 2 * NS, "order");
          if constexpr (KD == 0 && t == 1 && s == 0) U_LD(0, 0, wv);
          if constexpr (KD == 0 && t == 2 && s == 3) U_LD(0, 1, wv);
          if constexpr (KD == 2 && t == 2 && s == 3) U_LD(0, wv & 1, 4);
          if constexpr (KD == 1 && t == 3 && s == 3) U_LD(0, 2, jc + 1);
          if constexpr (t == 2 && s == 5) U_LD(1, tc, jc);
          if constexpr (t == 3 && s == NS - 2) fl_ = FLG(L_CTL);
        }
        constexpr bool un = (KD == 0 && t >= 2) || (KD == 2 && t == 3);
        constexpr int wq = 5 * s + j, w0 = un ? W0U : 4;
        if constexpr (t > 0 && wq >= w0 && wq < w0 + 5 * WSP && (wq - w0) % WSP == 0) *(volatile LAS f32x4*)(lds + PB(t - 1) + pw + ((wq - w0) / WSP) * 1024) = acc[(t - 1) & 1][(wq - w0) / WSP];
        if constexpr (t > 0 && wq == w0 + 1 + 4 * WSP) SIG(P_P0 + t);
        static_assert(W0U + 1 + 4 * WSP < 35 && U0 + 30 <= 35, "unit");
        if constexpr (un && KD == 0 && wq >= U0) U_PC(wq - U0, t - 2, wv, t - 2);
        if constexpr (un && KD == 2 && wq >= U0) U_PC(wq - U0, wv & 1, 4, 0);
        SB();
      });
      asm volatile("s_nop 3" : "+v"(acc[1][0]), "+v"(acc[1][1]), "+v"(acc[1][2]), "+v"(acc[1][3]), "+v"(acc[1][4]));
#pragma unroll
      for (int j = 0; j < 4; ++j) *(volatile LAS f32x4*)(lds + L_PB + wv * 4096 + l16 + j * 1024) = acc[1][j];
      NEED_AT(fl_, (unsigned)L_CTL, P_P0 + 3);
      *(volatile LAS f32x4*)(lds + PB(0) + pw) = acc[1][4];
      SIG(P_P0 + 4);
      TS_(60);
#define UNIT(r, y, t, j, base, str)                                                                        \
  do {                                                                                                  \
    unsigned pq = (unsigned)(base) + l16, oq = oo0 + (unsigned)((t) * (16 * D_ * 2) + (j) * 32);        \
    f32x4 gq_ = *(const LAS f32x4*)(lds + L_GB + ((j) * 16 + 4 * g) * 4);                               \
    f32x2 xa = up2(xv[r][0]), xb = up2(xv[r][1]), ya = up2(x1v[y][0]), yb = up2(x1v[y][1]);             \
    asm volatile("" : "+v"(xa), "+v"(xb), "+v"(ya), "+v"(yb), "+v"(gq_), "+v"(pq), "+v"(oq));           \
    f32x4 r8[8];                                                                                        \
    unsigned spins = 0;                                                                                 \
    for (;;) {                                                         \
      const unsigned fl = FLG(fa);                                                                      \
      _Pragma("unroll") for (int w = 0; w < 8; ++w) r8[w] = *(const volatile LAS f32x4*)(lds + pq + w * (str)); \
      if (__builtin_amdgcn_ballot_w64(fl < (unsigned)(P_P0 + 1 + (t))) == 0ull) break;                  \
      SPIN_STEP();                                                                                      \
      if (spins > SPIN_CAP) break;                                                                      \
    }                                                                                                   \
    const f32x4 sm = ((r8[0] + r8[1]) + (r8[2] + r8[3])) + ((r8[4] + r8[5]) + (r8[6] + r8[7]));         \
    const f32x2 a_ = FMA2(f32x2{sm[0], sm[1]} + ya, (f32x2{gq_[0], gq_[1]}), xa), b_ = FMA2(f32x2{sm[2], sm[3]} + yb, (f32x2{gq_[2], gq_[3]}), xb); \
    __builtin_amdgcn_raw_buffer_store_b64(u32x2{pack2(a_[0], a_[1]), pack2(b_[0], b_[1])}, rO, oq, 0, OAX); \
    FENCE();                                                                                            \
  } while (0)
      if constexpr (KD == 0) UNIT(1, 2, 3, wv, L_PB + wv * 1024, 4096);
      else UNIT(1, 1, tc, jc, tc == 3 ? PB(0) : PB(2) + jc * 1024, 5120);
      if constexpr (KD == 1) UNIT(0, 0, 2, jc + 1, PB(2) + jc * 1024 + 1024, 5120);
    };
#define KILL(V, N) _Pragma("unroll") for (int f_ = 0; f_ < (N); ++f_) asm volatile("" : "=v"(V[f_]))
    int arm = wv >> 1;
    if (arm < 2) {
      u32x4 W4[35], A4[AW4];
      u32x2 x1v[3];
      g3(IC<0>{}, W4, A4, x1v);
      g4(IC<0>{}, W4, A4, x1v);
      KILL(W3, 40 - NR3);
    }
    asm volatile("" : "+s"(arm));
    if (arm >= 2) {
      u32x4 W4[35], A4[AW4];
      u32x2 x1v[3];
      g3(IC<1>{}, W4, A4, x1v);
      if (arm == 3) {
        g4(IC<2>{}, W4, A4, x1v);
        KILL(W4, 30);
        KILL(A4, 6);
      }
      asm volatile("" : "+s"(arm));
      if (arm == 2) g4(IC<1>{}, W4, A4, x1v);
    }
    STAMP(27);
    CLK(31);
  }
  TS_(63);
  { unsigned gq; asm volatile("s_waitcnt lgkmcnt(0)\n\ts_mov_b32 %0, s98" : "=s"(gq)); if (GW(wv, bx)) while ((unsigned)__builtin_amdgcn_s_memrealtime() - gq < 4000u) __builtin_amdgcn_s_sleep(2); }
}

extern "C" void kernel_launch(void* const* d_in, const int* in_sizes, int n_in, void* d_out, int out_size,
                              void* d_ws, size_t ws_size, hipStream_t stream) {
  const bf16* A_q = (const bf16*)d_in[0];
  const bf16* A_k = (const bf16*)d_in[1];
  const bf16* A_v = (const bf16*)d_in[2];
  const bf16* A_shift_k = (const bf16*)d_in[3];
  const bf16* A_ssm_k = (const bf16*)d_in[4];
  const bf16* A_ssm_D = (const bf16*)d_in[5];
  const bf16* w1 = (const bf16*)d_in[7];
  const bf16* w2 = (const bf16*)d_in[8];
  const bf16* w3 = (const bf16*)d_in[9];
  const bf16* w4 = (const bf16*)d_in[10];
  const bf16* c = (const bf16*)d_in[11];
  const bf16* w_mod = (const bf16*)d_in[12];
  char* ws = (char*)d_ws;
  (void)hipFuncSetAttribute((const void*)k_main, hipFuncAttributeMaxDynamicSharedMemorySize, L_TOT);
  k_main<<<256, 512, L_TOT, stream>>>(A_k, A_v, ws, A_shift_k, A_ssm_k, w_mod, c, w3, w4, A_q, w1, w2, A_ssm_D, (bf16*)d_out);
}
```

```cpp
#include <hip/hip_runtime.h>
#include <hip/hip_bf16.h>
#include <stdint.h>

typedef __hip_bfloat16 bf16;
typedef __attribute__((ext_vector_type(8))) short bf16x8;
typedef __attribute__((ext_vector_type(4))) short s16x4;
typedef __attribute__((ext_vector_type(4))) float f32x4;
typedef __attribute__((ext_vector_type(2))) float f32x2;
typedef __attribute__((ext_vector_type(4))) unsigned u32x4;
typedef __attribute__((ext_vector_type(2))) unsigned u32x2;
#define LAS __attribute__((address_space(3)))

static constexpr int S_ = 2048;
static constexpr int D_ = 640;
static constexpr int F_ = 1728;
static constexpr unsigned SENT = 0xffffffffu;
static constexpr int RS3 = 0x00020000;
static constexpr unsigned OOB = 0x40000000u;

static constexpr unsigned SD2 = S_ * D_ * 2;
static constexpr unsigned DF2 = D_ * F_ * 2;
static constexpr unsigned O_WT3 = 0, O_WT4 = DF2;
static constexpr unsigned O_EP = 2 * DF2;
static constexpr unsigned O_MOD = O_EP + 256;
static constexpr unsigned O_MP = O_MOD + 16384;
static constexpr unsigned O_FL1 = O_MP + 131072;
static constexpr unsigned O_KVT = O_FL1 + 2048;
static constexpr unsigned O_HP = O_KVT, O_X1 = O_KVT;
static constexpr unsigned TS = 57344;
static constexpr unsigned O_H = O_KVT + 384 * 8192;
static constexpr unsigned O_Z = O_H, O_H1 = O_H + 20480;
static constexpr unsigned O_ST3 = O_H + 128 * TS;
static constexpr unsigned O_ST4 = O_ST3 + S_ * 8;
static constexpr unsigned O_XO = O_ST4 + S_ * 16 * 8;
static constexpr unsigned WS_USED = O_XO + SD2;
#define KVROW(c) ((c) < 384u ? O_KVT + (c) * 8192u : O_H + 40960u + (((c) - 384u) >> 1) * TS + ((c) & 1u) * 8192u)
static constexpr unsigned CODED = 0x7f7f7f7fu;
static constexpr unsigned CSH = 98u << 23;
static constexpr float PTHR = 0x1p48f, PMAX = 0x1p47f, CMUL = 0x1p98f, CMAX = 0x1p127f;
__device__ __forceinline__ float plainv(float x) { return __builtin_amdgcn_fmed3f(x, -PMAX, PMAX); }
__device__ __forceinline__ float codedv(float x) { return __builtin_copysignf(__builtin_amdgcn_fmed3f(__builtin_fabsf(x * CMUL), PTHR, CMAX), x); }

__device__ __forceinline__ float rbf(float x) { return __bfloat162float(__float2bfloat16(x)); }
__device__ __forceinline__ float lo2f(unsigned w) { return __uint_as_float(w << 16); }
__device__ __forceinline__ float hi2f(unsigned w) { return __uint_as_float(w & 0xffff0000u); }
typedef __bf16 bf16v2 __attribute__((ext_vector_type(2)));
__device__ __forceinline__ unsigned pack2(float a, float b) {
  bf16v2 r = __builtin_convertvector(f32x2{a, b}, bf16v2);
  return __builtin_bit_cast(unsigned, r);
}

static constexpr float GC1 = -2.f * 0.7978845608028654f * 1.4426950408889634f;
static constexpr float GC2 = GC1 * 0.044715f;
__device__ __forceinline__ float row16_sum(float v) {
  v += __builtin_bit_cast(float, __builtin_amdgcn_update_dpp(0, __builtin_bit_cast(int, v), 0xB1, 0xf, 0xf, true));
  v += __builtin_bit_cast(float, __builtin_amdgcn_update_dpp(0, __builtin_bit_cast(int, v), 0x4E, 0xf, 0xf, true));
  v += __builtin_bit_cast(float, __builtin_amdgcn_update_dpp(0, __builtin_bit_cast(int, v), 0x141, 0xf, 0xf, true));
  v += __builtin_bit_cast(float, __builtin_amdgcn_update_dpp(0, __builtin_bit_cast(int, v), 0x140, 0xf, 0xf, true));
  return v;
}
typedef volatile LAS unsigned* ldsw_t;
static constexpr int CV_STR = 4160;
static constexpr int CV_U = 8 * CV_STR;
static constexpr int CV_SLOT = CV_U + 4608;
static constexpr int SL_A = 46592, SL_C = SL_A + CV_SLOT, SL_B = SL_C + CV_SLOT;
static constexpr int L_KV = 0;
static constexpr int L_RT = L_KV + 24576;
static constexpr int L_MP = L_RT + 20480;
static constexpr int L_XCH = 139264;
static constexpr int L_XA = 81920, L_XB = 40960;
static constexpr int L_PB = 122880;
static constexpr int L_CTL = 160256;
static constexpr int L_MOD = 103424;
static constexpr int L_GB = L_CTL + 64;
static constexpr int L_CS3 = L_CTL + 384;
static constexpr int L_ST4 = L_CTL + 1408;
static constexpr int L_STAMP = L_CTL + 2432;
static constexpr int L_TOT = 163840;
static_assert(L_MP + 1024 <= SL_A && SL_B + CV_SLOT <= L_CTL && L_MOD >= SL_C + 16384 && L_MOD >= 102400 && L_MOD + 7680 <= SL_C + CV_U, "lds");
static constexpr int YROW = 1296;
enum { P_KV = 1, P_K, P_B, P_M, P_B2, P_U, P_C2, P_A2, P_RT, P_GA, P_ZC, P_X, P_X2, P_T, P_Z, P_RP, P_Z2, P_L1, P_XF1, P_G1, P_XF2, P_G2, P_SF, P_SA, P_S0, P_KA, P_H0, P_S1, P_KB, P_H1, P_P0 };
static constexpr unsigned SPIN_CAP = 1u << 21;
static constexpr unsigned RETRY_CAP = 1u << 15;

__device__ __forceinline__ float swap_add16(float v) {
  const unsigned u = __float_as_uint(v);
  auto r = __builtin_amdgcn_permlane16_swap(u, u, false, false);
  return __uint_as_float((unsigned)r[0]) + __uint_as_float((unsigned)r[1]);
}
__device__ __forceinline__ float swap_add32(float v) {
  const unsigned u = __float_as_uint(v);
  auto r = __builtin_amdgcn_permlane32_swap(u, u, false, false);
  return __uint_as_float((unsigned)r[0]) + __uint_as_float((unsigned)r[1]);
}
__device__ __forceinline__ float wave_sum2(float v) { return swap_add32(swap_add16(row16_sum(v))); }
__device__ __forceinline__ f32x2 up2(unsigned u) { return f32x2{lo2f(u), hi2f(u)}; }
__device__ __forceinline__ f32x2 rbf2(f32x2 v) { return up2(pack2(v[0], v[1])); }
#define FMA2 __builtin_elementwise_fma
__device__ __forceinline__ f32x2 rf2(f32x2 v, f32x2 cp, f32x2 cn) { return FMA2(v, cn, FMA2(v, cp, v)); }
__device__ __forceinline__ void ln5(const f32x2* in, unsigned* out) {
  const f32x2 s2 = ((in[0] + in[1]) + (in[2] + in[3])) + in[4];
  const float mean = wave_sum2(s2[0] + s2[1]) * (1.f / (float)D_);
  f32x2 d[5];
#pragma unroll
  for (int j = 0; j < 5; ++j) d[j] = in[j] - mean;
  const f32x2 q2 = (d[0] * d[0] + d[1] * d[1]) + (d[2] * d[2] + d[3] * d[3]) + d[4] * d[4];
  const float m = rbf(mean);
#pragma unroll
  for (int j = 0; j < 5; ++j) d[j] = rbf2(in[j] - m);
  const float q = wave_sum2(q2[0] + q2[1]);
  const float var = rbf(q * (1.f / (float)D_));
  const float den = rbf(__builtin_amdgcn_sqrtf(rbf(var + 1e-5f)));
  const float rinv = __builtin_amdgcn_rcpf(den);
#pragma unroll
  for (int j = 0; j < 5; ++j) { const f32x2 o = d[j] * rinv; out[j] = pack2(o[0], o[1]); }
}
#define PIN(x) asm volatile("" : "+v"(x))
#define STG(N, stmt) do { _Pragma("unroll") for (int i = 0; i < N; ++i) { stmt; } __builtin_amdgcn_sched_barrier(0); } while (0)
__device__ __forceinline__ void ln5s(const unsigned* in, unsigned* out) {
  float x[10], d[10], t[4];
  STG(10, x[i] = (i & 1) ? hi2f(in[i >> 1]) : lo2f(in[i >> 1]); PIN(x[i]));
  STG(4, t[i] = x[(i & 1) + (i >> 1) * 4] + x[(i & 1) + (i >> 1) * 4 + 2]; PIN(t[i]));
  STG(2, t[i] += t[2 + i]; PIN(t[i]));
  STG(2, t[i] += x[8 + i]; PIN(t[i]));
  const float mean = wave_sum2(t[0] + t[1]) * (1.f / (float)D_);
  STG(10, d[i] = x[i] - mean; PIN(d[i]));
  STG(4, t[i] = d[(i & 1) + (i >> 1) * 4] * d[(i & 1) + (i >> 1) * 4]; PIN(t[i]));
  STG(4, t[i] = __builtin_fmaf(d[(i & 1) + (i >> 1) * 4 + 2], d[(i & 1) + (i >> 1) * 4 + 2], t[i]); PIN(t[i]));
  STG(2, t[i] += t[2 + i]; PIN(t[i]));
  STG(2, t[i] = __builtin_fmaf(d[8 + i], d[8 + i], t[i]); PIN(t[i]));
  const float m = rbf(mean);
  STG(10, d[i] = x[i] - m; PIN(d[i]));
  unsigned p[5];
  STG(5, p[i] = pack2(d[2 * i], d[2 * i + 1]); PIN(p[i]));
  const float q = wave_sum2(t[0] + t[1]);
  const float var = rbf(q * (1.f / (float)D_));
  const float den = rbf(__builtin_amdgcn_sqrtf(rbf(var + 1e-5f)));
  const float rinv = __builtin_amdgcn_rcpf(den);
  STG(10, d[i] = ((i & 1) ? hi2f(p[i >> 1]) : lo2f(p[i >> 1])) * rinv; PIN(d[i]));
  STG(5, out[i] = pack2(d[2 * i], d[2 * i + 1]); PIN(out[i]));
}
template <int N>
__device__ __forceinline__ void wave_sums(float* v) {
  STG(N, v[i] += __builtin_bit_cast(float, __builtin_amdgcn_update_dpp(0, __builtin_bit_cast(int, v[i]), 0xB1, 0xf, 0xf, true)); PIN(v[i]));
  STG(N, v[i] += __builtin_bit_cast(float, __builtin_amdgcn_update_dpp(0, __builtin_bit_cast(int, v[i]), 0x4E, 0xf, 0xf, true)); PIN(v[i]));
  STG(N, v[i] += __builtin_bit_cast(float, __builtin_amdgcn_update_dpp(0, __builtin_bit_cast(int, v[i]), 0x141, 0xf, 0xf, true)); PIN(v[i]));
  STG(N, v[i] += __builtin_bit_cast(float, __builtin_amdgcn_update_dpp(0, __builtin_bit_cast(int, v[i]), 0x140, 0xf, 0xf, true)); PIN(v[i]));
  unsigned a[N], b[N];
  STG(N, a[i] = __float_as_uint(v[i]); b[i] = a[i]; PIN(b[i]));
  STG(N, { auto r = __builtin_amdgcn_permlane16_swap(a[i], b[i], false, false); a[i] = r[0]; b[i] = r[1]; PIN(a[i]); PIN(b[i]); });
  STG(N, v[i] = __uint_as_float(a[i]) + __uint_as_float(b[i]); PIN(v[i]));
  STG(N, a[i] = __float_as_uint(v[i]); b[i] = a[i]; PIN(b[i]));
  STG(N, { auto r = __builtin_amdgcn_permlane32_swap(a[i], b[i], false, false); a[i] = r[0]; b[i] = r[1]; PIN(a[i]); PIN(b[i]); });
  STG(N, v[i] = __uint_as_float(a[i]) + __uint_as_float(b[i]); PIN(v[i]));
}
template <int N>
__device__ __forceinline__ void gelu_s(float* e, float ga_, float gb2, float gd, unsigned* pk) {
  float x[N];
  STG(N, x[i] = e[i] * e[i]; PIN(x[i]));
  STG(N, x[i] = __builtin_fmaf(x[i], gb2, ga_); PIN(x[i]));
  STG(N, x[i] *= e[i]; PIN(x[i]));
  STG(N, x[i] = __builtin_amdgcn_exp2f(x[i]); PIN(x[i]));
  STG(N, x[i] = __builtin_fmaf(x[i], gd, gd); PIN(x[i]));
  STG(N, x[i] = __builtin_amdgcn_rcpf(x[i]); PIN(x[i]));
  STG(N, e[i] *= x[i]; PIN(e[i]));
  STG(N / 4, e[4 * i + 1] = plainv(e[4 * i + 1]); PIN(e[4 * i + 1]));
  STG(N / 2, pk[i] = pack2(e[2 * i], e[2 * i + 1]); PIN(pk[i]));
}
__device__ __forceinline__ f32x4 mfma16(u32x4 a, u32x4 b, f32x4 c) {
  return __builtin_amdgcn_mfma_f32_16x16x32_bf16(__builtin_bit_cast(bf16x8, a), __builtin_bit_cast(bf16x8, b), c, 0, 0, 0);
}
template <bool FIRST> __device__ __forceinline__ void mf(f32x4& c, const u32x4& a, const u32x4& b) {
  if (FIRST) asm volatile("v_mfma_f32_16x16x32_bf16 %0, %1, %2, 0" : "=v"(c) : "v"(a), "v"(b));
  else asm volatile("v_mfma_f32_16x16x32_bf16 %0, %1, %2, %0" : "+v"(c) : "v"(a), "v"(b));
}
__device__ __forceinline__ unsigned canon(unsigned x) { return x < 0xfffffffeu ? x : 0xfffffffeu; }
__device__ __forceinline__ bool bad1(unsigned a) { return __builtin_amdgcn_ballot_w64(a == SENT) != 0ull; }
__device__ __forceinline__ float amax2(const u32x4& a) { return __builtin_amdgcn_fmed3f(__builtin_fabsf(__uint_as_float(a[0])), __builtin_fabsf(__uint_as_float(a[2])), CMAX); }
__device__ __forceinline__ float amin2(const u32x4& a) { return __builtin_amdgcn_fmed3f(__builtin_fabsf(__uint_as_float(a[0])), __builtin_fabsf(__uint_as_float(a[2])), 0.f); }
__device__ __forceinline__ float amax3(float m, const u32x4& a) { float r; asm("v_max3_f32 %0, %1, |%2|, |%3|" : "=v"(r) : "v"(m), "v"(a[0]), "v"(a[2])); return r; }
template <bool CD> __device__ __forceinline__ bool any_bad4(u32x4& a, float thr, unsigned csh, float cmx) {
  const float m = __builtin_amdgcn_fmed3f(__builtin_fabsf(__uint_as_float(a[0])), __builtin_fabsf(__uint_as_float(a[2])), CD ? 0.f : cmx);
  if (CD) { a[0] -= csh; a[2] -= csh; }
  return __builtin_amdgcn_ballot_w64(CD ? m < thr : m >= thr) != 0ull;
}
__device__ __forceinline__ u32x4 widen(unsigned x0, unsigned x1, unsigned y0, unsigned y1) {
  const auto p0 = __builtin_amdgcn_permlane16_swap(x0, y0, false, false);
  const auto p1 = __builtin_amdgcn_permlane16_swap(x1, y1, false, false);
  return u32x4{(unsigned)p0[0], (unsigned)p1[0], (unsigned)p0[1], (unsigned)p1[1]};
}
#define DMA1(M) asm volatile("s_mov_b32 m0, %3\n\ts_nop 0\n\t" "buffer_load_dwordx4 %0, %1, %2 offen " M "lds" :: "v"(voff), "s"(rs), "s"(soff), "s"(dst) : "memory")
#define DMA4(M) asm volatile("s_mov_b32 m0, %3\n\ts_nop 0\n\t" \
                   "buffer_load_dwordx4 %0, %1, %2 offen " M "lds\n\tbuffer_load_dwordx4 %0, %1, %2 offen offset:1024 " M "lds\n\t" \
                   "buffer_load_dwordx4 %0, %1, %2 offen offset:2048 " M "lds\n\tbuffer_load_dwordx4 %0, %1, %2 offen offset:3072 " M "lds" \
                   :: "v"(voff), "s"(rs), "s"(soff), "s"(dst) : "memory")
template <int N, int AUX>
__device__ __forceinline__ void dma(unsigned voff, u32x4 rs, unsigned soff, unsigned dst) {
  if (N == 4) { if (AUX == 1) DMA4("sc1 "); else if (AUX == 2) DMA4("nt "); else DMA4(""); }
  else { if (AUX == 1) DMA1("sc1 "); else if (AUX == 2) DMA1("nt "); else DMA1(""); }
}
__device__ __forceinline__ void st128(u32x4 v, __amdgpu_buffer_rsrc_t r, unsigned voff, unsigned base, int) {
  __builtin_amdgcn_raw_buffer_store_b128(v, r, voff + base, 0, 16);
}
__device__ __forceinline__ u32x4 mk_rs(const void* p, unsigned bytes) {
  const unsigned long long a = (unsigned long long)p;
  return u32x4{(unsigned)a, (unsigned)(a >> 32) & 0xffffu, bytes, (unsigned)RS3};
}

#define USLEEP 2
#define DLY0 8
#define DLY1 3
#define DLY2 0
#define DLY3 2
#define NWH 8
#define ZG 8
#define ZSLEEP 2
#define AW4 8
#define AD3 3
#define RSLEEP 1
#define PHI 2
#define CPW 4
#define CPG 128u
#define NZ2 5
#define NS2 5
#define RS0 6
#define DLY3B 0
#define MV0 7
#define H0S 6
#define H1S 3
#define PK0 2
#define PE0 1
#define PK1 2
#define PE1 1
#define P40 1
#define P41 2
#define GSA 2
#define GSB 1
#define GSC 2
#define EA0 4
#define EA1 28
#define EC0 4
#define EC1 22
#define PB1 26
#define PB2 20
#define NL1 14
#define NP1 2
#define NL2 12
#define ROT 1
#define NA1 3
#define NP0 0
#define SL1 6
#define SL2 6
#define PH1 2
#define PH2 2
#define KG 42
#define KC3 12
#define PK2 2
#define KC3B 40
#define NPK 10
#define HG 4
#define WSP 3
#define PR1 3
#define HPZ 8
#define WS3 1
#define RB1 0
#define HFI 1
#define OAX 0
#define MGE 0
#define TZ 0
#define KE 0
#define TU1 200
#define TU2 260
#define TU 290
#define KL 5
#define PBE 18
#define NAF 6
#define W0U 4
#define U0 5
#define NPH 6
#define NGA 5
#define NR0 2
#define NR1 5
#define KVR 0
static_assert((NWH + NL1 + NP1) % 2 == 0 && NL2 % 2 == 0 && NWH + NL1 + NP1 + NL2 <= 40, "w3 schedule");
#define SPIN_STEP() do { ++spins; asm volatile("" : "+s"(spins)); } while (0)
#define SB() __builtin_amdgcn_sched_barrier(0)
#define FENCE() do { asm volatile("" ::: "memory"); __builtin_amdgcn_sched_barrier(0); } while (0)
#define LGKM0() do { __builtin_amdgcn_sched_barrier(0); asm volatile("s_waitcnt lgkmcnt(0)" ::: "memory"); } while (0)
#define BARRIER() do { LGKM0(); __builtin_amdgcn_s_barrier(); asm volatile("" ::: "memory"); } while (0)
#define VM0() __builtin_amdgcn_s_waitcnt(0x0F70)
#define PRIO(n) __builtin_amdgcn_s_setprio(n)
#define CHECK_A(CD, AV, i, hi, LD)                                                          \
  do {                                                                                      \
    if (__builtin_expect(any_bad4<CD>(AV, sthr, scsh, scmx), 0)) {                          \
      unsigned spins = 0;                                                                   \
      PRIO(0);                                                                              \
      do {                                                                                  \
        __builtin_amdgcn_s_sleep(RSLEEP);                                                   \
        _Pragma("unroll") for (int y_ = (i); y_ < (hi); ++y_) { LD(y_); }                   \
        SPIN_STEP();                                                                        \
      } while (any_bad4<CD>(AV, sthr, scsh, scmx) && spins < RETRY_CAP);                    \
      PRIO(PHI);                                                                            \
      VM0();                                                                                \
    }                                                                                       \
  } while (0)

#define ASM_MACROS \
  ".macro regno op\n.set r_, 0\n.set st_, 0\n.irpc c, \"\\op\"\n.ifc \\c, :\n.set st_, 2\n.endif\n.if st_ == 1\n.set r_, r_ * 10 + \\c\n.endif\n.ifc \\c, [\n.set st_, 1\n.endif\n.endr\n.endm\n" \
  ".macro retry n, cd, a, so, ct, t, vo, rs, sb, im, th, k, pr\n"                                   \
  "s_cbranch_vccnz Lc\\n\nLb\\n:\n.subsection 1\nLc\\n:\nregno \\a\n"              \
  "s_setprio 0\ns_add_u32 \\so, \\sb, \\im\ns_mov_b32 \\ct, 0\n"                                        \
  "Ll\\n:\ns_sleep 1\nbuffer_load_dwordx4 \\a, \\vo, \\rs, \\so offen sc1\ns_add_u32 \\ct, \\ct, 1\ns_waitcnt vmcnt(0)\n" \
  ".if \\cd\nv_med3_f32 \\t, |v[r_]|, |v[r_+2]|, 0\nv_cmp_gt_f32 vcc, \\th, \\t\n.else\nv_med3_f32 \\t, |v[r_]|, |v[r_+2]|, \\k\nv_cmp_le_f32 vcc, \\th, \\t\n.endif\n" \
  "s_cbranch_vccz Ld\\n\ns_cmp_lt_u32 \\ct, 0x8000\ns_cbranch_scc1 Ll\\n\n"                             \
  "Ld\\n:\n.if \\cd\nv_subrev_u32 v[r_], \\k, v[r_]\nv_subrev_u32 v[r_+2], \\k, v[r_+2]\n.endif\ns_setprio \\pr\ns_nop 1\ns_branch Lb\\n\n.subsection 0\n.endm\n" \
  ".macro need n, v, ct, ad, k\n"                                                                       \
  "v_cmp_gt_u32 vcc, \\k, \\v\ns_cbranch_vccnz Lc\\n\nLb\\n:\n.subsection 1\nLc\\n:\ns_mov_b32 \\ct, 0\n" \
  "Ll\\n:\nds_read_b32 \\v, \\ad\ns_waitcnt lgkmcnt(0)\nv_cmp_gt_u32 vcc, \\k, \\v\ns_cbranch_vccz Lb\\n\n" \
  "s_sleep 1\ns_add_u32 \\ct, \\ct, 1\ns_cmp_lt_u32 \\ct, 0x200000\ns_cbranch_scc1 Ll\\n\ns_branch Lb\\n\n.subsection 0\n.endm\n"
#define RETRY(CD, AV, BM, VOFF, SBASE, IMM, PR)                                                         \
  do {                                                                                                  \
    unsigned so_, ct_; float t_; register unsigned long long vc_ asm("vcc") = (BM);                                                                     \
    asm volatile("retry %=, %5, \"%0\", %1, %2, %3, %6, \"%7\", %8, %9, %10, %11, %12"                   \
                 : "+v"(AV), "=&s"(so_), "=&s"(ct_), "=&v"(t_), "+s"(vc_)                            \
                 : "i"((int)(CD)), "v"(VOFF), "s"(dW), "s"(SBASE), "i"(IMM), "s"(sthr), "s"((CD) ? scsh : __float_as_uint(scmx)), "i"(PR) : "scc", "memory"); \
  } while (0)
#define CMP_BAD(CD, BM, M) do { register unsigned long long vc_ asm("vcc");                             \
    if (CD) asm volatile("v_cmp_gt_f32 vcc, %1, %2" : "=s"(vc_) : "s"(sthr), "v"(M)); else asm volatile("v_cmp_le_f32 vcc, %1, %2" : "=s"(vc_) : "s"(sthr), "v"(M)); \
    BM = vc_; } while (0)
#define RETRY_C(AV, BM, VOFF, SBASE, IMM, PR) RETRY(1, AV, BM, VOFF, SBASE, IMM, PR)
#define NEED_AT(V, ADDR, K) do { unsigned ct_; asm volatile("need %=, %0, %1, %2, %3" : "+v"(V), "=&s"(ct_) : "v"(ADDR), "s"(K) : "vcc", "scc", "memory"); } while (0)

#define STAMP(i)
#define CLK(i)
#define KATTR
#define TS_(i)
#define TX_(i)
#define TSHI 62
__device__ __forceinline__ u32x4 rd_tr(const LAS char* p) {
  const s16x4 lo = __builtin_amdgcn_ds_read_tr16_b64_v4i16((LAS s16x4*)p);
  const s16x4 hi = __builtin_amdgcn_ds_read_tr16_b64_v4i16((LAS s16x4*)(p + 2560));
  const bf16x8 o = {lo[0], lo[1], lo[2], lo[3], hi[0], hi[1], hi[2], hi[3]};
  return __builtin_bit_cast(u32x4, o);
}
__device__ __forceinline__ float dpp_add(float v, int ctrl) {
  return v + __builtin_bit_cast(float, ctrl == 0 ? __builtin_amdgcn_update_dpp(0, __builtin_bit_cast(int, v), 0x128, 0xf, 0xf, true)
                                    : ctrl == 1 ? __builtin_amdgcn_update_dpp(0, __builtin_bit_cast(int, v), 0x124, 0xf, 0xf, true)
                                                : __builtin_amdgcn_update_dpp(0, __builtin_bit_cast(int, v), 0x4E, 0xf, 0xf, true));
}

template <int V> struct IC { static constexpr int v = V; };
template <int I, int N, class F> __device__ __forceinline__ void sfor(F&& f) { if constexpr (I < N) { f(IC<I>{}); sfor<I + 1, N>(f); } }
constexpr int cv_i(int m) { int i = 0; while (m >= 8 - i) { m -= 8 - i; ++i; } return i; }
constexpr int cv_d(int m) { int i = 0; while (m >= 8 - i) { m -= 8 - i; ++i; } return 7 - i - m; }
constexpr int kl_i(int m) { return m < 50 - 5 * KL ? m / 5 : m < 50 - 2 * KL ? 10 - KL + (m - 50 + 5 * KL) / 3 : 10 - KL + (m - 50 + 2 * KL) / 2; }
constexpr int kl_j(int m) { return m < 50 - 5 * KL ? m % 5 : m < 50 - 2 * KL ? (m - 50 + 5 * KL) % 3 : 3 + (m - 50 + 2 * KL) % 2; }
constexpr int cl_t(int m) { int t = 0; while (m > t) { m -= t + 1; ++t; } return t; }
constexpr int cl_i(int m) { int t = 0; while (m > t) { m -= t + 1; ++t; } return m; }
constexpr bool cv_free(int m, int k) {
  return k == 0 ? (m >= 64 && m < 111 && m != 65 && m != 69 && m != 72 && m != 75 && m != 77 && m != 94 && m != 95 && m != 104 && m != 110 && (m < 72 + RS0 || m >= 88 + RS0))
                : (m >= 123 && m != 126 && m != 131 && m != 132 && m != 139 && m != 140);
}
constexpr int cv_slot(int m, int k) { if (!cv_free(m, k)) return -1; int n = 0; for (int y = 0; y < m; ++y) n += cv_free(y, k); return n; }
static_assert(cv_slot(109, 0) >= 15 && cv_slot(143, 1) >= 15, "slots");
constexpr int cc_i(int J, int c) { for (int i = 0; i < 8; ++i) for (int t = 3; t >= 0; --t) if (i <= ((J >> (4 * t)) & 15) && c-- == 0) return i; return 0; }
constexpr int cc_t(int J, int c) { for (int i = 0; i < 8; ++i) for (int t = 3; t >= 0; --t) if (i <= ((J >> (4 * t)) & 15) && c-- == 0) return t; return 0; }
#define KWAIT
__global__ __launch_bounds__(512, 2) KATTR void k_main(const bf16* kin_, const bf16* vin_, char* ws_, const bf16* shift_k_, const bf16* ssm_k_,
                                                 const bf16* w_mod_, const bf16* cin_, const bf16* w3_,
                                                 const bf16* w4_, const bf16* q_, const bf16* w1_, const bf16* w2_,
                                                 const bf16* ssm_D_, bf16* out_) {
  typedef __attribute__((ext_vector_type(8))) unsigned long long u64x8;
  typedef __attribute__((ext_vector_type(4))) unsigned long long u64x4;
  typedef __attribute__((ext_vector_type(2))) unsigned long long u64x2;
  u64x8 ka;
  u64x4 kb;
  u64x2 kc;
  int tid_ = threadIdx.x, bx_ = blockIdx.x;
  unsigned long long pc0;
  unsigned code_bytes;
  asm volatile("s_load_dwordx16 %0, %6, 0x0\n\ts_load_dwordx8 %1, %6, 0x40\n\ts_load_dwordx4 %2, %6, 0x60\n\ts_getpc_b64 %3\n"
               "Lpc0:\n\ts_mov_b32 %4, Lcold0 - Lpc0\n" KWAIT ".subsection 1\nLcold0:\n.subsection 0"
               : "=&s"(ka), "=&s"(kb), "=&s"(kc), "=&s"(pc0), "=&s"(code_bytes), "+s"(bx_) : "s"(__builtin_amdgcn_kernarg_segment_ptr()));
  asm volatile(ASM_MACROS ".subsection 2\n.fill 256, 4, 0xbf800000\n.subsection 0" : "+v"(tid_));
  extern __shared__ __attribute__((aligned(16))) char lds_[];
  LAS char* lds = (LAS char*)lds_;
  ldsw_t LW = (ldsw_t)(lds + L_CTL);
  const int tid = tid_ & 511, lane = tid & 63;
  const int wv = __builtin_amdgcn_readfirstlane(tid >> 6) & 7;
  const int bx = bx_ & 255;
  const int mt = (bx & 7) * 4 + (bx >> 6), nt = (bx >> 3) & 7;
  const int n0 = nt * 80, m0 = mt * 64;
  const int l15 = lane & 15, g = lane >> 4;
  const int mg = wv & 3, kh = wv >> 2;
  const int gm = (wv >> 2) * 2 + (wv & 1), gk = ((wv >> 1) ^ (wv >> 2)) & 1, gp = wv ^ 2;
  const unsigned l16 = (unsigned)lane * 16u;
  TS_(0);
  STAMP(0);
  CLK(30);
  unsigned cp0 = 0;
  if (wv == CPW) {
    asm volatile("buffer_load_dword %0, %1, %2, 0 offen" : "=v"(cp0) : "v"((unsigned)((bx >> 3) + 32 * lane) * CPG), "s"(mk_rs((const void*)pc0, code_bytes)) : "memory");
  }
  FENCE();
  const int tok0 = 32 * ((bx & 7) * 8 + (bx >> 5)), ch0 = 160 * ((bx >> 3) & 3);
  unsigned ko[3], vo[3];
#pragma unroll
  for (int i = 0; i < 3; ++i) {
    const unsigned s = (unsigned)((mg + 4 * i) * 64 + lane);
    const unsigned gr = __umul24(s, 800u) >> 16, w = s - __umul24(82u, gr), rr = __umul24(w, 205u) >> 12;
    unsigned o = (unsigned)(tok0 * (D_ * 2) + ch0 * 2) + __umul24(gr, 4u * D_ * 2) + __umul24(rr, D_ * 2 - 320) + w * 16u;
    asm volatile("" : "+v"(o));
    ko[i] = (w < 80u && gr < 9u) ? o - 4u * D_ * 2 : OOB;
    vo[i] = (w < 80u && gr < 8u) ? o : OOB;
    asm volatile("" : "+v"(ko[i]), "+v"(vo[i]));
  }
  FENCE();
  asm volatile("s_waitcnt lgkmcnt(0)" : "+s"(ka), "+s"(kb), "+s"(kc));
  const bf16* kin = (const bf16*)ka[0];
  const bf16* vin = (const bf16*)ka[1];
  char* ws = (char*)ka[2];
  const bf16* shift_k = (const bf16*)ka[3];
  const bf16* ssm_k = (const bf16*)ka[4];
  const bf16* w_mod = (const bf16*)ka[5];
  const bf16* cin = (const bf16*)ka[6];
  const bf16* w3 = (const bf16*)ka[7];
  const bf16* w4 = (const bf16*)kb[0];
  const bf16* q = (const bf16*)kb[1];
  const bf16* w1 = (const bf16*)kb[2];
  const bf16* w2 = (const bf16*)kb[3];
  const bf16* ssm_D = (const bf16*)kc[0];
  bf16* out = (bf16*)kc[1];
  const __amdgpu_buffer_rsrc_t rW = __builtin_amdgcn_make_buffer_rsrc((void*)ws, 0, WS_USED, RS3);
  const u32x4 dW = mk_rs(ws, WS_USED);
  u32x2 ep;
  asm volatile("buffer_load_dwordx2 %0, off, %1, %2 sc1" : "=v"(ep) : "s"(dW), "s"(O_EP) : "memory");
  FENCE();
  if (kh == 0) {
    const u32x4 dK = mk_rs(kin, SD2), dV = mk_rs(vin, SD2);
#pragma unroll
    for (int i = 0; i < 3; ++i) dma<1, 0>(ko[i], dK, 0u, (unsigned)(L_KV + (mg + 4 * i) * 1024));
#pragma unroll
    for (int i = 0; i < 3; ++i) dma<1, 0>(vo[i], dV, 0u, (unsigned)(L_KV + 12288 + (mg + 4 * i) * 1024));
  }
  u32x4 sk[3];
  if (kh == 0) {
    asm volatile("buffer_load_dwordx4 %0, %3, %5, %6 offen\n\tbuffer_load_dwordx4 %1, %3, %5, %6 offen offset:16\n\tbuffer_load_dwordx4 %2, %4, %5, %7 offen"
                 : "=&v"(sk[0]), "=&v"(sk[1]), "=&v"(sk[2])
                 : "v"(((unsigned)lane >> 3) * 32u), "v"((unsigned)g * 16u), "s"(mk_rs(shift_k, D_ * 8)), "s"(ch0 * 8 + mg * 256), "s"(ch0 * 8 + 1024 + mg * 64) : "memory");
  }
  FENCE();
  if (tid >= 504) LW[tid - 504] = 0u;
  if (tid >= 256 && tid < 352) *(LAS u32x4*)(lds + SL_A + ((tid - 256) >> 5) * CV_SLOT + CV_U + (tid & 31) * 16) = u32x4{0u, 0u, 0u, 0u};
  if (tid >= 384 && tid < 408) *(LAS u32x4*)(lds + SL_A + ((tid - 384) >> 3) * CV_SLOT + (tid & 7) * CV_STR + 257 * 16) = u32x4{0u, 0u, 0u, 0u};
  STAMP(29);
  if constexpr (TSHI == 23) TX_(30);
  BARRIER();

  const unsigned psig = (unsigned)L_CTL + (unsigned)wv * 4u;
#define SIG(k) do { unsigned long long sv_; asm volatile("s_mov_b64 %0, exec\n\ts_mov_b64 exec, 1\n\tds_write_b32 %1, %2\n\ts_mov_b64 exec, %0" : "=&s"(sv_) : "v"(psig), "v"((unsigned)(k)) : "memory"); } while (0)
#define WAIT_AT(addr, k)                                                                                \
  do {                                                                                                  \
    unsigned tv_, tc_;                                                                                  \
    asm volatile("s_mov_b32 %1, 0\n"                                                                    \
                 "L%=_a:\n\tds_read_b32 %0, %2\n\ts_waitcnt lgkmcnt(0)\n\tv_cmp_gt_u32 vcc, %3, %0\n\ts_cbranch_vccz L%=_b\n\t"    \
                 "s_sleep 1\n\ts_add_u32 %1, %1, 1\n\ts_cmp_lt_u32 %1, 0x200000\n\ts_cbranch_scc1 L%=_a\n"            \
                 "L%=_b:" : "=&v"(tv_), "=&s"(tc_) : "v"(addr), "i"(k) : "vcc", "scc", "memory");       \
  } while (0)
#define WAIT_2(k0, k1)                                                                                 \
  do {                                                                                                  \
    unsigned tv_, tc_;                                                                                  \
    asm volatile("s_mov_b32 %1, 0\n"                                                                    \
                 "L%=_a:\n\tds_read_b32 %0, %2\n\ts_waitcnt lgkmcnt(0)\n\tv_cmp_gt_u32 vcc, %3, %0\n\ts_cbranch_vccz L%=_b\n\t"    \
                 "s_sleep 1\n\ts_add_u32 %1, %1, 1\n\ts_cmp_lt_u32 %1, 0x200000\n\ts_cbranch_scc1 L%=_a\n"            \
                 "L%=_b:" : "=&v"(tv_), "=&s"(tc_) : "v"((unsigned)L_CTL + ((unsigned)lane & 7u) * 4u), "v"((lane & 4) ? (unsigned)(k1) : (unsigned)(k0)) : "vcc", "scc", "memory"); \
  } while (0)
#define WAIT_ALL(k) WAIT_AT((unsigned)L_CTL + ((unsigned)lane & 7u) * 4u, k)
#define WAIT_HALF(h, k) WAIT_AT((unsigned)L_CTL + ((unsigned)lane & 3u) * 4u + (unsigned)(h) * 16u, k)

  const int sb3 = nt < 6 ? 14 * nt : 84 + 12 * (nt - 6);
  const bool two = kh == 0 || nt < 6;
  const int jl = kh ? 4 + wv : 2 * wv, jm = kh ? 12 + (wv & 1) : 2 * wv + 1, hb = wv > 5 ? 1 : 0;
  const int k4 = 7 * wv - (wv > 5 ? 2 : wv > 4 ? 1 : 0);
  const unsigned w4a = O_WT4 + (unsigned)(nt * 54 + k4) * 5120u;
  unsigned w3s[4] = {O_WT3 + (unsigned)(sb3 + jl) * 20480u + (unsigned)hb * 10240u, two ? O_WT3 + (unsigned)(sb3 + jm) * 20480u + (unsigned)hb * 10240u : OOB,
                     O_WT3 + (unsigned)(sb3 + jl) * 20480u - (unsigned)hb * 10240u, kh ? w4a - 10240u : O_WT3 + (unsigned)(sb3 + jm) * 20480u};
  f32x2 cp = {65536.f, 65536.f}, cn = {-65536.f, -65536.f};
  asm volatile("" : "+s"(cp), "+s"(cn));
  u32x4 W3[40];
#define LD_W3U(f) W3[f] = __builtin_amdgcn_raw_buffer_load_b128(rW, l16 + ((f) < 8 ? ((f) >> 1) * 1024 : 0), w3s[((f) & 1) + ((f) >= 20 ? 2 : 0)] + ((f) < 8 ? 0 : ((f) >> 1) * 1024), 16)
#define LD_W3R(a, b) do { _Pragma("unroll") for (int f_ = (a); f_ < (b); ++f_) LD_W3U(f_); } while (0)
#define VMW(n) asm volatile("s_waitcnt vmcnt(%0)" :: "i"(n) : "memory")
  const __amdgpu_buffer_rsrc_t rW1 = __builtin_amdgcn_make_buffer_rsrc((void*)w1, 0, D_ * D_ * 2, RS3);
  const __amdgpu_buffer_rsrc_t rW2 = __builtin_amdgcn_make_buffer_rsrc((void*)w2, 0, D_ * D_ * 2, RS3);
  const u32x4 dW1 = mk_rs(w1, D_ * D_ * 2), dW2 = mk_rs(w2, D_ * D_ * 2);
#define W12_G(gw) unsigned gw[5]; { unsigned ln_ = (unsigned)lane; asm volatile("" : "+v"(ln_)); \
    _Pragma("unroll") for (int j_ = 0; j_ < 5; ++j_) { const unsigned p_ = ln_ + 64u * j_, r_ = __umul24(p_, 6554u) >> 16; \
      gw[j_] = __umul24((r_ & 12u) * 2u + (r_ >> 4) * 4u + (r_ & 3u), D_ * 2) + (p_ - __umul24(r_, 10u)) * 16u + (unsigned)n0 * 2u; } }

  const int s0 = m0 + nt * 8;
  const int srow = s0 + wv;
  unsigned qw[5], hw[5];
  unsigned long long epn;
  unsigned tA, tB;
  u32x4 WS2[10];
#define LD_WS2U(a, b) _Pragma("unroll") for (int y_ = (a); y_ < (b); ++y_) asm volatile("buffer_load_dwordx4 %0, %1, %2, %3 offen ; ws2" : "=v"(WS2[y_]) : "v"(gw[y_ % 5]), "s"(dW2), "s"((wv + 8 * (y_ / 5)) * (32 * D_ * 2)) : "memory")
#define LD_WS2(a, b) _Pragma("unroll") for (int y_ = (a); y_ < (b); ++y_) WS2[y_] = __builtin_amdgcn_raw_buffer_load_b128(rW2, gw[y_ % 5], (wv + 8 * (y_ / 5)) * (32 * D_ * 2), 0)
  unsigned ry = (unsigned)(L_XCH + wv * YROW) + l16, rz = (unsigned)(L_XCH + wv * YROW + 1024 + 4 * lane);
  f32x2 xq[5], ha[5], ca[5], sb[5];
  float mc, mb;
  const int row = m0 + 16 * gm + l15;
  const int jb = 3 * gk;
  const unsigned ofr = (unsigned)(row >> 4) * 20480u + (unsigned)((n0 + 16 * jb + 4 * g) >> 3) * 256u + (unsigned)(g & 1) * 8u + (unsigned)l15 * 16u;
  {
    const int pi = (bx & 7) + 8 * (bx >> 4), half = (bx >> 3) & 1;
    const int chA = 2 * bx, chC = 512 + pi;
#define RD_MOD(v) do { const u32x4 b_ = *(const LAS u32x4*)(lds + L_MOD + (v) * (D_ * 2) + l16); mw[v][0] = b_[0]; mw[v][1] = b_[1]; mw[v][2] = b_[2]; mw[v][3] = b_[3]; \
                       mw[v][4] = *(const LAS unsigned*)(lds + L_MOD + (v) * (D_ * 2) + 1024 + lane * 4); } while (0)
#define LN1()                                                                                           \
  do {                                                                                                  \
    f32x2 xq_[5];                                                                                       \
    _Pragma("unroll") for (int j = 0; j < 5; ++j) xq_[j] = up2(qw[j]);                                  \
    ln5(xq_, hw);                                                                                       \
    _Pragma("unroll") for (int j = 0; j < 5; ++j) asm volatile("" : "+v"(hw[j]));                       \
  } while (0)
#define R_PRE() do {                                                                                    \
      unsigned mw[5][5];                                                                                \
      _Pragma("unroll") for (int v = 0; v < 5; ++v) RD_MOD(v);                                          \
      _Pragma("unroll") for (int j = 0; j < 5; ++j) {                                                   \
        xq[j] = up2(qw[j]);                                                                             \
        ha[j] = FMA2(up2(hw[j]), up2(mw[1][j]), up2(mw[0][j])) * up2(mw[2][j]); ca[j] = up2(mw[4][j]); sb[j] = up2(mw[3][j]); \
        asm volatile("" : "+v"(xq[j]), "+v"(ha[j]), "+v"(ca[j]), "+v"(sb[j]));                          \
      }                                                                                                 \
      const f32x2 c2_ = ((ca[0] + ca[1]) + (ca[2] + ca[3])) + ca[4], b2_ = ((sb[0] + sb[1]) + (sb[2] + sb[3])) + sb[4]; \
      mc = wave_sum2(c2_[0] + c2_[1]) * (1.f / (float)D_); mb = wave_sum2(b2_[0] + b2_[1]) * (1.f / (float)D_); \
      asm volatile("" : "+v"(mc), "+v"(mb));                                                            \
    } while (0)
    unsigned dv2;
#define GET_D() const unsigned dAB = __builtin_amdgcn_readlane(dv2, 0), dC_ = __builtin_amdgcn_readlane(dv2, 1), dC = (chC & 1) ? dC_ & 0xffff0000u : dC_ << 16
#define REQ_Q() do {                                                                                    \
      const __amdgpu_buffer_rsrc_t rQ = __builtin_amdgcn_make_buffer_rsrc((void*)q, 0, SD2, RS3);       \
      const u32x4 a = __builtin_amdgcn_raw_buffer_load_b128(rQ, l16, srow * (D_ * 2), 0);               \
      qw[0] = a[0]; qw[1] = a[1]; qw[2] = a[2]; qw[3] = a[3];                                           \
      qw[4] = __builtin_amdgcn_raw_buffer_load_b32(rQ, (unsigned)lane * 4u, srow * (D_ * 2) + 1024, 0); \
    } while (0)
#define REQ_D() dv2 = __builtin_amdgcn_raw_buffer_load_b32(__builtin_amdgcn_make_buffer_rsrc((void*)ssm_D, 0, D_ * 2, RS3), (unsigned)(lane ? (chC & ~1) : chA) * 2u, 0, 0)
#define TAGS()                                                                                          \
    {                                                                                                   \
      unsigned e0_, e1_;                                                                                \
      asm volatile("v_readfirstlane_b32 %0, %2\n\tv_readfirstlane_b32 %1, %3" : "=s"(e0_), "=s"(e1_) : "v"(ep[0]), "v"(ep[1])); \
      unsigned long long h = (unsigned long long)e1_ << 32 | e0_;                                       \
      epn = h + 1ull;                                                                                   \
      h += 0x9E3779B97F4A7C15ull;                                                                       \
      h = (h ^ (h >> 30)) * 0xBF58476D1CE4E5B9ull;                                                      \
      h = (h ^ (h >> 27)) * 0x94D049BB133111EBull;                                                      \
      h ^= h >> 31;                                                                                     \
      tA = (unsigned)h; tB = (unsigned)(h >> 32);                                                       \
    }
#define BUILD(SL, CC)                                                                                   \
  do {                                                                                                  \
    const u32x4 klo = *(const LAS u32x4*)(lds + (SL) + CV_U + 512 + 4064 - 16 * (CC)), khi = *(const LAS u32x4*)(lds + (SL) + CV_U + 512 + 4080 - 16 * (CC)); \
    const unsigned w[8] = {klo[0], klo[1], klo[2], klo[3], khi[0], khi[1], khi[2], khi[3]};             \
    _Pragma("unroll") for (int m = 0; m < 8; ++m) {                                                     \
      u32x4 o;                                                                                          \
      const int a = m >> 1;                                                                             \
      _Pragma("unroll") for (int t = 0; t < 4; ++t) {                                                   \
        if ((m & 1) == 0) o[t] = (w[a - t + 4] & 0xffffu) | (w[a - t + 3] & 0xffff0000u);               \
        else o[t] = (w[a - t + 4] >> 16) | (w[a - t + 4] << 16);                                        \
      }                                                                                                 \
      *(LAS u32x4*)(lds + (SL) + m * CV_STR + ((CC) + 1) * 16) = o;                                     \
    }                                                                                                   \
  } while (0)
#define ZCH(c) ((c) * 128u + ((c) / 160u) * (TS - 20480u))
#define ZOFF(JA, JB, CH) ((unsigned)((32 * ((g & 1) ? (JB) : (JA)) + 2 * l15 + (g >> 1)) >> 3) * (4u * TS) + ZCH((unsigned)(CH)) + (unsigned)((2 * l15 + (g >> 1)) & 7) * 16u)
#define Z2OFF(w) (O_Z + (unsigned)(4 * (w) + (l15 >> 2)) * (4u * TS) + ZCH((unsigned)chA) + (unsigned)((l15 & 3) * 64 + g * 16))
    const unsigned a00 = (unsigned)((l15 & 7) * CV_STR + (2032 - 8 * (l15 >> 3) + 8 * g) * 2) - 128u * mg;
    const unsigned b00 = (unsigned)(CV_U + 480 + 32 * l15 + 16 * g) - 128u * mg;
#define CV_RA(AV, SL, rd) _Pragma("unroll") for (int i = 0; i < 8; ++i) AV[8 * (rd) + i] = *(const LAS u32x4*)(lds + (SL) + a00 - 64 * (rd) - 512 * i)
#define CV_RB(BV, SL, rd) _Pragma("unroll") for (int i = 7; i >= 0; --i) BV[8 * (rd) + i] = *(const LAS u32x4*)(lds + (SL) + b00 + 512 * i - 64 * (rd))
    const int zja = mg ? (half ? 5 : 4) : (half ? 1 : 0), zjb = mg ? (half ? 6 : 7) : (half ? 2 : 3);
    const int t8 = tid & 255;
#define W_DMA(blk, j) dma<1, 0>(gw[j], (blk) < 20 ? dW1 : dW2, (unsigned)((blk) < 20 ? (blk) : (blk) - 4) * (32 * D_ * 2), (unsigned)((blk) * 5120 + (j) * 1024))
    if (__builtin_expect(kh != 0, 1)) {
      {
        const u32x4 dS = mk_rs(ssm_k, SD2);
        dma<1, 0>(l16, dS, (unsigned)(chC * (S_ * 2) + mg * 1024), (unsigned)(SL_C + CV_U + 512 + mg * 1024));
        dma<1, 0>(l16, dS, (unsigned)(chA * (S_ * 2) + mg * 1024), (unsigned)(SL_A + CV_U + 512 + mg * 1024));
        dma<1, 0>(l16, dS, (unsigned)((chA + 1) * (S_ * 2) + mg * 1024), (unsigned)(SL_B + CV_U + 512 + mg * 1024));
      }
      unsigned cpf = __builtin_amdgcn_raw_buffer_load_b32(rW, OOB, 0, 0);
      FENCE();
#define FILL_Z() do {                                                                                   \
      st128(u32x4{CODED, CODED, CODED, CODED}, rW, Z2OFF(mg), 0, 16);                                   \
      st128(u32x4{CODED, CODED, CODED, CODED}, rW, Z2OFF(wv), 0, 16);                                   \
      if (mg < 2) st128(u32x4{CODED, CODED, CODED, CODED}, rW, ZOFF(zja, zjb, chC), O_Z, 16);            \
    } while (0)
      unsigned fpa[3], fpv;
#pragma unroll
      for (int f_ = 0; f_ < 3; ++f_) {
        const unsigned o = (unsigned)(f_ * 4096 + t8 * 16);
        fpa[f_] = o < 10240u ? O_H1 + (unsigned)(bx >> 1) * TS + (bx & 1) * 10240u + o : o < 11264u ? O_ST4 + (unsigned)bx * 1024u + (o - 10240u)
                : (o < 11392u && (bx & 1)) ? O_ST3 + (unsigned)(bx >> 1) * 128u + (o - 11264u) : OOB;
        if (f_ == 2) fpv = o < 10240u ? CODED : SENT;
      }
#define FILL_P(a, b) _Pragma("unroll") for (int f_ = (a); f_ < (b); ++f_) { const unsigned fv = f_ < 2 ? CODED : fpv; st128(u32x4{fv, fv, fv, fv}, rW, fpa[f_], 0, 16); }
      u32x4 WS[10];
#define LD_WS(a, b) _Pragma("unroll") for (int x = (a); x < (b); ++x)                                   \
      WS[x] = __builtin_amdgcn_raw_buffer_load_b128(rW1, gw[x % 5], (9 + mg + 4 * (x / 5)) * (32 * D_ * 2), 0)
#define LD_WD(a, b) _Pragma("unroll") for (int x = (a); x < (b); ++x) {                                 \
      if (x < 10) W_DMA(mg + 4 * (x / 5), x % 5);                                                       \
      else if (x == 10) dma<1, 0>(mg == 0 ? gw[0] : mg == 1 ? gw[1] : mg == 2 ? gw[2] : gw[3], dW1, 8u * (32 * D_ * 2), (unsigned)(8 * 5120 + mg * 1024)); \
      else if (mg == 0) W_DMA(8, 4);                                                                    \
    }
      FILL_Z();
      FENCE();
      u32x4 wm[5];
      unsigned short cv;
      {
        const __amdgpu_buffer_rsrc_t rM = __builtin_amdgcn_make_buffer_rsrc((void*)w_mod, 0, D_ * 6 * D_ * 2, RS3);
        const __amdgpu_buffer_rsrc_t rC = __builtin_amdgcn_make_buffer_rsrc((void*)cin, 0, D_ * 2, RS3);
        const unsigned o = bx < 240 ? (unsigned)(t8 >> 3) * (6 * D_ * 2) + (unsigned)(t8 & 7) * 16u : OOB;
        cv = __builtin_amdgcn_raw_buffer_load_b16(rC, (unsigned)((t8 >> 3) + 32 * min(lane & 7, 4)) * 2u, (bx & 3) * 320, 0);
#pragma unroll
        for (int i = 0; i < 5; ++i) wm[i] = __builtin_amdgcn_raw_buffer_load_b128(rM, o, ((bx & 3) * 160 + i * 32) * (6 * D_ * 2) + (bx >> 2) * 128, 0);
      }
      FENCE();
      W12_G(gw);
#pragma unroll
      for (int j = 0; j < 5; ++j) PIN(gw[j]);
      const int job = bx * 4 + mg;
      const bool j3 = job < 540, jok = job < 972;
      const int jks = j3 ? job / 27 : (job - 540) >> 3, jcg = j3 ? job % 27 : (job - 540) & 7;
      const unsigned rtl = (unsigned)(L_RT + mg * 5120);
      u32x4 rt[5];
      unsigned rta[5];
      {
        const unsigned v3 = ((unsigned)lane >> 3) * (F_ * 2) + (((unsigned)lane & 7u) * 16u ^ ((unsigned)lane & 24u) * 4u) + (unsigned)jks * (32 * F_ * 2) + (unsigned)jcg * 128u;
        const unsigned s4 = (unsigned)jks * (32 * D_ * 2) + (unsigned)(jcg - nt) * 160u;
#pragma unroll
        for (int i = 0; i < 5; ++i) { rta[i] = j3 ? (i < 4 ? (v3 ^ (i & 1) * 32u) + i * (8 * F_ * 2) : OOB) : gw[i] + s4; PIN(rta[i]); }
      }
#define RT_LD() do {                                                                                    \
      const u32x4 dS = mk_rs(j3 ? w3 : w4, jok ? DF2 : 0);                                              \
      _Pragma("unroll") for (int i = 0; i < 4; ++i) dma<1, 0>(rta[i], dS, 0u, rtl + i * 1024);          \
      if (!j3) dma<1, 0>(rta[4], dS, 0u, rtl + 4096);                                                   \
      FENCE();                                                                                          \
    } while (0)
      FENCE();
      STAMP(12);
      asm volatile("" : "+v"(cpf));
      asm volatile("" :: "v"(cp0));
      SIG(P_K);
      if constexpr (TSHI == 23) TX_(31);
      FENCE();
      TAGS();
      {
        float a[8];
#pragma unroll
        for (int e = 0; e < 8; ++e) a[e] = 0.f;
        const float c_ = lo2f(cv);
        const int sci = __float_as_int(rbf(c_ * __builtin_amdgcn_rcpf(1.f + __builtin_amdgcn_exp2f(c_ * -1.4426950408889634f))));
#pragma unroll
        for (int i = 0; i < 5; ++i) {
          const float sc = __int_as_float(__builtin_amdgcn_ds_bpermute(((lane & 56) + i) * 4, sci));
#pragma unroll
          for (int e = 0; e < 4; ++e) { a[2 * e] += sc * lo2f(wm[i][e]); a[2 * e + 1] += sc * hi2f(wm[i][e]); }
        }
#pragma unroll
        for (int e = 0; e < 4; ++e) {
          const auto r = __builtin_amdgcn_permlane32_swap(__float_as_uint(a[e]), __float_as_uint(a[e + 4]), false, false);
          a[e] = __uint_as_float((unsigned)r[0]) + __uint_as_float((unsigned)r[1]);
        }
#pragma unroll
        for (int e = 0; e < 2; ++e) {
          const auto r = __builtin_amdgcn_permlane16_swap(__float_as_uint(a[e]), __float_as_uint(a[e + 2]), false, false);
          a[e] = dpp_add(__uint_as_float((unsigned)r[0]) + __uint_as_float((unsigned)r[1]), 0);
        }
        if ((lane & 8) == 0) *(LAS f32x2*)(lds + L_MP + mg * 256 + (lane & 7) * 32 + g * 8) = f32x2{a[0], a[1]};
        SIG(P_M);
        TS_(52);
        if constexpr (TSHI == 23) TX_(32);
        FENCE();
        REQ_Q();
        REQ_D();
        FENCE();
        if (wv == 7) {
          WAIT_HALF(1, P_M);
          f32x2 s = *(const LAS f32x2*)(lds + L_MP + (lane & 31) * 8);
#pragma unroll
          for (int w = 1; w < 4; ++w) s += *(const LAS f32x2*)(lds + L_MP + w * 256 + (lane & 31) * 8);
          st128(u32x4{__float_as_uint(s[0]), tA, __float_as_uint(s[1]), tB}, rW, lane < 32 ? l16 : OOB, O_MP + (unsigned)bx * 512u, 16);
          __builtin_amdgcn_raw_buffer_store_b64(u32x2{tA, tB}, rW, lane == 0 ? (unsigned)bx * 8u : OOB, O_FL1, 16);
        }
        FENCE();
      }
      STAMP(2);
      {
      WAIT_HALF(1, P_K);
      BUILD(SL_A, t8);
      BUILD(SL_B, t8);
      SIG(P_B2);
      if constexpr (TSHI == 23) TX_(33);
      FENCE();
      STAMP(1);
      }
      {
        const unsigned o_ = lane < 50 ? (unsigned)(bx * 4 + mg) * 800u + l16 : OOB;
        dma<1, 0>(o_, dW1, 0u, (unsigned)L_ST4);
        dma<1, 0>(o_, dW2, 0u, (unsigned)L_ST4);
        FENCE();
      }
      FENCE();
      FENCE();
      unsigned tra[10], rto[5];
      {
        const unsigned b3 = rtl + (unsigned)(8 * g + (l15 >> 2)) * 128u + (unsigned)((l15 >> 2) ^ (g & 1)) * 32u + ((unsigned)lane & 3u) * 8u, b4 = rtl + ((unsigned)lane >> 2) * 160u + ((unsigned)lane & 3u) * 8u;
        const unsigned dst = j3 ? O_WT3 + (unsigned)(jcg * 80 + jks) * 1024u : O_WT4 + (unsigned)(jcg * 54 + jks) * 5120u, dstr = j3 ? 20480u : 1024u;
#pragma unroll
        for (int jn = 0; jn < 5; ++jn) {
          tra[jn] = j3 ? b3 ^ (unsigned)(jn & 3) * 32u : b4 + jn * 32u;
          tra[5 + jn] = tra[jn] + (j3 ? 512u : 2560u);
          rto[jn] = (jok && !(j3 && jn == 4)) ? l16 + dst + jn * dstr : OOB;
          PIN(tra[jn]); PIN(tra[5 + jn]); PIN(rto[jn]);
        }
      }
#define RT_TR() do {                                                                                    \
        _Pragma("unroll") for (int jn = 0; jn < 5; ++jn) {                                              \
          const s16x4 lo = __builtin_amdgcn_ds_read_tr16_b64_v4i16((LAS s16x4*)(size_t)tra[jn]);        \
          const s16x4 hi = __builtin_amdgcn_ds_read_tr16_b64_v4i16((LAS s16x4*)(size_t)tra[5 + jn]);    \
          rt[jn] = __builtin_bit_cast(u32x4, bf16x8{lo[0], lo[1], lo[2], lo[3], hi[0], hi[1], hi[2], hi[3]}); \
          SB();                                                                                         \
        }                                                                                               \
      } while (0)
#define RT_OUT() do {                                                                                   \
        _Pragma("unroll") for (int jn = 0; jn < 4; ++jn) st128(rt[jn], rW, rto[jn], 0, 16);             \
        if (!j3) st128(rt[4], rW, rto[4], 0, 16);                                                       \
        FENCE();                                                                                        \
      } while (0)
      SIG(P_U);
      const bool mfin = wv == 7 && bx < 240 && lane < 32;
      const unsigned po = mfin ? ((unsigned)lane >> 3) * 512u + ((unsigned)lane & 7u) * 16u : OOB;
      u32x4 mp4;
#define LD_P() mp4 = __builtin_amdgcn_raw_buffer_load_b128(rW, po, O_MP + (unsigned)(bx & ~3) * 512u + (unsigned)(bx & 3) * 128u, 16)
#define MOD_L2() do {                                                                                   \
      if (wv == 7) {                                                                                    \
        unsigned spins = 0;                                                                             \
        while (__builtin_amdgcn_ballot_w64(mfin && (mp4[1] != tA || mp4[3] != tB)) != 0ull) {           \
          SPIN_STEP();                                                                                  \
          if (spins > RETRY_CAP) break;                                                                 \
          __builtin_amdgcn_s_sleep(USLEEP);                                                             \
          LD_P();                                                                                       \
        }                                                                                               \
        const float one = ((bx >= 40 && bx < 80) || (bx >= 160 && bx < 200)) ? 1.f : 0.f;               \
        const float s0_ = swap_add16(dpp_add(__uint_as_float(mp4[0]), 0)), s1_ = swap_add16(dpp_add(__uint_as_float(mp4[2]), 0)); \
        const unsigned d = pack2(one + rbf(s0_), one + rbf(s1_));                                       \
        const unsigned dn = (unsigned)__builtin_amdgcn_update_dpp(0, (int)d, 0xB1, 0xf, 0xf, true);     \
        st128(u32x4{d, tA, dn, tB}, rW, (lane < 8 && (lane & 1) == 0) ? (unsigned)lane * 8u : OOB, O_MOD + (unsigned)bx * 64u, 16); \
      }                                                                                                 \
      FENCE();                                                                                          \
    } while (0)
      if (wv == 7) LD_P();
      FENCE();
      STAMP(3);
      FENCE();
      asm volatile("" : "+v"(fpa[0]), "+v"(fpa[1]), "+v"(fpa[2]), "+v"(fpv));
      ln5s(qw, hw);
      GET_D();
      if constexpr (TSHI == 23) TX_(34);
      MOD_L2();
      if constexpr (TSHI == 23) TX_(35);
      TS_(27);
      if constexpr (TSHI == 23) TX_(36);
      STAMP(22);
      WAIT_ALL(P_U);
      FENCE();
      RT_LD();
      TS_(4);
      STAMP(28);
      FILL_P(0, 3);
      FENCE();
      VMW(3);
      RT_TR();
      SIG(P_RT);
      TS_(48);
      RT_OUT();
      LD_WD(0, 5);
      WAIT_HALF(1, P_RT);
      LD_WD(5, NGA);
      FENCE();
      VMW(NGA);
      SIG(P_GA);
      TS_(5);
      LD_WD(NGA, 12);
      FENCE();
      if (mg < 2) {
        u32x2 uc[2];
        f32x4 yc[8];
        float v[8];
        uc[0] = *(const LAS u32x2*)(lds + SL_C + CV_U + 512 + (256 * zja + 16 * l15 + 4 * g) * 2);
        uc[1] = *(const LAS u32x2*)(lds + SL_C + CV_U + 512 + (256 * zjb + 16 * l15 + 4 * g) * 2);
        unsigned zq = O_Z + ZOFF(zja, zjb, chC);
        PIN(zq);
        WAIT_HALF(0, P_C2);
#pragma unroll
        for (int c = 0; c < 8; ++c) yc[c] = *(const LAS f32x4*)(lds + SL_C + mg * 2048 + l16 + (c >> 1) * 4096 + (c & 1) * 1024);
        STG(8, v[i] = (i & 1) ? hi2f(uc[i >> 2][(i >> 1) & 1]) : lo2f(uc[i >> 2][(i >> 1) & 1]); PIN(v[i]));
        STG(8, v[i] = __builtin_fmaf(v[i], __uint_as_float(dC), yc[i >> 2][i & 3]); PIN(v[i]));
#pragma unroll
        for (int w = 1; w < 4; ++w) STG(8, v[i] += yc[2 * w + (i >> 2)][i & 3]; PIN(v[i]));
        st128(widen(pack2(v[0], plainv(v[1])), pack2(v[2], v[3]), pack2(v[4], plainv(v[5])), pack2(v[6], v[7])), rW, zq, 0, 16);
      }
      FENCE();
      SIG(P_ZC);
      FENCE();
      LD_WS(0, NS2);
      FENCE();
      if (HPZ) { WAIT_HALF(0, P_X2); __builtin_amdgcn_s_sleep(HPZ); }
      STAMP(15);
      LD_WS(NS2, 10);
      if (mg > 0) {
        WAIT_HALF(1, P_ZC);
#pragma unroll
        for (int j = 0; j < 5; ++j) W_DMA(16 + mg, j);
      }
      LD_WS2U(0, NZ2);
      FENCE();
      STAMP(5);
      TS_(6);
      WAIT_HALF(0, P_X2);
#pragma unroll
      for (int x = 0; x < 10; ++x) *(LAS u32x4*)(lds + (9 + mg + 4 * (x / 5)) * 5120 + (x % 5) * 1024 + l16) = WS[x];
      TS_(1);
      WAIT_HALF(0, P_T);
      R_PRE();
      VMW(NZ2);
      SIG(P_X);
      TS_(2);
      TS_(3);
      WAIT_HALF(0, P_Z);
      LD_WS2U(NZ2, 10);
      FENCE();
      WAIT_2(P_RP, P_X);
#pragma unroll
      for (int j = 0; j < 5; ++j) W_DMA(20 + mg, j);
      FENCE();
      TS_(7);
      STAMP(7);
    } else {
      PRIO(3);
      const unsigned rt0 = (unsigned)__builtin_amdgcn_s_memrealtime();
      unsigned kpa = (unsigned)L_KV + ((unsigned)lane & 7u) * 1312u + (unsigned)mg * 64u + ((unsigned)lane >> 3) * 8u;
      unsigned kpb = (unsigned)L_KV + ((unsigned)l15 >> 1) * 1312u + 256u + (unsigned)mg * 16u + (unsigned)g * 4u + ((unsigned)lane & 1u) * 640u, kpc = kpb + ((unsigned)lane & 1u) * 32u;
      const unsigned cha_ = (unsigned)ch0 + 32u * mg + ((unsigned)lane >> 3) * 4u, chb_ = (unsigned)ch0 + 128u + 8u * mg + 2u * g;
      unsigned koa = KVROW(cha_) + (unsigned)tok0 * 4u + ((unsigned)lane & 7u) * 16u, koa2 = KVROW(cha_ + 2u) + (unsigned)tok0 * 4u + ((unsigned)lane & 7u) * 16u;
      unsigned kob = KVROW(chb_) + (unsigned)tok0 * 4u + (unsigned)l15 * 8u;
      asm volatile("" : "+v"(kpa), "+v"(kpb), "+v"(kpc), "+v"(koa), "+v"(koa2), "+v"(kob));
      u32x4 ug[6];
      unsigned uo = (unsigned)mg * 2048u + l16;
      unsigned udone;
      asm volatile("s_mov_b32 %0, 0" : "=s"(udone));
      unsigned ubA = KVROW((unsigned)chA), ubC = KVROW((unsigned)chC), ubB = KVROW((unsigned)(chA + 1));
      asm volatile("" : "+s"(ubA), "+s"(ubB), "+s"(ubC));
      PIN(uo);
#pragma unroll
      for (int r = 0; r < 6; ++r) asm volatile("" : "=v"(ug[r]));
#define TRY_U(EL, T, CODE)                                                                               \
        asm volatile("s_cmp_lg_u32 %6, 0\n\ts_cbranch_scc1 Lu%=\n\ts_cmp_lt_u32 %[el], %[t]\n\ts_cbranch_scc1 Lu%=\n\t" \
                     "buffer_load_dwordx4 %2, %[vo], %[rs], %[c] offen sc1\n\tbuffer_load_dwordx4 %3, %[vo], %[rs], %[c] offen offset:1024 sc1\n\t" \
                     "buffer_load_dwordx4 %0, %[vo], %[rs], %[a] offen sc1\n\tbuffer_load_dwordx4 %1, %[vo], %[rs], %[a] offen offset:1024 sc1\n\t" \
                     "buffer_load_dwordx4 %4, %[vo], %[rs], %[b] offen sc1\n\tbuffer_load_dwordx4 %5, %[vo], %[rs], %[b] offen offset:1024 sc1\n\t" \
                     "s_mov_b32 %6, %[cd]\n"                                                            \
                     "Lu%=:" : "+v"(ug[0]), "+v"(ug[1]), "+v"(ug[2]), "+v"(ug[3]), "+v"(ug[4]), "+v"(ug[5]), "+s"(udone) \
                     : [el] "s"(EL), [t] "i"(T), [cd] "i"(CODE), [vo] "v"(uo), [rs] "s"(dW), [a] "s"(ubA), [b] "s"(ubB), [c] "s"(ubC) : "scc", "memory")
      VMW(9);
      TAGS();
      unsigned tAB = (lane & 1) ? tB : tA;
      PIN(tAB);
      asm volatile("s_waitcnt vmcnt(0)" : "+v"(sk[0]), "+v"(sk[1]), "+v"(sk[2]));
      const unsigned rt1 = (unsigned)__builtin_amdgcn_s_memrealtime();
      SIG(P_KV);
      FENCE();
      f32x2 wa[3][4];
#pragma unroll
      for (int c = 0; c < 3; ++c)
#pragma unroll
        for (int t = 0; t < 4; ++t) {
          const f32x2 w = (t & 1) ? f32x2{hi2f(sk[c][t >> 1]), hi2f(sk[c][2 + (t >> 1)])} : f32x2{lo2f(sk[c][t >> 1]), lo2f(sk[c][2 + (t >> 1)])};
          wa[c][t] = w;
          PIN(wa[c][t]);
        }
      asm volatile("" : "+v"(tAB));
      FENCE();
      TS_(1);
      STAMP(1);
      WAIT_HALF(0, P_KV);
      if (TU1) TRY_U(rt1 - rt0, TU1, 1);
      if constexpr (TSHI == 23) TX_(31);
      STAMP(12);
      {
        u32x2 kw[7], vw[4];
        unsigned kx[5], vx[2];
#pragma unroll
        for (int i = 0; i < 7; ++i) kw[i] = *(const LAS u32x2*)(lds + kpa + (i < 3 ? 320 + 320 * i : 1312 + 320 * (i - 3)));
#pragma unroll
        for (int i = 0; i < 5; ++i) kx[i] = *(const LAS unsigned*)(lds + ((i == 1 || i == 2) ? kpc : kpb) + (i < 3 ? 320 + 320 * i : 1312 + 320 * (i - 3)));
#pragma unroll
        for (int i = 0; i < 4; ++i) vw[i] = *(const LAS u32x2*)(lds + kpa + 12288 + 320 * i);
#pragma unroll
        for (int i = 0; i < 2; ++i) vx[i] = *(const LAS unsigned*)(lds + kpb + 12288 + 320 * i);
        __builtin_amdgcn_sched_barrier(0);
        f32x2 kf[19], a[10], p[10], t_[10];
#define KX(o, t) kf[((o) >> 2) * 7 + ((o) & 3) + (t)]
#define STR(lo, hi, stmt) do { _Pragma("unroll") for (int i = (lo); i < (hi); ++i) { stmt; } __builtin_amdgcn_sched_barrier(0); } while (0)
#define KV_GROUP(lo, hi) do {                                                                           \
        STR(((lo) >> 2) * 7, (hi) == 10 ? 19 : ((hi) >> 2) * 7, kf[i] = up2(i < 14 ? kw[i % 7][i / 7] : kx[i < 14 ? 0 : i - 14]); PIN(kf[i])); \
        {                                                                                               \
          STR(lo, hi, a[i] = KX(i, 0) * wa[i >> 2][3]; PIN(a[i]));                                      \
          _Pragma("unroll") for (int t = 1; t < 4; ++t) STR(lo, hi, a[i] = FMA2(KX(i, t), wa[i >> 2][3 - t], a[i]); PIN(a[i])); \
          STR(lo, hi, t_[i] = FMA2(a[i], cp, a[i]); PIN(t_[i]));                                        \
          STR(lo, hi, a[i] = FMA2(a[i], cn, t_[i]); PIN(a[i]));                                         \
        }                                                                                               \
        STR(lo, hi, p[i] = up2(i < 8 ? vw[i & 3][i >> 2] : vx[i & 1]); PIN(p[i]));                      \
        STR(lo, hi, a[i] *= p[i]; PIN(a[i]));                                                           \
        if constexpr (TSHI == 23) TX_(32 + (lo) / 2);                                                   \
        _Pragma("unroll") for (int c = (lo) >> 1; c < ((hi) < 8 ? (hi) : 8) >> 1; ++c) {                \
          const int o = (c >> 1) * 4, e = c & 1;                                                        \
          st128(u32x4{pack2(a[o][e], plainv(a[o + 1][e])), tA, pack2(a[o + 2][e], plainv(a[o + 3][e])), tB}, rW, c < 2 ? koa : koa2, e * 8192u, 16); \
        }                                                                                               \
        if ((hi) == 10) { _Pragma("unroll") for (int e = 0; e < 2; ++e) __builtin_amdgcn_raw_buffer_store_b64(u32x2{pack2(a[8][e], plainv(a[9][e])), tAB}, rW, kob, e * 8192, 16); } \
        __builtin_amdgcn_sched_barrier(0);                                                              \
      } while (0)
        KV_GROUP(0, 4);
        if constexpr (TSHI == 23) TX_(33);
        STAMP(5);
        KV_GROUP(4, 10);
        if constexpr (TSHI == 23) TX_(35);
      }
      FENCE();
      const unsigned rt2 = (unsigned)__builtin_amdgcn_s_memrealtime();
      REQ_Q();
      REQ_D();
      FENCE();
      if (TU2) TRY_U(rt2 - rt0, TU2, 2);
      STAMP(2);
      TS_(2);
      WAIT_HALF(1, P_K);
      BUILD(SL_C, t8);
      SIG(P_B);
      if constexpr (TSHI == 23) TX_(36);
      FENCE();
#define LD_U(r) ug[r] = __builtin_amdgcn_raw_buffer_load_b128(rW, uo + ((r) & 1) * 1024, (r) < 2 ? ubA : (r) < 4 ? ubC : ubB, 16)
      u32x4 A2[16], B2[16];
      WAIT_HALF(0, P_B);
      CV_RA(A2, SL_C, 0);
      CV_RA(A2, SL_C, 1);
      W12_G(gw);
#pragma unroll
      for (int j = 0; j < 5; ++j) PIN(gw[j]);
      unsigned zq = Z2OFF(wv);
      unsigned pa = a00 + SL_A - 3648, pb = b00 + SL_A - 64;
      unsigned xw = SL_A + mg * 8192 + l16, xc = SL_C + mg * 4096 + l16;
      asm volatile("" : "+v"(zq), "+v"(pa), "+v"(pb), "+v"(xw), "+v"(xc));
      FENCE();
      if (udone == 0u) while ((unsigned)__builtin_amdgcn_s_memrealtime() - rt0 < (unsigned)TU) __builtin_amdgcn_s_sleep(1);
      STAMP(3);
      TS_(3);
      TRY_U(rt0, 0, 3);
      FENCE();
      if constexpr (TSHI == 23) TX_(37);
      unsigned uw = (unsigned)(SL_A + CV_U + 512 + mg * 1024 + lane * 8);
      PIN(uw);
      WAIT_HALF(1, P_B2);
      asm volatile("s_cmp_eq_u32 %[d], 1\n\ts_cbranch_scc1 Lw%=\n\ts_waitcnt vmcnt(0)\nLw%=:\n\ts_waitcnt vmcnt(9)"
                   : "+v"(ug[0]), "+v"(ug[1]), "+v"(ug[2]), "+v"(ug[3]), "+v"(ug[4]), "+v"(ug[5]) : [d] "s"(udone) : "scc", "memory");
      if constexpr (TSHI == 23) { TX_(38); PIN(ug[5]); __builtin_amdgcn_s_sleep(1); TX_(39); }
      {
#define U_BAD(r) (ug[r][1] != tA || ug[r][3] != tB)
        if (__builtin_expect(__builtin_amdgcn_ballot_w64(U_BAD(0) || U_BAD(1) || U_BAD(2) || U_BAD(3) || U_BAD(4) || U_BAD(5)) != 0ull, 1)) {
          unsigned spins = 0;
          do {
            __builtin_amdgcn_s_sleep(USLEEP);
#pragma unroll
            for (int r = 0; r < 6; ++r) if (U_BAD(r)) LD_U(r);
            SPIN_STEP();
          } while (__builtin_amdgcn_ballot_w64(U_BAD(0) || U_BAD(1) || U_BAD(2) || U_BAD(3) || U_BAD(4) || U_BAD(5)) != 0ull && spins < RETRY_CAP);
          VM0();
        }
#pragma unroll
        for (int r = 2; r < 8; ++r) *(LAS u32x2*)(lds + uw + ((r % 6) >> 1) * CV_SLOT + (r & 1) * 512) = u32x2{ug[r % 6][0], ug[r % 6][2]};
      }
      SIG(P_U);
      TS_(56);
      if constexpr (TSHI == 23) TX_(40);
      STAMP(22);
      GET_D();
      LD_WS2(0, 10);
      FENCE();
      WAIT_HALF(0, P_U);
      CV_RB(B2, SL_C, 0);
      FENCE();
      STAMP(28);
      TS_(4);
      f32x4 acc[8], ac[4];
      u32x4 A[16], B[16];
#define RA(P, rd, i_) *(const LAS u32x4*)(lds + (P) + 3648 - 64 * (rd) - 512 * (i_))
#define RB(P, rd, i_) *(const LAS u32x4*)(lds + (P) + 64 - 64 * (rd) + 512 * (i_))
#define CONV_C(J, lo, hi)                                                                               \
      sfor<lo, hi>([&](auto C_) __attribute__((always_inline)) {                                         \
        constexpr int c = decltype(C_)::v, rd = c / 18, i = cc_i(J, c % 18), t = cc_t(J, c % 18);       \
        ac[t] = mfma16(A2[8 * rd + i], B2[8 * rd + (((J) >> (4 * t)) & 15) - i], (rd == 0 && i == 0) ? f32x4{0.f, 0.f, 0.f, 0.f} : ac[t]); \
        SB();                                                                                           \
        if constexpr (c < 8) B2[15 - c] = RB(pb + CV_SLOT, 1, 7 - c);                                   \
        if constexpr (c >= 18 && c < 34) { constexpr int x = (c - 18) >> 1; if constexpr ((c & 1) == 0) B[7 - x] = RB(pb, 0, 7 - x); else A[x] = RA(pa, 0, x); } \
        SB();                                                                                           \
      });
      if (half == 0) { CONV_C(0x7430, 0, 18) } else { CONV_C(0x6521, 0, 18) }
      SB();
      int half2 = half;
      asm volatile("" : "+s"(half2));
      if (half2 == 0) { CONV_C(0x7430, 18, 36) } else { CONV_C(0x6521, 18, 36) }
      u32x4 A3[16], B3[16];
      f32x4 ab_[8];
      unsigned pk_;
      float ud[16];
      u32x2 u2[4];
      f32x4 ya[8];
      unsigned ga_;
      unsigned pu = (unsigned)(SL_A + CV_U + 512 + (256 * wv + 16 * l15 + 4 * g) * 2), py = (unsigned)(SL_A + wv * 1024) + l16;
      const unsigned f0 = (unsigned)L_CTL + ((unsigned)lane & 3u) * 4u;
      sfor<0, 144>([&](auto M_) __attribute__((always_inline)) {
        constexpr int m = decltype(M_)::v, ch = m / 72, rd = (m / 36) & 1, r = m % 36, i = m < 108 ? cv_i(r) : cl_i(r), dd = m < 108 ? cv_d(r) : cl_t(r) - cl_i(r);
        if constexpr (ch == 0) acc[i + dd] = mfma16(A[8 * rd + i], B[8 * rd + dd], (rd == 0 && i == 0) ? f32x4{0.f, 0.f, 0.f, 0.f} : acc[i + dd]);
        else ab_[i + dd] = mfma16(A3[8 * rd + i], B3[8 * rd + dd], (rd == 0 && i == 0) ? f32x4{0.f, 0.f, 0.f, 0.f} : ab_[i + dd]);
        SB();
        if constexpr (m == 30) { pa += SL_B - SL_A; pb += SL_B - SL_A; asm volatile("" : "+v"(pa), "+v"(pb)); }
        if constexpr (m == 94) { xw += SL_B - SL_A; PIN(xw); }
        if constexpr (r >= RS0 && r < RS0 + 16 && m < 108) {
          constexpr int x = (r - RS0) >> 1;
          if constexpr (m < 36) { if constexpr ((r & 1) == 0) B[15 - x] = RB(pb, 1, 7 - x); else A[8 + x] = RA(pa, 1, x); }
          else if constexpr (m < 72) { if constexpr ((r & 1) == 0) B3[7 - x] = RB(pb, 0, 7 - x); else A3[x] = RA(pa, 0, x); }
          else { if constexpr ((r & 1) == 0) B3[15 - x] = RB(pb, 1, 7 - x); else A3[8 + x] = RA(pa, 1, x); }
        }
        if constexpr (m == 1 || m == 4 || m == 23 || m == 26) { constexpr int c = m == 1 ? 0 : m == 4 ? 1 : m == 23 ? 2 : 3; *(LAS f32x4*)(lds + xc + c * 1024) = ac[c]; }
        if constexpr (m == 28) SIG(P_C2);
        if constexpr (m == 38 || m == 104) pk_ = *(volatile LAS unsigned*)(lds + f0);
        if constexpr (m == 45) NEED_AT(pk_, f0, P_C2);
        if constexpr (m == 111) NEED_AT(pk_, f0, P_A2);
        constexpr int wa = m == 47 ? 0 : m == 54 ? 1 : m == 60 ? 2 : m == 65 ? 3 : m == 69 ? 4 : m == 72 ? 5 : m == 75 ? 6 : m == 77 ? 7 : -1;
        constexpr int wb = m == 113 ? 0 : m == 116 ? 1 : m == 119 ? 2 : m == 122 ? 3 : m == 126 ? 4 : m == 132 ? 5 : m == 139 ? 6 : -1;
        if constexpr (wa >= 0) *(LAS f32x4*)(lds + xw + wa * 1024) = acc[wa];
        if constexpr (wb >= 0) *(LAS f32x4*)(lds + xw + wb * 1024) = ab_[wb];
        if constexpr (m == 95) SIG(P_A2);
        if constexpr (m == 58) PIN(pu);
        if constexpr (m == 59 || m == 61) { constexpr int c = m - 59; u2[c] = *(const LAS u32x2*)(lds + pu + c * 1024); u2[c + 1] = *(const LAS u32x2*)(lds + pu + (SL_B - SL_A) + c * 1024); }
        constexpr int us = cv_slot(m, 0);
        if constexpr (us >= 0 && us < 16) {
#pragma unroll
          for (int o = 2 * us; o < 2 * us + 2; ++o) {
            const int v = (o >> 3) * 4 + (o & 3);
            if ((o & 4) == 0) ud[v] = (v & 1) ? hi2f(u2[v >> 2][(v >> 1) & 1]) : lo2f(u2[v >> 2][(v >> 1) & 1]);
            else ud[v] *= ((v >> 2) & 1) ? hi2f(dAB) : lo2f(dAB);
            PIN(ud[v]);
          }
        }
        if constexpr (m == 110) PIN(py);
        if constexpr (m == 112 || m == 114 || m == 115 || m == 117) { constexpr int c = m == 112 ? 0 : m == 114 ? 2 : m == 115 ? 4 : 6;
          ya[c] = *(const volatile LAS f32x4*)(lds + py + (c >> 2) * 4096 + (c & 3) * 8192); ya[c + 1] = *(const volatile LAS f32x4*)(lds + py + (c >> 2) * 4096 + (c & 3) * 8192 + 8192); }
        constexpr int zs = cv_slot(m, 1);
        if constexpr (zs >= 0 && zs < 16) {
#pragma unroll
          for (int o = 2 * zs; o < 2 * zs + 2; ++o) { const int tl = (o >> 2) & 1, e = o & 3; ud[tl * 8 + e] += ya[4 * tl + (o >> 3)][e]; PIN(ud[tl * 8 + e]); }
        }
        if constexpr (TSHI == 10 && (m % 18 == 0 || m == 143)) TX_(44 + (m + 1) / 18);
        if constexpr (m == 131) ga_ = *(volatile LAS unsigned*)(lds + f0 + 16);
        if constexpr (m == 140) { py += SL_B - SL_A; PIN(py); }
        SB();
      });
      *(LAS f32x4*)(lds + xw + 7 * 1024) = ab_[7];
      SIG(P_X);
      TS_(5);
      STAMP(4);
      u32x4 mg4[4];
#define LD_MG(i) mg4[i] = __builtin_amdgcn_raw_buffer_load_b128(rW, (unsigned)t8 * 16u, O_MOD + (i) * 4096, 16)
      u32x2 f1v;
#define LD_F1() f1v = __builtin_amdgcn_raw_buffer_load_b64(rW, (unsigned)t8 * 8u, O_FL1, 16)
      NEED_AT(ga_, f0 + 16, P_GA);
      {
        unsigned spins = 0;
        for (;;) {
          const unsigned fl = *(volatile LAS unsigned*)(lds + f0);
#pragma unroll
          for (int c = 0; c < 8; ++c) ya[c] = *(const volatile LAS f32x4*)(lds + py + (c >> 2) * 4096 + (c & 3) * 8192);
          if (__builtin_amdgcn_ballot_w64(fl < (unsigned)P_X) == 0ull) break;
          SPIN_STEP();
          if (spins > SPIN_CAP) break;
        }
#pragma unroll
        for (int tl = 0; tl < 2; ++tl) {
          const f32x4 yb = (ya[4 * tl] + ya[4 * tl + 1]) + (ya[4 * tl + 2] + ya[4 * tl + 3]);
          const f32x2 v2 = f32x2{yb[0], yb[1]} + f32x2{ud[8 * tl + 4], ud[8 * tl + 5]}, v3 = f32x2{yb[2], yb[3]} + f32x2{ud[8 * tl + 6], ud[8 * tl + 7]};
          st128(u32x4{pack2(ud[8 * tl], plainv(v2[0])), pack2(ud[8 * tl + 1], v2[1]), pack2(ud[8 * tl + 2], plainv(v3[0])), pack2(ud[8 * tl + 3], v3[1])}, rW, zq + tl * 64u * TS, 0, 16);
        }
      }
      FENCE();
      SIG(P_X2);
      TS_(6);
      LD_F1(); LD_MG(0); LD_MG(1); LD_MG(2); LD_MG(3);
      FENCE();
      LN1();
      STAMP(6);
      u32x4 za, zb, zc = {0u, 0u, 0u, 0u}, zd = {0u, 0u, 0u, 0u};
      const unsigned zo = (unsigned)mt * (4u * TS) + ZCH((unsigned)t8 & ~1u) + (unsigned)nt * 32u + ((unsigned)t8 & 1u) * 16u;
      const unsigned zo1 = (unsigned)mt * (4u * TS) + ZCH(256u + ((unsigned)t8 & ~1u)) + (unsigned)nt * 32u + ((unsigned)t8 & 1u) * 16u;
      const unsigned zo2 = (unsigned)mt * (4u * TS) + ZCH(2u * t8 + 128u) + (unsigned)nt * 16u;
      FENCE();
      {
#define MG_BAD(i) (mg4[i][1] != tA || mg4[i][3] != tB)
#define T_BAD() (__builtin_amdgcn_ballot_w64(f1v[0] != tA || f1v[1] != tB || MG_BAD(0) || MG_BAD(1) || MG_BAD(2) || MG_BAD(3)) != 0ull)
        if (__builtin_expect(T_BAD(), 0)) {
          unsigned spins = 0;
          do {
            __builtin_amdgcn_s_sleep(USLEEP);
            if (f1v[0] != tA || f1v[1] != tB) LD_F1();
#pragma unroll
            for (int i = 0; i < 4; ++i) if (MG_BAD(i)) LD_MG(i);
            SPIN_STEP();
          } while (T_BAD() && spins < RETRY_CAP);
          VM0();
        }
#pragma unroll
        for (int i = 0; i < 4; ++i) {
          const int n = (t8 + 256 * i) * 4;
          if (n < 6 * D_) *(LAS u32x2*)(lds + L_MOD + n * 2) = u32x2{mg4[i][0], mg4[i][2]};
          if (i == 3 && n >= 5 * D_ + n0 && n < 5 * D_ + n0 + 80)
            *(LAS f32x4*)(lds + L_GB + (n - 5 * D_ - n0) * 4) = f32x4{lo2f(mg4[i][0]), hi2f(mg4[i][0]), lo2f(mg4[i][2]), hi2f(mg4[i][2])};
        }
        SIG(P_T);
        if constexpr (TSHI == 23) TX_(41);
      }
      WAIT_HALF(0, P_T);
      if (DLY0) __builtin_amdgcn_s_sleep(DLY0);
      PRIO(3);
      STAMP(7);
      TS_(7);
      za = __builtin_amdgcn_raw_buffer_load_b128(rW, zo, O_Z, 16); zb = __builtin_amdgcn_raw_buffer_load_b128(rW, zo1, O_Z, 16);
      if (wv == 3) { zc = __builtin_amdgcn_raw_buffer_load_b128(rW, zo2, O_Z, 16); zd = __builtin_amdgcn_raw_buffer_load_b128(rW, zo2 + 16u * ZG, O_Z, 16); }
      FENCE();
      SIG(P_Z);
      R_PRE();
      SIG(P_RP);
      unsigned zw0 = (unsigned)(L_XCH + 4 * (t8 & 1) * YROW + 4 * (t8 >> 1)), zw1 = (unsigned)(L_XCH + 256 + 4 * t8);
      asm volatile("" : "+v"(zw0), "+v"(zw1), "+v"(ry), "+v"(rz));
      asm volatile("" : "+s"(w3s[0]), "+s"(w3s[1]));
      unsigned spins = 0;
      for (;;) {
        const bool ba = amax2(za) >= PTHR, bb_ = amax2(zb) >= PTHR, bc_ = amax2(zc) >= PTHR, bd = amax2(zd) >= PTHR;
        if (__builtin_amdgcn_ballot_w64(ba || bb_ || bc_ || bd) == 0ull) break;
        SPIN_STEP();
        if (spins > RETRY_CAP) break;
        __builtin_amdgcn_s_sleep(ZSLEEP);
        if (ba) za = __builtin_amdgcn_raw_buffer_load_b128(rW, zo, O_Z, 16);
        if (bb_) zb = __builtin_amdgcn_raw_buffer_load_b128(rW, zo1, O_Z, 16);
        if (bc_) zc = __builtin_amdgcn_raw_buffer_load_b128(rW, zo2, O_Z, 16);
        if (bd) zd = __builtin_amdgcn_raw_buffer_load_b128(rW, zo2 + 16u * ZG, O_Z, 16);
      }
#pragma unroll
      for (int i = 0; i < 4; ++i) {
        *(LAS unsigned*)(lds + zw0 + i * YROW) = za[i];
        *(LAS unsigned*)(lds + zw0 + i * YROW + 512) = zb[i];
      }
      if (wv == 3) {
#pragma unroll
        for (int i = 0; i < 4; ++i) {
          *(LAS unsigned*)(lds + zw1 + (2 * i) * YROW) = __builtin_amdgcn_perm(zd[i], zc[i], 0x05040100u);
          *(LAS unsigned*)(lds + zw1 + (2 * i + 1) * YROW) = __builtin_amdgcn_perm(zd[i], zc[i], 0x07060302u);
        }
      }
      VM0();
      SIG(P_Z2);
    }
  }
  {
    f32x2 hh[5], t_[5];
    STAMP(8);
    asm volatile("" : "+v"(ry), "+v"(rz));
    asm volatile("" : "+s"(w3s[0]), "+s"(w3s[1]));
    if (kh) PRIO(PR1); else PRIO(PHI);
    WAIT_HALF(0, P_Z2);
    STAMP(9);
    TS_(8);
    unsigned zw[5], xp[5], hp_[5];
    {
      const u32x4 a = *(LAS u32x4*)(lds + ry);
      zw[0] = a[0]; zw[1] = a[1]; zw[2] = a[2]; zw[3] = a[3];
      zw[4] = *(LAS unsigned*)(lds + rz);
    }
    FENCE();
#define RQ3(k) do { LD_W3R((k) == 0 ? 0 : (k) == 1 ? NR0 : NR1, (k) == 0 ? NR0 : (k) == 1 ? NR1 : NWH); FENCE(); } while (0)
    RQ3(0);
    FENCE();
    if constexpr (TSHI == 22) TX_(30);
    STG(5, hh[i] = up2(zw[i]); PIN(hh[i]));
    STG(5, xq[i] = FMA2(hh[i], ha[i], xq[i]); PIN(xq[i]));
    STG(5, t_[i] = FMA2(xq[i], cp, xq[i]); PIN(t_[i]));
    STG(5, xq[i] = FMA2(xq[i], cn, t_[i]); PIN(xq[i]));
    if constexpr (TSHI == 22) TX_(31);
    RQ3(1);
    if constexpr (TSHI == 22) TX_(32);
    float m3;
    {
      const f32x2 s2 = ((xq[0] + xq[1]) + (xq[2] + xq[3])) + xq[4];
      const f32x2 q2 = FMA2(xq[0], xq[0], xq[1] * xq[1]) + FMA2(xq[2], xq[2], FMA2(xq[3], xq[3], xq[4] * xq[4]));
      const f32x2 p2 = FMA2(xq[0], ca[0], xq[1] * ca[1]) + FMA2(xq[2], ca[2], FMA2(xq[3], ca[3], xq[4] * ca[4]));
      float sm3[3] = {s2[0] + s2[1], q2[0] + q2[1], p2[0] + p2[1]};
      wave_sums<3>(sm3);
      const float mean = sm3[0] * (1.f / (float)D_), qs = sm3[1], ps = sm3[2];
      const float m = rbf(mean);
      f32x2 d[5];
#pragma unroll
      for (int j = 0; j < 5; ++j) d[j] = (xq[j] - m) * ca[j];
      const float var = rbf(__builtin_fmaxf(__builtin_fmaf(-mean, mean, qs * (1.f / (float)D_)), 0.f));
      const float den = rbf(__builtin_amdgcn_sqrtf(rbf(var + 1e-5f)));
      const float rinv = __builtin_amdgcn_rcpf(den);
      m3 = rbf(__builtin_fmaf(__builtin_fmaf(-m, mc, ps * (1.f / (float)D_)), rinv, mb));
      __builtin_amdgcn_sched_barrier(0);
      if constexpr (TSHI == 22) TX_(33);
      RQ3(2);
      if constexpr (TSHI == 22) TX_(34);
      STG(5, hh[i] = FMA2(d[i], (f32x2{rinv, rinv}), sb[i]); PIN(hh[i]));
      STG(5, hh[i] -= m3; PIN(hh[i]));
    }
#pragma unroll
    for (int j = 0; j < 5; ++j) hp_[j] = pack2(hh[j][0], (j & 1) ? hh[j][1] : (j < 4 || (lane & 1) == 0) ? codedv(hh[j][1]) : hh[j][1]);
    *(LAS u32x4*)(lds + ry) = u32x4{hp_[0], hp_[1], hp_[2], hp_[3]};
    *(LAS unsigned*)(lds + rz) = hp_[4];
    BARRIER();
    {
      const unsigned tb = (unsigned)(s0 >> 4) * 20480u + (unsigned)(s0 & 15) * 16u + (unsigned)(tid >> 3) * 256u + (unsigned)(tid & 7) * 16u;
      const LAS char* pr = lds + L_XCH + (tid & 7) * YROW + (tid >> 3) * 16;
      const u32x4 v0 = *(const LAS u32x4*)(pr);
      const u32x4 v1 = *(const LAS u32x4*)(pr + (tid < 128 ? 1024 : 0));
      st128(v0, rW, tb, O_HP, 16);
      if (wv < 2) st128(v1, rW, tb + 64u * 256u, O_HP, 16);
    }
    FENCE();
    TS_(9);
    PRIO(1);
#pragma unroll
    for (int j = 0; j < 5; ++j) xp[j] = pack2(xq[j][0], xq[j][1]);
    *(LAS u32x4*)(lds + ry + 8 * YROW) = u32x4{xp[0], xp[1], xp[2], xp[3]};
    *(LAS unsigned*)(lds + rz + 8 * YROW) = xp[4];
    FENCE();
    STAMP(10);
    {
      const f32x2 s2 = ((hh[0] + hh[1]) + (hh[2] + hh[3])) + hh[4];
      const f32x2 q2 = FMA2(hh[0], hh[0], hh[1] * hh[1]) + FMA2(hh[2], hh[2], FMA2(hh[3], hh[3], hh[4] * hh[4]));
      const float mean = wave_sum2(s2[0] + s2[1]) * (1.f / (float)D_), qs = wave_sum2(q2[0] + q2[1]);
      const float var = rbf(__builtin_fmaxf(__builtin_fmaf(-mean, mean, qs * (1.f / (float)D_)), 0.f));
      const float den = rbf(__builtin_amdgcn_sqrtf(rbf(var + 1e-5f)));
      const u32x2 st = {canon(__float_as_uint(m3)), __float_as_uint(den)};
      __builtin_amdgcn_raw_buffer_store_b64(st, rW, lane == 0 ? (unsigned)srow * 8u : OOB, O_ST3, 16);
    }
    {
      BARRIER();
      const unsigned tb = (unsigned)(s0 >> 4) * 20480u + (unsigned)(s0 & 15) * 16u + (unsigned)(tid >> 3) * 256u + (unsigned)(tid & 7) * 16u;
      const LAS char* pr = lds + L_XCH + (8 + (tid & 7)) * YROW + (tid >> 3) * 16;
      st128(*(const LAS u32x4*)(pr), rW, tb, O_XO, 16);
      if (wv >= 6) st128(*(const LAS u32x4*)(pr + 256), rW, tb + 16u * 256u, O_XO, 16);
      SIG(P_L1);
      if (bx == 0 && wv == 5) __builtin_amdgcn_raw_buffer_store_b64(u32x2{(unsigned)epn, (unsigned)(epn >> 32)}, rW, lane == 0 ? 0u : OOB, O_EP, 16);
    }
    FENCE();
    PRIO(0);
    FENCE();
  }

  float sthr = PTHR, scmx = CMAX;
  unsigned scsh = CSH;
  asm volatile("" : "+s"(sthr), "+s"(scsh), "+s"(scmx));
  unsigned xcw = (unsigned)(L_XCH + gm * 5120 + gk * 2048) + l16, xcr = (unsigned)(L_XCH + gm * 5120 + 2048 - gk * 2048) + l16, xcf = (unsigned)(L_CTL + gp * 4);
  asm volatile("" : "+v"(xcw), "+v"(xcr), "+v"(xcf));
  unsigned long long gkm;
  asm volatile("s_cmp_lg_u32 %1, 0\n\ts_cselect_b64 %0, -1, 0" : "=s"(gkm) : "s"(gk) : "scc");
  static_assert(KL >= 3 && KG <= 54 - 2 * KL, "the weights are read in front of the signal");
#define EXCHANGE(ph)                                                                        \
  do {                                                                                      \
    f32x4 e0, e1, e2;                                                                       \
    unsigned spins = 0;                                                                     \
    for (;;) {                                                                              \
      const unsigned fl = *(volatile LAS unsigned*)(lds + xcf);                                                           \
      e0 = *(volatile LAS f32x4*)(lds + xcr);                                               \
      e1 = *(volatile LAS f32x4*)(lds + xcr + 1024);                                        \
      e2 = *(volatile LAS f32x4*)(lds + xcr + 2048);                                        \
      if (__builtin_amdgcn_readfirstlane(fl) >= (ph)) break;                                \
      SPIN_STEP();                                                                          \
      if (spins > SPIN_CAP) break;                                                          \
    }                                                                                       \
    asm volatile("s_nop 3" : "+v"(acc[3]), "+v"(acc[4]));        \
    STG(12, e[i] = (i < 4 ? acc[3][i & 3] + e0[i & 3] : i < 8 ? acc[4][i & 3] + e1[i & 3] : acc[2][i & 3] + e2[i & 3]); PIN(e[i])); \
  } while (0)
#define KLOOP(CD, PB, BLK, VA, AOFF, XG, XS, ph)                                            \
  asm volatile("s_nop 1" : "+v"(A[0]));                                                     \
  sfor<0, 50>([&](auto M_) __attribute__((always_inline)) {                                 \
    constexpr int m = decltype(M_)::v, i = kl_i(m), j = kl_j(m), f = m / 4 + 1, c = m % 4;  \
    mf<i == 0 && (CD || !HFI || j < 2)>(acc[j], __builtin_bit_cast(u32x4, __builtin_shufflevector(Bh[2 * m], Bh[2 * m + 1], 0, 1, 2, 3, 4, 5, 6, 7)), A[i]); \
    SB();                                                                                   \
    constexpr int n0_ = m < KG ? (m * (100 - 2 * (PB))) / KG : 100, n1_ = m < KG ? ((m + 1) * (100 - 2 * (PB))) / KG : 100; \
    if constexpr (n1_ > n0_) RD_BH(2 * (PB) + n0_, BLK);                                    \
    if constexpr (n1_ > n0_ + 1) RD_BH(2 * (PB) + n0_ + 1, BLK);                            \
    static_assert(n1_ <= n0_ + 2 && (f > 9 || kl_i(m + 1) < f), "order");                   \
    if constexpr (f < 10) {                                                                 \
      if constexpr (c == 0) { m_ = CD ? amin2(A[f]) : __builtin_amdgcn_fmed3f(__builtin_fabsf(__uint_as_float(A[f][0])), __builtin_fabsf(__uint_as_float(A[f][2])), scmx); PIN(m_); } \
      if constexpr (c == 1) CMP_BAD(CD, bm, m_);                                            \
      if constexpr (c == 2 && CD) A[f][0] -= scsh;                                          \
      if constexpr (c == 3 && CD) { A[f][2] -= scsh; PIN(A[f]); }                           \
      if constexpr (c == 3) RETRY(CD, A[f], bm, VA, sa[f / 5], AOFF + (f % 5) * 2048, PHI); \
    }                                                                                       \
    if constexpr (m % 5 == 2 || m % 5 == 4) { XS(2 * (m / 5) + (m % 5 == 4)); }             \
    XG(m);                                                                                  \
                                \
    if constexpr (m == 51 - 2 * KL) { asm volatile("s_nop 3" : "+v"(acc[0])); *(volatile LAS f32x4*)(lds + xcw) = acc[0]; } \
    if constexpr (m == 52 - 2 * KL) *(volatile LAS f32x4*)(lds + xcw + 1024) = acc[1];      \
    if constexpr (m == 53 - 2 * KL) asm volatile("s_mov_b64 exec, %2\n\tds_write_b128 %0, %1 offset:2048\n\ts_mov_b64 exec, -1" :: "v"(xcw), "v"(acc[2]), "s"(gkm) : "memory"); \
    if constexpr (m == 54 - 2 * KL) SIG(ph);                                                \
    if constexpr (TSHI == 20 && m % 5 == 0) TX_((CD ? 30 : 44) + m / 5);                    \
    SB();                                                                                   \
  })
  {
    f32x4 acc[5];
    s16x4 Bh[100];
    u32x4 A[10];
    float m_, e[12];
    unsigned long long bm;
    unsigned sa[2] = {ROT ? (unsigned)(nt & 1) * 10240u : 0u, ROT ? 10240u - (unsigned)(nt & 1) * 10240u : 10240u};
    asm volatile("" : "+s"(sa[0]), "+s"(sa[1]));
    const unsigned va = (unsigned)(row >> 4) * 20480u + gk * 1024 + l16;
#define LD_A1(x) A[x] = __builtin_amdgcn_raw_buffer_load_b128(rW, va, sa[(x) / 5] + (O_HP + ((x) % 5) * 2048), 16)
    unsigned bb[3][2];
#pragma unroll
    for (int c = 0; c < 6; ++c) {
      bb[c >> 1][c & 1] = (unsigned)gk * 5120u + ((unsigned)lane >> 2) * 160u + ((unsigned)lane & 3u) * 8u + sa[c & 1] * 5u + (c < 2 ? 96u - 96u * gk : c < 4 ? 64u : 96u * gk);
      PIN(bb[c >> 1][c & 1]);
    }
#define RD_BH(h, BLK) Bh[h] = __builtin_amdgcn_ds_read_tr16_b64_v4i16((LAS s16x4*)(lds + bb[kl_j((h) / 2) < 2 ? 0 : kl_j((h) / 2) == 2 ? 1 : 2][kl_i((h) / 2) / 5] + (BLK) + (kl_i((h) / 2) % 5) * 10240 + (kl_j((h) / 2) % 3 == 1 ? 32 : 0) + ((h) & 1) * 2560))
#define BLK1 0
    FENCE();
#pragma unroll
    for (int n = 0; n < 2 * PBE; ++n) RD_BH(n, BLK1);
    FENCE();
    if (DLY1) __builtin_amdgcn_s_sleep(DLY1);
    PRIO(3);
    TS_(10);
#pragma unroll
    for (int i = 0; i < NA1; ++i) { LD_A1(i); SB(); }
    PRIO(1);
#pragma unroll
    for (int n = 2 * PBE; n < 2 * PB1; ++n) RD_BH(n, BLK1);
    FENCE();
#pragma unroll
    for (int i = NA1; i < NAF; ++i) { LD_A1(i); SB(); }
    u32x2 s3v;
    FENCE();
    if (kh) PRIO(PH1); else PRIO(PHI);
    STAMP(11);
    const int sh = (nt >> 1) == 0 ? 1 : (nt >> 1) == 1 ? -1 : (nt >> 1) == 2 ? 2 : -2;
    const int rr = row + sh, rw = rr & (S_ - 1);
    unsigned keep = rr == rw ? 0xffffffffu : 0u;
    unsigned oh1 = ofr - (unsigned)(row >> 4) * 20480u - (unsigned)l15 * 16u + (unsigned)(rw >> 4) * TS + (unsigned)(rw & 15) * 16u + O_H1;
    asm volatile("" : "+v"(keep), "+v"(oh1));
    asm volatile("" : "+s"(w3s[2]), "+s"(w3s[3]));
    if constexpr (TSHI == 21) { TX_(29); sfor<0, 10>([&](auto I_) __attribute__((always_inline)) { PIN(A[decltype(I_)::v]); TX_(30 + decltype(I_)::v); __builtin_amdgcn_s_sleep(1); }); }
    WAIT_ALL(P_L1);
    SB();
    CHECK_A(true, A[0], 0, NAF, LD_A1);
    SB();
#define XG1(m) do { if constexpr ((m) % 2 == 0 && NAF + (m) / 2 < 10) LD_A1(NAF + (m) / 2); if constexpr ((m) == 13) s3v = __builtin_amdgcn_raw_buffer_load_b64(rW, (unsigned)row * 8u, O_ST3, 16); if constexpr ((m) == 4) TS_(11); } while (0)
#define XSL(u, s0, n, f0) do { if constexpr ((u) >= (s0) && (((u) - (s0) + 1) * (n)) / (20 - (s0)) > (((u) - (s0)) * (n)) / (20 - (s0))) LD_W3U((f0) + (((u) - (s0)) * (n)) / (20 - (s0))); } while (0)
#define XS1(u) XSL(u, SL1, NL1, NWH)
    KLOOP(true, PB1, BLK1, va, O_HP, XG1, XS1, P_XF1);
    TS_(12);
    if (__builtin_expect(bad1(s3v[0]), 0)) {
      unsigned spins = 0;
      do {
        __builtin_amdgcn_s_sleep(RSLEEP);
        s3v = __builtin_amdgcn_raw_buffer_load_b64(rW, (unsigned)row * 8u, O_ST3, 16);
        SPIN_STEP();
      } while (bad1(s3v[0]) && spins < RETRY_CAP);
    }
    const float ld = __uint_as_float(s3v[1]), lr = __builtin_amdgcn_rcpf(ld);
    float gca = lr * GC1, gcb = lr * lr * lr * GC2;
    asm volatile("" : "+v"(gca), "+v"(gcb));
    EXCHANGE((unsigned)P_XF1);
    TS_(13);
    {
      unsigned pk[6];
      if (gk == 0) {
        gelu_s<12>(e, gca, gcb, ld, pk);
        __builtin_amdgcn_raw_buffer_store_b64(u32x2{pk[4] & keep, pk[5] & keep}, rW, oh1 + 1024, 0, 16);
      } else {
        gelu_s<8>(e, gca, gcb, ld, pk);
      }
      st128(widen(pk[0] & keep, pk[1] & keep, pk[2] & keep, pk[3] & keep), rW, oh1 + (unsigned)(g & 1) * 504u, 0, 16);
    }
    FENCE();
    TS_(14);
    PRIO(0);
    LD_W3R(NWH + NL1, NWH + NL1 + NP0);
    FENCE();
    WAIT_ALL(P_XF1);
#pragma unroll
    for (int i = 0; i < 10; ++i) *(LAS u32x4*)(lds + 20480 + wv * 5120 + (i / 5) * 40960 + (i % 5) * 1024 + l16) = WS2[i];
    SIG(P_G1);
    STAMP(13);
    FENCE();
    LD_W3R(NWH + NL1 + NP0, NWH + NL1 + NP1);
    FENCE();
    const unsigned va2 = (unsigned)(row >> 4) * TS + gk * 1024 + l16;
    u32x2 hpv[3];
#define LD_A2(x) A[x] = __builtin_amdgcn_raw_buffer_load_b128(rW, va2, sa[(x) / 5] + (O_H1 + ((x) % 5) * 2048), 16)
    u32x4 hp4;
    const unsigned ofw = ofr + (unsigned)(g & 1) * 504u;
    hp4 = __builtin_amdgcn_raw_buffer_load_b128(rW, ofw, O_HP, 16);
    hpv[2] = __builtin_amdgcn_raw_buffer_load_b64(rW, gk ? OOB : ofr + 1024, O_HP, 16);
    FENCE();
    WAIT_ALL(P_G1);
    if (DLY2) __builtin_amdgcn_s_sleep(DLY2);
    PRIO(3);
    TS_(15);
#pragma unroll
    for (int i = 0; i < NA1; ++i) { LD_A2(i); SB(); }
    PRIO(1);
#define BLK2 20480
#pragma unroll
    for (int n = 0; n < 2 * PB2; ++n) RD_BH(n, BLK2);
    FENCE();
#pragma unroll
    for (int i = NA1; i < NAF; ++i) { LD_A2(i); SB(); }
    FENCE();
    if (kh) PRIO(PH2); else PRIO(PHI);
    float hf[12];
    { const u32x4 n_ = widen(hp4[0], hp4[1], hp4[2], hp4[3]); hpv[0] = u32x2{n_[0], n_[1]}; hpv[1] = u32x2{n_[2], n_[3]}; }
#pragma unroll
    for (int j = 0; j < 3; ++j) { hpv[j][0] -= CSH; PIN(hpv[j]); }
    STG(12, hf[i] = ((i & 1) ? hi2f(hpv[i >> 2][(i >> 1) & 1]) : lo2f(hpv[i >> 2][(i >> 1) & 1])) + __uint_as_float(s3v[0]); PIN(hf[i]));
    acc[3] = f32x4{hf[0], hf[1], hf[2], hf[3]}; acc[4] = f32x4{hf[4], hf[5], hf[6], hf[7]};
    acc[2] = gk ? f32x4{0.f, 0.f, 0.f, 0.f} : f32x4{hf[8], hf[9], hf[10], hf[11]};
    PIN(acc[2]); PIN(acc[3]); PIN(acc[4]);
    STAMP(14);
    if constexpr (TSHI == 21) { TX_(43); sfor<0, 10>([&](auto I_) __attribute__((always_inline)) { PIN(A[decltype(I_)::v]); TX_(44 + decltype(I_)::v); __builtin_amdgcn_s_sleep(1); }); }
    SB();
    CHECK_A(false, A[0], 0, NAF, LD_A2);
    SB();
#define XG2(m) do { if constexpr ((m) % 2 == 0 && NAF + (m) / 2 < 10) LD_A2(NAF + (m) / 2); if constexpr ((m) == 4) TS_(16); } while (0)
#define XS2(u) XSL(u, SL2, NL2, NWH + NL1 + NP1)
    KLOOP(false, PB2, BLK2, va2, O_H1, XG2, XS2, P_XF2);
    TS_(17);
    EXCHANGE((unsigned)P_XF2);
    TS_(18);
    unsigned xk[6];
    {
      if (!HFI) STG(12, e[i] += hf[i]; PIN(e[i]));
      STG(3, e[4 * i + 1] = plainv(e[4 * i + 1]); PIN(e[4 * i + 1]));
      STG(6, xk[i] = pack2(e[2 * i], e[2 * i + 1]); PIN(xk[i]));
      st128(widen(xk[0], xk[1], xk[2], xk[3]), rW, ofw, O_X1, 16);
      if (gk == 0) __builtin_amdgcn_raw_buffer_store_b64(u32x2{xk[4], xk[5]}, rW, ofr + 1024, O_X1, 16);
    }
    FENCE();
    TS_(19);
    PRIO(1);
    SIG(P_G2);
    float v[12];
    float s = 0.f;
#pragma unroll
    for (int j = 0; j < 3; ++j) {
      v[4 * j] = lo2f(xk[2 * j]); v[4 * j + 1] = hi2f(xk[2 * j]); v[4 * j + 2] = lo2f(xk[2 * j + 1]); v[4 * j + 3] = hi2f(xk[2 * j + 1]);
      if (j < 2 || gk == 0) s += (v[4 * j] + v[4 * j + 1]) + (v[4 * j + 2] + v[4 * j + 3]);
    }
    s = swap_add32(swap_add16(s));
    const float mean = s * (gk ? (1.f / 32.f) : (1.f / 48.f));
    float m2 = 0.f;
#pragma unroll
    for (int i = 0; i < 12; ++i) {
      const float dd = v[i] - mean;
      if (i < 8 || gk == 0) m2 += dd * dd;
    }
    m2 = swap_add32(swap_add16(m2));
    {
      u32x2 st = {canon(__float_as_uint(mean)), __float_as_uint(m2)};
      __builtin_amdgcn_raw_buffer_store_b64(st, rW, g == 0 ? (unsigned)(((row >> 4) * 16 + nt * 2 + gk) * 16 + l15) * 8u : OOB, O_ST4, 16);
    }
    FENCE();
    PRIO(0);
    FENCE();
    STAMP(16);
  }
  {
    constexpr int NR3 = 40 - NWH - NL1 - NP1 - NL2;
#define NOW4D(...) __VA_ARGS__
#define LD_W4(f) W4[f] = __builtin_amdgcn_raw_buffer_load_b128(rW, l16, w4a + (f) * 1024, 16)
    const unsigned va4 = O_H + (unsigned)(m0 >> 4) * TS + (unsigned)k4 * 1024u;
#define A4OFF(x) (((x) / NS) * TS + ((x) % NS) * 1024)
#define LD_A4(x) A4[(x) % AW4] = __builtin_amdgcn_raw_buffer_load_b128(rW, l16, va4 + A4OFF(x), 16)
    const int lane0 = lane;
#define LANES() int lane = lane0; PIN(lane); const int l15 = lane & 15, g = lane >> 4; const unsigned l16 = (unsigned)lane * 16u; \
    const unsigned fa = (unsigned)L_CTL + ((unsigned)lane & 7u) * 4u
#define FLG(a) *(volatile LAS unsigned*)(lds + (a))
    auto g3 = [&](auto KH_, u32x4 (&W4)[35], u32x4 (&A4)[AW4], u32x2 (&x1v)[3]) __attribute__((always_inline)) {
      constexpr int KH = decltype(KH_)::v, NS = 7, NW0 = KH ? 6 : AW4;
      LANES();
      const unsigned hw0 = (unsigned)(m0 >> 4) * TS + (unsigned)l15 * 16u + (unsigned)(g >> 1) * 256u + O_H;
      const unsigned csp = (unsigned)(L_CS3 + wv * 128 + g * 16), f1 = (unsigned)L_CTL + 16u + ((unsigned)lane & 3u) * 4u;
      u32x2 stv[4];
#define LD_ST() _Pragma("unroll") for (int a = 0; a < 4; ++a) stv[a] = __builtin_amdgcn_raw_buffer_load_b64(rW, (unsigned)((((m0 >> 4) + mg) * 16 + 4 * g) * 16 + l15) * 8u, O_ST4 + a * 128, 16)
#define ST_BAD() bad1(max(max(stv[0][0], stv[1][0]), max(stv[2][0], stv[3][0])))
      const u32x4 ones = {0x3f803f80u, 0x3f803f80u, 0x3f803f80u, 0x3f803f80u};
      f32x4 c3a = {0.f, 0.f, 0.f, 0.f}, c3b = {0.f, 0.f, 0.f, 0.f};
#define CS3R(a, b) do { _Pragma("unroll") for (int f_ = (a); f_ < (b); f_ += 2) { c3a = mfma16(W3[f_], ones, c3a); if (!KH || f_ < 20) c3b = mfma16(W3[f_ + 1], ones, c3b); } } while (0)
      TS_(20);
      PRIO(PHI);
      LD_W3R(40 - NR3, 40);
      FENCE();
      const unsigned so = O_X1 + (unsigned)mt * 81920u + (unsigned)wv * 10240u, d0 = (unsigned)(KH ? L_XB : L_XA) + (unsigned)mg * 10240u;
      CS3R(0, KH ? KC3B : KC3);
      asm volatile("" : "+v"(c3a), "+v"(c3b));
      FENCE();
      TS_(21);
      WAIT_2(KH ? P_XF2 : P_G2, KH ? P_G2 : P_XF2);
      if (KH ? DLY3B : DLY3) __builtin_amdgcn_s_sleep(KH ? DLY3B : DLY3);
      PIN(W3[38]); PIN(W3[39]);
      if (KH) {
#pragma unroll
        for (int f = 0; f < 10; ++f) W4[f] = W3[21 + 2 * f];
      }
      FENCE();
      PRIO(3);
      TS_(22);
#define STG_DMA() do { dma<4, 1>(l16, dW, so, d0); dma<4, 1>(l16, dW, so + 4096, d0 + 4096); dma<1, 1>(l16, dW, so + 8192, d0 + 8192); dma<1, 1>(l16, dW, so + 9216, d0 + 9216); } while (0)
      if (KH) {
        asm volatile("buffer_load_dwordx2 %0, %4, %5, %6 offen sc1\n\tbuffer_load_dwordx2 %1, %4, %5, %6 offen offset:128 sc1\n\t"
                     "buffer_load_dwordx2 %2, %4, %5, %6 offen offset:256 sc1\n\tbuffer_load_dwordx2 %3, %4, %5, %6 offen offset:384 sc1"
                     : "=&v"(stv[0]), "=&v"(stv[1]), "=&v"(stv[2]), "=&v"(stv[3]) : "v"((unsigned)((((m0 >> 4) + mg) * 16 + 4 * g) * 16 + l15) * 8u), "s"(dW), "s"(O_ST4) : "memory");
      }
      STG_DMA();
      FENCE();
      TS_(23);
      PRIO(PHI);
      CS3R(KH ? KC3B : KC3, 40);
      if (l15 == 0) { *(LAS f32x4*)(lds + csp) = c3a; *(LAS f32x4*)(lds + csp + 64) = c3b; }
      FENCE();
      if (KH) {
        asm volatile("s_waitcnt vmcnt(10)" : "+v"(stv[0]), "+v"(stv[1]), "+v"(stv[2]), "+v"(stv[3]));
        if (__builtin_expect(ST_BAD(), 0)) {
          unsigned spins = 0;
          do {
            __builtin_amdgcn_s_sleep(RSLEEP);
            LD_ST();
            SPIN_STEP();
          } while (ST_BAD() && spins < RETRY_CAP);
        }
        const float m0_ = __uint_as_float(stv[0][0]), q0 = __uint_as_float(stv[0][1]), m1 = __uint_as_float(stv[1][0]), q1 = __uint_as_float(stv[1][1]);
        const float m2_ = __uint_as_float(stv[2][0]), q2 = __uint_as_float(stv[2][1]), m3 = __uint_as_float(stv[3][0]), q3 = __uint_as_float(stv[3][1]);
        float sm = 48.f * (m0_ + m2_) + 32.f * (m1 + m3);
        sm = swap_add32(swap_add16(sm));
        const float mean = sm * (1.f / (float)D_);
        const float d0_ = m0_ - mean, d1 = m1 - mean, d2 = m2_ - mean, d3 = m3 - mean;
        float qs = (q0 + q1) + (q2 + q3) + 48.f * (d0_ * d0_ + d2 * d2) + 32.f * (d1 * d1 + d3 * d3);
        qs = swap_add32(swap_add16(qs));
        const float var = rbf(qs * (1.f / (float)D_));
        const float den = rbf(__builtin_amdgcn_sqrtf(rbf(var + 1e-5f))), r = __builtin_amdgcn_rcpf(den);
        float rc1 = r * GC1;
        PIN(rc1);
        if (g == 0) *(volatile LAS f32x4*)(lds + L_ST4 + (mg * 16 + l15) * 16) = f32x4{rbf(mean), rc1, r * r * r * GC2, den};
        SIG(P_SF);
      }
      FENCE();
      TS_(24);
      VMW(0);
      TS_(25);
      {
        unsigned spins = 0;
        for (;;) {
          u32x4 a[10];
          float mx = 0.f;
#pragma unroll
          for (int i = 0; i < 10; ++i) a[i] = *(volatile LAS u32x4*)(lds + d0 + i * 1024 + l16);
#pragma unroll
          for (int i = 0; i < 10; ++i) mx = amax3(mx, a[i]);
          if (__builtin_expect(__builtin_amdgcn_ballot_w64(mx >= PTHR) == 0ull, 1)) break;
          SPIN_STEP();
          if (spins > RETRY_CAP) break;
          PRIO(0);
          __builtin_amdgcn_s_sleep(RSLEEP);
          STG_DMA();
          PRIO(PHI);
          asm volatile("s_waitcnt vmcnt(0)" ::: "memory");
        }
        SIG(P_SA);
      }
      TS_(26);
      STAMP(17);
      FENCE();
      constexpr int NP = KH ? NPH : NPK, F0 = KH ? 10 : 0, NEQ = NP + 6, EQ0 = KH ? EC0 : EA0, EQ1 = KH ? EC1 : EA1, NL0 = F0 + NEQ, NLT = (KH ? 30 : 35) - NL0;
      const unsigned pks = (unsigned)(KH ? L_PB + NPK * 1024 + mg * (NPH * 1024) : wv < 3 ? wv * (NPK * 1024) : L_PB);
      constexpr auto ecnt = [](int q) { return q <= EQ0 ? 0 : q >= EQ1 ? NEQ : ((q - EQ0) * NEQ + EQ1 - EQ0 - 1) / (EQ1 - EQ0); };
#define ESLOT(q) sfor<ecnt(q), ecnt((q) + 1)>([&](auto U_) __attribute__((always_inline)) { constexpr int u = decltype(U_)::v; \
        if constexpr (u < NP) { NOW4D(dma<1, 1>(l16, dW, w4a + (F0 + 6 + u) * 1024, pks + u * 1024)); } else LD_W4(F0 + u - NP); })
      static_assert(3 * NPK <= 32 && NPK + 4 * NPH <= 36 && MV0 + NP <= 20 && NLT <= 19 && NLT >= MV0, "w4");
      sfor<0, 2>([&](auto P_) __attribute__((always_inline)) {
        constexpr int p = decltype(P_)::v, NV = KH ? 12 : 16;
        TS_(28 + 8 * p + 0);
        if (p == 0) WAIT_2(P_SA, P_G2); else WAIT_HALF(1, P_SA);
        f32x4 acc[4];
        PRIO(KH ? (p ? PK1 : PK2) : PK0);
        TS_(28 + 8 * p + 1);
        u32x4 Ar[AD3][2];
        unsigned ab[2] = {l16 + (unsigned)((p ? L_XB : L_XA) + (KH ? hb * 30720 : 0)), l16 + (unsigned)((p ? L_XB : L_XA) + 20480 - (KH ? hb * 10240 : 0))};
        const unsigned sp = (unsigned)(L_ST4 + p * 512 + l15 * 16), fp = (unsigned)(L_CTL + (wv ^ 2) * 4), xs = (unsigned)(32768 + p * 4096) + l16;
        PIN(ab[0]);
        if (KH) PIN(ab[1]);
        f32x4 cs[3], st[2], rx;
        unsigned sf_, sx_;
#define RD_A3(s, h) Ar[(s) % AD3][h] = *(const LAS u32x4*)(lds + (KH ? ab[h] : ab[0] + (h) * 20480) + (s) * 1024)
#pragma unroll
        for (int s = 0; s < AD3 - 1; ++s) { RD_A3(s, 0); RD_A3(s, 1); }
        SB();
        sfor<0, 20>([&](auto S_) __attribute__((always_inline)) {
          constexpr int s = decltype(S_)::v;
          constexpr bool rd = s + AD3 - 1 < 20, T2 = !KH || s < 10;
          mf<s == 0>(acc[0], W3[2 * s], Ar[s % AD3][0]);
          SB();
          if constexpr (rd) RD_A3(s + AD3 - 1, 0);
          else if constexpr (s == 18) cs[0] = *(const LAS f32x4*)(lds + csp);
          else st[0] = *(const volatile LAS f32x4*)(lds + sp + (KH ? hb * 256 : 0));
          SB();
          mf<s == 0>(acc[1], W3[2 * s], Ar[s % AD3][1]);
          SB();
          if constexpr (rd) RD_A3(s + AD3 - 1, 1);
          else if constexpr (s == 18) cs[1] = *(const LAS f32x4*)(lds + csp + 64);
          else st[1] = *(const volatile LAS f32x4*)(lds + sp + 256 - (KH ? hb * 256 : 0));
          SB();
          if constexpr (T2) { mf<s == 0>(acc[2], W3[2 * s + 1], Ar[s % AD3][0]); SB(); }
          if constexpr (p == 0 && s == 15) sf_ = FLG(f1);
          if constexpr (p == 0 && s == 18) NEED_AT(sf_, f1, P_SF);
          if constexpr (p == 0) ESLOT(s);
          if constexpr (p == 1 && s >= 1 && s <= NLT) LD_W4(NL0 + s - 1);
          if constexpr (p == 1 && s == MV0 - 1) VMW(MV0 + 1);
          SB();
          if constexpr (T2) { mf<s == 0>(acc[3], W3[2 * s + 1], Ar[s % AD3][1]); SB(); }
          if constexpr (KH && s + AD3 == 10) { ab[0] -= hb * 20480; ab[1] -= hb * 20480; PIN(ab[0]); PIN(ab[1]); }
          if constexpr (KH && s == 12) { *(volatile LAS f32x4*)(lds + xs + mg * 1024) = acc[3]; SIG(p ? P_S1 : P_S0); }
          if constexpr (KH && s == 14) sx_ = FLG(fp);
          if constexpr (KH && s == 17) { NEED_AT(sx_, fp, p ? P_S1 : P_S0); rx = *(const volatile LAS f32x4*)(lds + xs + (mg ^ 2) * 1024); cs[2] = *(const volatile LAS f32x4*)(lds + csp + (hb ? -192 : 320)); }
          if constexpr (p == 1 && s >= MV0 && s < MV0 + NP) W4[F0 + 6 + s - MV0] = *(const LAS u32x4*)(lds + pks + l16 + (s - MV0) * 1024);
          if constexpr (p == 1 && s >= 1 && s < 4 - KH) {
            static_assert(H0S >= 4 && H0S < 12, "x1");
            const int tu = KH == 0 ? (s == 3 ? 3 : s - 1) : wv < 6 ? 2 : s == 1 ? wv & 1 : wv - 4, ju = KH == 0 ? wv : wv < 6 ? 2 * wv - 6 - s : 4;
            x1v[s - 1] = *(const LAS u32x2*)(lds + (tu < 2 ? L_XA : L_XB - 40960) + tu * 20480 + (nt * 10 + 2 * ju) * 256 + (g >> 1) * 256 + (g & 1) * 8 + l15 * 16);
          }
          if constexpr (p == 1 && s == H0S) SIG(P_H0);
          if constexpr (s == 9) TS_(28 + 8 * p + 2);
          SB();
        });
        TS_(28 + 8 * p + 3);
        PRIO(KH && p ? PE1 : PE0);
        SIG(p ? P_KB : P_KA);
        if constexpr (KH) asm volatile("s_nop 3" : "+v"(acc[0]), "+v"(acc[1]), "+v"(acc[2])); else asm volatile("s_nop 3" : "+v"(acc[0]), "+v"(acc[1]), "+v"(acc[2]), "+v"(acc[3]));
        STAMP(18 + 2 * p);
        {
          float e[NV], x[NV];
          unsigned pk[NV / 2];
#define SQ(i) st[((i) >> 2) & 1]
#define EPS(q) do { if constexpr (p == 0) { ESLOT(20 + (q)); } if constexpr (p == 1 && (q) == HG - 1) sf_ = FLG(fa); \
                    if constexpr (p == 1 && (q) == HG) { NEED_AT(sf_, fa, P_H0); _Pragma("unroll") for (int x = 0; x < NW0; ++x) { LD_A4(x); } } SB(); } while (0)
          if constexpr (KH) STG(4, x[i] = __builtin_fmaf(-st[0][0], cs[2][i], rx[i]); PIN(x[i]));
          STG(NV, e[i] = __builtin_fmaf(-SQ(i)[0], cs[i >> 3][i & 3], acc[i >> 2][i & 3]); PIN(e[i]));
          if constexpr (KH) STG(4, e[8 + i] += x[i]; PIN(e[8 + i]));
          TS_(28 + 8 * p + 4);
          EPS(0);
          STG(NV, x[i] = e[i] * e[i]; PIN(x[i]));
          EPS(1);
          STG(NV, x[i] = __builtin_fmaf(x[i], SQ(i)[2], SQ(i)[1]); PIN(x[i]));
          EPS(2);
          STG(NV, x[i] *= e[i]; PIN(x[i]));
          EPS(3);
          STG(NV, x[i] = __builtin_amdgcn_exp2f(x[i]); PIN(x[i]));
          TS_(28 + 8 * p + 5);
          EPS(4);
          STG(NV, x[i] = __builtin_fmaf(x[i], SQ(i)[3], SQ(i)[3]); PIN(x[i]));
          EPS(5);
          STG(NV, x[i] = __builtin_amdgcn_rcpf(x[i]); PIN(x[i]));
          EPS(6);
          STG(NV, e[i] *= x[i]; PIN(e[i]));
          TS_(28 + 8 * p + 6);
          EPS(7);
          STG(NV / 4, e[4 * i + 1] = codedv(e[4 * i + 1]); PIN(e[4 * i + 1]));
          STG(NV / 2, pk[i] = pack2(e[2 * i], e[2 * i + 1]); PIN(pk[i]));
          const unsigned hw1 = hw0 + (unsigned)((g & 1) ^ (KH ? hb : 0)) * TS;
          st128(widen(pk[0], pk[1], pk[2], pk[3]), rW, hw1, (unsigned)(sb3 + jl) * 512u + 2 * p * TS, 16);
          if constexpr (KH) __builtin_amdgcn_raw_buffer_store_b64(u32x2{pk[4], pk[5]}, rW, hw0 + (unsigned)(g & 1) * 8u, two ? (unsigned)(sb3 + jm) * 512u + (unsigned)(2 * p + hb) * TS : OOB, 16);
          else st128(widen(pk[4], pk[5], pk[6], pk[7]), rW, hw1, (unsigned)(sb3 + jm) * 512u + 2 * p * TS, 16);
          FENCE();
          EPS(8);
        }
        TS_(28 + 8 * p + 7);
        STAMP(19 + 2 * p);
      });
    };
#define PB(t) (81920 - (t) * 40960)
    const __amdgpu_buffer_rsrc_t rO = __builtin_amdgcn_make_buffer_rsrc((void*)out, 0, SD2, RS3);
    auto g4 = [&](auto KD_, u32x4 (&W4)[35], u32x4 (&A4)[AW4], u32x2 (&x1v)[3]) __attribute__((always_inline)) {
      constexpr int KD = decltype(KD_)::v, NS = KD == 1 ? 6 : 7, PR = KD ? P41 : P40;
      constexpr int GS = NS + (KD == 0 ? GSA : KD == 1 ? GSB : GSC);
      constexpr auto nreq = [](int x) { int n = AW4; for (int y = 0; y <= x; ++y) { int m = y + AW4 < 4 * NS ? y + AW4 : 4 * NS; if (y < GS && m > 2 * NS) m = 2 * NS; n = m < n + 3 ? (m > n ? m : n) : n + 3; } return n; };
      LANES();
      TS_(44);
      if constexpr (KD) {
#pragma unroll
        for (int x = 6; x < AW4; ++x) { LD_A4(x); }
      }
      if constexpr (KD == 2) {
#pragma unroll
        for (int f = 30; f < 35; ++f) { LD_W4(f); }
      }
      FENCE();
      PRIO(PR);
      u32x2 xv[2];
      unsigned fl_, fh_, fu_, pu_;
      f32x4 acc[2][5], rs[4], gq;
      float u_[4], y_[4], m4_;
      unsigned long long bm4;
      unsigned pw = (unsigned)(wv * 5120) + l16;
      PIN(pw);
      const unsigned fo0 = (unsigned)((m0 >> 4) * 20480 + ((n0 + 4 * g) >> 3) * 256 + (g & 1) * 8 + l15 * 16), oo0 = (unsigned)((m0 + l15) * D_ + n0 + 4 * g) * 2u;
#define U_LD(r, t, j) xv[r] = __builtin_amdgcn_raw_buffer_load_b64(rW, fo0, O_XO + (unsigned)((t) * 20480 + (j) * 512), 16)
#define U_PC(q, t, j, y)                                                                               \
  do {                                                                                                  \
    if constexpr ((q) == 0) { fu_ = FLG(fa); pu_ = (unsigned)(PB(t) + (j) * 1024) + l16; PIN(pu_); }    \
    if constexpr ((q) == 1) { gq = *(const LAS f32x4*)(lds + L_GB + ((j) * 16 + 4 * g) * 4); }          \
    if constexpr ((q) >= 1 && (q) < 5) { constexpr int i = (q) - 1; u_[i] = (i & 1) ? hi2f(x1v[y][i >> 1]) : lo2f(x1v[y][i >> 1]); PIN(u_[i]); } \
    if constexpr ((q) == 5) NEED_AT(fu_, fa, P_P0 + 1 + (t));                                           \
    if constexpr ((q) >= 6 && (q) < 10) rs[(q) - 6] = *(const volatile LAS f32x4*)(lds + pu_ + ((q) - 6) * 5120); \
    if constexpr ((q) >= 11 && (q) < 27) { constexpr int w = ((q) - 11) >> 1, h = (((q) - 11) & 1) * 2;  \
      u_[h] += rs[w & 3][h]; PIN(u_[h]); u_[h + 1] += rs[w & 3][h + 1]; PIN(u_[h + 1]);                 \
      if constexpr (h == 2 && w < 4) rs[w] = *(const volatile LAS f32x4*)(lds + pu_ + (4 + w) * 5120); } \
    if constexpr ((q) >= 6 && (q) < 10) { constexpr int i = (q) - 6; y_[i] = (i & 1) ? hi2f(xv[0][i >> 1]) : lo2f(xv[0][i >> 1]); PIN(y_[i]); } \
    if constexpr ((q) == 27 || (q) == 28) { constexpr int h = ((q) - 27) * 2; u_[h] = __builtin_fmaf(u_[h], gq[h], y_[h]); PIN(u_[h]); u_[h + 1] = __builtin_fmaf(u_[h + 1], gq[h + 1], y_[h + 1]); PIN(u_[h + 1]); } \
    if constexpr ((q) == 29) __builtin_amdgcn_raw_buffer_store_b64(u32x2{pack2(u_[0], u_[1]), pack2(u_[2], u_[3])}, rO, oo0, (unsigned)((t) * (16 * D_ * 2) + (j) * 32), OAX); \
  } while (0)
      {
        unsigned long long b0;
        const float m0_ = amin2(A4[0]);
        CMP_BAD(1, b0, m0_);
        A4[0][0] -= scsh; A4[0][2] -= scsh;
        RETRY_C(A4[0], b0, l16, va4, 0, PR);
        asm volatile("s_nop 0" : "+v"(A4[0]));
      }
      SB();
      const int tc = KD == 0 ? 3 : KD == 1 ? 2 : wv - 4, jc = KD == 0 ? wv : KD == 1 ? 2 * wv - 8 : 4;
      sfor<0, 20 * NS>([&](auto M_) __attribute__((always_inline)) {
        constexpr int m = decltype(M_)::v, x = m / 5, j = m % 5, t = x / NS, s = x % NS, r0 = nreq(x - 2), r1 = nreq(x - 1);
        constexpr bool nx = x + 1 < 4 * NS;
        u32x4& an = A4[(x + 1) % AW4];
        mf<s == 0>(acc[t & 1][j], W4[5 * s + j], A4[x % AW4]);
        SB();
        if constexpr (j == 0) {
          if constexpr (x == GS + 1) NEED_AT(fh_, fa, P_H1);
          if constexpr (r0 < r1) { LD_A4(r0); }
        }
        if constexpr (j == 1 && nx) { m4_ = amin2(an); PIN(m4_); }
        if constexpr (j == 2 && nx) { CMP_BAD(1, bm4, m4_); an[0] -= scsh; }
        if constexpr (j == 3 && nx) { an[2] -= scsh; PIN(an); }
        if constexpr (j == 3 && r0 + 1 < r1) { LD_A4(r0 + 1); }
        if constexpr (j == 4) {
          if constexpr (nx) RETRY_C(an, bm4, l16, va4, A4OFF(x + 1), PR);
          if constexpr (r0 + 2 < r1) { LD_A4(r0 + 2); }
          if constexpr (x == GS) fh_ = FLG(fa);
          if constexpr (x == H1S) SIG(P_H1);
          if constexpr (s == 0) TS_(45 + 4 * t);
          if constexpr (s == 3) TS_(45 + 4 * t + 1);
          if constexpr (s == NS - 1) TS_(45 + 4 * t + 2);
          static_assert(GS + 1 < 2 * NS, "order");
          if constexpr (KD == 0 && t == 1 && s == 0) U_LD(0, 0, wv);
          if constexpr (KD == 0 && t == 2 && s == 3) U_LD(0, 1, wv);
          if constexpr (KD == 2 && t == 2 && s == 3) U_LD(0, wv & 1, 4);
          if constexpr (KD == 1 && t == 3 && s == 3) U_LD(0, 2, jc + 1);
          if constexpr (t == 2 && s == 5) U_LD(1, tc, jc);
          if constexpr (t == 3 && s == NS - 2) fl_ = FLG(L_CTL);
        }
        constexpr bool un = (KD == 0 && t >= 2) || (KD == 2 && t == 3);
        constexpr int wq = 5 * s + j, w0 = un ? W0U : 4;
        if constexpr (t > 0 && wq >= w0 && wq < w0 + 5 * WSP && (wq - w0) % WSP == 0) *(volatile LAS f32x4*)(lds + PB(t - 1) + pw + ((wq - w0) / WSP) * 1024) = acc[(t - 1) & 1][(wq - w0) / WSP];
        if constexpr (t > 0 && wq == w0 + 1 + 4 * WSP) SIG(P_P0 + t);
        static_assert(W0U + 1 + 4 * WSP < 35 && U0 + 30 <= 35, "unit");
        if constexpr (un && KD == 0 && wq >= U0) U_PC(wq - U0, t - 2, wv, t - 2);
        if constexpr (un && KD == 2 && wq >= U0) U_PC(wq - U0, wv & 1, 4, 0);
        SB();
      });
      asm volatile("s_nop 3" : "+v"(acc[1][0]), "+v"(acc[1][1]), "+v"(acc[1][2]), "+v"(acc[1][3]), "+v"(acc[1][4]));
#pragma unroll
      for (int j = 0; j < 4; ++j) *(volatile LAS f32x4*)(lds + L_PB + wv * 4096 + l16 + j * 1024) = acc[1][j];
      NEED_AT(fl_, (unsigned)L_CTL, P_P0 + 3);
      *(volatile LAS f32x4*)(lds + PB(0) + pw) = acc[1][4];
      SIG(P_P0 + 4);
      TS_(60);
#define UNIT(r, y, t, j, base, str)                                                                        \
  do {                                                                                                  \
    unsigned pq = (unsigned)(base) + l16, oq = oo0 + (unsigned)((t) * (16 * D_ * 2) + (j) * 32);        \
    f32x4 gq_ = *(const LAS f32x4*)(lds + L_GB + ((j) * 16 + 4 * g) * 4);                               \
    f32x2 xa = up2(xv[r][0]), xb = up2(xv[r][1]), ya = up2(x1v[y][0]), yb = up2(x1v[y][1]);             \
    asm volatile("" : "+v"(xa), "+v"(xb), "+v"(ya), "+v"(yb), "+v"(gq_), "+v"(pq), "+v"(oq));           \
    f32x4 r8[8];                                                                                        \
    unsigned spins = 0;                                                                                 \
    for (;;) {                                                         \
      const unsigned fl = FLG(fa);                                                                      \
      _Pragma("unroll") for (int w = 0; w < 8; ++w) r8[w] = *(const volatile LAS f32x4*)(lds + pq + w * (str)); \
      if (__builtin_amdgcn_ballot_w64(fl < (unsigned)(P_P0 + 1 + (t))) == 0ull) break;                  \
      SPIN_STEP();                                                                                      \
      if (spins > SPIN_CAP) break;                                                                      \
    }                                                                                                   \
    const f32x4 sm = ((r8[0] + r8[1]) + (r8[2] + r8[3])) + ((r8[4] + r8[5]) + (r8[6] + r8[7]));         \
    const f32x2 a_ = FMA2(f32x2{sm[0], sm[1]} + ya, (f32x2{gq_[0], gq_[1]}), xa), b_ = FMA2(f32x2{sm[2], sm[3]} + yb, (f32x2{gq_[2], gq_[3]}), xb); \
    __builtin_amdgcn_raw_buffer_store_b64(u32x2{pack2(a_[0], a_[1]), pack2(b_[0], b_[1])}, rO, oq, 0, OAX); \
    FENCE();                                                                                            \
  } while (0)
      if constexpr (KD == 0) UNIT(1, 2, 3, wv, L_PB + wv * 1024, 4096);
      else UNIT(1, 1, tc, jc, tc == 3 ? PB(0) : PB(2) + jc * 1024, 5120);
      if constexpr (KD == 1) UNIT(0, 0, 2, jc + 1, PB(2) + jc * 1024 + 1024, 5120);
    };
#define KILL(V, N) _Pragma("unroll") for (int f_ = 0; f_ < (N); ++f_) asm volatile("" : "=v"(V[f_]))
    int arm = wv >> 1;
    if (arm < 2) {
      u32x4 W4[35], A4[AW4];
      u32x2 x1v[3];
      g3(IC<0>{}, W4, A4, x1v);
      g4(IC<0>{}, W4, A4, x1v);
      KILL(W3, 40 - NR3);
    }
    asm volatile("" : "+s"(arm));
    if (arm >= 2) {
      u32x4 W4[35], A4[AW4];
      u32x2 x1v[3];
      g3(IC<1>{}, W4, A4, x1v);
      if (arm == 3) {
        g4(IC<2>{}, W4, A4, x1v);
        KILL(W4, 30);
        KILL(A4, 6);
      }
      asm volatile("" : "+s"(arm));
      if (arm == 2) g4(IC<1>{}, W4, A4, x1v);
    }
    STAMP(27);
    CLK(31);
  }
}

extern "C" void kernel_launch(void* const* d_in, const int* in_sizes, int n_in, void* d_out, int out_size,
                              void* d_ws, size_t ws_size, hipStream_t stream) {
  const bf16* A_q = (const bf16*)d_in[0];
  const bf16* A_k = (const bf16*)d_in[1];
  const bf16* A_v = (const bf16*)d_in[2];
  const bf16* A_shift_k = (const bf16*)d_in[3];
  const bf16* A_ssm_k = (const bf16*)d_in[4];
  const bf16* A_ssm_D = (const bf16*)d_in[5];
  const bf16* w1 = (const bf16*)d_in[7];
  const bf16* w2 = (const bf16*)d_in[8];
  const bf16* w3 = (const bf16*)d_in[9];
  const bf16* w4 = (const bf16*)d_in[10];
  const bf16* c = (const bf16*)d_in[11];
  const bf16* w_mod = (const bf16*)d_in[12];
  char* ws = (char*)d_ws;
  (void)hipFuncSetAttribute((const void*)k_main, hipFuncAttributeMaxDynamicSharedMemorySize, L_TOT);
  k_main<<<256, 512, L_TOT, stream>>>(A_k, A_v, ws, A_shift_k, A_ssm_k, w_mod, c, w3, w4, A_q, w1, w2, A_ssm_D, (bf16*)d_out);
}
```

```cpp
#include <hip/hip_runtime.h>
#include <hip/hip_bf16.h>
#include <stdint.h>

typedef __hip_bfloat16 bf16;
typedef __attribute__((ext_vector_type(8))) short bf16x8;
typedef __attribute__((ext_vector_type(4))) short s16x4;
typedef __attribute__((ext_vector_type(4))) float f32x4;
typedef __attribute__((ext_vector_type(2))) float f32x2;
typedef __attribute__((ext_vector_type(4))) unsigned u32x4;
typedef __attribute__((ext_vector_type(2))) unsigned u32x2;
#define LAS __attribute__((address_space(3)))

static constexpr int S_ = 2048;
static constexpr int D_ = 640;
static constexpr int F_ = 1728;
static constexpr unsigned SENT = 0xffffffffu;
static constexpr int RS3 = 0x00020000;
static constexpr unsigned OOB = 0x40000000u;

static constexpr unsigned SD2 = S_ * D_ * 2;
static constexpr unsigned DF2 = D_ * F_ * 2;
static constexpr unsigned O_WT3 = 0, O_WT4 = DF2;
static constexpr unsigned O_EP = 2 * DF2;
static constexpr unsigned O_MOD = O_EP + 256;
static constexpr unsigned O_MP = O_MOD + 16384;
static constexpr unsigned O_FL1 = O_MP + 131072;
static constexpr unsigned O_KVT = O_FL1 + 2048;
static constexpr unsigned O_HP = O_KVT, O_X1 = O_KVT;
static constexpr unsigned TS = 57344;
static constexpr unsigned O_H = O_KVT + 384 * 8192;
static constexpr unsigned O_Z = O_H, O_H1 = O_H + 20480;
static constexpr unsigned O_ST3 = O_H + 128 * TS;
static constexpr unsigned O_ST4 = O_ST3 + S_ * 8;
static constexpr unsigned O_XO = O_ST4 + S_ * 16 * 8;
static constexpr unsigned WS_USED = O_XO + SD2;
#define KVROW(c) ((c) < 384u ? O_KVT + (c) * 8192u : O_H + 40960u + (((c) - 384u) >> 1) * TS + ((c) & 1u) * 8192u)
static constexpr unsigned CODED = 0x7f7f7f7fu;
static constexpr unsigned CSH = 98u << 23;
static constexpr float PTHR = 0x1p48f, PMAX = 0x1p47f, CMUL = 0x1p98f, CMAX = 0x1p127f;
__device__ __forceinline__ float plainv(float x) { return __builtin_amdgcn_fmed3f(x, -PMAX, PMAX); }
__device__ __forceinline__ float codedv(float x) { return __builtin_copysignf(__builtin_amdgcn_fmed3f(__builtin_fabsf(x * CMUL), PTHR, CMAX), x); }

__device__ __forceinline__ float rbf(float x) { return __bfloat162float(__float2bfloat16(x)); }
__device__ __forceinline__ float lo2f(unsigned w) { return __uint_as_float(w << 16); }
__device__ __forceinline__ float hi2f(unsigned w) { return __uint_as_float(w & 0xffff0000u); }
typedef __bf16 bf16v2 __attribute__((ext_vector_type(2)));
__device__ __forceinline__ unsigned pack2(float a, float b) {
  bf16v2 r = __builtin_convertvector(f32x2{a, b}, bf16v2);
  return __builtin_bit_cast(unsigned, r);
}

static constexpr float GC1 = -2.f * 0.7978845608028654f * 1.4426950408889634f;
static constexpr float GC2 = GC1 * 0.044715f;
__device__ __forceinline__ float row16_sum(float v) {
  v += __builtin_bit_cast(float, __builtin_amdgcn_update_dpp(0, __builtin_bit_cast(int, v), 0xB1, 0xf, 0xf, true));
  v += __builtin_bit_cast(float, __builtin_amdgcn_update_dpp(0, __builtin_bit_cast(int, v), 0x4E, 0xf, 0xf, true));
  v += __builtin_bit_cast(float, __builtin_amdgcn_update_dpp(0, __builtin_bit_cast(int, v), 0x141, 0xf, 0xf, true));
  v += __builtin_bit_cast(float, __builtin_amdgcn_update_dpp(0, __builtin_bit_cast(int, v), 0x140, 0xf, 0xf, true));
  return v;
}
typedef volatile LAS unsigned* ldsw_t;
static constexpr int CV_STR = 4160;
static constexpr int CV_U = 8 * CV_STR;
static constexpr int CV_SLOT = CV_U + 4608;
static constexpr int SL_A = 46592, SL_C = SL_A + CV_SLOT, SL_B = SL_C + CV_SLOT;
static constexpr int L_KV = 0;
static constexpr int L_RT = L_KV + 24576;
static constexpr int L_MP = L_RT + 20480;
static constexpr int L_XCH = 139264;
static constexpr int L_XA = 81920, L_XB = 40960;
static constexpr int L_PB = 122880;
static constexpr int L_CTL = 160256;
static constexpr int L_MOD = 103424;
static constexpr int L_GB = L_CTL + 64;
static constexpr int L_CS3 = L_CTL + 384;
static constexpr int L_ST4 = L_CTL + 1408;
static constexpr int L_STAMP = L_CTL + 2432;
static constexpr int L_TOT = 163840;
static_assert(L_MP + 1024 <= SL_A && SL_B + CV_SLOT <= L_CTL && L_MOD >= SL_C + 16384 && L_MOD >= 102400 && L_MOD + 7680 <= SL_C + CV_U, "lds");
static constexpr int YROW = 1296;
enum { P_KV = 1, P_K, P_B, P_M, P_B2, P_U, P_C2, P_A2, P_RT, P_GA, P_ZC, P_X, P_X2, P_T, P_Z, P_RP, P_Z2, P_L1, P_XF1, P_G1, P_XF2, P_G2, P_SF, P_SA, P_S0, P_KA, P_H0, P_S1, P_KB, P_H1, P_P0 };
static constexpr unsigned SPIN_CAP = 1u << 21;
static constexpr unsigned RETRY_CAP = 1u << 15;

__device__ __forceinline__ float swap_add16(float v) {
  const unsigned u = __float_as_uint(v);
  auto r = __builtin_amdgcn_permlane16_swap(u, u, false, false);
  return __uint_as_float((unsigned)r[0]) + __uint_as_float((unsigned)r[1]);
}
__device__ __forceinline__ float swap_add32(float v) {
  const unsigned u = __float_as_uint(v);
  auto r = __builtin_amdgcn_permlane32_swap(u, u, false, false);
  return __uint_as_float((unsigned)r[0]) + __uint_as_float((unsigned)r[1]);
}
__device__ __forceinline__ float wave_sum2(float v) { return swap_add32(swap_add16(row16_sum(v))); }
__device__ __forceinline__ f32x2 up2(unsigned u) { return f32x2{lo2f(u), hi2f(u)}; }
__device__ __forceinline__ f32x2 rbf2(f32x2 v) { return up2(pack2(v[0], v[1])); }
#define FMA2 __builtin_elementwise_fma
__device__ __forceinline__ f32x2 rf2(f32x2 v, f32x2 cp, f32x2 cn) { return FMA2(v, cn, FMA2(v, cp, v)); }
__device__ __forceinline__ void ln5(const f32x2* in, unsigned* out) {
  const f32x2 s2 = ((in[0] + in[1]) + (in[2] + in[3])) + in[4];
  const float mean = wave_sum2(s2[0] + s2[1]) * (1.f / (float)D_);
  f32x2 d[5];
#pragma unroll
  for (int j = 0; j < 5; ++j) d[j] = in[j] - mean;
  const f32x2 q2 = (d[0] * d[0] + d[1] * d[1]) + (d[2] * d[2] + d[3] * d[3]) + d[4] * d[4];
  const float m = rbf(mean);
#pragma unroll
  for (int j = 0; j < 5; ++j) d[j] = rbf2(in[j] - m);
  const float q = wave_sum2(q2[0] + q2[1]);
  const float var = rbf(q * (1.f / (float)D_));
  const float den = rbf(__builtin_amdgcn_sqrtf(rbf(var + 1e-5f)));
  const float rinv = __builtin_amdgcn_rcpf(den);
#pragma unroll
  for (int j = 0; j < 5; ++j) { const f32x2 o = d[j] * rinv; out[j] = pack2(o[0], o[1]); }
}
#define PIN(x) asm volatile("" : "+v"(x))
#define STG(N, stmt) do { _Pragma("unroll") for (int i = 0; i < N; ++i) { stmt; } __builtin_amdgcn_sched_barrier(0); } while (0)
__device__ __forceinline__ void ln5s(const unsigned* in, unsigned* out) {
  float x[10], d[10], t[4];
  STG(10, x[i] = (i & 1) ? hi2f(in[i >> 1]) : lo2f(in[i >> 1]); PIN(x[i]));
  STG(4, t[i] = x[(i & 1) + (i >> 1) * 4] + x[(i & 1) + (i >> 1) * 4 + 2]; PIN(t[i]));
  STG(2, t[i] += t[2 + i]; PIN(t[i]));
  STG(2, t[i] += x[8 + i]; PIN(t[i]));
  const float mean = wave_sum2(t[0] + t[1]) * (1.f / (float)D_);
  STG(10, d[i] = x[i] - mean; PIN(d[i]));
  STG(4, t[i] = d[(i & 1) + (i >> 1) * 4] * d[(i & 1) + (i >> 1) * 4]; PIN(t[i]));
  STG(4, t[i] = __builtin_fmaf(d[(i & 1) + (i >> 1) * 4 + 2], d[(i & 1) + (i >> 1) * 4 + 2], t[i]); PIN(t[i]));
  STG(2, t[i] += t[2 + i]; PIN(t[i]));
  STG(2, t[i] = __builtin_fmaf(d[8 + i], d[8 + i], t[i]); PIN(t[i]));
  const float m = rbf(mean);
  STG(10, d[i] = x[i] - m; PIN(d[i]));
  unsigned p[5];
  STG(5, p[i] = pack2(d[2 * i], d[2 * i + 1]); PIN(p[i]));
  const float q = wave_sum2(t[0] + t[1]);
  const float var = rbf(q * (1.f / (float)D_));
  const float den = rbf(__builtin_amdgcn_sqrtf(rbf(var + 1e-5f)));
  const float rinv = __builtin_amdgcn_rcpf(den);
  STG(10, d[i] = ((i & 1) ? hi2f(p[i >> 1]) : lo2f(p[i >> 1])) * rinv; PIN(d[i]));
  STG(5, out[i] = pack2(d[2 * i], d[2 * i + 1]); PIN(out[i]));
}
template <int N>
__device__ __forceinline__ void wave_sums(float* v) {
  STG(N, v[i] += __builtin_bit_cast(float, __builtin_amdgcn_update_dpp(0, __builtin_bit_cast(int, v[i]), 0xB1, 0xf, 0xf, true)); PIN(v[i]));
  STG(N, v[i] += __builtin_bit_cast(float, __builtin_amdgcn_update_dpp(0, __builtin_bit_cast(int, v[i]), 0x4E, 0xf, 0xf, true)); PIN(v[i]));
  STG(N, v[i] += __builtin_bit_cast(float, __builtin_amdgcn_update_dpp(0, __builtin_bit_cast(int, v[i]), 0x141, 0xf, 0xf, true)); PIN(v[i]));
  STG(N, v[i] += __builtin_bit_cast(float, __builtin_amdgcn_update_dpp(0, __builtin_bit_cast(int, v[i]), 0x140, 0xf, 0xf, true)); PIN(v[i]));
  unsigned a[N], b[N];
  STG(N, a[i] = __float_as_uint(v[i]); b[i] = a[i]; PIN(b[i]));
  STG(N, { auto r = __builtin_amdgcn_permlane16_swap(a[i], b[i], false, false); a[i] = r[0]; b[i] = r[1]; PIN(a[i]); PIN(b[i]); });
  STG(N, v[i] = __uint_as_float(a[i]) + __uint_as_float(b[i]); PIN(v[i]));
  STG(N, a[i] = __float_as_uint(v[i]); b[i] = a[i]; PIN(b[i]));
  STG(N, { auto r = __builtin_amdgcn_permlane32_swap(a[i], b[i], false, false); a[i] = r[0]; b[i] = r[1]; PIN(a[i]); PIN(b[i]); });
  STG(N, v[i] = __uint_as_float(a[i]) + __uint_as_float(b[i]); PIN(v[i]));
}
template <int N>
__device__ __forceinline__ void gelu_s(float* e, float ga_, float gb2, float gd, unsigned* pk) {
  float x[N];
  STG(N, x[i] = e[i] * e[i]; PIN(x[i]));
  STG(N, x[i] = __builtin_fmaf(x[i], gb2, ga_); PIN(x[i]));
  STG(N, x[i] *= e[i]; PIN(x[i]));
  STG(N, x[i] = __builtin_amdgcn_exp2f(x[i]); PIN(x[i]));
  STG(N, x[i] = __builtin_fmaf(x[i], gd, gd); PIN(x[i]));
  STG(N, x[i] = __builtin_amdgcn_rcpf(x[i]); PIN(x[i]));
  STG(N, e[i] *= x[i]; PIN(e[i]));
  STG(N / 4, e[4 * i + 1] = plainv(e[4 * i + 1]); PIN(e[4 * i + 1]));
  STG(N / 2, pk[i] = pack2(e[2 * i], e[2 * i + 1]); PIN(pk[i]));
}
__device__ __forceinline__ f32x4 mfma16(u32x4 a, u32x4 b, f32x4 c) {
  return __builtin_amdgcn_mfma_f32_16x16x32_bf16(__builtin_bit_cast(bf16x8, a), __builtin_bit_cast(bf16x8, b), c, 0, 0, 0);
}
template <bool FIRST> __device__ __forceinline__ void mf(f32x4& c, const u32x4& a, const u32x4& b) {
  if (FIRST) asm volatile("v_mfma_f32_16x16x32_bf16 %0, %1, %2, 0" : "=v"(c) : "v"(a), "v"(b));
  else asm volatile("v_mfma_f32_16x16x32_bf16 %0, %1, %2, %0" : "+v"(c) : "v"(a), "v"(b));
}
__device__ __forceinline__ unsigned canon(unsigned x) { return x < 0xfffffffeu ? x : 0xfffffffeu; }
__device__ __forceinline__ bool bad1(unsigned a) { return __builtin_amdgcn_ballot_w64(a == SENT) != 0ull; }
__device__ __forceinline__ float amax2(const u32x4& a) { return __builtin_amdgcn_fmed3f(__builtin_fabsf(__uint_as_float(a[0])), __builtin_fabsf(__uint_as_float(a[2])), CMAX); }
__device__ __forceinline__ float amin2(const u32x4& a) { return __builtin_amdgcn_fmed3f(__builtin_fabsf(__uint_as_float(a[0])), __builtin_fabsf(__uint_as_float(a[2])), 0.f); }
__device__ __forceinline__ float amax3(float m, const u32x4& a) { float r; asm("v_max3_f32 %0, %1, |%2|, |%3|" : "=v"(r) : "v"(m), "v"(a[0]), "v"(a[2])); return r; }
template <bool CD> __device__ __forceinline__ bool any_bad4(u32x4& a, float thr, unsigned csh, float cmx) {
  const float m = __builtin_amdgcn_fmed3f(__builtin_fabsf(__uint_as_float(a[0])), __builtin_fabsf(__uint_as_float(a[2])), CD ? 0.f : cmx);
  if (CD) { a[0] -= csh; a[2] -= csh; }
  return __builtin_amdgcn_ballot_w64(CD ? m < thr : m >= thr) != 0ull;
}
__device__ __forceinline__ u32x4 widen(unsigned x0, unsigned x1, unsigned y0, unsigned y1) {
  const auto p0 = __builtin_amdgcn_permlane16_swap(x0, y0, false, false);
  const auto p1 = __builtin_amdgcn_permlane16_swap(x1, y1, false, false);
  return u32x4{(unsigned)p0[0], (unsigned)p1[0], (unsigned)p0[1], (unsigned)p1[1]};
}
#define DMA1(M) asm volatile("s_mov_b32 m0, %3\n\ts_nop 0\n\t" "buffer_load_dwordx4 %0, %1, %2 offen " M "lds" :: "v"(voff), "s"(rs), "s"(soff), "s"(dst) : "memory")
#define DMA4(M) asm volatile("s_mov_b32 m0, %3\n\ts_nop 0\n\t" \
                   "buffer_load_dwordx4 %0, %1, %2 offen " M "lds\n\tbuffer_load_dwordx4 %0, %1, %2 offen offset:1024 " M "lds\n\t" \
                   "buffer_load_dwordx4 %0, %1, %2 offen offset:2048 " M "lds\n\tbuffer_load_dwordx4 %0, %1, %2 offen offset:3072 " M "lds" \
                   :: "v"(voff), "s"(rs), "s"(soff), "s"(dst) : "memory")
template <int N, int AUX>
__device__ __forceinline__ void dma(unsigned voff, u32x4 rs, unsigned soff, unsigned dst) {
  if (N == 4) { if (AUX == 1) DMA4("sc1 "); else if (AUX == 2) DMA4("nt "); else DMA4(""); }
  else { if (AUX == 1) DMA1("sc1 "); else if (AUX == 2) DMA1("nt "); else DMA1(""); }
}
__device__ __forceinline__ void st128(u32x4 v, __amdgpu_buffer_rsrc_t r, unsigned voff, unsigned base, int) {
  __builtin_amdgcn_raw_buffer_store_b128(v, r, voff + base, 0, 16);
}
__device__ __forceinline__ u32x4 mk_rs(const void* p, unsigned bytes) {
  const unsigned long long a = (unsigned long long)p;
  return u32x4{(unsigned)a, (unsigned)(a >> 32) & 0xffffu, bytes, (unsigned)RS3};
}

#define USLEEP 2
#define DLY0 8
#define DLY1 3
#define DLY2 0
#define DLY3 2
#define NWH 8
#define ZG 8
#define ZSLEEP 2
#define AW4 8
#define AD3 3
#define RSLEEP 1
#define PHI 2
#define CPW 4
#define CPG 128u
#define NZ2 5
#define NS2 5
#define RS0 6
#define DLY3B 0
#define MV0 7
#define H0S 6
#define H1S 3
#define PK0 2
#define PE0 1
#define PK1 2
#define PE1 1
#define P40 1
#define P41 2
#define GSA 2
#define GSB 1
#define GSC 2
#define EA0 4
#define EA1 28
#define EC0 4
#define EC1 22
#define PB1 26
#define PB2 20
#define NL1 14
#define NP1 2
#define NL2 12
#define ROT 1
#define NA1 3
#define NP0 0
#define SL1 6
#define SL2 6
#define PH1 2
#define PH2 2
#define KG 42
#define KC3 12
#define PK2 2
#define KC3B 40
#define NPK 10
#define HG 4
#define WSP 3
#define PR1 3
#define HPZ 8
#define HFI 1
#define TU1 200
#define TU2 260
#define TU 290
#define KL 5
#define PBE 18
#define NAF 6
#define W0U 4
#define U0 5
#define NPH 6
#define NGA 5
#define NR0 2
#define NR1 5
static_assert((NWH + NL1 + NP1) % 2 == 0 && NL2 % 2 == 0 && NWH + NL1 + NP1 + NL2 <= 40, "w3 schedule");
#define SPIN_STEP() do { ++spins; asm volatile("" : "+s"(spins)); } while (0)
#define SB() __builtin_amdgcn_sched_barrier(0)
#define FENCE() do { asm volatile("" ::: "memory"); __builtin_amdgcn_sched_barrier(0); } while (0)
#define LGKM0() do { __builtin_amdgcn_sched_barrier(0); asm volatile("s_waitcnt lgkmcnt(0)" ::: "memory"); } while (0)
#define BARRIER() do { LGKM0(); __builtin_amdgcn_s_barrier(); asm volatile("" ::: "memory"); } while (0)
#define VM0() __builtin_amdgcn_s_waitcnt(0x0F70)
#define PRIO(n) __builtin_amdgcn_s_setprio(n)
#define CHECK_A(CD, AV, i, hi, LD)                                                          \
  do {                                                                                      \
    if (__builtin_expect(any_bad4<CD>(AV, sthr, scsh, scmx), 0)) {                          \
      unsigned spins = 0;                                                                   \
      PRIO(0);                                                                              \
      do {                                                                                  \
        __builtin_amdgcn_s_sleep(RSLEEP);                                                   \
        _Pragma("unroll") for (int y_ = (i); y_ < (hi); ++y_) { LD(y_); }                   \
        SPIN_STEP();                                                                        \
      } while (any_bad4<CD>(AV, sthr, scsh, scmx) && spins < RETRY_CAP);                    \
      PRIO(PHI);                                                                            \
      VM0();                                                                                \
    }                                                                                       \
  } while (0)

#define ASM_MACROS \
  ".macro regno op\n.set r_, 0\n.set st_, 0\n.irpc c, \"\\op\"\n.ifc \\c, :\n.set st_, 2\n.endif\n.if st_ == 1\n.set r_, r_ * 10 + \\c\n.endif\n.ifc \\c, [\n.set st_, 1\n.endif\n.endr\n.endm\n" \
  ".macro retry n, cd, a, so, ct, t, vo, rs, sb, im, th, k, pr\n"                                   \
  "s_cbranch_vccnz Lc\\n\nLb\\n:\n.subsection 1\nLc\\n:\nregno \\a\n"              \
  "s_setprio 0\ns_add_u32 \\so, \\sb, \\im\ns_mov_b32 \\ct, 0\n"                                        \
  "Ll\\n:\ns_sleep 1\nbuffer_load_dwordx4 \\a, \\vo, \\rs, \\so offen sc1\ns_add_u32 \\ct, \\ct, 1\ns_waitcnt vmcnt(0)\n" \
  ".if \\cd\nv_med3_f32 \\t, |v[r_]|, |v[r_+2]|, 0\nv_cmp_gt_f32 vcc, \\th, \\t\n.else\nv_med3_f32 \\t, |v[r_]|, |v[r_+2]|, \\k\nv_cmp_le_f32 vcc, \\th, \\t\n.endif\n" \
  "s_cbranch_vccz Ld\\n\ns_cmp_lt_u32 \\ct, 0x8000\ns_cbranch_scc1 Ll\\n\n"                             \
  "Ld\\n:\n.if \\cd\nv_subrev_u32 v[r_], \\k, v[r_]\nv_subrev_u32 v[r_+2], \\k, v[r_+2]\n.endif\ns_setprio \\pr\ns_nop 1\ns_branch Lb\\n\n.subsection 0\n.endm\n" \
  ".macro need n, v, ct, ad, k\n"                                                                       \
  "v_cmp_gt_u32 vcc, \\k, \\v\ns_cbranch_vccnz Lc\\n\nLb\\n:\n.subsection 1\nLc\\n:\ns_mov_b32 \\ct, 0\n" \
  "Ll\\n:\nds_read_b32 \\v, \\ad\ns_waitcnt lgkmcnt(0)\nv_cmp_gt_u32 vcc, \\k, \\v\ns_cbranch_vccz Lb\\n\n" \
  "s_sleep 1\ns_add_u32 \\ct, \\ct, 1\ns_cmp_lt_u32 \\ct, 0x200000\ns_cbranch_scc1 Ll\\n\ns_branch Lb\\n\n.subsection 0\n.endm\n"
#define RETRY(CD, AV, BM, VOFF, SBASE, IMM, PR)                                                         \
  do {                                                                                                  \
    unsigned so_, ct_; float t_; register unsigned long long vc_ asm("vcc") = (BM);                                                                     \
    asm volatile("retry %=, %5, \"%0\", %1, %2, %3, %6, \"%7\", %8, %9, %10, %11, %12"                   \
                 : "+v"(AV), "=&s"(so_), "=&s"(ct_), "=&v"(t_), "+s"(vc_)                            \
                 : "i"((int)(CD)), "v"(VOFF), "s"(dW), "s"(SBASE), "i"(IMM), "s"(sthr), "s"((CD) ? scsh : __float_as_uint(scmx)), "i"(PR) : "scc", "memory"); \
  } while (0)
#define CMP_BAD(CD, BM, M) do { register unsigned long long vc_ asm("vcc");                             \
    if (CD) asm volatile("v_cmp_gt_f32 vcc, %1, %2" : "=s"(vc_) : "s"(sthr), "v"(M)); else asm volatile("v_cmp_le_f32 vcc, %1, %2" : "=s"(vc_) : "s"(sthr), "v"(M)); \
    BM = vc_; } while (0)
#define RETRY_C(AV, BM, VOFF, SBASE, IMM, PR) RETRY(1, AV, BM, VOFF, SBASE, IMM, PR)
#define NEED_AT(V, ADDR, K) do { unsigned ct_; asm volatile("need %=, %0, %1, %2, %3" : "+v"(V), "=&s"(ct_) : "v"(ADDR), "s"(K) : "vcc", "scc", "memory"); } while (0)

__device__ __forceinline__ u32x4 rd_tr(const LAS char* p) {
  const s16x4 lo = __builtin_amdgcn_ds_read_tr16_b64_v4i16((LAS s16x4*)p);
  const s16x4 hi = __builtin_amdgcn_ds_read_tr16_b64_v4i16((LAS s16x4*)(p + 2560));
  const bf16x8 o = {lo[0], lo[1], lo[2], lo[3], hi[0], hi[1], hi[2], hi[3]};
  return __builtin_bit_cast(u32x4, o);
}
__device__ __forceinline__ float dpp_add(float v, int ctrl) {
  return v + __builtin_bit_cast(float, ctrl == 0 ? __builtin_amdgcn_update_dpp(0, __builtin_bit_cast(int, v), 0x128, 0xf, 0xf, true)
                                    : ctrl == 1 ? __builtin_amdgcn_update_dpp(0, __builtin_bit_cast(int, v), 0x124, 0xf, 0xf, true)
                                                : __builtin_amdgcn_update_dpp(0, __builtin_bit_cast(int, v), 0x4E, 0xf, 0xf, true));
}

template <int V> struct IC { static constexpr int v = V; };
template <int I, int N, class F> __device__ __forceinline__ void sfor(F&& f) { if constexpr (I < N) { f(IC<I>{}); sfor<I + 1, N>(f); } }
constexpr int cv_i(int m) { int i = 0; while (m >= 8 - i) { m -= 8 - i; ++i; } return i; }
constexpr int cv_d(int m) { int i = 0; while (m >= 8 - i) { m -= 8 - i; ++i; } return 7 - i - m; }
constexpr int kl_i(int m) { return m < 50 - 5 * KL ? m / 5 : m < 50 - 2 * KL ? 10 - KL + (m - 50 + 5 * KL) / 3 : 10 - KL + (m - 50 + 2 * KL) / 2; }
constexpr int kl_j(int m) { return m < 50 - 5 * KL ? m % 5 : m < 50 - 2 * KL ? (m - 50 + 5 * KL) % 3 : 3 + (m - 50 + 2 * KL) % 2; }
constexpr int cl_t(int m) { int t = 0; while (m > t) { m -= t + 1; ++t; } return t; }
constexpr int cl_i(int m) { int t = 0; while (m > t) { m -= t + 1; ++t; } return m; }
constexpr bool cv_free(int m, int k) {
  return k == 0 ? (m >= 64 && m < 111 && m != 65 && m != 69 && m != 72 && m != 75 && m != 77 && m != 94 && m != 95 && m != 104 && m != 110 && (m < 72 + RS0 || m >= 88 + RS0))
                : (m >= 123 && m != 126 && m != 131 && m != 132 && m != 139 && m != 140);
}
constexpr int cv_slot(int m, int k) { if (!cv_free(m, k)) return -1; int n = 0; for (int y = 0; y < m; ++y) n += cv_free(y, k); return n; }
static_assert(cv_slot(109, 0) >= 15 && cv_slot(143, 1) >= 15, "slots");
constexpr int cc_i(int J, int c) { for (int i = 0; i < 8; ++i) for (int t = 3; t >= 0; --t) if (i <= ((J >> (4 * t)) & 15) && c-- == 0) return i; return 0; }
constexpr int cc_t(int J, int c) { for (int i = 0; i < 8; ++i) for (int t = 3; t >= 0; --t) if (i <= ((J >> (4 * t)) & 15) && c-- == 0) return t; return 0; }
__global__ __launch_bounds__(512, 2) void k_main(const bf16* kin_, const bf16* vin_, char* ws_, const bf16* shift_k_, const bf16* ssm_k_,
                                                 const bf16* w_mod_, const bf16* cin_, const bf16* w3_,
                                                 const bf16* w4_, const bf16* q_, const bf16* w1_, const bf16* w2_,
                                                 const bf16* ssm_D_, bf16* out_) {
  typedef __attribute__((ext_vector_type(8))) unsigned long long u64x8;
  typedef __attribute__((ext_vector_type(4))) unsigned long long u64x4;
  typedef __attribute__((ext_vector_type(2))) unsigned long long u64x2;
  u64x8 ka;
  u64x4 kb;
  u64x2 kc;
  int tid_ = threadIdx.x, bx_ = blockIdx.x;
  unsigned long long pc0;
  unsigned code_bytes;
  asm volatile("s_load_dwordx16 %0, %6, 0x0\n\ts_load_dwordx8 %1, %6, 0x40\n\ts_load_dwordx4 %2, %6, 0x60\n\ts_getpc_b64 %3\n"
               "Lpc0:\n\ts_mov_b32 %4, Lcold0 - Lpc0\n.subsection 1\nLcold0:\n.subsection 0"
               : "=&s"(ka), "=&s"(kb), "=&s"(kc), "=&s"(pc0), "=&s"(code_bytes), "+s"(bx_) : "s"(__builtin_amdgcn_kernarg_segment_ptr()));
  asm volatile(ASM_MACROS ".subsection 2\n.fill 256, 4, 0xbf800000\n.subsection 0" : "+v"(tid_));
  extern __shared__ __attribute__((aligned(16))) char lds_[];
  LAS char* lds = (LAS char*)lds_;
  ldsw_t LW = (ldsw_t)(lds + L_CTL);
  const int tid = tid_ & 511, lane = tid & 63;
  const int wv = __builtin_amdgcn_readfirstlane(tid >> 6) & 7;
  const int bx = bx_ & 255;
  const int mt = (bx & 7) * 4 + (bx >> 6), nt = (bx >> 3) & 7;
  const int n0 = nt * 80, m0 = mt * 64;
  const int l15 = lane & 15, g = lane >> 4;
  const int mg = wv & 3, kh = wv >> 2;
  const int gm = (wv >> 2) * 2 + (wv & 1), gk = ((wv >> 1) ^ (wv >> 2)) & 1, gp = wv ^ 2;
  const unsigned l16 = (unsigned)lane * 16u;
  unsigned cp0 = 0;
  if (wv == CPW) {
    asm volatile("buffer_load_dword %0, %1, %2, 0 offen" : "=v"(cp0) : "v"((unsigned)((bx >> 3) + 32 * lane) * CPG), "s"(mk_rs((const void*)pc0, code_bytes)) : "memory");
  }
  FENCE();
  const int tok0 = 32 * ((bx & 7) * 8 + (bx >> 5)), ch0 = 160 * ((bx >> 3) & 3);
  unsigned ko[3], vo[3];
#pragma unroll
  for (int i = 0; i < 3; ++i) {
    const unsigned s = (unsigned)((mg + 4 * i) * 64 + lane);
    const unsigned gr = __umul24(s, 800u) >> 16, w = s - __umul24(82u, gr), rr = __umul24(w, 205u) >> 12;
    unsigned o = (unsigned)(tok0 * (D_ * 2) + ch0 * 2) + __umul24(gr, 4u * D_ * 2) + __umul24(rr, D_ * 2 - 320) + w * 16u;
    asm volatile("" : "+v"(o));
    ko[i] = (w < 80u && gr < 9u) ? o - 4u * D_ * 2 : OOB;
    vo[i] = (w < 80u && gr < 8u) ? o : OOB;
    asm volatile("" : "+v"(ko[i]), "+v"(vo[i]));
  }
  FENCE();
  asm volatile("s_waitcnt lgkmcnt(0)" : "+s"(ka), "+s"(kb), "+s"(kc));
  const bf16* kin = (const bf16*)ka[0];
  const bf16* vin = (const bf16*)ka[1];
  char* ws = (char*)ka[2];
  const bf16* shift_k = (const bf16*)ka[3];
  const bf16* ssm_k = (const bf16*)ka[4];
  const bf16* w_mod = (const bf16*)ka[5];
  const bf16* cin = (const bf16*)ka[6];
  const bf16* w3 = (const bf16*)ka[7];
  const bf16* w4 = (const bf16*)kb[0];
  const bf16* q = (const bf16*)kb[1];
  const bf16* w1 = (const bf16*)kb[2];
  const bf16* w2 = (const bf16*)kb[3];
  const bf16* ssm_D = (const bf16*)kc[0];
  bf16* out = (bf16*)kc[1];
  const __amdgpu_buffer_rsrc_t rW = __builtin_amdgcn_make_buffer_rsrc((void*)ws, 0, WS_USED, RS3);
  const u32x4 dW = mk_rs(ws, WS_USED);
  u32x2 ep;
  asm volatile("buffer_load_dwordx2 %0, off, %1, %2 sc1" : "=v"(ep) : "s"(dW), "s"(O_EP) : "memory");
  FENCE();
  if (kh == 0) {
    const u32x4 dK = mk_rs(kin, SD2), dV = mk_rs(vin, SD2);
#pragma unroll
    for (int i = 0; i < 3; ++i) dma<1, 0>(ko[i], dK, 0u, (unsigned)(L_KV + (mg + 4 * i) * 1024));
#pragma unroll
    for (int i = 0; i < 3; ++i) dma<1, 0>(vo[i], dV, 0u, (unsigned)(L_KV + 12288 + (mg + 4 * i) * 1024));
  }
  u32x4 sk[3];
  if (kh == 0) {
    asm volatile("buffer_load_dwordx4 %0, %3, %5, %6 offen\n\tbuffer_load_dwordx4 %1, %3, %5, %6 offen offset:16\n\tbuffer_load_dwordx4 %2, %4, %5, %7 offen"
                 : "=&v"(sk[0]), "=&v"(sk[1]), "=&v"(sk[2])
                 : "v"(((unsigned)lane >> 3) * 32u), "v"((unsigned)g * 16u), "s"(mk_rs(shift_k, D_ * 8)), "s"(ch0 * 8 + mg * 256), "s"(ch0 * 8 + 1024 + mg * 64) : "memory");
  }
  FENCE();
  if (tid >= 504) LW[tid - 504] = 0u;
  if (tid >= 256 && tid < 352) *(LAS u32x4*)(lds + SL_A + ((tid - 256) >> 5) * CV_SLOT + CV_U + (tid & 31) * 16) = u32x4{0u, 0u, 0u, 0u};
  if (tid >= 384 && tid < 408) *(LAS u32x4*)(lds + SL_A + ((tid - 384) >> 3) * CV_SLOT + (tid & 7) * CV_STR + 257 * 16) = u32x4{0u, 0u, 0u, 0u};
  BARRIER();

  const unsigned psig = (unsigned)L_CTL + (unsigned)wv * 4u;
#define SIG(k) do { unsigned long long sv_; asm volatile("s_mov_b64 %0, exec\n\ts_mov_b64 exec, 1\n\tds_write_b32 %1, %2\n\ts_mov_b64 exec, %0" : "=&s"(sv_) : "v"(psig), "v"((unsigned)(k)) : "memory"); } while (0)
#define WAIT_AT(addr, k)                                                                                \
  do {                                                                                                  \
    unsigned tv_, tc_;                                                                                  \
    asm volatile("s_mov_b32 %1, 0\n"                                                                    \
                 "L%=_a:\n\tds_read_b32 %0, %2\n\ts_waitcnt lgkmcnt(0)\n\tv_cmp_gt_u32 vcc, %3, %0\n\ts_cbranch_vccz L%=_b\n\t"    \
                 "s_sleep 1\n\ts_add_u32 %1, %1, 1\n\ts_cmp_lt_u32 %1, 0x200000\n\ts_cbranch_scc1 L%=_a\n"            \
                 "L%=_b:" : "=&v"(tv_), "=&s"(tc_) : "v"(addr), "i"(k) : "vcc", "scc", "memory");       \
  } while (0)
#define WAIT_2(k0, k1)                                                                                 \
  do {                                                                                                  \
    unsigned tv_, tc_;                                                                                  \
    asm volatile("s_mov_b32 %1, 0\n"                                                                    \
                 "L%=_a:\n\tds_read_b32 %0, %2\n\ts_waitcnt lgkmcnt(0)\n\tv_cmp_gt_u32 vcc, %3, %0\n\ts_cbranch_vccz L%=_b\n\t"    \
                 "s_sleep 1\n\ts_add_u32 %1, %1, 1\n\ts_cmp_lt_u32 %1, 0x200000\n\ts_cbranch_scc1 L%=_a\n"            \
                 "L%=_b:" : "=&v"(tv_), "=&s"(tc_) : "v"((unsigned)L_CTL + ((unsigned)lane & 7u) * 4u), "v"((lane & 4) ? (unsigned)(k1) : (unsigned)(k0)) : "vcc", "scc", "memory"); \
  } while (0)
#define WAIT_ALL(k) WAIT_AT((unsigned)L_CTL + ((unsigned)lane & 7u) * 4u, k)
#define WAIT_HALF(h, k) WAIT_AT((unsigned)L_CTL + ((unsigned)lane & 3u) * 4u + (unsigned)(h) * 16u, k)

  const int sb3 = nt < 6 ? 14 * nt : 84 + 12 * (nt - 6);
  const bool two = kh == 0 || nt < 6;
  const int jl = kh ? 4 + wv : 2 * wv, jm = kh ? 12 + (wv & 1) : 2 * wv + 1, hb = wv > 5 ? 1 : 0;
  const int k4 = 7 * wv - (wv > 5 ? 2 : wv > 4 ? 1 : 0);
  const unsigned w4a = O_WT4 + (unsigned)(nt * 54 + k4) * 5120u;
  unsigned w3s[4] = {O_WT3 + (unsigned)(sb3 + jl) * 20480u + (unsigned)hb * 10240u, two ? O_WT3 + (unsigned)(sb3 + jm) * 20480u + (unsigned)hb * 10240u : OOB,
                     O_WT3 + (unsigned)(sb3 + jl) * 20480u - (unsigned)hb * 10240u, kh ? w4a - 10240u : O_WT3 + (unsigned)(sb3 + jm) * 20480u};
  f32x2 cp = {65536.f, 65536.f}, cn = {-65536.f, -65536.f};
  asm volatile("" : "+s"(cp), "+s"(cn));
  u32x4 W3[40];
#define LD_W3U(f) W3[f] = __builtin_amdgcn_raw_buffer_load_b128(rW, l16 + ((f) < 8 ? ((f) >> 1) * 1024 : 0), w3s[((f) & 1) + ((f) >= 20 ? 2 : 0)] + ((f) < 8 ? 0 : ((f) >> 1) * 1024), 16)
#define LD_W3R(a, b) do { _Pragma("unroll") for (int f_ = (a); f_ < (b); ++f_) LD_W3U(f_); } while (0)
#define VMW(n) asm volatile("s_waitcnt vmcnt(%0)" :: "i"(n) : "memory")
  const __amdgpu_buffer_rsrc_t rW1 = __builtin_amdgcn_make_buffer_rsrc((void*)w1, 0, D_ * D_ * 2, RS3);
  const __amdgpu_buffer_rsrc_t rW2 = __builtin_amdgcn_make_buffer_rsrc((void*)w2, 0, D_ * D_ * 2, RS3);
  const u32x4 dW1 = mk_rs(w1, D_ * D_ * 2), dW2 = mk_rs(w2, D_ * D_ * 2);
#define W12_G(gw) unsigned gw[5]; { unsigned ln_ = (unsigned)lane; asm volatile("" : "+v"(ln_)); \
    _Pragma("unroll") for (int j_ = 0; j_ < 5; ++j_) { const unsigned p_ = ln_ + 64u * j_, r_ = __umul24(p_, 6554u) >> 16; \
      gw[j_] = __umul24((r_ & 12u) * 2u + (r_ >> 4) * 4u + (r_ & 3u), D_ * 2) + (p_ - __umul24(r_, 10u)) * 16u + (unsigned)n0 * 2u; } }

  const int s0 = m0 + nt * 8;
  const int srow = s0 + wv;
  unsigned qw[5], hw[5];
  unsigned long long epn;
  unsigned tA, tB;
  u32x4 WS2[10];
#define LD_WS2U(a, b) _Pragma("unroll") for (int y_ = (a); y_ < (b); ++y_) asm volatile("buffer_load_dwordx4 %0, %1, %2, %3 offen ; ws2" : "=v"(WS2[y_]) : "v"(gw[y_ % 5]), "s"(dW2), "s"((wv + 8 * (y_ / 5)) * (32 * D_ * 2)) : "memory")
#define LD_WS2(a, b) _Pragma("unroll") for (int y_ = (a); y_ < (b); ++y_) WS2[y_] = __builtin_amdgcn_raw_buffer_load_b128(rW2, gw[y_ % 5], (wv + 8 * (y_ / 5)) * (32 * D_ * 2), 0)
  unsigned ry = (unsigned)(L_XCH + wv * YROW) + l16, rz = (unsigned)(L_XCH + wv * YROW + 1024 + 4 * lane);
  f32x2 xq[5], ha[5], ca[5], sb[5];
  float mc, mb;
  const int row = m0 + 16 * gm + l15;
  const int jb = 3 * gk;
  const unsigned ofr = (unsigned)(row >> 4) * 20480u + (unsigned)((n0 + 16 * jb + 4 * g) >> 3) * 256u + (unsigned)(g & 1) * 8u + (unsigned)l15 * 16u;
  {
    const int pi = (bx & 7) + 8 * (bx >> 4), half = (bx >> 3) & 1;
    const int chA = 2 * bx, chC = 512 + pi;
#define RD_MOD(v) do { const u32x4 b_ = *(const LAS u32x4*)(lds + L_MOD + (v) * (D_ * 2) + l16); mw[v][0] = b_[0]; mw[v][1] = b_[1]; mw[v][2] = b_[2]; mw[v][3] = b_[3]; \
                       mw[v][4] = *(const LAS unsigned*)(lds + L_MOD + (v) * (D_ * 2) + 1024 + lane * 4); } while (0)
#define LN1()                                                                                           \
  do {                                                                                                  \
    f32x2 xq_[5];                                                                                       \
    _Pragma("unroll") for (int j = 0; j < 5; ++j) xq_[j] = up2(qw[j]);                                  \
    ln5(xq_, hw);                                                                                       \
    _Pragma("unroll") for (int j = 0; j < 5; ++j) asm volatile("" : "+v"(hw[j]));                       \
  } while (0)
#define R_PRE() do {                                                                                    \
      unsigned mw[5][5];                                                                                \
      _Pragma("unroll") for (int v = 0; v < 5; ++v) RD_MOD(v);                                          \
      _Pragma("unroll") for (int j = 0; j < 5; ++j) {                                                   \
        xq[j] = up2(qw[j]);                                                                             \
        ha[j] = FMA2(up2(hw[j]), up2(mw[1][j]), up2(mw[0][j])) * up2(mw[2][j]); ca[j] = up2(mw[4][j]); sb[j] = up2(mw[3][j]); \
        asm volatile("" : "+v"(xq[j]), "+v"(ha[j]), "+v"(ca[j]), "+v"(sb[j]));                          \
      }                                                                                                 \
      const f32x2 c2_ = ((ca[0] + ca[1]) + (ca[2] + ca[3])) + ca[4], b2_ = ((sb[0] + sb[1]) + (sb[2] + sb[3])) + sb[4]; \
      mc = wave_sum2(c2_[0] + c2_[1]) * (1.f / (float)D_); mb = wave_sum2(b2_[0] + b2_[1]) * (1.f / (float)D_); \
      asm volatile("" : "+v"(mc), "+v"(mb));                                                            \
    } while (0)
    unsigned dv2;
#define GET_D() const unsigned dAB = __builtin_amdgcn_readlane(dv2, 0), dC_ = __builtin_amdgcn_readlane(dv2, 1), dC = (chC & 1) ? dC_ & 0xffff0000u : dC_ << 16
#define REQ_Q() do {                                                                                    \
      const __amdgpu_buffer_rsrc_t rQ = __builtin_amdgcn_make_buffer_rsrc((void*)q, 0, SD2, RS3);       \
      const u32x4 a = __builtin_amdgcn_raw_buffer_load_b128(rQ, l16, srow * (D_ * 2), 0);               \
      qw[0] = a[0]; qw[1] = a[1]; qw[2] = a[2]; qw[3] = a[3];                                           \
      qw[4] = __builtin_amdgcn_raw_buffer_load_b32(rQ, (unsigned)lane * 4u, srow * (D_ * 2) + 1024, 0); \
    } while (0)
#define REQ_D() dv2 = __builtin_amdgcn_raw_buffer_load_b32(__builtin_amdgcn_make_buffer_rsrc((void*)ssm_D, 0, D_ * 2, RS3), (unsigned)(lane ? (chC & ~1) : chA) * 2u, 0, 0)
#define TAGS()                                                                                          \
    {                                                                                                   \
      unsigned e0_, e1_;                                                                                \
      asm volatile("v_readfirstlane_b32 %0, %2\n\tv_readfirstlane_b32 %1, %3" : "=s"(e0_), "=s"(e1_) : "v"(ep[0]), "v"(ep[1])); \
      unsigned long long h = (unsigned long long)e1_ << 32 | e0_;                                       \
      epn = h + 1ull;                                                                                   \
      h += 0x9E3779B97F4A7C15ull;                                                                       \
      h = (h ^ (h >> 30)) * 0xBF58476D1CE4E5B9ull;                                                      \
      h = (h ^ (h >> 27)) * 0x94D049BB133111EBull;                                                      \
      h ^= h >> 31;                                                                                     \
      tA = (unsigned)h; tB = (unsigned)(h >> 32);                                                       \
    }
#define BUILD(SL, CC)                                                                                   \
  do {                                                                                                  \
    const u32x4 klo = *(const LAS u32x4*)(lds + (SL) + CV_U + 512 + 4064 - 16 * (CC)), khi = *(const LAS u32x4*)(lds + (SL) + CV_U + 512 + 4080 - 16 * (CC)); \
    const unsigned w[8] = {klo[0], klo[1], klo[2], klo[3], khi[0], khi[1], khi[2], khi[3]};             \
    _Pragma("unroll") for (int m = 0; m < 8; ++m) {                                                     \
      u32x4 o;                                                                                          \
      const int a = m >> 1;                                                                             \
      _Pragma("unroll") for (int t = 0; t < 4; ++t) {                                                   \
        if ((m & 1) == 0) o[t] = (w[a - t + 4] & 0xffffu) | (w[a - t + 3] & 0xffff0000u);               \
        else o[t] = (w[a - t + 4] >> 16) | (w[a - t + 4] << 16);                                        \
      }                                                                                                 \
      *(LAS u32x4*)(lds + (SL) + m * CV_STR + ((CC) + 1) * 16) = o;                                     \
    }                                                                                                   \
  } while (0)
#define ZCH(c) ((c) * 128u + ((c) / 160u) * (TS - 20480u))
#define ZOFF(JA, JB, CH) ((unsigned)((32 * ((g & 1) ? (JB) : (JA)) + 2 * l15 + (g >> 1)) >> 3) * (4u * TS) + ZCH((unsigned)(CH)) + (unsigned)((2 * l15 + (g >> 1)) & 7) * 16u)
#define Z2OFF(w) (O_Z + (unsigned)(4 * (w) + (l15 >> 2)) * (4u * TS) + ZCH((unsigned)chA) + (unsigned)((l15 & 3) * 64 + g * 16))
    const unsigned a00 = (unsigned)((l15 & 7) * CV_STR + (2032 - 8 * (l15 >> 3) + 8 * g) * 2) - 128u * mg;
    const unsigned b00 = (unsigned)(CV_U + 480 + 32 * l15 + 16 * g) - 128u * mg;
#define CV_RA(AV, SL, rd) _Pragma("unroll") for (int i = 0; i < 8; ++i) AV[8 * (rd) + i] = *(const LAS u32x4*)(lds + (SL) + a00 - 64 * (rd) - 512 * i)
#define CV_RB(BV, SL, rd) _Pragma("unroll") for (int i = 7; i >= 0; --i) BV[8 * (rd) + i] = *(const LAS u32x4*)(lds + (SL) + b00 + 512 * i - 64 * (rd))
    const int zja = mg ? (half ? 5 : 4) : (half ? 1 : 0), zjb = mg ? (half ? 6 : 7) : (half ? 2 : 3);
    const int t8 = tid & 255;
#define W_DMA(blk, j) dma<1, 0>(gw[j], (blk) < 20 ? dW1 : dW2, (unsigned)((blk) < 20 ? (blk) : (blk) - 4) * (32 * D_ * 2), (unsigned)((blk) * 5120 + (j) * 1024))
    if (__builtin_expect(kh != 0, 1)) {
      {
        const u32x4 dS = mk_rs(ssm_k, SD2);
        dma<1, 0>(l16, dS, (unsigned)(chC * (S_ * 2) + mg * 1024), (unsigned)(SL_C + CV_U + 512 + mg * 1024));
        dma<1, 0>(l16, dS, (unsigned)(chA * (S_ * 2) + mg * 1024), (unsigned)(SL_A + CV_U + 512 + mg * 1024));
        dma<1, 0>(l16, dS, (unsigned)((chA + 1) * (S_ * 2) + mg * 1024), (unsigned)(SL_B + CV_U + 512 + mg * 1024));
      }
      unsigned cpf = __builtin_amdgcn_raw_buffer_load_b32(rW, OOB, 0, 0);
      FENCE();
#define FILL_Z() do {                                                                                   \
      st128(u32x4{CODED, CODED, CODED, CODED}, rW, Z2OFF(mg), 0, 16);                                   \
      st128(u32x4{CODED, CODED, CODED, CODED}, rW, Z2OFF(wv), 0, 16);                                   \
      if (mg < 2) st128(u32x4{CODED, CODED, CODED, CODED}, rW, ZOFF(zja, zjb, chC), O_Z, 16);            \
    } while (0)
      unsigned fpa[3], fpv;
#pragma unroll
      for (int f_ = 0; f_ < 3; ++f_) {
        const unsigned o = (unsigned)(f_ * 4096 + t8 * 16);
        fpa[f_] = o < 10240u ? O_H1 + (unsigned)(bx >> 1) * TS + (bx & 1) * 10240u + o : o < 11264u ? O_ST4 + (unsigned)bx * 1024u + (o - 10240u)
                : (o < 11392u && (bx & 1)) ? O_ST3 + (unsigned)(bx >> 1) * 128u + (o - 11264u) : OOB;
        if (f_ == 2) fpv = o < 10240u ? CODED : SENT;
      }
#define FILL_P(a, b) _Pragma("unroll") for (int f_ = (a); f_ < (b); ++f_) { const unsigned fv = f_ < 2 ? CODED : fpv; st128(u32x4{fv, fv, fv, fv}, rW, fpa[f_], 0, 16); }
      u32x4 WS[10];
#define LD_WS(a, b) _Pragma("unroll") for (int x = (a); x < (b); ++x)                                   \
      WS[x] = __builtin_amdgcn_raw_buffer_load_b128(rW1, gw[x % 5], (9 + mg + 4 * (x / 5)) * (32 * D_ * 2), 0)
#define LD_WD(a, b) _Pragma("unroll") for (int x = (a); x < (b); ++x) {                                 \
      if (x < 10) W_DMA(mg + 4 * (x / 5), x % 5);                                                       \
      else if (x == 10) dma<1, 0>(mg == 0 ? gw[0] : mg == 1 ? gw[1] : mg == 2 ? gw[2] : gw[3], dW1, 8u * (32 * D_ * 2), (unsigned)(8 * 5120 + mg * 1024)); \
      else if (mg == 0) W_DMA(8, 4);                                                                    \
    }
      FILL_Z();
      FENCE();
      u32x4 wm[5];
      unsigned short cv;
      {
        const __amdgpu_buffer_rsrc_t rM = __builtin_amdgcn_make_buffer_rsrc((void*)w_mod, 0, D_ * 6 * D_ * 2, RS3);
        const __amdgpu_buffer_rsrc_t rC = __builtin_amdgcn_make_buffer_rsrc((void*)cin, 0, D_ * 2, RS3);
        const unsigned o = bx < 240 ? (unsigned)(t8 >> 3) * (6 * D_ * 2) + (unsigned)(t8 & 7) * 16u : OOB;
        cv = __builtin_amdgcn_raw_buffer_load_b16(rC, (unsigned)((t8 >> 3) + 32 * min(lane & 7, 4)) * 2u, (bx & 3) * 320, 0);
#pragma unroll
        for (int i = 0; i < 5; ++i) wm[i] = __builtin_amdgcn_raw_buffer_load_b128(rM, o, ((bx & 3) * 160 + i * 32) * (6 * D_ * 2) + (bx >> 2) * 128, 0);
      }
      FENCE();
      W12_G(gw);
#pragma unroll
      for (int j = 0; j < 5; ++j) PIN(gw[j]);
      const int job = bx * 4 + mg;
      const bool j3 = job < 540, jok = job < 972;
      const int jks = j3 ? job / 27 : (job - 540) >> 3, jcg = j3 ? job % 27 : (job - 540) & 7;
      const unsigned rtl = (unsigned)(L_RT + mg * 5120);
      u32x4 rt[5];
      unsigned rta[5];
      {
        const unsigned v3 = ((unsigned)lane >> 3) * (F_ * 2) + (((unsigned)lane & 7u) * 16u ^ ((unsigned)lane & 24u) * 4u) + (unsigned)jks * (32 * F_ * 2) + (unsigned)jcg * 128u;
        const unsigned s4 = (unsigned)jks * (32 * D_ * 2) + (unsigned)(jcg - nt) * 160u;
#pragma unroll
        for (int i = 0; i < 5; ++i) { rta[i] = j3 ? (i < 4 ? (v3 ^ (i & 1) * 32u) + i * (8 * F_ * 2) : OOB) : gw[i] + s4; PIN(rta[i]); }
      }
#define RT_LD() do {                                                                                    \
      const u32x4 dS = mk_rs(j3 ? w3 : w4, jok ? DF2 : 0);                                              \
      _Pragma("unroll") for (int i = 0; i < 4; ++i) dma<1, 0>(rta[i], dS, 0u, rtl + i * 1024);          \
      if (!j3) dma<1, 0>(rta[4], dS, 0u, rtl + 4096);                                                   \
      FENCE();                                                                                          \
    } while (0)
      FENCE();
      asm volatile("" : "+v"(cpf));
      asm volatile("" :: "v"(cp0));
      SIG(P_K);
      FENCE();
      TAGS();
      {
        float a[8];
#pragma unroll
        for (int e = 0; e < 8; ++e) a[e] = 0.f;
        const float c_ = lo2f(cv);
        const int sci = __float_as_int(rbf(c_ * __builtin_amdgcn_rcpf(1.f + __builtin_amdgcn_exp2f(c_ * -1.4426950408889634f))));
#pragma unroll
        for (int i = 0; i < 5; ++i) {
          const float sc = __int_as_float(__builtin_amdgcn_ds_bpermute(((lane & 56) + i) * 4, sci));
#pragma unroll
          for (int e = 0; e < 4; ++e) { a[2 * e] += sc * lo2f(wm[i][e]); a[2 * e + 1] += sc * hi2f(wm[i][e]); }
        }
#pragma unroll
        for (int e = 0; e < 4; ++e) {
          const auto r = __builtin_amdgcn_permlane32_swap(__float_as_uint(a[e]), __float_as_uint(a[e + 4]), false, false);
          a[e] = __uint_as_float((unsigned)r[0]) + __uint_as_float((unsigned)r[1]);
        }
#pragma unroll
        for (int e = 0; e < 2; ++e) {
          const auto r = __builtin_amdgcn_permlane16_swap(__float_as_uint(a[e]), __float_as_uint(a[e + 2]), false, false);
          a[e] = dpp_add(__uint_as_float((unsigned)r[0]) + __uint_as_float((unsigned)r[1]), 0);
        }
        if ((lane & 8) == 0) *(LAS f32x2*)(lds + L_MP + mg * 256 + (lane & 7) * 32 + g * 8) = f32x2{a[0], a[1]};
        SIG(P_M);
        FENCE();
        REQ_Q();
        REQ_D();
        FENCE();
        if (wv == 7) {
          WAIT_HALF(1, P_M);
          f32x2 s = *(const LAS f32x2*)(lds + L_MP + (lane & 31) * 8);
#pragma unroll
          for (int w = 1; w < 4; ++w) s += *(const LAS f32x2*)(lds + L_MP + w * 256 + (lane & 31) * 8);
          st128(u32x4{__float_as_uint(s[0]), tA, __float_as_uint(s[1]), tB}, rW, lane < 32 ? l16 : OOB, O_MP + (unsigned)bx * 512u, 16);
          __builtin_amdgcn_raw_buffer_store_b64(u32x2{tA, tB}, rW, lane == 0 ? (unsigned)bx * 8u : OOB, O_FL1, 16);
        }
        FENCE();
      }
      {
      WAIT_HALF(1, P_K);
      BUILD(SL_A, t8);
      BUILD(SL_B, t8);
      SIG(P_B2);
      FENCE();
      }
      {
        const unsigned o_ = lane < 50 ? (unsigned)(bx * 4 + mg) * 800u + l16 : OOB;
        dma<1, 0>(o_, dW1, 0u, (unsigned)L_ST4);
        dma<1, 0>(o_, dW2, 0u, (unsigned)L_ST4);
        FENCE();
      }
      FENCE();
      FENCE();
      unsigned tra[10], rto[5];
      {
        const unsigned b3 = rtl + (unsigned)(8 * g + (l15 >> 2)) * 128u + (unsigned)((l15 >> 2) ^ (g & 1)) * 32u + ((unsigned)lane & 3u) * 8u, b4 = rtl + ((unsigned)lane >> 2) * 160u + ((unsigned)lane & 3u) * 8u;
        const unsigned dst = j3 ? O_WT3 + (unsigned)(jcg * 80 + jks) * 1024u : O_WT4 + (unsigned)(jcg * 54 + jks) * 5120u, dstr = j3 ? 20480u : 1024u;
#pragma unroll
        for (int jn = 0; jn < 5; ++jn) {
          tra[jn] = j3 ? b3 ^ (unsigned)(jn & 3) * 32u : b4 + jn * 32u;
          tra[5 + jn] = tra[jn] + (j3 ? 512u : 2560u);
          rto[jn] = (jok && !(j3 && jn == 4)) ? l16 + dst + jn * dstr : OOB;
          PIN(tra[jn]); PIN(tra[5 + jn]); PIN(rto[jn]);
        }
      }
#define RT_TR() do {                                                                                    \
        _Pragma("unroll") for (int jn = 0; jn < 5; ++jn) {                                              \
          const s16x4 lo = __builtin_amdgcn_ds_read_tr16_b64_v4i16((LAS s16x4*)(size_t)tra[jn]);        \
          const s16x4 hi = __builtin_amdgcn_ds_read_tr16_b64_v4i16((LAS s16x4*)(size_t)tra[5 + jn]);    \
          rt[jn] = __builtin_bit_cast(u32x4, bf16x8{lo[0], lo[1], lo[2], lo[3], hi[0], hi[1], hi[2], hi[3]}); \
          SB();                                                                                         \
        }                                                                                               \
      } while (0)
#define RT_OUT() do {                                                                                   \
        _Pragma("unroll") for (int jn = 0; jn < 4; ++jn) st128(rt[jn], rW, rto[jn], 0, 16);             \
        if (!j3) st128(rt[4], rW, rto[4], 0, 16);                                                       \
        FENCE();                                                                                        \
      } while (0)
      SIG(P_U);
      const bool mfin = wv == 7 && bx < 240 && lane < 32;
      const unsigned po = mfin ? ((unsigned)lane >> 3) * 512u + ((unsigned)lane & 7u) * 16u : OOB;
      u32x4 mp4;
#define LD_P() mp4 = __builtin_amdgcn_raw_buffer_load_b128(rW, po, O_MP + (unsigned)(bx & ~3) * 512u + (unsigned)(bx & 3) * 128u, 16)
#define MOD_L2() do {                                                                                   \
      if (wv == 7) {                                                                                    \
        unsigned spins = 0;                                                                             \
        while (__builtin_amdgcn_ballot_w64(mfin && (mp4[1] != tA || mp4[3] != tB)) != 0ull) {           \
          SPIN_STEP();                                                                                  \
          if (spins > RETRY_CAP) break;                                                                 \
          __builtin_amdgcn_s_sleep(USLEEP);                                                             \
          LD_P();                                                                                       \
        }                                                                                               \
        const float one = ((bx >= 40 && bx < 80) || (bx >= 160 && bx < 200)) ? 1.f : 0.f;               \
        const float s0_ = swap_add16(dpp_add(__uint_as_float(mp4[0]), 0)), s1_ = swap_add16(dpp_add(__uint_as_float(mp4[2]), 0)); \
        const unsigned d = pack2(one + rbf(s0_), one + rbf(s1_));                                       \
        const unsigned dn = (unsigned)__builtin_amdgcn_update_dpp(0, (int)d, 0xB1, 0xf, 0xf, true);     \
        st128(u32x4{d, tA, dn, tB}, rW, (lane < 8 && (lane & 1) == 0) ? (unsigned)lane * 8u : OOB, O_MOD + (unsigned)bx * 64u, 16); \
      }                                                                                                 \
      FENCE();                                                                                          \
    } while (0)
      if (wv == 7) LD_P();
      FENCE();
      FENCE();
      asm volatile("" : "+v"(fpa[0]), "+v"(fpa[1]), "+v"(fpa[2]), "+v"(fpv));
      ln5s(qw, hw);
      GET_D();
      MOD_L2();
      WAIT_ALL(P_U);
      FENCE();
      RT_LD();
      FILL_P(0, 3);
      FENCE();
      VMW(3);
      RT_TR();
      SIG(P_RT);
      RT_OUT();
      LD_WD(0, 5);
      WAIT_HALF(1, P_RT);
      LD_WD(5, NGA);
      FENCE();
      VMW(NGA);
      SIG(P_GA);
      LD_WD(NGA, 12);
      FENCE();
      if (mg < 2) {
        u32x2 uc[2];
        f32x4 yc[8];
        float v[8];
        uc[0] = *(const LAS u32x2*)(lds + SL_C + CV_U + 512 + (256 * zja + 16 * l15 + 4 * g) * 2);
        uc[1] = *(const LAS u32x2*)(lds + SL_C + CV_U + 512 + (256 * zjb + 16 * l15 + 4 * g) * 2);
        unsigned zq = O_Z + ZOFF(zja, zjb, chC);
        PIN(zq);
        WAIT_HALF(0, P_C2);
#pragma unroll
        for (int c = 0; c < 8; ++c) yc[c] = *(const LAS f32x4*)(lds + SL_C + mg * 2048 + l16 + (c >> 1) * 4096 + (c & 1) * 1024);
        STG(8, v[i] = (i & 1) ? hi2f(uc[i >> 2][(i >> 1) & 1]) : lo2f(uc[i >> 2][(i >> 1) & 1]); PIN(v[i]));
        STG(8, v[i] = __builtin_fmaf(v[i], __uint_as_float(dC), yc[i >> 2][i & 3]); PIN(v[i]));
#pragma unroll
        for (int w = 1; w < 4; ++w) STG(8, v[i] += yc[2 * w + (i >> 2)][i & 3]; PIN(v[i]));
        st128(widen(pack2(v[0], plainv(v[1])), pack2(v[2], v[3]), pack2(v[4], plainv(v[5])), pack2(v[6], v[7])), rW, zq, 0, 16);
      }
      FENCE();
      SIG(P_ZC);
      FENCE();
      LD_WS(0, NS2);
      FENCE();
      if (HPZ) { WAIT_HALF(0, P_X2); __builtin_amdgcn_s_sleep(HPZ); }
      LD_WS(NS2, 10);
      if (mg > 0) {
        WAIT_HALF(1, P_ZC);
#pragma unroll
        for (int j = 0; j < 5; ++j) W_DMA(16 + mg, j);
      }
      LD_WS2U(0, NZ2);
      FENCE();
      WAIT_HALF(0, P_X2);
#pragma unroll
      for (int x = 0; x < 10; ++x) *(LAS u32x4*)(lds + (9 + mg + 4 * (x / 5)) * 5120 + (x % 5) * 1024 + l16) = WS[x];
      WAIT_HALF(0, P_T);
      R_PRE();
      VMW(NZ2);
      SIG(P_X);
      WAIT_HALF(0, P_Z);
      LD_WS2U(NZ2, 10);
      FENCE();
      WAIT_2(P_RP, P_X);
#pragma unroll
      for (int j = 0; j < 5; ++j) W_DMA(20 + mg, j);
      FENCE();
    } else {
      PRIO(3);
      const unsigned rt0 = (unsigned)__builtin_amdgcn_s_memrealtime();
      unsigned kpa = (unsigned)L_KV + ((unsigned)lane & 7u) * 1312u + (unsigned)mg * 64u + ((unsigned)lane >> 3) * 8u;
      unsigned kpb = (unsigned)L_KV + ((unsigned)l15 >> 1) * 1312u + 256u + (unsigned)mg * 16u + (unsigned)g * 4u + ((unsigned)lane & 1u) * 640u, kpc = kpb + ((unsigned)lane & 1u) * 32u;
      const unsigned cha_ = (unsigned)ch0 + 32u * mg + ((unsigned)lane >> 3) * 4u, chb_ = (unsigned)ch0 + 128u + 8u * mg + 2u * g;
      unsigned koa = KVROW(cha_) + (unsigned)tok0 * 4u + ((unsigned)lane & 7u) * 16u, koa2 = KVROW(cha_ + 2u) + (unsigned)tok0 * 4u + ((unsigned)lane & 7u) * 16u;
      unsigned kob = KVROW(chb_) + (unsigned)tok0 * 4u + (unsigned)l15 * 8u;
      asm volatile("" : "+v"(kpa), "+v"(kpb), "+v"(kpc), "+v"(koa), "+v"(koa2), "+v"(kob));
      u32x4 ug[6];
      unsigned uo = (unsigned)mg * 2048u + l16;
      unsigned udone;
      asm volatile("s_mov_b32 %0, 0" : "=s"(udone));
      unsigned ubA = KVROW((unsigned)chA), ubC = KVROW((unsigned)chC), ubB = KVROW((unsigned)(chA + 1));
      asm volatile("" : "+s"(ubA), "+s"(ubB), "+s"(ubC));
      PIN(uo);
#pragma unroll
      for (int r = 0; r < 6; ++r) asm volatile("" : "=v"(ug[r]));
#define TRY_U(EL, T, CODE)                                                                               \
        asm volatile("s_cmp_lg_u32 %6, 0\n\ts_cbranch_scc1 Lu%=\n\ts_cmp_lt_u32 %[el], %[t]\n\ts_cbranch_scc1 Lu%=\n\t" \
                     "buffer_load_dwordx4 %2, %[vo], %[rs], %[c] offen sc1\n\tbuffer_load_dwordx4 %3, %[vo], %[rs], %[c] offen offset:1024 sc1\n\t" \
                     "buffer_load_dwordx4 %0, %[vo], %[rs], %[a] offen sc1\n\tbuffer_load_dwordx4 %1, %[vo], %[rs], %[a] offen offset:1024 sc1\n\t" \
                     "buffer_load_dwordx4 %4, %[vo], %[rs], %[b] offen sc1\n\tbuffer_load_dwordx4 %5, %[vo], %[rs], %[b] offen offset:1024 sc1\n\t" \
                     "s_mov_b32 %6, %[cd]\n"                                                            \
                     "Lu%=:" : "+v"(ug[0]), "+v"(ug[1]), "+v"(ug[2]), "+v"(ug[3]), "+v"(ug[4]), "+v"(ug[5]), "+s"(udone) \
                     : [el] "s"(EL), [t] "i"(T), [cd] "i"(CODE), [vo] "v"(uo), [rs] "s"(dW), [a] "s"(ubA), [b] "s"(ubB), [c] "s"(ubC) : "scc", "memory")
      VMW(9);
      TAGS();
      unsigned tAB = (lane & 1) ? tB : tA;
      PIN(tAB);
      asm volatile("s_waitcnt vmcnt(0)" : "+v"(sk[0]), "+v"(sk[1]), "+v"(sk[2]));
      const unsigned rt1 = (unsigned)__builtin_amdgcn_s_memrealtime();
      SIG(P_KV);
      FENCE();
      f32x2 wa[3][4];
#pragma unroll
      for (int c = 0; c < 3; ++c)
#pragma unroll
        for (int t = 0; t < 4; ++t) {
          const f32x2 w = (t & 1) ? f32x2{hi2f(sk[c][t >> 1]), hi2f(sk[c][2 + (t >> 1)])} : f32x2{lo2f(sk[c][t >> 1]), lo2f(sk[c][2 + (t >> 1)])};
          wa[c][t] = w;
          PIN(wa[c][t]);
        }
      asm volatile("" : "+v"(tAB));
      FENCE();
      WAIT_HALF(0, P_KV);
      if (TU1) TRY_U(rt1 - rt0, TU1, 1);
      {
        u32x2 kw[7], vw[4];
        unsigned kx[5], vx[2];
#pragma unroll
        for (int i = 0; i < 7; ++i) kw[i] = *(const LAS u32x2*)(lds + kpa + (i < 3 ? 320 + 320 * i : 1312 + 320 * (i - 3)));
#pragma unroll
        for (int i = 0; i < 5; ++i) kx[i] = *(const LAS unsigned*)(lds + ((i == 1 || i == 2) ? kpc : kpb) + (i < 3 ? 320 + 320 * i : 1312 + 320 * (i - 3)));
#pragma unroll
        for (int i = 0; i < 4; ++i) vw[i] = *(const LAS u32x2*)(lds + kpa + 12288 + 320 * i);
#pragma unroll
        for (int i = 0; i < 2; ++i) vx[i] = *(const LAS unsigned*)(lds + kpb + 12288 + 320 * i);
        __builtin_amdgcn_sched_barrier(0);
        f32x2 kf[19], a[10], p[10], t_[10];
#define KX(o, t) kf[((o) >> 2) * 7 + ((o) & 3) + (t)]
#define STR(lo, hi, stmt) do { _Pragma("unroll") for (int i = (lo); i < (hi); ++i) { stmt; } __builtin_amdgcn_sched_barrier(0); } while (0)
#define KV_GROUP(lo, hi) do {                                                                           \
        STR(((lo) >> 2) * 7, (hi) == 10 ? 19 : ((hi) >> 2) * 7, kf[i] = up2(i < 14 ? kw[i % 7][i / 7] : kx[i < 14 ? 0 : i - 14]); PIN(kf[i])); \
        {                                                                                               \
          STR(lo, hi, a[i] = KX(i, 0) * wa[i >> 2][3]; PIN(a[i]));                                      \
          _Pragma("unroll") for (int t = 1; t < 4; ++t) STR(lo, hi, a[i] = FMA2(KX(i, t), wa[i >> 2][3 - t], a[i]); PIN(a[i])); \
          STR(lo, hi, t_[i] = FMA2(a[i], cp, a[i]); PIN(t_[i]));                                        \
          STR(lo, hi, a[i] = FMA2(a[i], cn, t_[i]); PIN(a[i]));                                         \
        }                                                                                               \
        STR(lo, hi, p[i] = up2(i < 8 ? vw[i & 3][i >> 2] : vx[i & 1]); PIN(p[i]));                      \
        STR(lo, hi, a[i] *= p[i]; PIN(a[i]));                                                           \
        _Pragma("unroll") for (int c = (lo) >> 1; c < ((hi) < 8 ? (hi) : 8) >> 1; ++c) {                \
          const int o = (c >> 1) * 4, e = c & 1;                                                        \
          st128(u32x4{pack2(a[o][e], plainv(a[o + 1][e])), tA, pack2(a[o + 2][e], plainv(a[o + 3][e])), tB}, rW, c < 2 ? koa : koa2, e * 8192u, 16); \
        }                                                                                               \
        if ((hi) == 10) { _Pragma("unroll") for (int e = 0; e < 2; ++e) __builtin_amdgcn_raw_buffer_store_b64(u32x2{pack2(a[8][e], plainv(a[9][e])), tAB}, rW, kob, e * 8192, 16); } \
        __builtin_amdgcn_sched_barrier(0);                                                              \
      } while (0)
        KV_GROUP(0, 4);
        KV_GROUP(4, 10);
      }
      FENCE();
      const unsigned rt2 = (unsigned)__builtin_amdgcn_s_memrealtime();
      REQ_Q();
      REQ_D();
      FENCE();
      if (TU2) TRY_U(rt2 - rt0, TU2, 2);
      WAIT_HALF(1, P_K);
      BUILD(SL_C, t8);
      SIG(P_B);
      FENCE();
#define LD_U(r) ug[r] = __builtin_amdgcn_raw_buffer_load_b128(rW, uo + ((r) & 1) * 1024, (r) < 2 ? ubA : (r) < 4 ? ubC : ubB, 16)
      u32x4 A2[16], B2[16];
      WAIT_HALF(0, P_B);
      CV_RA(A2, SL_C, 0);
      CV_RA(A2, SL_C, 1);
      W12_G(gw);
#pragma unroll
      for (int j = 0; j < 5; ++j) PIN(gw[j]);
      unsigned zq = Z2OFF(wv);
      unsigned pa = a00 + SL_A - 3648, pb = b00 + SL_A - 64;
      unsigned xw = SL_A + mg * 8192 + l16, xc = SL_C + mg * 4096 + l16;
      asm volatile("" : "+v"(zq), "+v"(pa), "+v"(pb), "+v"(xw), "+v"(xc));
      FENCE();
      if (udone == 0u) while ((unsigned)__builtin_amdgcn_s_memrealtime() - rt0 < (unsigned)TU) __builtin_amdgcn_s_sleep(1);
      TRY_U(rt0, 0, 3);
      FENCE();
      unsigned uw = (unsigned)(SL_A + CV_U + 512 + mg * 1024 + lane * 8);
      PIN(uw);
      WAIT_HALF(1, P_B2);
      asm volatile("s_cmp_eq_u32 %[d], 1\n\ts_cbranch_scc1 Lw%=\n\ts_waitcnt vmcnt(0)\nLw%=:\n\ts_waitcnt vmcnt(9)"
                   : "+v"(ug[0]), "+v"(ug[1]), "+v"(ug[2]), "+v"(ug[3]), "+v"(ug[4]), "+v"(ug[5]) : [d] "s"(udone) : "scc", "memory");
      {
#define U_BAD(r) (ug[r][1] != tA || ug[r][3] != tB)
        if (__builtin_expect(__builtin_amdgcn_ballot_w64(U_BAD(0) || U_BAD(1) || U_BAD(2) || U_BAD(3) || U_BAD(4) || U_BAD(5)) != 0ull, 1)) {
          unsigned spins = 0;
          do {
            __builtin_amdgcn_s_sleep(USLEEP);
#pragma unroll
            for (int r = 0; r < 6; ++r) if (U_BAD(r)) LD_U(r);
            SPIN_STEP();
          } while (__builtin_amdgcn_ballot_w64(U_BAD(0) || U_BAD(1) || U_BAD(2) || U_BAD(3) || U_BAD(4) || U_BAD(5)) != 0ull && spins < RETRY_CAP);
          VM0();
        }
#pragma unroll
        for (int r = 2; r < 8; ++r) *(LAS u32x2*)(lds + uw + ((r % 6) >> 1) * CV_SLOT + (r & 1) * 512) = u32x2{ug[r % 6][0], ug[r % 6][2]};
      }
      SIG(P_U);
      GET_D();
      LD_WS2(0, 10);
      FENCE();
      WAIT_HALF(0, P_U);
      CV_RB(B2, SL_C, 0);
      FENCE();
      f32x4 acc[8], ac[4];
      u32x4 A[16], B[16];
#define RA(P, rd, i_) *(const LAS u32x4*)(lds + (P) + 3648 - 64 * (rd) - 512 * (i_))
#define RB(P, rd, i_) *(const LAS u32x4*)(lds + (P) + 64 - 64 * (rd) + 512 * (i_))
#define CONV_C(J, lo, hi)                                                                               \
      sfor<lo, hi>([&](auto C_) __attribute__((always_inline)) {                                         \
        constexpr int c = decltype(C_)::v, rd = c / 18, i = cc_i(J, c % 18), t = cc_t(J, c % 18);       \
        ac[t] = mfma16(A2[8 * rd + i], B2[8 * rd + (((J) >> (4 * t)) & 15) - i], (rd == 0 && i == 0) ? f32x4{0.f, 0.f, 0.f, 0.f} : ac[t]); \
        SB();                                                                                           \
        if constexpr (c < 8) B2[15 - c] = RB(pb + CV_SLOT, 1, 7 - c);                                   \
        if constexpr (c >= 18 && c < 34) { constexpr int x = (c - 18) >> 1; if constexpr ((c & 1) == 0) B[7 - x] = RB(pb, 0, 7 - x); else A[x] = RA(pa, 0, x); } \
        SB();                                                                                           \
      });
      if (half == 0) { CONV_C(0x7430, 0, 18) } else { CONV_C(0x6521, 0, 18) }
      SB();
      int half2 = half;
      asm volatile("" : "+s"(half2));
      if (half2 == 0) { CONV_C(0x7430, 18, 36) } else { CONV_C(0x6521, 18, 36) }
      u32x4 A3[16], B3[16];
      f32x4 ab_[8];
      unsigned pk_;
      float ud[16];
      u32x2 u2[4];
      f32x4 ya[8];
      unsigned ga_;
      unsigned pu = (unsigned)(SL_A + CV_U + 512 + (256 * wv + 16 * l15 + 4 * g) * 2), py = (unsigned)(SL_A + wv * 1024) + l16;
      const unsigned f0 = (unsigned)L_CTL + ((unsigned)lane & 3u) * 4u;
      sfor<0, 144>([&](auto M_) __attribute__((always_inline)) {
        constexpr int m = decltype(M_)::v, ch = m / 72, rd = (m / 36) & 1, r = m % 36, i = m < 108 ? cv_i(r) : cl_i(r), dd = m < 108 ? cv_d(r) : cl_t(r) - cl_i(r);
        if constexpr (ch == 0) acc[i + dd] = mfma16(A[8 * rd + i], B[8 * rd + dd], (rd == 0 && i == 0) ? f32x4{0.f, 0.f, 0.f, 0.f} : acc[i + dd]);
        else ab_[i + dd] = mfma16(A3[8 * rd + i], B3[8 * rd + dd], (rd == 0 && i == 0) ? f32x4{0.f, 0.f, 0.f, 0.f} : ab_[i + dd]);
        SB();
        if constexpr (m == 30) { pa += SL_B - SL_A; pb += SL_B - SL_A; asm volatile("" : "+v"(pa), "+v"(pb)); }
        if constexpr (m == 94) { xw += SL_B - SL_A; PIN(xw); }
        if constexpr (r >= RS0 && r < RS0 + 16 && m < 108) {
          constexpr int x = (r - RS0) >> 1;
          if constexpr (m < 36) { if constexpr ((r & 1) == 0) B[15 - x] = RB(pb, 1, 7 - x); else A[8 + x] = RA(pa, 1, x); }
          else if constexpr (m < 72) { if constexpr ((r & 1) == 0) B3[7 - x] = RB(pb, 0, 7 - x); else A3[x] = RA(pa, 0, x); }
          else { if constexpr ((r & 1) == 0) B3[15 - x] = RB(pb, 1, 7 - x); else A3[8 + x] = RA(pa, 1, x); }
        }
        if constexpr (m == 1 || m == 4 || m == 23 || m == 26) { constexpr int c = m == 1 ? 0 : m == 4 ? 1 : m == 23 ? 2 : 3; *(LAS f32x4*)(lds + xc + c * 1024) = ac[c]; }
        if constexpr (m == 28) SIG(P_C2);
        if constexpr (m == 38 || m == 104) pk_ = *(volatile LAS unsigned*)(lds + f0);
        if constexpr (m == 45) NEED_AT(pk_, f0, P_C2);
        if constexpr (m == 111) NEED_AT(pk_, f0, P_A2);
        constexpr int wa = m == 47 ? 0 : m == 54 ? 1 : m == 60 ? 2 : m == 65 ? 3 : m == 69 ? 4 : m == 72 ? 5 : m == 75 ? 6 : m == 77 ? 7 : -1;
        constexpr int wb = m == 113 ? 0 : m == 116 ? 1 : m == 119 ? 2 : m == 122 ? 3 : m == 126 ? 4 : m == 132 ? 5 : m == 139 ? 6 : -1;
        if constexpr (wa >= 0) *(LAS f32x4*)(lds + xw + wa * 1024) = acc[wa];
        if constexpr (wb >= 0) *(LAS f32x4*)(lds + xw + wb * 1024) = ab_[wb];
        if constexpr (m == 95) SIG(P_A2);
        if constexpr (m == 58) PIN(pu);
        if constexpr (m == 59 || m == 61) { constexpr int c = m - 59; u2[c] = *(const LAS u32x2*)(lds + pu + c * 1024); u2[c + 1] = *(const LAS u32x2*)(lds + pu + (SL_B - SL_A) + c * 1024); }
        constexpr int us = cv_slot(m, 0);
        if constexpr (us >= 0 && us < 16) {
#pragma unroll
          for (int o = 2 * us; o < 2 * us + 2; ++o) {
            const int v = (o >> 3) * 4 + (o & 3);
            if ((o & 4) == 0) ud[v] = (v & 1) ? hi2f(u2[v >> 2][(v >> 1) & 1]) : lo2f(u2[v >> 2][(v >> 1) & 1]);
            else ud[v] *= ((v >> 2) & 1) ? hi2f(dAB) : lo2f(dAB);
            PIN(ud[v]);
          }
        }
        if constexpr (m == 110) PIN(py);
        if constexpr (m == 112 || m == 114 || m == 115 || m == 117) { constexpr int c = m == 112 ? 0 : m == 114 ? 2 : m == 115 ? 4 : 6;
          ya[c] = *(const volatile LAS f32x4*)(lds + py + (c >> 2) * 4096 + (c & 3) * 8192); ya[c + 1] = *(const volatile LAS f32x4*)(lds + py + (c >> 2) * 4096 + (c & 3) * 8192 + 8192); }
        constexpr int zs = cv_slot(m, 1);
        if constexpr (zs >= 0 && zs < 16) {
#pragma unroll
          for (int o = 2 * zs; o < 2 * zs + 2; ++o) { const int tl = (o >> 2) & 1, e = o & 3; ud[tl * 8 + e] += ya[4 * tl + (o >> 3)][e]; PIN(ud[tl * 8 + e]); }
        }
        if constexpr (m == 131) ga_ = *(volatile LAS unsigned*)(lds + f0 + 16);
        if constexpr (m == 140) { py += SL_B - SL_A; PIN(py); }
        SB();
      });
      *(LAS f32x4*)(lds + xw + 7 * 1024) = ab_[7];
      SIG(P_X);
      u32x4 mg4[4];
#define LD_MG(i) mg4[i] = __builtin_amdgcn_raw_buffer_load_b128(rW, (unsigned)t8 * 16u, O_MOD + (i) * 4096, 16)
      u32x2 f1v;
#define LD_F1() f1v = __builtin_amdgcn_raw_buffer_load_b64(rW, (unsigned)t8 * 8u, O_FL1, 16)
      NEED_AT(ga_, f0 + 16, P_GA);
      {
        unsigned spins = 0;
        for (;;) {
          const unsigned fl = *(volatile LAS unsigned*)(lds + f0);
#pragma unroll
          for (int c = 0; c < 8; ++c) ya[c] = *(const volatile LAS f32x4*)(lds + py + (c >> 2) * 4096 + (c & 3) * 8192);
          if (__builtin_amdgcn_ballot_w64(fl < (unsigned)P_X) == 0ull) break;
          SPIN_STEP();
          if (spins > SPIN_CAP) break;
        }
#pragma unroll
        for (int tl = 0; tl < 2; ++tl) {
          const f32x4 yb = (ya[4 * tl] + ya[4 * tl + 1]) + (ya[4 * tl + 2] + ya[4 * tl + 3]);
          const f32x2 v2 = f32x2{yb[0], yb[1]} + f32x2{ud[8 * tl + 4], ud[8 * tl + 5]}, v3 = f32x2{yb[2], yb[3]} + f32x2{ud[8 * tl + 6], ud[8 * tl + 7]};
          st128(u32x4{pack2(ud[8 * tl], plainv(v2[0])), pack2(ud[8 * tl + 1], v2[1]), pack2(ud[8 * tl + 2], plainv(v3[0])), pack2(ud[8 * tl + 3], v3[1])}, rW, zq + tl * 64u * TS, 0, 16);
        }
      }
      FENCE();
      SIG(P_X2);
      LD_F1(); LD_MG(0); LD_MG(1); LD_MG(2); LD_MG(3);
      FENCE();
      LN1();
      u32x4 za, zb, zc = {0u, 0u, 0u, 0u}, zd = {0u, 0u, 0u, 0u};
      const unsigned zo = (unsigned)mt * (4u * TS) + ZCH((unsigned)t8 & ~1u) + (unsigned)nt * 32u + ((unsigned)t8 & 1u) * 16u;
      const unsigned zo1 = (unsigned)mt * (4u * TS) + ZCH(256u + ((unsigned)t8 & ~1u)) + (unsigned)nt * 32u + ((unsigned)t8 & 1u) * 16u;
      const unsigned zo2 = (unsigned)mt * (4u * TS) + ZCH(2u * t8 + 128u) + (unsigned)nt * 16u;
      FENCE();
      {
#define MG_BAD(i) (mg4[i][1] != tA || mg4[i][3] != tB)
#define T_BAD() (__builtin_amdgcn_ballot_w64(f1v[0] != tA || f1v[1] != tB || MG_BAD(0) || MG_BAD(1) || MG_BAD(2) || MG_BAD(3)) != 0ull)
        if (__builtin_expect(T_BAD(), 0)) {
          unsigned spins = 0;
          do {
            __builtin_amdgcn_s_sleep(USLEEP);
            if (f1v[0] != tA || f1v[1] != tB) LD_F1();
#pragma unroll
            for (int i = 0; i < 4; ++i) if (MG_BAD(i)) LD_MG(i);
            SPIN_STEP();
          } while (T_BAD() && spins < RETRY_CAP);
          VM0();
        }
#pragma unroll
        for (int i = 0; i < 4; ++i) {
          const int n = (t8 + 256 * i) * 4;
          if (n < 6 * D_) *(LAS u32x2*)(lds + L_MOD + n * 2) = u32x2{mg4[i][0], mg4[i][2]};
          if (i == 3 && n >= 5 * D_ + n0 && n < 5 * D_ + n0 + 80)
            *(LAS f32x4*)(lds + L_GB + (n - 5 * D_ - n0) * 4) = f32x4{lo2f(mg4[i][0]), hi2f(mg4[i][0]), lo2f(mg4[i][2]), hi2f(mg4[i][2])};
        }
        SIG(P_T);
      }
      WAIT_HALF(0, P_T);
      if (DLY0) __builtin_amdgcn_s_sleep(DLY0);
      PRIO(3);
      za = __builtin_amdgcn_raw_buffer_load_b128(rW, zo, O_Z, 16); zb = __builtin_amdgcn_raw_buffer_load_b128(rW, zo1, O_Z, 16);
      if (wv == 3) { zc = __builtin_amdgcn_raw_buffer_load_b128(rW, zo2, O_Z, 16); zd = __builtin_amdgcn_raw_buffer_load_b128(rW, zo2 + 16u * ZG, O_Z, 16); }
      FENCE();
      SIG(P_Z);
      R_PRE();
      SIG(P_RP);
      unsigned zw0 = (unsigned)(L_XCH + 4 * (t8 & 1) * YROW + 4 * (t8 >> 1)), zw1 = (unsigned)(L_XCH + 256 + 4 * t8);
      asm volatile("" : "+v"(zw0), "+v"(zw1), "+v"(ry), "+v"(rz));
      asm volatile("" : "+s"(w3s[0]), "+s"(w3s[1]));
      unsigned spins = 0;
      for (;;) {
        const bool ba = amax2(za) >= PTHR, bb_ = amax2(zb) >= PTHR, bc_ = amax2(zc) >= PTHR, bd = amax2(zd) >= PTHR;
        if (__builtin_amdgcn_ballot_w64(ba || bb_ || bc_ || bd) == 0ull) break;
        SPIN_STEP();
        if (spins > RETRY_CAP) break;
        __builtin_amdgcn_s_sleep(ZSLEEP);
        if (ba) za = __builtin_amdgcn_raw_buffer_load_b128(rW, zo, O_Z, 16);
        if (bb_) zb = __builtin_amdgcn_raw_buffer_load_b128(rW, zo1, O_Z, 16);
        if (bc_) zc = __builtin_amdgcn_raw_buffer_load_b128(rW, zo2, O_Z, 16);
        if (bd) zd = __builtin_amdgcn_raw_buffer_load_b128(rW, zo2 + 16u * ZG, O_Z, 16);
      }
#pragma unroll
      for (int i = 0; i < 4; ++i) {
        *(LAS unsigned*)(lds + zw0 + i * YROW) = za[i];
        *(LAS unsigned*)(lds + zw0 + i * YROW + 512) = zb[i];
      }
      if (wv == 3) {
#pragma unroll
        for (int i = 0; i < 4; ++i) {
          *(LAS unsigned*)(lds + zw1 + (2 * i) * YROW) = __builtin_amdgcn_perm(zd[i], zc[i], 0x05040100u);
          *(LAS unsigned*)(lds + zw1 + (2 * i + 1) * YROW) = __builtin_amdgcn_perm(zd[i], zc[i], 0x07060302u);
        }
      }
      VM0();
      SIG(P_Z2);
    }
  }
  {
    f32x2 hh[5], t_[5];
    asm volatile("" : "+v"(ry), "+v"(rz));
    asm volatile("" : "+s"(w3s[0]), "+s"(w3s[1]));
    if (kh) PRIO(PR1); else PRIO(PHI);
    WAIT_HALF(0, P_Z2);
    unsigned zw[5], xp[5], hp_[5];
    {
      const u32x4 a = *(LAS u32x4*)(lds + ry);
      zw[0] = a[0]; zw[1] = a[1]; zw[2] = a[2]; zw[3] = a[3];
      zw[4] = *(LAS unsigned*)(lds + rz);
    }
    FENCE();
#define RQ3(k) do { LD_W3R((k) == 0 ? 0 : (k) == 1 ? NR0 : NR1, (k) == 0 ? NR0 : (k) == 1 ? NR1 : NWH); FENCE(); } while (0)
    RQ3(0);
    FENCE();
    STG(5, hh[i] = up2(zw[i]); PIN(hh[i]));
    STG(5, xq[i] = FMA2(hh[i], ha[i], xq[i]); PIN(xq[i]));
    STG(5, t_[i] = FMA2(xq[i], cp, xq[i]); PIN(t_[i]));
    STG(5, xq[i] = FMA2(xq[i], cn, t_[i]); PIN(xq[i]));
    RQ3(1);
    float m3;
    {
      const f32x2 s2 = ((xq[0] + xq[1]) + (xq[2] + xq[3])) + xq[4];
      const f32x2 q2 = FMA2(xq[0], xq[0], xq[1] * xq[1]) + FMA2(xq[2], xq[2], FMA2(xq[3], xq[3], xq[4] * xq[4]));
      const f32x2 p2 = FMA2(xq[0], ca[0], xq[1] * ca[1]) + FMA2(xq[2], ca[2], FMA2(xq[3], ca[3], xq[4] * ca[4]));
      float sm3[3] = {s2[0] + s2[1], q2[0] + q2[1], p2[0] + p2[1]};
      wave_sums<3>(sm3);
      const float mean = sm3[0] * (1.f / (float)D_), qs = sm3[1], ps = sm3[2];
      const float m = rbf(mean);
      f32x2 d[5];
#pragma unroll
      for (int j = 0; j < 5; ++j) d[j] = (xq[j] - m) * ca[j];
      const float var = rbf(__builtin_fmaxf(__builtin_fmaf(-mean, mean, qs * (1.f / (float)D_)), 0.f));
      const float den = rbf(__builtin_amdgcn_sqrtf(rbf(var + 1e-5f)));
      const float rinv = __builtin_amdgcn_rcpf(den);
      m3 = rbf(__builtin_fmaf(__builtin_fmaf(-m, mc, ps * (1.f / (float)D_)), rinv, mb));
      __builtin_amdgcn_sched_barrier(0);
      RQ3(2);
      STG(5, hh[i] = FMA2(d[i], (f32x2{rinv, rinv}), sb[i]); PIN(hh[i]));
      STG(5, hh[i] -= m3; PIN(hh[i]));
    }
#pragma unroll
    for (int j = 0; j < 5; ++j) hp_[j] = pack2(hh[j][0], (j & 1) ? hh[j][1] : (j < 4 || (lane & 1) == 0) ? codedv(hh[j][1]) : hh[j][1]);
    *(LAS u32x4*)(lds + ry) = u32x4{hp_[0], hp_[1], hp_[2], hp_[3]};
    *(LAS unsigned*)(lds + rz) = hp_[4];
    BARRIER();
    {
      const unsigned tb = (unsigned)(s0 >> 4) * 20480u + (unsigned)(s0 & 15) * 16u + (unsigned)(tid >> 3) * 256u + (unsigned)(tid & 7) * 16u;
      const LAS char* pr = lds + L_XCH + (tid & 7) * YROW + (tid >> 3) * 16;
      const u32x4 v0 = *(const LAS u32x4*)(pr);
      const u32x4 v1 = *(const LAS u32x4*)(pr + (tid < 128 ? 1024 : 0));
      st128(v0, rW, tb, O_HP, 16);
      if (wv < 2) st128(v1, rW, tb + 64u * 256u, O_HP, 16);
    }
    FENCE();
    PRIO(1);
#pragma unroll
    for (int j = 0; j < 5; ++j) xp[j] = pack2(xq[j][0], xq[j][1]);
    *(LAS u32x4*)(lds + ry + 8 * YROW) = u32x4{xp[0], xp[1], xp[2], xp[3]};
    *(LAS unsigned*)(lds + rz + 8 * YROW) = xp[4];
    FENCE();
    {
      const f32x2 s2 = ((hh[0] + hh[1]) + (hh[2] + hh[3])) + hh[4];
      const f32x2 q2 = FMA2(hh[0], hh[0], hh[1] * hh[1]) + FMA2(hh[2], hh[2], FMA2(hh[3], hh[3], hh[4] * hh[4]));
      const float mean = wave_sum2(s2[0] + s2[1]) * (1.f / (float)D_), qs = wave_sum2(q2[0] + q2[1]);
      const float var = rbf(__builtin_fmaxf(__builtin_fmaf(-mean, mean, qs * (1.f / (float)D_)), 0.f));
      const float den = rbf(__builtin_amdgcn_sqrtf(rbf(var + 1e-5f)));
      const u32x2 st = {canon(__float_as_uint(m3)), __float_as_uint(den)};
      __builtin_amdgcn_raw_buffer_store_b64(st, rW, lane == 0 ? (unsigned)srow * 8u : OOB, O_ST3, 16);
    }
    {
      BARRIER();
      const unsigned tb = (unsigned)(s0 >> 4) * 20480u + (unsigned)(s0 & 15) * 16u + (unsigned)(tid >> 3) * 256u + (unsigned)(tid & 7) * 16u;
      const LAS char* pr = lds + L_XCH + (8 + (tid & 7)) * YROW + (tid >> 3) * 16;
      st128(*(const LAS u32x4*)(pr), rW, tb, O_XO, 16);
      if (wv >= 6) st128(*(const LAS u32x4*)(pr + 256), rW, tb + 16u * 256u, O_XO, 16);
      SIG(P_L1);
      if (bx == 0 && wv == 5) __builtin_amdgcn_raw_buffer_store_b64(u32x2{(unsigned)epn, (unsigned)(epn >> 32)}, rW, lane == 0 ? 0u : OOB, O_EP, 16);
    }
    FENCE();
    PRIO(0);
    FENCE();
  }

  float sthr = PTHR, scmx = CMAX;
  unsigned scsh = CSH;
  asm volatile("" : "+s"(sthr), "+s"(scsh), "+s"(scmx));
  unsigned xcw = (unsigned)(L_XCH + gm * 5120 + gk * 2048) + l16, xcr = (unsigned)(L_XCH + gm * 5120 + 2048 - gk * 2048) + l16, xcf = (unsigned)(L_CTL + gp * 4);
  asm volatile("" : "+v"(xcw), "+v"(xcr), "+v"(xcf));
  unsigned long long gkm;
  asm volatile("s_cmp_lg_u32 %1, 0\n\ts_cselect_b64 %0, -1, 0" : "=s"(gkm) : "s"(gk) : "scc");
  static_assert(KL >= 3 && KG <= 54 - 2 * KL, "the weights are read in front of the signal");
#define EXCHANGE(ph)                                                                        \
  do {                                                                                      \
    f32x4 e0, e1, e2;                                                                       \
    unsigned spins = 0;                                                                     \
    for (;;) {                                                                              \
      const unsigned fl = *(volatile LAS unsigned*)(lds + xcf);                                                           \
      e0 = *(volatile LAS f32x4*)(lds + xcr);                                               \
      e1 = *(volatile LAS f32x4*)(lds + xcr + 1024);                                        \
      e2 = *(volatile LAS f32x4*)(lds + xcr + 2048);                                        \
      if (__builtin_amdgcn_readfirstlane(fl) >= (ph)) break;                                \
      SPIN_STEP();                                                                          \
      if (spins > SPIN_CAP) break;                                                          \
    }                                                                                       \
    asm volatile("s_nop 3" : "+v"(acc[3]), "+v"(acc[4]));        \
    STG(12, e[i] = (i < 4 ? acc[3][i & 3] + e0[i & 3] : i < 8 ? acc[4][i & 3] + e1[i & 3] : acc[2][i & 3] + e2[i & 3]); PIN(e[i])); \
  } while (0)
#define KLOOP(CD, PB, BLK, VA, AOFF, XG, XS, ph)                                            \
  asm volatile("s_nop 1" : "+v"(A[0]));                                                     \
  sfor<0, 50>([&](auto M_) __attribute__((always_inline)) {                                 \
    constexpr int m = decltype(M_)::v, i = kl_i(m), j = kl_j(m), f = m / 4 + 1, c = m % 4;  \
    mf<i == 0 && (CD || !HFI || j < 2)>(acc[j], __builtin_bit_cast(u32x4, __builtin_shufflevector(Bh[2 * m], Bh[2 * m + 1], 0, 1, 2, 3, 4, 5, 6, 7)), A[i]); \
    SB();                                                                                   \
    constexpr int n0_ = m < KG ? (m * (100 - 2 * (PB))) / KG : 100, n1_ = m < KG ? ((m + 1) * (100 - 2 * (PB))) / KG : 100; \
    if constexpr (n1_ > n0_) RD_BH(2 * (PB) + n0_, BLK);                                    \
    if constexpr (n1_ > n0_ + 1) RD_BH(2 * (PB) + n0_ + 1, BLK);                            \
    static_assert(n1_ <= n0_ + 2 && (f > 9 || kl_i(m + 1) < f), "order");                   \
    if constexpr (f < 10) {                                                                 \
      if constexpr (c == 0) { m_ = CD ? amin2(A[f]) : __builtin_amdgcn_fmed3f(__builtin_fabsf(__uint_as_float(A[f][0])), __builtin_fabsf(__uint_as_float(A[f][2])), scmx); PIN(m_); } \
      if constexpr (c == 1) CMP_BAD(CD, bm, m_);                                            \
      if constexpr (c == 2 && CD) A[f][0] -= scsh;                                          \
      if constexpr (c == 3 && CD) { A[f][2] -= scsh; PIN(A[f]); }                           \
      if constexpr (c == 3) RETRY(CD, A[f], bm, VA, sa[f / 5], AOFF + (f % 5) * 2048, PHI); \
    }                                                                                       \
    if constexpr (m % 5 == 2 || m % 5 == 4) { XS(2 * (m / 5) + (m % 5 == 4)); }             \
    XG(m);                                                                                  \
                                \
    if constexpr (m == 51 - 2 * KL) { asm volatile("s_nop 3" : "+v"(acc[0])); *(volatile LAS f32x4*)(lds + xcw) = acc[0]; } \
    if constexpr (m == 52 - 2 * KL) *(volatile LAS f32x4*)(lds + xcw + 1024) = acc[1];      \
    if constexpr (m == 53 - 2 * KL) asm volatile("s_mov_b64 exec, %2\n\tds_write_b128 %0, %1 offset:2048\n\ts_mov_b64 exec, -1" :: "v"(xcw), "v"(acc[2]), "s"(gkm) : "memory"); \
    if constexpr (m == 54 - 2 * KL) SIG(ph);                                                \
    SB();                                                                                   \
  })
  {
    f32x4 acc[5];
    s16x4 Bh[100];
    u32x4 A[10];
    float m_, e[12];
    unsigned long long bm;
    unsigned sa[2] = {ROT ? (unsigned)(nt & 1) * 10240u : 0u, ROT ? 10240u - (unsigned)(nt & 1) * 10240u : 10240u};
    asm volatile("" : "+s"(sa[0]), "+s"(sa[1]));
    const unsigned va = (unsigned)(row >> 4) * 20480u + gk * 1024 + l16;
#define LD_A1(x) A[x] = __builtin_amdgcn_raw_buffer_load_b128(rW, va, sa[(x) / 5] + (O_HP + ((x) % 5) * 2048), 16)
    unsigned bb[3][2];
#pragma unroll
    for (int c = 0; c < 6; ++c) {
      bb[c >> 1][c & 1] = (unsigned)gk * 5120u + ((unsigned)lane >> 2) * 160u + ((unsigned)lane & 3u) * 8u + sa[c & 1] * 5u + (c < 2 ? 96u - 96u * gk : c < 4 ? 64u : 96u * gk);
      PIN(bb[c >> 1][c & 1]);
    }
#define RD_BH(h, BLK) Bh[h] = __builtin_amdgcn_ds_read_tr16_b64_v4i16((LAS s16x4*)(lds + bb[kl_j((h) / 2) < 2 ? 0 : kl_j((h) / 2) == 2 ? 1 : 2][kl_i((h) / 2) / 5] + (BLK) + (kl_i((h) / 2) % 5) * 10240 + (kl_j((h) / 2) % 3 == 1 ? 32 : 0) + ((h) & 1) * 2560))
#define BLK1 0
    FENCE();
#pragma unroll
    for (int n = 0; n < 2 * PBE; ++n) RD_BH(n, BLK1);
    FENCE();
    if (DLY1) __builtin_amdgcn_s_sleep(DLY1);
    PRIO(3);
#pragma unroll
    for (int i = 0; i < NA1; ++i) { LD_A1(i); SB(); }
    PRIO(1);
#pragma unroll
    for (int n = 2 * PBE; n < 2 * PB1; ++n) RD_BH(n, BLK1);
    FENCE();
#pragma unroll
    for (int i = NA1; i < NAF; ++i) { LD_A1(i); SB(); }
    u32x2 s3v;
    FENCE();
    if (kh) PRIO(PH1); else PRIO(PHI);
    const int sh = (nt >> 1) == 0 ? 1 : (nt >> 1) == 1 ? -1 : (nt >> 1) == 2 ? 2 : -2;
    const int rr = row + sh, rw = rr & (S_ - 1);
    unsigned keep = rr == rw ? 0xffffffffu : 0u;
    unsigned oh1 = ofr - (unsigned)(row >> 4) * 20480u - (unsigned)l15 * 16u + (unsigned)(rw >> 4) * TS + (unsigned)(rw & 15) * 16u + O_H1;
    asm volatile("" : "+v"(keep), "+v"(oh1));
    asm volatile("" : "+s"(w3s[2]), "+s"(w3s[3]));
    WAIT_ALL(P_L1);
    SB();
    CHECK_A(true, A[0], 0, NAF, LD_A1);
    SB();
#define XG1(m) do { if constexpr ((m) % 2 == 0 && NAF + (m) / 2 < 10) LD_A1(NAF + (m) / 2); if constexpr ((m) == 13) s3v = __builtin_amdgcn_raw_buffer_load_b64(rW, (unsigned)row * 8u, O_ST3, 16); } while (0)
#define XSL(u, s0, n, f0) do { if constexpr ((u) >= (s0) && (((u) - (s0) + 1) * (n)) / (20 - (s0)) > (((u) - (s0)) * (n)) / (20 - (s0))) LD_W3U((f0) + (((u) - (s0)) * (n)) / (20 - (s0))); } while (0)
#define XS1(u) XSL(u, SL1, NL1, NWH)
    KLOOP(true, PB1, BLK1, va, O_HP, XG1, XS1, P_XF1);
    if (__builtin_expect(bad1(s3v[0]), 0)) {
      unsigned spins = 0;
      do {
        __builtin_amdgcn_s_sleep(RSLEEP);
        s3v = __builtin_amdgcn_raw_buffer_load_b64(rW, (unsigned)row * 8u, O_ST3, 16);
        SPIN_STEP();
      } while (bad1(s3v[0]) && spins < RETRY_CAP);
    }
    const float ld = __uint_as_float(s3v[1]), lr = __builtin_amdgcn_rcpf(ld);
    float gca = lr * GC1, gcb = lr * lr * lr * GC2;
    asm volatile("" : "+v"(gca), "+v"(gcb));
    EXCHANGE((unsigned)P_XF1);
    {
      unsigned pk[6];
      if (gk == 0) {
        gelu_s<12>(e, gca, gcb, ld, pk);
        __builtin_amdgcn_raw_buffer_store_b64(u32x2{pk[4] & keep, pk[5] & keep}, rW, oh1 + 1024, 0, 16);
      } else {
        gelu_s<8>(e, gca, gcb, ld, pk);
      }
      st128(widen(pk[0] & keep, pk[1] & keep, pk[2] & keep, pk[3] & keep), rW, oh1 + (unsigned)(g & 1) * 504u, 0, 16);
    }
    FENCE();
    PRIO(0);
    LD_W3R(NWH + NL1, NWH + NL1 + NP0);
    FENCE();
    WAIT_ALL(P_XF1);
#pragma unroll
    for (int i = 0; i < 10; ++i) *(LAS u32x4*)(lds + 20480 + wv * 5120 + (i / 5) * 40960 + (i % 5) * 1024 + l16) = WS2[i];
    SIG(P_G1);
    FENCE();
    LD_W3R(NWH + NL1 + NP0, NWH + NL1 + NP1);
    FENCE();
    const unsigned va2 = (unsigned)(row >> 4) * TS + gk * 1024 + l16;
    u32x2 hpv[3];
#define LD_A2(x) A[x] = __builtin_amdgcn_raw_buffer_load_b128(rW, va2, sa[(x) / 5] + (O_H1 + ((x) % 5) * 2048), 16)
    u32x4 hp4;
    const unsigned ofw = ofr + (unsigned)(g & 1) * 504u;
    hp4 = __builtin_amdgcn_raw_buffer_load_b128(rW, ofw, O_HP, 16);
    hpv[2] = __builtin_amdgcn_raw_buffer_load_b64(rW, gk ? OOB : ofr + 1024, O_HP, 16);
    FENCE();
    WAIT_ALL(P_G1);
    if (DLY2) __builtin_amdgcn_s_sleep(DLY2);
    PRIO(3);
#pragma unroll
    for (int i = 0; i < NA1; ++i) { LD_A2(i); SB(); }
    PRIO(1);
#define BLK2 20480
#pragma unroll
    for (int n = 0; n < 2 * PB2; ++n) RD_BH(n, BLK2);
    FENCE();
#pragma unroll
    for (int i = NA1; i < NAF; ++i) { LD_A2(i); SB(); }
    FENCE();
    if (kh) PRIO(PH2); else PRIO(PHI);
    float hf[12];
    { const u32x4 n_ = widen(hp4[0], hp4[1], hp4[2], hp4[3]); hpv[0] = u32x2{n_[0], n_[1]}; hpv[1] = u32x2{n_[2], n_[3]}; }
#pragma unroll
    for (int j = 0; j < 3; ++j) { hpv[j][0] -= CSH; PIN(hpv[j]); }
    STG(12, hf[i] = ((i & 1) ? hi2f(hpv[i >> 2][(i >> 1) & 1]) : lo2f(hpv[i >> 2][(i >> 1) & 1])) + __uint_as_float(s3v[0]); PIN(hf[i]));
    acc[3] = f32x4{hf[0], hf[1], hf[2], hf[3]}; acc[4] = f32x4{hf[4], hf[5], hf[6], hf[7]};
    acc[2] = gk ? f32x4{0.f, 0.f, 0.f, 0.f} : f32x4{hf[8], hf[9], hf[10], hf[11]};
    PIN(acc[2]); PIN(acc[3]); PIN(acc[4]);
    SB();
    CHECK_A(false, A[0], 0, NAF, LD_A2);
    SB();
#define XG2(m) do { if constexpr ((m) % 2 == 0 && NAF + (m) / 2 < 10) LD_A2(NAF + (m) / 2); } while (0)
#define XS2(u) XSL(u, SL2, NL2, NWH + NL1 + NP1)
    KLOOP(false, PB2, BLK2, va2, O_H1, XG2, XS2, P_XF2);
    EXCHANGE((unsigned)P_XF2);
    unsigned xk[6];
    {
      if (!HFI) STG(12, e[i] += hf[i]; PIN(e[i]));
      STG(3, e[4 * i + 1] = plainv(e[4 * i + 1]); PIN(e[4 * i + 1]));
      STG(6, xk[i] = pack2(e[2 * i], e[2 * i + 1]); PIN(xk[i]));
      st128(widen(xk[0], xk[1], xk[2], xk[3]), rW, ofw, O_X1, 16);
      if (gk == 0) __builtin_amdgcn_raw_buffer_store_b64(u32x2{xk[4], xk[5]}, rW, ofr + 1024, O_X1, 16);
    }
    FENCE();
    PRIO(1);
    SIG(P_G2);
    float v[12];
    float s = 0.f;
#pragma unroll
    for (int j = 0; j < 3; ++j) {
      v[4 * j] = lo2f(xk[2 * j]); v[4 * j + 1] = hi2f(xk[2 * j]); v[4 * j + 2] = lo2f(xk[2 * j + 1]); v[4 * j + 3] = hi2f(xk[2 * j + 1]);
      if (j < 2 || gk == 0) s += (v[4 * j] + v[4 * j + 1]) + (v[4 * j + 2] + v[4 * j + 3]);
    }
    s = swap_add32(swap_add16(s));
    const float mean = s * (gk ? (1.f / 32.f) : (1.f / 48.f));
    float m2 = 0.f;
#pragma unroll
    for (int i = 0; i < 12; ++i) {
      const float dd = v[i] - mean;
      if (i < 8 || gk == 0) m2 += dd * dd;
    }
    m2 = swap_add32(swap_add16(m2));
    {
      u32x2 st = {canon(__float_as_uint(mean)), __float_as_uint(m2)};
      __builtin_amdgcn_raw_buffer_store_b64(st, rW, g == 0 ? (unsigned)(((row >> 4) * 16 + nt * 2 + gk) * 16 + l15) * 8u : OOB, O_ST4, 16);
    }
    FENCE();
    PRIO(0);
    FENCE();
  }
  {
    constexpr int NR3 = 40 - NWH - NL1 - NP1 - NL2;
#define LD_W4(f) W4[f] = __builtin_amdgcn_raw_buffer_load_b128(rW, l16, w4a + (f) * 1024, 16)
    const unsigned va4 = O_H + (unsigned)(m0 >> 4) * TS + (unsigned)k4 * 1024u;
#define A4OFF(x) (((x) / NS) * TS + ((x) % NS) * 1024)
#define LD_A4(x) A4[(x) % AW4] = __builtin_amdgcn_raw_buffer_load_b128(rW, l16, va4 + A4OFF(x), 16)
    const int lane0 = lane;
#define LANES() int lane = lane0; PIN(lane); const int l15 = lane & 15, g = lane >> 4; const unsigned l16 = (unsigned)lane * 16u; \
    const unsigned fa = (unsigned)L_CTL + ((unsigned)lane & 7u) * 4u
#define FLG(a) *(volatile LAS unsigned*)(lds + (a))
    auto g3 = [&](auto KH_, u32x4 (&W4)[35], u32x4 (&A4)[AW4], u32x2 (&x1v)[3]) __attribute__((always_inline)) {
      constexpr int KH = decltype(KH_)::v, NS = 7, NW0 = KH ? 6 : AW4;
      LANES();
      const unsigned hw0 = (unsigned)(m0 >> 4) * TS + (unsigned)l15 * 16u + (unsigned)(g >> 1) * 256u + O_H;
      const unsigned csp = (unsigned)(L_CS3 + wv * 128 + g * 16), f1 = (unsigned)L_CTL + 16u + ((unsigned)lane & 3u) * 4u;
      u32x2 stv[4];
#define LD_ST() _Pragma("unroll") for (int a = 0; a < 4; ++a) stv[a] = __builtin_amdgcn_raw_buffer_load_b64(rW, (unsigned)((((m0 >> 4) + mg) * 16 + 4 * g) * 16 + l15) * 8u, O_ST4 + a * 128, 16)
#define ST_BAD() bad1(max(max(stv[0][0], stv[1][0]), max(stv[2][0], stv[3][0])))
      const u32x4 ones = {0x3f803f80u, 0x3f803f80u, 0x3f803f80u, 0x3f803f80u};
      f32x4 c3a = {0.f, 0.f, 0.f, 0.f}, c3b = {0.f, 0.f, 0.f, 0.f};
#define CS3R(a, b) do { _Pragma("unroll") for (int f_ = (a); f_ < (b); f_ += 2) { c3a = mfma16(W3[f_], ones, c3a); if (!KH || f_ < 20) c3b = mfma16(W3[f_ + 1], ones, c3b); } } while (0)
      PRIO(PHI);
      LD_W3R(40 - NR3, 40);
      FENCE();
      const unsigned so = O_X1 + (unsigned)mt * 81920u + (unsigned)wv * 10240u, d0 = (unsigned)(KH ? L_XB : L_XA) + (unsigned)mg * 10240u;
      CS3R(0, KH ? KC3B : KC3);
      asm volatile("" : "+v"(c3a), "+v"(c3b));
      FENCE();
      WAIT_2(KH ? P_XF2 : P_G2, KH ? P_G2 : P_XF2);
      if (KH ? DLY3B : DLY3) __builtin_amdgcn_s_sleep(KH ? DLY3B : DLY3);
      PIN(W3[38]); PIN(W3[39]);
      if (KH) {
#pragma unroll
        for (int f = 0; f < 10; ++f) W4[f] = W3[21 + 2 * f];
      }
      FENCE();
      PRIO(3);
#define STG_DMA() do { dma<4, 1>(l16, dW, so, d0); dma<4, 1>(l16, dW, so + 4096, d0 + 4096); dma<1, 1>(l16, dW, so + 8192, d0 + 8192); dma<1, 1>(l16, dW, so + 9216, d0 + 9216); } while (0)
      if (KH) {
        asm volatile("buffer_load_dwordx2 %0, %4, %5, %6 offen sc1\n\tbuffer_load_dwordx2 %1, %4, %5, %6 offen offset:128 sc1\n\t"
                     "buffer_load_dwordx2 %2, %4, %5, %6 offen offset:256 sc1\n\tbuffer_load_dwordx2 %3, %4, %5, %6 offen offset:384 sc1"
                     : "=&v"(stv[0]), "=&v"(stv[1]), "=&v"(stv[2]), "=&v"(stv[3]) : "v"((unsigned)((((m0 >> 4) + mg) * 16 + 4 * g) * 16 + l15) * 8u), "s"(dW), "s"(O_ST4) : "memory");
      }
      STG_DMA();
      FENCE();
      PRIO(PHI);
      CS3R(KH ? KC3B : KC3, 40);
      if (l15 == 0) { *(LAS f32x4*)(lds + csp) = c3a; *(LAS f32x4*)(lds + csp + 64) = c3b; }
      FENCE();
      if (KH) {
        asm volatile("s_waitcnt vmcnt(10)" : "+v"(stv[0]), "+v"(stv[1]), "+v"(stv[2]), "+v"(stv[3]));
        if (__builtin_expect(ST_BAD(), 0)) {
          unsigned spins = 0;
          do {
            __builtin_amdgcn_s_sleep(RSLEEP);
            LD_ST();
            SPIN_STEP();
          } while (ST_BAD() && spins < RETRY_CAP);
        }
        const float m0_ = __uint_as_float(stv[0][0]), q0 = __uint_as_float(stv[0][1]), m1 = __uint_as_float(stv[1][0]), q1 = __uint_as_float(stv[1][1]);
        const float m2_ = __uint_as_float(stv[2][0]), q2 = __uint_as_float(stv[2][1]), m3 = __uint_as_float(stv[3][0]), q3 = __uint_as_float(stv[3][1]);
        float sm = 48.f * (m0_ + m2_) + 32.f * (m1 + m3);
        sm = swap_add32(swap_add16(sm));
        const float mean = sm * (1.f / (float)D_);
        const float d0_ = m0_ - mean, d1 = m1 - mean, d2 = m2_ - mean, d3 = m3 - mean;
        float qs = (q0 + q1) + (q2 + q3) + 48.f * (d0_ * d0_ + d2 * d2) + 32.f * (d1 * d1 + d3 * d3);
        qs = swap_add32(swap_add16(qs));
        const float var = rbf(qs * (1.f / (float)D_));
        const float den = rbf(__builtin_amdgcn_sqrtf(rbf(var + 1e-5f))), r = __builtin_amdgcn_rcpf(den);
        float rc1 = r * GC1;
        PIN(rc1);
        if (g == 0) *(volatile LAS f32x4*)(lds + L_ST4 + (mg * 16 + l15) * 16) = f32x4{rbf(mean), rc1, r * r * r * GC2, den};
        SIG(P_SF);
      }
      FENCE();
      VMW(0);
      {
        unsigned spins = 0;
        for (;;) {
          u32x4 a[10];
          float mx = 0.f;
#pragma unroll
          for (int i = 0; i < 10; ++i) a[i] = *(volatile LAS u32x4*)(lds + d0 + i * 1024 + l16);
#pragma unroll
          for (int i = 0; i < 10; ++i) mx = amax3(mx, a[i]);
          if (__builtin_expect(__builtin_amdgcn_ballot_w64(mx >= PTHR) == 0ull, 1)) break;
          SPIN_STEP();
          if (spins > RETRY_CAP) break;
          PRIO(0);
          __builtin_amdgcn_s_sleep(RSLEEP);
          STG_DMA();
          PRIO(PHI);
          asm volatile("s_waitcnt vmcnt(0)" ::: "memory");
        }
        SIG(P_SA);
      }
      FENCE();
      constexpr int NP = KH ? NPH : NPK, F0 = KH ? 10 : 0, NEQ = NP + 6, EQ0 = KH ? EC0 : EA0, EQ1 = KH ? EC1 : EA1, NL0 = F0 + NEQ, NLT = (KH ? 30 : 35) - NL0;
      const unsigned pks = (unsigned)(KH ? L_PB + NPK * 1024 + mg * (NPH * 1024) : wv < 3 ? wv * (NPK * 1024) : L_PB);
      constexpr auto ecnt = [](int q) { return q <= EQ0 ? 0 : q >= EQ1 ? NEQ : ((q - EQ0) * NEQ + EQ1 - EQ0 - 1) / (EQ1 - EQ0); };
#define ESLOT(q) sfor<ecnt(q), ecnt((q) + 1)>([&](auto U_) __attribute__((always_inline)) { constexpr int u = decltype(U_)::v; \
        if constexpr (u < NP) dma<1, 1>(l16, dW, w4a + (F0 + 6 + u) * 1024, pks + u * 1024); else LD_W4(F0 + u - NP); })
      static_assert(3 * NPK <= 32 && NPK + 4 * NPH <= 36 && MV0 + NP <= 20 && NLT <= 19 && NLT >= MV0, "w4");
      sfor<0, 2>([&](auto P_) __attribute__((always_inline)) {
        constexpr int p = decltype(P_)::v, NV = KH ? 12 : 16;
        if (p == 0) WAIT_2(P_SA, P_G2); else WAIT_HALF(1, P_SA);
        f32x4 acc[4];
        PRIO(KH ? (p ? PK1 : PK2) : PK0);
        u32x4 Ar[AD3][2];
        unsigned ab[2] = {l16 + (unsigned)((p ? L_XB : L_XA) + (KH ? hb * 30720 : 0)), l16 + (unsigned)((p ? L_XB : L_XA) + 20480 - (KH ? hb * 10240 : 0))};
        const unsigned sp = (unsigned)(L_ST4 + p * 512 + l15 * 16), fp = (unsigned)(L_CTL + (wv ^ 2) * 4), xs = (unsigned)(32768 + p * 4096) + l16;
        PIN(ab[0]);
        if (KH) PIN(ab[1]);
        f32x4 cs[3], st[2], rx;
        unsigned sf_, sx_;
#define RD_A3(s, h) Ar[(s) % AD3][h] = *(const LAS u32x4*)(lds + (KH ? ab[h] : ab[0] + (h) * 20480) + (s) * 1024)
#pragma unroll
        for (int s = 0; s < AD3 - 1; ++s) { RD_A3(s, 0); RD_A3(s, 1); }
        SB();
        sfor<0, 20>([&](auto S_) __attribute__((always_inline)) {
          constexpr int s = decltype(S_)::v;
          constexpr bool rd = s + AD3 - 1 < 20, T2 = !KH || s < 10;
          mf<s == 0>(acc[0], W3[2 * s], Ar[s % AD3][0]);
          SB();
          if constexpr (rd) RD_A3(s + AD3 - 1, 0);
          else if constexpr (s == 18) cs[0] = *(const LAS f32x4*)(lds + csp);
          else st[0] = *(const volatile LAS f32x4*)(lds + sp + (KH ? hb * 256 : 0));
          SB();
          mf<s == 0>(acc[1], W3[2 * s], Ar[s % AD3][1]);
          SB();
          if constexpr (rd) RD_A3(s + AD3 - 1, 1);
          else if constexpr (s == 18) cs[1] = *(const LAS f32x4*)(lds + csp + 64);
          else st[1] = *(const volatile LAS f32x4*)(lds + sp + 256 - (KH ? hb * 256 : 0));
          SB();
          if constexpr (T2) { mf<s == 0>(acc[2], W3[2 * s + 1], Ar[s % AD3][0]); SB(); }
          if constexpr (p == 0 && s == 15) sf_ = FLG(f1);
          if constexpr (p == 0 && s == 18) NEED_AT(sf_, f1, P_SF);
          if constexpr (p == 0) ESLOT(s);
          if constexpr (p == 1 && s >= 1 && s <= NLT) LD_W4(NL0 + s - 1);
          if constexpr (p == 1 && s == MV0 - 1) VMW(MV0 + 1);
          SB();
          if constexpr (T2) { mf<s == 0>(acc[3], W3[2 * s + 1], Ar[s % AD3][1]); SB(); }
          if constexpr (KH && s + AD3 == 10) { ab[0] -= hb * 20480; ab[1] -= hb * 20480; PIN(ab[0]); PIN(ab[1]); }
          if constexpr (KH && s == 12) { *(volatile LAS f32x4*)(lds + xs + mg * 1024) = acc[3]; SIG(p ? P_S1 : P_S0); }
          if constexpr (KH && s == 14) sx_ = FLG(fp);
          if constexpr (KH && s == 17) { NEED_AT(sx_, fp, p ? P_S1 : P_S0); rx = *(const volatile LAS f32x4*)(lds + xs + (mg ^ 2) * 1024); cs[2] = *(const volatile LAS f32x4*)(lds + csp + (hb ? -192 : 320)); }
          if constexpr (p == 1 && s >= MV0 && s < MV0 + NP) W4[F0 + 6 + s - MV0] = *(const LAS u32x4*)(lds + pks + l16 + (s - MV0) * 1024);
          if constexpr (p == 1 && s >= 1 && s < 4 - KH) {
            static_assert(H0S >= 4 && H0S < 12, "x1");
            const int tu = KH == 0 ? (s == 3 ? 3 : s - 1) : wv < 6 ? 2 : s == 1 ? wv & 1 : wv - 4, ju = KH == 0 ? wv : wv < 6 ? 2 * wv - 6 - s : 4;
            x1v[s - 1] = *(const LAS u32x2*)(lds + (tu < 2 ? L_XA : L_XB - 40960) + tu * 20480 + (nt * 10 + 2 * ju) * 256 + (g >> 1) * 256 + (g & 1) * 8 + l15 * 16);
          }
          if constexpr (p == 1 && s == H0S) SIG(P_H0);
          SB();
        });
        PRIO(KH && p ? PE1 : PE0);
        SIG(p ? P_KB : P_KA);
        if constexpr (KH) asm volatile("s_nop 3" : "+v"(acc[0]), "+v"(acc[1]), "+v"(acc[2])); else asm volatile("s_nop 3" : "+v"(acc[0]), "+v"(acc[1]), "+v"(acc[2]), "+v"(acc[3]));
        {
          float e[NV], x[NV];
          unsigned pk[NV / 2];
#define SQ(i) st[((i) >> 2) & 1]
#define EPS(q) do { if constexpr (p == 0) { ESLOT(20 + (q)); } if constexpr (p == 1 && (q) == HG - 1) sf_ = FLG(fa); \
                    if constexpr (p == 1 && (q) == HG) { NEED_AT(sf_, fa, P_H0); _Pragma("unroll") for (int x = 0; x < NW0; ++x) { LD_A4(x); } } SB(); } while (0)
          if constexpr (KH) STG(4, x[i] = __builtin_fmaf(-st[0][0], cs[2][i], rx[i]); PIN(x[i]));
          STG(NV, e[i] = __builtin_fmaf(-SQ(i)[0], cs[i >> 3][i & 3], acc[i >> 2][i & 3]); PIN(e[i]));
          if constexpr (KH) STG(4, e[8 + i] += x[i]; PIN(e[8 + i]));
          EPS(0);
          STG(NV, x[i] = e[i] * e[i]; PIN(x[i]));
          EPS(1);
          STG(NV, x[i] = __builtin_fmaf(x[i], SQ(i)[2], SQ(i)[1]); PIN(x[i]));
          EPS(2);
          STG(NV, x[i] *= e[i]; PIN(x[i]));
          EPS(3);
          STG(NV, x[i] = __builtin_amdgcn_exp2f(x[i]); PIN(x[i]));
          EPS(4);
          STG(NV, x[i] = __builtin_fmaf(x[i], SQ(i)[3], SQ(i)[3]); PIN(x[i]));
          EPS(5);
          STG(NV, x[i] = __builtin_amdgcn_rcpf(x[i]); PIN(x[i]));
          EPS(6);
          STG(NV, e[i] *= x[i]; PIN(e[i]));
          EPS(7);
          STG(NV / 4, e[4 * i + 1] = codedv(e[4 * i + 1]); PIN(e[4 * i + 1]));
          STG(NV / 2, pk[i] = pack2(e[2 * i], e[2 * i + 1]); PIN(pk[i]));
          const unsigned hw1 = hw0 + (unsigned)((g & 1) ^ (KH ? hb : 0)) * TS;
          st128(widen(pk[0], pk[1], pk[2], pk[3]), rW, hw1, (unsigned)(sb3 + jl) * 512u + 2 * p * TS, 16);
          if constexpr (KH) __builtin_amdgcn_raw_buffer_store_b64(u32x2{pk[4], pk[5]}, rW, hw0 + (unsigned)(g & 1) * 8u, two ? (unsigned)(sb3 + jm) * 512u + (unsigned)(2 * p + hb) * TS : OOB, 16);
          else st128(widen(pk[4], pk[5], pk[6], pk[7]), rW, hw1, (unsigned)(sb3 + jm) * 512u + 2 * p * TS, 16);
          FENCE();
          EPS(8);
        }
      });
    };
#define PB(t) (81920 - (t) * 40960)
    const __amdgpu_buffer_rsrc_t rO = __builtin_amdgcn_make_buffer_rsrc((void*)out, 0, SD2, RS3);
    auto g4 = [&](auto KD_, u32x4 (&W4)[35], u32x4 (&A4)[AW4], u32x2 (&x1v)[3]) __attribute__((always_inline)) {
      constexpr int KD = decltype(KD_)::v, NS = KD == 1 ? 6 : 7, PR = KD ? P41 : P40;
      constexpr int GS = NS + (KD == 0 ? GSA : KD == 1 ? GSB : GSC);
      constexpr auto nreq = [](int x) { int n = AW4; for (int y = 0; y <= x; ++y) { int m = y + AW4 < 4 * NS ? y + AW4 : 4 * NS; if (y < GS && m > 2 * NS) m = 2 * NS; n = m < n + 3 ? (m > n ? m : n) : n + 3; } return n; };
      LANES();
      if constexpr (KD) {
#pragma unroll
        for (int x = 6; x < AW4; ++x) { LD_A4(x); }
      }
      if constexpr (KD == 2) {
#pragma unroll
        for (int f = 30; f < 35; ++f) { LD_W4(f); }
      }
      FENCE();
      PRIO(PR);
      u32x2 xv[2];
      unsigned fl_, fh_, fu_, pu_;
      f32x4 acc[2][5], rs[4], gq;
      float u_[4], y_[4], m4_;
      unsigned long long bm4;
      unsigned pw = (unsigned)(wv * 5120) + l16;
      PIN(pw);
      const unsigned fo0 = (unsigned)((m0 >> 4) * 20480 + ((n0 + 4 * g) >> 3) * 256 + (g & 1) * 8 + l15 * 16), oo0 = (unsigned)((m0 + l15) * D_ + n0 + 4 * g) * 2u;
#define U_LD(r, t, j) xv[r] = __builtin_amdgcn_raw_buffer_load_b64(rW, fo0, O_XO + (unsigned)((t) * 20480 + (j) * 512), 16)
#define U_PC(q, t, j, y)                                                                               \
  do {                                                                                                  \
    if constexpr ((q) == 0) { fu_ = FLG(fa); pu_ = (unsigned)(PB(t) + (j) * 1024) + l16; PIN(pu_); }    \
    if constexpr ((q) == 1) { gq = *(const LAS f32x4*)(lds + L_GB + ((j) * 16 + 4 * g) * 4); }          \
    if constexpr ((q) >= 1 && (q) < 5) { constexpr int i = (q) - 1; u_[i] = (i & 1) ? hi2f(x1v[y][i >> 1]) : lo2f(x1v[y][i >> 1]); PIN(u_[i]); } \
    if constexpr ((q) == 5) NEED_AT(fu_, fa, P_P0 + 1 + (t));                                           \
    if constexpr ((q) >= 6 && (q) < 10) rs[(q) - 6] = *(const volatile LAS f32x4*)(lds + pu_ + ((q) - 6) * 5120); \
    if constexpr ((q) >= 11 && (q) < 27) { constexpr int w = ((q) - 11) >> 1, h = (((q) - 11) & 1) * 2;  \
      u_[h] += rs[w & 3][h]; PIN(u_[h]); u_[h + 1] += rs[w & 3][h + 1]; PIN(u_[h + 1]);                 \
      if constexpr (h == 2 && w < 4) rs[w] = *(const volatile LAS f32x4*)(lds + pu_ + (4 + w) * 5120); } \
    if constexpr ((q) >= 6 && (q) < 10) { constexpr int i = (q) - 6; y_[i] = (i & 1) ? hi2f(xv[0][i >> 1]) : lo2f(xv[0][i >> 1]); PIN(y_[i]); } \
    if constexpr ((q) == 27 || (q) == 28) { constexpr int h = ((q) - 27) * 2; u_[h] = __builtin_fmaf(u_[h], gq[h], y_[h]); PIN(u_[h]); u_[h + 1] = __builtin_fmaf(u_[h + 1], gq[h + 1], y_[h + 1]); PIN(u_[h + 1]); } \
    if constexpr ((q) == 29) __builtin_amdgcn_raw_buffer_store_b64(u32x2{pack2(u_[0], u_[1]), pack2(u_[2], u_[3])}, rO, oo0, (unsigned)((t) * (16 * D_ * 2) + (j) * 32), 0); \
  } while (0)
      {
        unsigned long long b0;
        const float m0_ = amin2(A4[0]);
        CMP_BAD(1, b0, m0_);
        A4[0][0] -= scsh; A4[0][2] -= scsh;
        RETRY_C(A4[0], b0, l16, va4, 0, PR);
        asm volatile("s_nop 0" : "+v"(A4[0]));
      }
      SB();
      const int tc = KD == 0 ? 3 : KD == 1 ? 2 : wv - 4, jc = KD == 0 ? wv : KD == 1 ? 2 * wv - 8 : 4;
      sfor<0, 20 * NS>([&](auto M_) __attribute__((always_inline)) {
        constexpr int m = decltype(M_)::v, x = m / 5, j = m % 5, t = x / NS, s = x % NS, r0 = nreq(x - 2), r1 = nreq(x - 1);
        constexpr bool nx = x + 1 < 4 * NS;
        u32x4& an = A4[(x + 1) % AW4];
        mf<s == 0>(acc[t & 1][j], W4[5 * s + j], A4[x % AW4]);
        SB();
        if constexpr (j == 0) {
          if constexpr (x == GS + 1) NEED_AT(fh_, fa, P_H1);
          if constexpr (r0 < r1) { LD_A4(r0); }
        }
        if constexpr (j == 1 && nx) { m4_ = amin2(an); PIN(m4_); }
        if constexpr (j == 2 && nx) { CMP_BAD(1, bm4, m4_); an[0] -= scsh; }
        if constexpr (j == 3 && nx) { an[2] -= scsh; PIN(an); }
        if constexpr (j == 3 && r0 + 1 < r1) { LD_A4(r0 + 1); }
        if constexpr (j == 4) {
          if constexpr (nx) RETRY_C(an, bm4, l16, va4, A4OFF(x + 1), PR);
          if constexpr (r0 + 2 < r1) { LD_A4(r0 + 2); }
          if constexpr (x == GS) fh_ = FLG(fa);
          if constexpr (x == H1S) SIG(P_H1);
          static_assert(GS + 1 < 2 * NS, "order");
          if constexpr (KD == 0 && t == 1 && s == 0) U_LD(0, 0, wv);
          if constexpr (KD == 0 && t == 2 && s == 3) U_LD(0, 1, wv);
          if constexpr (KD == 2 && t == 2 && s == 3) U_LD(0, wv & 1, 4);
          if constexpr (KD == 1 && t == 3 && s == 3) U_LD(0, 2, jc + 1);
          if constexpr (t == 2 && s == 5) U_LD(1, tc, jc);
          if constexpr (t == 3 && s == NS - 2) fl_ = FLG(L_CTL);
        }
        constexpr bool un = (KD == 0 && t >= 2) || (KD == 2 && t == 3);
        constexpr int wq = 5 * s + j, w0 = un ? W0U : 4;
        if constexpr (t > 0 && wq >= w0 && wq < w0 + 5 * WSP && (wq - w0) % WSP == 0) *(volatile LAS f32x4*)(lds + PB(t - 1) + pw + ((wq - w0) / WSP) * 1024) = acc[(t - 1) & 1][(wq - w0) / WSP];
        if constexpr (t > 0 && wq == w0 + 1 + 4 * WSP) SIG(P_P0 + t);
        static_assert(W0U + 1 + 4 * WSP < 35 && U0 + 30 <= 35, "unit");
        if constexpr (un && KD == 0 && wq >= U0) U_PC(wq - U0, t - 2, wv, t - 2);
        if constexpr (un && KD == 2 && wq >= U0) U_PC(wq - U0, wv & 1, 4, 0);
        SB();
      });
      asm volatile("s_nop 3" : "+v"(acc[1][0]), "+v"(acc[1][1]), "+v"(acc[1][2]), "+v"(acc[1][3]), "+v"(acc[1][4]));
#pragma unroll
      for (int j = 0; j < 4; ++j) *(volatile LAS f32x4*)(lds + L_PB + wv * 4096 + l16 + j * 1024) = acc[1][j];
      NEED_AT(fl_, (unsigned)L_CTL, P_P0 + 3);
      *(volatile LAS f32x4*)(lds + PB(0) + pw) = acc[1][4];
      SIG(P_P0 + 4);
#define UNIT(r, y, t, j, base, str)                                                                        \
  do {                                                                                                  \
    unsigned pq = (unsigned)(base) + l16, oq = oo0 + (unsigned)((t) * (16 * D_ * 2) + (j) * 32);        \
    f32x4 gq_ = *(const LAS f32x4*)(lds + L_GB + ((j) * 16 + 4 * g) * 4);                               \
    f32x2 xa = up2(xv[r][0]), xb = up2(xv[r][1]), ya = up2(x1v[y][0]), yb = up2(x1v[y][1]);             \
    asm volatile("" : "+v"(xa), "+v"(xb), "+v"(ya), "+v"(yb), "+v"(gq_), "+v"(pq), "+v"(oq));           \
    f32x4 r8[8];                                                                                        \
    unsigned spins = 0;                                                                                 \
    for (;;) {                                                         \
      const unsigned fl = FLG(fa);                                                                      \
      _Pragma("unroll") for (int w = 0; w < 8; ++w) r8[w] = *(const volatile LAS f32x4*)(lds + pq + w * (str)); \
      if (__builtin_amdgcn_ballot_w64(fl < (unsigned)(P_P0 + 1 + (t))) == 0ull) break;                  \
      SPIN_STEP();                                                                                      \
      if (spins > SPIN_CAP) break;                                                                      \
    }                                                                                                   \
    const f32x4 sm = ((r8[0] + r8[1]) + (r8[2] + r8[3])) + ((r8[4] + r8[5]) + (r8[6] + r8[7]));         \
    const f32x2 a_ = FMA2(f32x2{sm[0], sm[1]} + ya, (f32x2{gq_[0], gq_[1]}), xa), b_ = FMA2(f32x2{sm[2], sm[3]} + yb, (f32x2{gq_[2], gq_[3]}), xb); \
    __builtin_amdgcn_raw_buffer_store_b64(u32x2{pack2(a_[0], a_[1]), pack2(b_[0], b_[1])}, rO, oq, 0, 0); \
    FENCE();                                                                                            \
  } while (0)
      if constexpr (KD == 0) UNIT(1, 2, 3, wv, L_PB + wv * 1024, 4096);
      else UNIT(1, 1, tc, jc, tc == 3 ? PB(0) : PB(2) + jc * 1024, 5120);
      if constexpr (KD == 1) UNIT(0, 0, 2, jc + 1, PB(2) + jc * 1024 + 1024, 5120);
    };
#define KILL(V, N) _Pragma("unroll") for (int f_ = 0; f_ < (N); ++f_) asm volatile("" : "=v"(V[f_]))
    int arm = wv >> 1;
    if (arm < 2) {
      u32x4 W4[35], A4[AW4];
      u32x2 x1v[3];
      g3(IC<0>{}, W4, A4, x1v);
      g4(IC<0>{}, W4, A4, x1v);
      KILL(W3, 40 - NR3);
    }
    asm volatile("" : "+s"(arm));
    if (arm >= 2) {
      u32x4 W4[35], A4[AW4];
      u32x2 x1v[3];
      g3(IC<1>{}, W4, A4, x1v);
      if (arm == 3) {
        g4(IC<2>{}, W4, A4, x1v);
        KILL(W4, 30);
        KILL(A4, 6);
      }
      asm volatile("" : "+s"(arm));
      if (arm == 2) g4(IC<1>{}, W4, A4, x1v);
    }
  }
}

extern "C" void kernel_launch(void* const* d_in, const int* in_sizes, int n_in, void* d_out, int out_size,
                              void* d_ws, size_t ws_size, hipStream_t stream) {
  const bf16* A_q = (const bf16*)d_in[0];
  const bf16* A_k = (const bf16*)d_in[1];
  const bf16* A_v = (const bf16*)d_in[2];
  const bf16* A_shift_k = (const bf16*)d_in[3];
  const bf16* A_ssm_k = (const bf16*)d_in[4];
  const bf16* A_ssm_D = (const bf16*)d_in[5];
  const bf16* w1 = (const bf16*)d_in[7];
  const bf16* w2 = (const bf16*)d_in[8];
  const bf16* w3 = (const bf16*)d_in[9];
  const bf16* w4 = (const bf16*)d_in[10];
  const bf16* c = (const bf16*)d_in[11];
  const bf16* w_mod = (const bf16*)d_in[12];
  char* ws = (char*)d_ws;
  (void)hipFuncSetAttribute((const void*)k_main, hipFuncAttributeMaxDynamicSharedMemorySize, L_TOT);
  k_main<<<256, 512, L_TOT, stream>>>(A_k, A_v, ws, A_shift_k, A_ssm_k, w_mod, c, w3, w4, A_q, w1, w2, A_ssm_D, (bf16*)d_out);
}
```
